```python
import torch

def _softmax(x, axis=-1):
    return torch.softmax(x, dim=axis)

def _gelu(x):
    return torch.nn.functional.gelu(x, approximate='tanh')

def _silu(x):
    return torch.nn.functional.silu(x)

def _sigmoid(x):
    return torch.sigmoid(x)

def _layernorm(x, axis=-1, eps=1e-05):
    m = x.mean(dim=axis, keepdim=True)
    v = x.var(dim=axis, keepdim=True, unbiased=False)
    return (x - m) / torch.sqrt(v + eps)

def _scatter_add(tgt, idx, src, axis=0):
    return tgt.clone().index_add_(axis, idx, src)

def _segment_sum(src, seg, n):
    out = torch.zeros((n,) + src.shape[1:], dtype=src.dtype)
    return out.index_add_(0, seg, src)

def _pad(x, pw):
    flat = tuple((v for pair in reversed(pw) for v in pair))
    return torch.nn.functional.pad(x, flat)
kernel_name = 'standalone__autoregressive_conv_wavernn__speech'

def setup_inputs():

    _w = lambda a: (lambda t: t.to(torch.bfloat16) if t.is_floating_point() else t)(torch.as_tensor(a))
    B, T, D, K = (2, 16000, 512, 5)
    return {'x': _w((lambda *s: torch.randn(*s, dtype=torch.bfloat16) * 0.05)(B, T, D)), 'w': _w((lambda *s: torch.randn(*s, dtype=torch.bfloat16) * 0.05)(K, D, 2 * D))}

def reference(x, w):
    B, T, D = x.shape
    K = w.shape[0]
    dil = 2
    pad = (K - 1) * dil
    xp = _pad(x, ((0, 0), (pad, 0), (0, 0)))
    h = torch.einsum('btd,de->bte', xp[:, 0:T, :], w[0])
    for i in range(1, K):
        h = h + torch.einsum('btd,de->bte', xp[:, i * dil:i * dil + T, :], w[i])
    f, g = (h[..., :D].clone(), h[..., D:].clone())
    return torch.tanh(f) * _sigmoid(g)
```

```cpp
#include <hip/hip_runtime.h>
#include <hip/hip_bf16.h>
typedef unsigned short u16;
__device__ inline float bf2f(u16 b){ return __uint_as_float(((unsigned)b)<<16); }
__global__ void naive_kernel(const u16* __restrict__ x, const u16* __restrict__ w, __hip_bfloat16* __restrict__ out, int B, int T){
    int e = blockIdx.x*256 + threadIdx.x;
    int row = blockIdx.y;
    int b = row / T, t = row % T;
    float f=0.f,g=0.f;
    for(int i=0;i<5;i++){
        int tt = t + 2*i - 8;
        if(tt<0) continue;
        const u16* xr = x + ((size_t)b*T+tt)*512;
        const u16* wr = w + (size_t)i*512*1024;
        for(int d=0;d<512;d++){
            float xv = bf2f(xr[d]);
            f += xv*bf2f(wr[d*1024+e]);
            g += xv*bf2f(wr[d*1024+512+e]);
        }
    }
    float r = tanhf(f) * (1.f/(1.f+expf(-g)));
    out[(size_t)row*512+e] = __float2bfloat16(r);
}
extern "C" void kernel_launch(void* const* d_in, const int* in_sizes, int n_in,
                              void* d_out, int out_size, void* d_ws, size_t ws_size,
                              hipStream_t stream) {
    naive_kernel<<<dim3(2,32000),256,0,stream>>>((const u16*)d_in[0],(const u16*)d_in[1],(__hip_bfloat16*)d_out,2,16000);
}
```

```cpp
#include <hip/hip_runtime.h>
#include <hip/hip_bf16.h>
typedef unsigned short u16;
typedef unsigned u32x4 __attribute__((ext_vector_type(4)));
typedef unsigned u32x2 __attribute__((ext_vector_type(2)));
typedef float f32x2 __attribute__((ext_vector_type(2)));
typedef __bf16 bf16x2 __attribute__((ext_vector_type(2)));

__device__ __forceinline__ unsigned add_bf16x2(unsigned a, unsigned b) {
    f32x2 v = {__uint_as_float(a << 16) + __uint_as_float(b << 16), __uint_as_float(a & 0xffff0000u) + __uint_as_float(b & 0xffff0000u)};
    return __builtin_bit_cast(unsigned, __builtin_convertvector(v, bf16x2));
}
#define LDW(p) __builtin_nontemporal_load((const u32x4*)(p))
__global__ __launch_bounds__(256) void wprep_kernel(const u16* __restrict__ w, u16* __restrict__ wt) {
    __shared__ __attribute__((aligned(16))) char tile[3 * 32 * 272];
    const unsigned tid = threadIdx.x, lane = tid & 63u, wv = tid >> 6;
    const unsigned bid = blockIdx.x, cb = bid & 7u, ks = (bid >> 3) & 15u, a = bid >> 7;
    if (bid == 0 && tid == 0) {
        unsigned xcc; asm volatile("s_getreg_b32 %0, hwreg(HW_REG_XCC_ID)" : "=s"(xcc));
        *(unsigned*)(wt + 32 * 131072) = xcc & 1u;
    }
    if (bid == 1 && tid < 128) *(unsigned*)(wt + 32 * 131072 + 128 + tid * 32) = 0u;
    const u16* src = w + ((size_t)(2 * a) * 512 + ks * 32 + (tid >> 4)) * 1024 + cb * 128 + (tid & 15u) * 8;
    const unsigned lo = (tid >> 4) * 272 + (tid & 15u) * 16;
    const u32x4 e0 = LDW(src), e1 = LDW(src + 16 * 1024);
    u32x4 o0 = {0, 0, 0, 0}, o1 = {0, 0, 0, 0};
    if (a < 2) { o0 = LDW(src + 512 * 1024); o1 = LDW(src + 528 * 1024); }
    u32x4 s0 = e0, s1 = e1;
    if (a < 2) {
#pragma unroll
        for (int i = 0; i < 4; ++i) { s0[i] = add_bf16x2(e0[i], o0[i]); s1[i] = add_bf16x2(e1[i], o1[i]); }
    }
    *(u32x4*)(tile + lo) = s0;            *(u32x4*)(tile + lo + 16 * 272) = s1;
    *(u32x4*)(tile + 8704 + lo) = e0;     *(u32x4*)(tile + 8704 + lo + 16 * 272) = e1;
    *(u32x4*)(tile + 17408 + lo) = o0;    *(u32x4*)(tile + 17408 + lo + 16 * 272) = o1;
    __syncthreads();
    const unsigned q = lane >> 4, ra = (lane & 15u) >> 2, pb = lane & 3u, sw = ((pb & 1u) << 1) | (pb >> 1);
    const unsigned fg = cb >> 2, z0 = (cb & 3u) * 8u + wv * 2u, S = ks >> 1, s = ks & 1u;
    const unsigned fidx[3] = {S * 12u + fg * 6u + s * 3u + a, 160u + S * 12u + fg * 6u + s * 3u + a, 96u + S * 8u + fg * 4u + s * 2u + a};
    const unsigned addr = (unsigned)(size_t)tile + (8u * q + ra) * 272u + wv * 64u + sw * 8u;
    u32x2 r[12];
    asm volatile("ds_read_b64_tr_b16 %0, %12\n\tds_read_b64_tr_b16 %1, %12 offset:1088\n\tds_read_b64_tr_b16 %2, %12 offset:32\n\tds_read_b64_tr_b16 %3, %12 offset:1120\n\t"
                 "ds_read_b64_tr_b16 %4, %12 offset:8704\n\tds_read_b64_tr_b16 %5, %12 offset:9792\n\tds_read_b64_tr_b16 %6, %12 offset:8736\n\tds_read_b64_tr_b16 %7, %12 offset:9824\n\t"
                 "ds_read_b64_tr_b16 %8, %12 offset:17408\n\tds_read_b64_tr_b16 %9, %12 offset:18496\n\tds_read_b64_tr_b16 %10, %12 offset:17440\n\tds_read_b64_tr_b16 %11, %12 offset:18528\n\t"
                 "s_waitcnt lgkmcnt(0)"
                 : "=&v"(r[0]), "=&v"(r[1]), "=&v"(r[2]), "=&v"(r[3]), "=&v"(r[4]), "=&v"(r[5]), "=&v"(r[6]), "=&v"(r[7]), "=&v"(r[8]), "=&v"(r[9]), "=&v"(r[10]), "=&v"(r[11])
                 : "v"(addr) : "memory");
#pragma unroll
    for (int t = 0; t < 3; ++t) {
        if (t < 2 || a < 2) {
            u16* dst = wt + ((size_t)z0 * 256 + fidx[t]) * 512 + lane * 8;
            const u32x4 va = {r[4*t][0], r[4*t][1], r[4*t+1][0], r[4*t+1][1]}, vb = {r[4*t+2][0], r[4*t+2][1], r[4*t+3][0], r[4*t+3][1]};
            asm volatile("global_store_dwordx4 %0, %1, off sc0 sc1\n\tglobal_store_dwordx4 %2, %3, off sc0 sc1\n\ts_nop 1" :: "v"(dst), "v"(va), "v"(dst + 256 * 512), "v"(vb) : "memory");
        }
    }
}

__global__ __launch_bounds__(256, 1) void conv_main_kernel(const u16* __restrict__ x, u16* wt, u16* __restrict__ out) {
    extern __shared__ __attribute__((aligned(16))) char lds[];
    const unsigned tid = threadIdx.x, lane = tid & 63u;
    const unsigned wv = __builtin_amdgcn_readfirstlane(tid >> 6);
    const unsigned bid = blockIdx.x, xcd = bid & 7u, idx = bid >> 3;
    const unsigned side = xcd & 1u, b = xcd >> 2, p = (idx >> 2) & 1u;
    const unsigned nt = ((xcd >> 1) & 1u) * 4u + (idx & 3u);
    const int u0 = (int)(idx >> 3) * 2000 + (side ? 1492 : 0);
    const unsigned r = lane & 15u, q = lane >> 4, r1 = r + 1u;
    const unsigned ldsb = (unsigned)(size_t)lds;
    unsigned rd0 = ldsb + 128u * r + 16u * (q ^ ((r >> 1) & 7u)), rd1 = rd0 ^ 64u;
    unsigned re0 = ldsb + 128u * r1 + 16u * (q ^ ((r1 >> 1) & 7u)), re1 = re0 ^ 64u;
    const unsigned gpar = wv & 1u, jb = wv >> 1;
    const unsigned rr = 8u * gpar + (lane >> 3), piece = (lane & 7u) ^ ((rr >> 1) & 7u);
    const unsigned od0 = (unsigned)((2 * (u0 - 4 + (int)(32u * rr + 2u * jb + 1u)) + (int)p) * 1024 + (int)(piece * 16u));
    unsigned od1 = od0 + 8192u;
    const unsigned gam = 4u * nt + wv, rs = gam >> 1;
    unsigned xr1 = (unsigned)((2 * (u0 + (int)(16u * gam + 8u * (lane >> 5))) + (int)p) * 1024 + (int)((lane & 31u) * 16u));
    const unsigned vst = ((lane >> 3) & 3u) * 32768u + (gam & 1u) * 16384u + (lane >> 5) * 8192u + rs * 128u + (((lane & 7u) ^ ((rs >> 1) & 7u)) * 16u);
    const unsigned l16 = lane * 16u;
    const unsigned rb = 4u * wv + (lane >> 4);
    unsigned oo = (32u * rb + 2u * ((lane >> 3) & 1u)) * 2048u + nt * 128u + (((lane & 7u) ^ (rb & 7u)) * 16u);
    const unsigned cg = ((q & 1u) << 1) | (q >> 1);
    const unsigned vsw = ldsb + 135168u + r * 256u + (((2u * wv + (cg >> 1)) ^ (r & 7u)) * 16u) + (cg & 1u) * 8u;
    const unsigned m0b = ldsb + 2048u * jb + 1024u * gpar;
    const unsigned wso = (nt * 4u + wv) * 262144u;
    __amdgpu_buffer_rsrc_t srdX = __builtin_amdgcn_make_buffer_rsrc((void*)(x + (size_t)b * 16000 * 512), 0, 16000 * 1024, 0x00020000);
    __amdgpu_buffer_rsrc_t srdW = __builtin_amdgcn_make_buffer_rsrc((void*)wt, 0, 32 * 262144 + 16384, 0x00020000);
    const unsigned grp = (b * 2u + side) * 8u + (idx >> 2);
    __amdgpu_buffer_rsrc_t srdD = __builtin_amdgcn_make_buffer_rsrc((void*)(wt + (5u << 20) + (size_t)grp * 524288u), 0, 1 << 20, 0x00020000);
    const size_t ob = (size_t)(out + ((size_t)b * 16000 + 2 * u0 + p) * 512);
    const unsigned so0 = (unsigned)ob, so1 = (unsigned)(ob >> 32) & 0xffffu;
    asm volatile(
    "s_nop 4\n"
    "s_mov_b32 s55, 0\n"
    "s_mov_b32 s54, %[wso]\n"
    "s_mov_b32 s69, %[m0b]\n"
    "buffer_load_dwordx4 a[80:83], %[l16], %[srdW], s54 offen offset:0\n"
    "buffer_load_dwordx4 a[84:87], %[l16], %[srdW], s54 offen offset:1024\n"
    "buffer_load_dwordx4 a[88:91], %[l16], %[srdW], s54 offen offset:2048\n"
    "buffer_load_dwordx4 a[92:95], %[l16], %[srdW], s54 offen offset:3072\n"
    "s_add_u32 s54, s54, 4096\n"
    "buffer_load_dwordx4 a[96:99], %[l16], %[srdW], s54 offen offset:0\n"
    "buffer_load_dwordx4 a[100:103], %[l16], %[srdW], s54 offen offset:1024\n"
    "buffer_load_dwordx4 a[104:107], %[l16], %[srdW], s54 offen offset:2048\n"
    "buffer_load_dwordx4 a[108:111], %[l16], %[srdW], s54 offen offset:3072\n"
    "s_add_u32 s54, s54, 4096\n"
    "buffer_load_dwordx4 a[112:115], %[l16], %[srdW], s54 offen offset:0\n"
    "buffer_load_dwordx4 a[116:119], %[l16], %[srdW], s54 offen offset:1024\n"
    "buffer_load_dwordx4 a[120:123], %[l16], %[srdW], s54 offen offset:2048\n"
    "buffer_load_dwordx4 a[124:127], %[l16], %[srdW], s54 offen offset:3072\n"
    "s_add_u32 s54, s54, 4096\n"
    "s_add_u32 m0, s69, 0\n"
    "s_add_u32 s57, s55, 0\n"
    "v_subrev_u32 v13, 0x2000, %[od1]\n"
    "buffer_load_dwordx4 v13, %[srdX], s57 offen  lds\n"
    "s_add_u32 m0, s69, 4096\n"
    "s_add_u32 s57, s55, 0\n"
    "buffer_load_dwordx4 %[od1], %[srdX], s57 offen  lds\n"
    "s_add_u32 m0, s69, 8192\n"
    "s_add_u32 s57, s55, 8192\n"
    "buffer_load_dwordx4 %[od1], %[srdX], s57 offen  lds\n"
    "s_add_u32 m0, s69, 12288\n"
    "s_add_u32 s57, s55, 16384\n"
    "buffer_load_dwordx4 %[od1], %[srdX], s57 offen  lds\n"
    "s_add_u32 m0, s69, 16384\n"
    "s_add_u32 s57, s55, 24576\n"
    "buffer_load_dwordx4 %[od1], %[srdX], s57 offen  lds\n"
    "s_add_u32 m0, s69, 20480\n"
    "s_add_u32 s57, s55, 32768\n"
    "buffer_load_dwordx4 %[od1], %[srdX], s57 offen  lds\n"
    "s_add_u32 m0, s69, 24576\n"
    "s_add_u32 s57, s55, 40960\n"
    "buffer_load_dwordx4 %[od1], %[srdX], s57 offen  lds\n"
    "s_add_u32 m0, s69, 28672\n"
    "s_add_u32 s57, s55, 49152\n"
    "buffer_load_dwordx4 %[od1], %[srdX], s57 offen  lds\n"
    "s_add_u32 s69, s69, 0x8000\n"
    "s_and_b32 s69, s69, 0x1ffff\n"
    "s_add_u32 s55, s55, 128\n"
    "v_mov_b32 v22, 0x800000\n"
    "buffer_load_dword v22, v22, %[srdW], 0 offen\n"
    "s_add_u32 m0, s69, 0\n"
    "s_add_u32 s57, s55, 0\n"
    "v_subrev_u32 v13, 0x2000, %[od1]\n"
    "buffer_load_dwordx4 v13, %[srdX], s57 offen  lds\n"
    "s_add_u32 m0, s69, 4096\n"
    "s_add_u32 s57, s55, 0\n"
    "buffer_load_dwordx4 %[od1], %[srdX], s57 offen  lds\n"
    "s_add_u32 m0, s69, 8192\n"
    "s_add_u32 s57, s55, 8192\n"
    "buffer_load_dwordx4 %[od1], %[srdX], s57 offen  lds\n"
    "s_add_u32 m0, s69, 12288\n"
    "s_add_u32 s57, s55, 16384\n"
    "buffer_load_dwordx4 %[od1], %[srdX], s57 offen  lds\n"
    "s_add_u32 m0, s69, 16384\n"
    "s_add_u32 s57, s55, 24576\n"
    "buffer_load_dwordx4 %[od1], %[srdX], s57 offen  lds\n"
    "s_add_u32 m0, s69, 20480\n"
    "s_add_u32 s57, s55, 32768\n"
    "buffer_load_dwordx4 %[od1], %[srdX], s57 offen  lds\n"
    "s_add_u32 m0, s69, 24576\n"
    "s_add_u32 s57, s55, 40960\n"
    "buffer_load_dwordx4 %[od1], %[srdX], s57 offen  lds\n"
    "s_add_u32 m0, s69, 28672\n"
    "s_add_u32 s57, s55, 49152\n"
    "buffer_load_dwordx4 %[od1], %[srdX], s57 offen  lds\n"
    "s_add_u32 s69, s69, 0x8000\n"
    "s_and_b32 s69, s69, 0x1ffff\n"
    "s_add_u32 s55, s55, 128\n"
    "s_getpc_b64 s[74:75]\n"
    "Lpc%=:\n"
    "v_mov_b32 v17, Lend%=-Lpc%=-4\n"
    "v_lshlrev_b32 v16, 3, %[l16]\n"
    "v_add_u32 v16, %[w8k], v16\n"
    "v_min_u32 v18, v16, v17\n"
    "global_load_dword v24, v18, s[74:75]\n"
    "v_add_u32 v16, 0x8000, v16\n"
    "v_min_u32 v18, v16, v17\n"
    "global_load_dword v25, v18, s[74:75]\n"
    "v_add_u32 v16, 0x8000, v16\n"
    "v_min_u32 v18, v16, v17\n"
    "global_load_dword v26, v18, s[74:75]\n"
    "s_mov_b32 s76, %[so0]\n"
    "s_mov_b32 s77, %[so1]\n"
    "s_mov_b32 s78, 0\n"
    "s_mov_b32 s79, 0x00020000\n"
    "s_mov_b32 s72, 0x4038aa3b\n"
    "s_mov_b32 s73, 0xbfb8aa3b\n"
    "s_mov_b32 s61, 0x7fffffff\n"
    "s_mov_b32 s62, 0\n"
    "s_mov_b32 s65, 0xffff0000\n"
    "s_mov_b32 s56, 0\n"
    "s_mov_b32 s71, 0\n"
    "s_lshr_b32 s70, %[w8k], 3\n"
    "v_mbcnt_lo_u32_b32 v13, -1, 0\n"
    "v_mbcnt_hi_u32_b32 v13, -1, v13\n"
    "v_and_b32 v12, 3, v13\n"
    "v_lshl_add_u32 v12, v12, 10, %[flb]\n"
    "v_lshl_add_u32 v14, v13, 4, %[flw]\n"
    "v_mov_b32 v13, 0\n"
    "s_mov_b32 s63, 0\n"
    "ds_write_b32 v14, v13\n"
    "s_waitcnt lgkmcnt(0)\n"
    "s_waitcnt vmcnt(11)\n"
    "s_barrier\n"
    "ds_read_b128 a[32:35], %[rd1] offset:0\n"
    "ds_read_b128 a[0:3], %[rd0] offset:0\n"
    "ds_read_b128 a[36:39], %[rd1] offset:2048\n"
    "ds_read_b128 a[4:7], %[rd0] offset:2048\n"
    "ds_read_b128 a[40:43], %[rd1] offset:4096\n"
    "ds_read_b128 a[8:11], %[rd0] offset:4096\n"
    "ds_read_b128 a[44:47], %[rd1] offset:6144\n"
    "ds_read_b128 a[12:15], %[rd0] offset:6144\n"
    "ds_read_b128 a[48:51], %[rd1] offset:8192\n"
    "ds_read_b128 a[16:19], %[rd0] offset:8192\n"
    "ds_read_b128 a[52:55], %[rd1] offset:10240\n"
    "ds_read_b128 a[20:23], %[rd0] offset:10240\n"
    "ds_read_b128 a[56:59], %[rd1] offset:12288\n"
    "ds_read_b128 a[24:27], %[rd0] offset:12288\n"
    "ds_read_b128 a[60:63], %[rd1] offset:14336\n"
    "ds_read_b128 a[28:31], %[rd0] offset:14336\n"
    "v_readfirstlane_b32 s66, v22\n"
    "s_add_u32 m0, s69, 0\n"
    "s_add_u32 s57, s55, 0\n"
    "v_subrev_u32 v13, 0x2000, %[od1]\n"
    "buffer_load_dwordx4 v13, %[srdX], s57 offen  lds\n"
    "s_add_u32 m0, s69, 4096\n"
    "s_add_u32 s57, s55, 0\n"
    "buffer_load_dwordx4 %[od1], %[srdX], s57 offen  lds\n"
    "s_add_u32 m0, s69, 8192\n"
    "s_add_u32 s57, s55, 8192\n"
    "buffer_load_dwordx4 %[od1], %[srdX], s57 offen  lds\n"
    "s_add_u32 m0, s69, 12288\n"
    "s_add_u32 s57, s55, 16384\n"
    "buffer_load_dwordx4 %[od1], %[srdX], s57 offen  lds\n"
    "s_add_u32 m0, s69, 16384\n"
    "s_add_u32 s57, s55, 24576\n"
    "buffer_load_dwordx4 %[od1], %[srdX], s57 offen  lds\n"
    "s_add_u32 m0, s69, 20480\n"
    "s_add_u32 s57, s55, 32768\n"
    "buffer_load_dwordx4 %[od1], %[srdX], s57 offen  lds\n"
    "s_add_u32 m0, s69, 24576\n"
    "s_add_u32 s57, s55, 40960\n"
    "buffer_load_dwordx4 %[od1], %[srdX], s57 offen  lds\n"
    "s_add_u32 m0, s69, 28672\n"
    "s_add_u32 s57, s55, 49152\n"
    "buffer_load_dwordx4 %[od1], %[srdX], s57 offen  lds\n"
    "s_add_u32 s69, s69, 0x8000\n"
    "s_and_b32 s69, s69, 0x1ffff\n"
    "s_add_u32 s55, s55, 128\n"
    "s_xor_b32 s66, s66, %[side]\n"
    "s_and_b32 s66, s66, 1\n"
    "s_mov_b32 s67, 0\n"
    "s_lshl_b32 s68, s66, 16\n"
    "s_sub_u32 s68, s68, 1040384\n"
    "s_cmp_eq_u32 %[side], 0\n"
    "s_cmov_b32 s68, 1040384\n"
    "s_waitcnt lgkmcnt(8)\n"
    "s_cmp_lg_u32 s67, 0\n"
    "v_mfma_f32_16x16x32_bf16 v[208:211], a[100:103], a[40:43], 0\n"
    "v_mfma_f32_16x16x32_bf16 v[208:211], a[96:99], a[36:39], v[208:211]\n"
    "v_mfma_f32_16x16x32_bf16 v[208:211], a[92:95], a[32:35], v[208:211]\n"
    "v_mfma_f32_16x16x32_bf16 v[208:211], a[88:91], a[8:11], v[208:211]\n"
    "v_mfma_f32_16x16x32_bf16 v[208:211], a[84:87], a[4:7], v[208:211]\n"
    "v_mfma_f32_16x16x32_bf16 v[208:211], a[80:83], a[0:3], v[208:211]\n"
    "v_mfma_f32_16x16x32_bf16 v[212:215], a[104:107], a[0:3], 0\n"
    "v_mfma_f32_16x16x32_bf16 v[212:215], a[108:111], a[4:7], v[212:215]\n"
    "v_mfma_f32_16x16x32_bf16 v[212:215], a[112:115], a[8:11], v[212:215]\n"
    "v_mfma_f32_16x16x32_bf16 v[212:215], a[116:119], a[32:35], v[212:215]\n"
    "v_mfma_f32_16x16x32_bf16 v[212:215], a[120:123], a[36:39], v[212:215]\n"
    "v_mfma_f32_16x16x32_bf16 v[212:215], a[124:127], a[40:43], v[212:215]\n"
    "v_mfma_f32_16x16x32_bf16 v[216:219], a[100:103], a[44:47], 0\n"
    "ds_read_b128 a[0:3], %[rd0] offset:16384\n"
    "v_mfma_f32_16x16x32_bf16 v[216:219], a[96:99], a[40:43], v[216:219]\n"
    "v_mfma_f32_16x16x32_bf16 v[216:219], a[92:95], a[36:39], v[216:219]\n"
    "buffer_load_dwordx4 a[128:131], %[l16], %[srdW], s54 offen offset:0\n"
    "v_mfma_f32_16x16x32_bf16 v[216:219], a[88:91], a[12:15], v[216:219]\n"
    "v_mfma_f32_16x16x32_bf16 v[216:219], a[84:87], a[8:11], v[216:219]\n"
    "ds_read_b128 a[32:35], %[rd1] offset:16384\n"
    "v_mfma_f32_16x16x32_bf16 v[216:219], a[80:83], a[4:7], v[216:219]\n"
    "v_subrev_u32 v76, 0x2000, %[xr1]\n"
    "v_mfma_f32_16x16x32_bf16 v[220:223], a[104:107], a[4:7], 0\n"
    "ds_read_b128 a[4:7], %[rd0] offset:18432\n"
    "v_mfma_f32_16x16x32_bf16 v[220:223], a[108:111], a[8:11], v[220:223]\n"
    "v_mfma_f32_16x16x32_bf16 v[220:223], a[112:115], a[12:15], v[220:223]\n"
    "v_mfma_f32_16x16x32_bf16 v[220:223], a[116:119], a[36:39], v[220:223]\n"
    "ds_read_b128 a[36:39], %[rd1] offset:18432\n"
    "v_mfma_f32_16x16x32_bf16 v[220:223], a[120:123], a[40:43], v[220:223]\n"
    "buffer_load_dwordx4 a[132:135], %[l16], %[srdW], s54 offen offset:1024\n"
    "v_mfma_f32_16x16x32_bf16 v[220:223], a[124:127], a[44:47], v[220:223]\n"
    "s_waitcnt lgkmcnt(8)\n"
    "v_mfma_f32_16x16x32_bf16 v[224:227], a[100:103], a[48:51], 0\n"
    "v_mfma_f32_16x16x32_bf16 v[224:227], a[96:99], a[44:47], v[224:227]\n"
    "ds_read_b128 a[64:67], %[re0] offset:0\n"
    "v_mfma_f32_16x16x32_bf16 v[224:227], a[92:95], a[40:43], v[224:227]\n"
    "buffer_load_dwordx4 a[136:139], %[l16], %[srdW], s54 offen offset:2048\n"
    "v_mfma_f32_16x16x32_bf16 v[224:227], a[88:91], a[16:19], v[224:227]\n"
    "v_mfma_f32_16x16x32_bf16 v[224:227], a[84:87], a[12:15], v[224:227]\n"
    "v_mfma_f32_16x16x32_bf16 v[224:227], a[80:83], a[8:11], v[224:227]\n"
    "v_mfma_f32_16x16x32_bf16 v[228:231], a[104:107], a[8:11], 0\n"
    "ds_read_b128 a[8:11], %[rd0] offset:20480\n"
    "v_mfma_f32_16x16x32_bf16 v[228:231], a[108:111], a[12:15], v[228:231]\n"
    "v_mfma_f32_16x16x32_bf16 v[228:231], a[112:115], a[16:19], v[228:231]\n"
    "v_mfma_f32_16x16x32_bf16 v[228:231], a[116:119], a[40:43], v[228:231]\n"
    "ds_read_b128 a[40:43], %[rd1] offset:20480\n"
    "v_mfma_f32_16x16x32_bf16 v[228:231], a[120:123], a[44:47], v[228:231]\n"
    "buffer_load_dwordx4 a[140:143], %[l16], %[srdW], s54 offen offset:3072\n"
    "v_mfma_f32_16x16x32_bf16 v[228:231], a[124:127], a[48:51], v[228:231]\n"
    "v_mfma_f32_16x16x32_bf16 v[232:235], a[100:103], a[52:55], 0\n"
    "s_add_u32 s54, s54, 4096\n"
    "v_mfma_f32_16x16x32_bf16 v[232:235], a[96:99], a[48:51], v[232:235]\n"
    "ds_read_b128 a[72:75], %[re1] offset:0\n"
    "v_mfma_f32_16x16x32_bf16 v[232:235], a[92:95], a[44:47], v[232:235]\n"
    "buffer_load_dwordx4 a[144:147], %[l16], %[srdW], s54 offen offset:0\n"
    "v_mfma_f32_16x16x32_bf16 v[232:235], a[88:91], a[20:23], v[232:235]\n"
    "v_mfma_f32_16x16x32_bf16 v[232:235], a[84:87], a[16:19], v[232:235]\n"
    "v_mfma_f32_16x16x32_bf16 v[232:235], a[80:83], a[12:15], v[232:235]\n"
    "v_mfma_f32_16x16x32_bf16 v[236:239], a[104:107], a[12:15], 0\n"
    "ds_read_b128 a[12:15], %[rd0] offset:22528\n"
    "v_mfma_f32_16x16x32_bf16 v[236:239], a[108:111], a[16:19], v[236:239]\n"
    "v_mfma_f32_16x16x32_bf16 v[236:239], a[112:115], a[20:23], v[236:239]\n"
    "v_mfma_f32_16x16x32_bf16 v[236:239], a[116:119], a[44:47], v[236:239]\n"
    "ds_read_b128 a[44:47], %[rd1] offset:22528\n"
    "v_mfma_f32_16x16x32_bf16 v[236:239], a[120:123], a[48:51], v[236:239]\n"
    "buffer_load_dwordx4 a[148:151], %[l16], %[srdW], s54 offen offset:1024\n"
    "v_mfma_f32_16x16x32_bf16 v[236:239], a[124:127], a[52:55], v[236:239]\n"
    "s_waitcnt lgkmcnt(10)\n"
    "v_mfma_f32_16x16x32_bf16 v[240:243], a[100:103], a[56:59], 0\n"
    "v_mfma_f32_16x16x32_bf16 v[240:243], a[96:99], a[52:55], v[240:243]\n"
    "ds_read_b128 a[68:71], %[re0] offset:2048\n"
    "v_mfma_f32_16x16x32_bf16 v[240:243], a[92:95], a[48:51], v[240:243]\n"
    "buffer_load_dwordx4 a[152:155], %[l16], %[srdW], s54 offen offset:2048\n"
    "v_mfma_f32_16x16x32_bf16 v[240:243], a[88:91], a[24:27], v[240:243]\n"
    "v_mfma_f32_16x16x32_bf16 v[240:243], a[84:87], a[20:23], v[240:243]\n"
    "v_mfma_f32_16x16x32_bf16 v[240:243], a[80:83], a[16:19], v[240:243]\n"
    "v_mfma_f32_16x16x32_bf16 v[244:247], a[104:107], a[16:19], 0\n"
    "ds_read_b128 a[16:19], %[rd0] offset:24576\n"
    "v_mfma_f32_16x16x32_bf16 v[244:247], a[108:111], a[20:23], v[244:247]\n"
    "v_mfma_f32_16x16x32_bf16 v[244:247], a[112:115], a[24:27], v[244:247]\n"
    "v_mfma_f32_16x16x32_bf16 v[244:247], a[116:119], a[48:51], v[244:247]\n"
    "ds_read_b128 a[48:51], %[rd1] offset:24576\n"
    "v_mfma_f32_16x16x32_bf16 v[244:247], a[120:123], a[52:55], v[244:247]\n"
    "buffer_load_dwordx4 a[156:159], %[l16], %[srdW], s54 offen offset:3072\n"
    "v_mfma_f32_16x16x32_bf16 v[244:247], a[124:127], a[56:59], v[244:247]\n"
    "v_mfma_f32_16x16x32_bf16 v[248:251], a[100:103], a[60:63], 0\n"
    "s_add_u32 s54, s54, 4096\n"
    "v_mfma_f32_16x16x32_bf16 v[248:251], a[96:99], a[56:59], v[248:251]\n"
    "ds_read_b128 a[76:79], %[re1] offset:2048\n"
    "v_mfma_f32_16x16x32_bf16 v[248:251], a[92:95], a[52:55], v[248:251]\n"
    "buffer_load_dwordx4 a[160:163], %[l16], %[srdW], s54 offen offset:0\n"
    "v_mfma_f32_16x16x32_bf16 v[248:251], a[88:91], a[28:31], v[248:251]\n"
    "v_mfma_f32_16x16x32_bf16 v[248:251], a[84:87], a[24:27], v[248:251]\n"
    "v_mfma_f32_16x16x32_bf16 v[248:251], a[80:83], a[20:23], v[248:251]\n"
    "v_mfma_f32_16x16x32_bf16 v[252:255], a[104:107], a[20:23], 0\n"
    "ds_read_b128 a[20:23], %[rd0] offset:26624\n"
    "v_mfma_f32_16x16x32_bf16 v[252:255], a[108:111], a[24:27], v[252:255]\n"
    "v_mfma_f32_16x16x32_bf16 v[252:255], a[112:115], a[28:31], v[252:255]\n"
    "v_mfma_f32_16x16x32_bf16 v[252:255], a[116:119], a[52:55], v[252:255]\n"
    "ds_read_b128 a[52:55], %[rd1] offset:26624\n"
    "v_mfma_f32_16x16x32_bf16 v[252:255], a[120:123], a[56:59], v[252:255]\n"
    "buffer_load_dwordx4 a[164:167], %[l16], %[srdW], s54 offen offset:1024\n"
    "v_mfma_f32_16x16x32_bf16 v[252:255], a[124:127], a[60:63], v[252:255]\n"
    "s_waitcnt lgkmcnt(12)\n"
    "v_mfma_f32_16x16x32_bf16 a[176:179], a[100:103], a[32:35], 0\n"
    "v_mfma_f32_16x16x32_bf16 a[176:179], a[96:99], a[60:63], a[176:179]\n"
    "v_mfma_f32_16x16x32_bf16 a[176:179], a[92:95], a[56:59], a[176:179]\n"
    "buffer_load_dwordx4 a[168:171], %[l16], %[srdW], s54 offen offset:2048\n"
    "v_mfma_f32_16x16x32_bf16 a[176:179], a[88:91], a[0:3], a[176:179]\n"
    "v_mfma_f32_16x16x32_bf16 a[176:179], a[84:87], a[28:31], a[176:179]\n"
    "v_mfma_f32_16x16x32_bf16 a[176:179], a[80:83], a[24:27], a[176:179]\n"
    "v_mfma_f32_16x16x32_bf16 a[180:183], a[104:107], a[24:27], 0\n"
    "ds_read_b128 a[24:27], %[rd0] offset:28672\n"
    "v_mfma_f32_16x16x32_bf16 a[180:183], a[108:111], a[28:31], a[180:183]\n"
    "v_mfma_f32_16x16x32_bf16 a[180:183], a[112:115], a[0:3], a[180:183]\n"
    "v_mfma_f32_16x16x32_bf16 a[180:183], a[116:119], a[56:59], a[180:183]\n"
    "ds_read_b128 a[56:59], %[rd1] offset:28672\n"
    "v_mfma_f32_16x16x32_bf16 a[180:183], a[120:123], a[60:63], a[180:183]\n"
    "buffer_load_dwordx4 a[172:175], %[l16], %[srdW], s54 offen offset:3072\n"
    "v_mfma_f32_16x16x32_bf16 a[180:183], a[124:127], a[32:35], a[180:183]\n"
    "v_mfma_f32_16x16x32_bf16 a[184:187], a[100:103], a[36:39], 0\n"
    "s_add_u32 s54, s54, 4096\n"
    "v_mfma_f32_16x16x32_bf16 a[184:187], a[96:99], a[32:35], a[184:187]\n"
    "s_add_u32 m0, s69, 0\n"
    "s_add_u32 s57, s55, 0\n"
    "v_subrev_u32 v13, 0x2000, %[od1]\n"
    "v_mfma_f32_16x16x32_bf16 a[184:187], a[92:95], a[60:63], a[184:187]\n"
    "buffer_load_dwordx4 v13, %[srdX], s57 offen  lds\n"
    "v_mfma_f32_16x16x32_bf16 a[184:187], a[88:91], a[4:7], a[184:187]\n"
    "s_add_u32 m0, s69, 4096\n"
    "s_add_u32 s57, s55, 0\n"
    "v_mfma_f32_16x16x32_bf16 a[184:187], a[84:87], a[0:3], a[184:187]\n"
    "buffer_load_dwordx4 %[od1], %[srdX], s57 offen  lds\n"
    "v_mfma_f32_16x16x32_bf16 a[184:187], a[80:83], a[28:31], a[184:187]\n"
    "s_add_u32 s64, s56, 0\n"
    "buffer_load_dwordx4 v[32:35], v76, %[srdX], s64 offen\n"
    "v_mfma_f32_16x16x32_bf16 a[188:191], a[104:107], a[28:31], 0\n"
    "ds_read_b128 a[28:31], %[rd0] offset:30720\n"
    "v_mfma_f32_16x16x32_bf16 a[188:191], a[108:111], a[0:3], a[188:191]\n"
    "v_mfma_f32_16x16x32_bf16 a[188:191], a[112:115], a[4:7], a[188:191]\n"
    "v_mfma_f32_16x16x32_bf16 a[188:191], a[116:119], a[60:63], a[188:191]\n"
    "ds_read_b128 a[60:63], %[rd1] offset:30720\n"
    "v_mfma_f32_16x16x32_bf16 a[188:191], a[120:123], a[32:35], a[188:191]\n"
    "v_mfma_f32_16x16x32_bf16 a[188:191], a[124:127], a[36:39], a[188:191]\n"
    "s_waitcnt lgkmcnt(10)\n"
    "v_mfma_f32_16x16x32_bf16 a[192:195], a[100:103], a[40:43], 0\n"
    "v_mfma_f32_16x16x32_bf16 a[192:195], a[96:99], a[36:39], a[192:195]\n"
    "v_mfma_f32_16x16x32_bf16 a[192:195], a[92:95], a[32:35], a[192:195]\n"
    "s_add_u32 s63, s63, 1\n"
    "v_mfma_f32_16x16x32_bf16 a[192:195], a[88:91], a[8:11], a[192:195]\n"
    "v_mfma_f32_16x16x32_bf16 a[192:195], a[84:87], a[4:7], a[192:195]\n"
    "v_mfma_f32_16x16x32_bf16 a[192:195], a[80:83], a[0:3], a[192:195]\n"
    "v_mfma_f32_16x16x32_bf16 a[196:199], a[104:107], a[0:3], 0\n"
    "s_waitcnt vmcnt(26)\n"
    "s_waitcnt lgkmcnt(0)\n"
    "v_mov_b32 v13, s63\n"
    "v_mfma_f32_16x16x32_bf16 a[196:199], a[108:111], a[4:7], a[196:199]\n"
    "ds_write_b32 v14, v13\n"
    "v_mfma_f32_16x16x32_bf16 a[196:199], a[112:115], a[8:11], a[196:199]\n"
    "v_mfma_f32_16x16x32_bf16 a[196:199], a[116:119], a[32:35], a[196:199]\n"
    "v_mfma_f32_16x16x32_bf16 a[196:199], a[120:123], a[36:39], a[196:199]\n"
    "v_mfma_f32_16x16x32_bf16 a[196:199], a[124:127], a[40:43], a[196:199]\n"
    "v_mfma_f32_16x16x32_bf16 a[200:203], a[100:103], a[44:47], 0\n"
    "v_mfma_f32_16x16x32_bf16 a[200:203], a[96:99], a[40:43], a[200:203]\n"
    "v_mfma_f32_16x16x32_bf16 a[200:203], a[92:95], a[36:39], a[200:203]\n"
    "v_mfma_f32_16x16x32_bf16 a[200:203], a[88:91], a[12:15], a[200:203]\n"
    "v_mfma_f32_16x16x32_bf16 a[200:203], a[84:87], a[8:11], a[200:203]\n"
    "ds_read_b32 v13, v12\n"
    "v_mfma_f32_16x16x32_bf16 a[200:203], a[80:83], a[4:7], a[200:203]\n"
    "v_mfma_f32_16x16x32_bf16 a[204:207], a[104:107], a[4:7], 0\n"
    "v_mfma_f32_16x16x32_bf16 a[204:207], a[108:111], a[8:11], a[204:207]\n"
    "v_mfma_f32_16x16x32_bf16 a[204:207], a[112:115], a[12:15], a[204:207]\n"
    "v_mfma_f32_16x16x32_bf16 a[204:207], a[116:119], a[36:39], a[204:207]\n"
    "v_mfma_f32_16x16x32_bf16 a[204:207], a[120:123], a[40:43], a[204:207]\n"
    "s_waitcnt lgkmcnt(0)\n"
    "v_cmp_gt_u32 vcc, s63, v13\n"
    "v_mfma_f32_16x16x32_bf16 a[204:207], a[124:127], a[44:47], a[204:207]\n"
    "s_cbranch_vccnz Ls41%=\n"
    "Lb41%=:\n"
    "v_mfma_f32_16x16x32_bf16 a[208:211], a[100:103], a[48:51], 0\n"
    "ds_read_b128 a[0:3], %[rd0] offset:32768\n"
    "v_mfma_f32_16x16x32_bf16 a[208:211], a[96:99], a[44:47], a[208:211]\n"
    "ds_read_b128 a[32:35], %[rd1] offset:32768\n"
    "v_mfma_f32_16x16x32_bf16 a[208:211], a[92:95], a[40:43], a[208:211]\n"
    "ds_read_b128 a[4:7], %[rd0] offset:34816\n"
    "v_mfma_f32_16x16x32_bf16 a[208:211], a[88:91], a[16:19], a[208:211]\n"
    "ds_read_b128 a[36:39], %[rd1] offset:34816\n"
    "v_mfma_f32_16x16x32_bf16 a[208:211], a[84:87], a[12:15], a[208:211]\n"
    "s_add_u32 s64, s56, 2048\n"
    "buffer_load_dwordx4 v[36:39], v76, %[srdX], s64 offen\n"
    "v_mfma_f32_16x16x32_bf16 a[208:211], a[80:83], a[8:11], a[208:211]\n"
    "v_mfma_f32_16x16x32_bf16 a[212:215], a[104:107], a[8:11], 0\n"
    "ds_read_b128 a[8:11], %[rd0] offset:36864\n"
    "v_mfma_f32_16x16x32_bf16 a[212:215], a[108:111], a[12:15], a[212:215]\n"
    "s_add_u32 m0, s69, 8192\n"
    "s_add_u32 s57, s55, 8192\n"
    "v_mfma_f32_16x16x32_bf16 a[212:215], a[112:115], a[16:19], a[212:215]\n"
    "buffer_load_dwordx4 %[od1], %[srdX], s57 offen  lds\n"
    "v_mfma_f32_16x16x32_bf16 a[212:215], a[116:119], a[40:43], a[212:215]\n"
    "ds_read_b128 a[40:43], %[rd1] offset:36864\n"
    "v_mfma_f32_16x16x32_bf16 a[212:215], a[120:123], a[44:47], a[212:215]\n"
    "v_mfma_f32_16x16x32_bf16 a[212:215], a[124:127], a[48:51], a[212:215]\n"
    "v_mfma_f32_16x16x32_bf16 a[216:219], a[100:103], a[52:55], 0\n"
    "v_mfma_f32_16x16x32_bf16 a[216:219], a[96:99], a[48:51], a[216:219]\n"
    "v_mfma_f32_16x16x32_bf16 a[216:219], a[92:95], a[44:47], a[216:219]\n"
    "s_add_u32 m0, s69, 12288\n"
    "s_add_u32 s57, s55, 16384\n"
    "v_mfma_f32_16x16x32_bf16 a[216:219], a[88:91], a[20:23], a[216:219]\n"
    "buffer_load_dwordx4 %[od1], %[srdX], s57 offen  lds\n"
    "v_mfma_f32_16x16x32_bf16 a[216:219], a[84:87], a[16:19], a[216:219]\n"
    "v_mfma_f32_16x16x32_bf16 a[216:219], a[80:83], a[12:15], a[216:219]\n"
    "v_mfma_f32_16x16x32_bf16 a[220:223], a[104:107], a[12:15], 0\n"
    "ds_read_b128 a[12:15], %[rd0] offset:38912\n"
    "v_mfma_f32_16x16x32_bf16 a[220:223], a[108:111], a[16:19], a[220:223]\n"
    "v_mfma_f32_16x16x32_bf16 a[220:223], a[112:115], a[20:23], a[220:223]\n"
    "v_mfma_f32_16x16x32_bf16 a[220:223], a[116:119], a[44:47], a[220:223]\n"
    "ds_read_b128 a[44:47], %[rd1] offset:38912\n"
    "v_mfma_f32_16x16x32_bf16 a[220:223], a[120:123], a[48:51], a[220:223]\n"
    "v_mfma_f32_16x16x32_bf16 a[220:223], a[124:127], a[52:55], a[220:223]\n"
    "v_mfma_f32_16x16x32_bf16 a[224:227], a[100:103], a[56:59], 0\n"
    "v_mfma_f32_16x16x32_bf16 a[224:227], a[96:99], a[52:55], a[224:227]\n"
    "v_mfma_f32_16x16x32_bf16 a[224:227], a[92:95], a[48:51], a[224:227]\n"
    "s_add_u32 m0, s69, 16384\n"
    "s_add_u32 s57, s55, 24576\n"
    "v_mfma_f32_16x16x32_bf16 a[224:227], a[88:91], a[24:27], a[224:227]\n"
    "buffer_load_dwordx4 %[od1], %[srdX], s57 offen  lds\n"
    "v_mfma_f32_16x16x32_bf16 a[224:227], a[84:87], a[20:23], a[224:227]\n"
    "v_mfma_f32_16x16x32_bf16 a[224:227], a[80:83], a[16:19], a[224:227]\n"
    "v_mfma_f32_16x16x32_bf16 a[228:231], a[104:107], a[16:19], 0\n"
    "ds_read_b128 a[16:19], %[rd0] offset:40960\n"
    "v_mfma_f32_16x16x32_bf16 a[228:231], a[108:111], a[20:23], a[228:231]\n"
    "v_mfma_f32_16x16x32_bf16 a[228:231], a[112:115], a[24:27], a[228:231]\n"
    "v_mfma_f32_16x16x32_bf16 a[228:231], a[116:119], a[48:51], a[228:231]\n"
    "ds_read_b128 a[48:51], %[rd1] offset:40960\n"
    "v_mfma_f32_16x16x32_bf16 a[228:231], a[120:123], a[52:55], a[228:231]\n"
    "s_add_u32 s64, s56, 4096\n"
    "buffer_load_dwordx4 v[40:43], v76, %[srdX], s64 offen\n"
    "v_mfma_f32_16x16x32_bf16 a[228:231], a[124:127], a[56:59], a[228:231]\n"
    "v_mfma_f32_16x16x32_bf16 a[232:235], a[100:103], a[60:63], 0\n"
    "v_mfma_f32_16x16x32_bf16 a[232:235], a[96:99], a[56:59], a[232:235]\n"
    "v_mfma_f32_16x16x32_bf16 a[232:235], a[92:95], a[52:55], a[232:235]\n"
    "s_add_u32 m0, s69, 20480\n"
    "s_add_u32 s57, s55, 32768\n"
    "v_mfma_f32_16x16x32_bf16 a[232:235], a[88:91], a[28:31], a[232:235]\n"
    "buffer_load_dwordx4 %[od1], %[srdX], s57 offen  lds\n"
    "v_mfma_f32_16x16x32_bf16 a[232:235], a[84:87], a[24:27], a[232:235]\n"
    "v_mfma_f32_16x16x32_bf16 a[232:235], a[80:83], a[20:23], a[232:235]\n"
    "v_mfma_f32_16x16x32_bf16 a[236:239], a[104:107], a[20:23], 0\n"
    "ds_read_b128 a[20:23], %[rd0] offset:43008\n"
    "v_mfma_f32_16x16x32_bf16 a[236:239], a[108:111], a[24:27], a[236:239]\n"
    "v_mfma_f32_16x16x32_bf16 a[236:239], a[112:115], a[28:31], a[236:239]\n"
    "v_mfma_f32_16x16x32_bf16 a[236:239], a[116:119], a[52:55], a[236:239]\n"
    "ds_read_b128 a[52:55], %[rd1] offset:43008\n"
    "v_mfma_f32_16x16x32_bf16 a[236:239], a[120:123], a[56:59], a[236:239]\n"
    "v_mfma_f32_16x16x32_bf16 a[236:239], a[124:127], a[60:63], a[236:239]\n"
    "v_mfma_f32_16x16x32_bf16 a[240:243], a[100:103], a[72:75], 0\n"
    "v_mfma_f32_16x16x32_bf16 a[240:243], a[96:99], a[60:63], a[240:243]\n"
    "v_mfma_f32_16x16x32_bf16 a[240:243], a[92:95], a[56:59], a[240:243]\n"
    "s_add_u32 m0, s69, 24576\n"
    "s_add_u32 s57, s55, 40960\n"
    "v_mfma_f32_16x16x32_bf16 a[240:243], a[88:91], a[64:67], a[240:243]\n"
    "buffer_load_dwordx4 %[od1], %[srdX], s57 offen  lds\n"
    "v_mfma_f32_16x16x32_bf16 a[240:243], a[84:87], a[28:31], a[240:243]\n"
    "v_mfma_f32_16x16x32_bf16 a[240:243], a[80:83], a[24:27], a[240:243]\n"
    "v_mfma_f32_16x16x32_bf16 a[244:247], a[104:107], a[24:27], 0\n"
    "ds_read_b128 a[24:27], %[rd0] offset:45056\n"
    "v_mfma_f32_16x16x32_bf16 a[244:247], a[108:111], a[28:31], a[244:247]\n"
    "v_mfma_f32_16x16x32_bf16 a[244:247], a[112:115], a[64:67], a[244:247]\n"
    "v_mfma_f32_16x16x32_bf16 a[244:247], a[116:119], a[56:59], a[244:247]\n"
    "ds_read_b128 a[56:59], %[rd1] offset:45056\n"
    "v_mfma_f32_16x16x32_bf16 a[244:247], a[120:123], a[60:63], a[244:247]\n"
    "v_mfma_f32_16x16x32_bf16 a[244:247], a[124:127], a[72:75], a[244:247]\n"
    "v_mfma_f32_16x16x32_bf16 a[248:251], a[100:103], a[76:79], 0\n"
    "v_mfma_f32_16x16x32_bf16 a[248:251], a[96:99], a[72:75], a[248:251]\n"
    "v_mfma_f32_16x16x32_bf16 a[248:251], a[92:95], a[60:63], a[248:251]\n"
    "s_add_u32 m0, s69, 28672\n"
    "s_add_u32 s57, s55, 49152\n"
    "v_mfma_f32_16x16x32_bf16 a[248:251], a[88:91], a[68:71], a[248:251]\n"
    "buffer_load_dwordx4 %[od1], %[srdX], s57 offen  lds\n"
    "v_mfma_f32_16x16x32_bf16 a[248:251], a[84:87], a[64:67], a[248:251]\n"
    "s_add_u32 s64, s56, 6144\n"
    "buffer_load_dwordx4 v[44:47], v76, %[srdX], s64 offen\n"
    "v_mfma_f32_16x16x32_bf16 a[248:251], a[80:83], a[28:31], a[248:251]\n"
    "s_add_u32 s69, s69, 0x8000\n"
    "s_and_b32 s69, s69, 0x1ffff\n"
    "v_mfma_f32_16x16x32_bf16 a[252:255], a[104:107], a[28:31], 0\n"
    "ds_read_b128 a[28:31], %[rd0] offset:47104\n"
    "v_mfma_f32_16x16x32_bf16 a[252:255], a[108:111], a[64:67], a[252:255]\n"
    "s_add_u32 s55, s55, 128\n"
    "v_mfma_f32_16x16x32_bf16 a[252:255], a[112:115], a[68:71], a[252:255]\n"
    "v_mfma_f32_16x16x32_bf16 a[252:255], a[116:119], a[60:63], a[252:255]\n"
    "ds_read_b128 a[60:63], %[rd1] offset:47104\n"
    "v_mfma_f32_16x16x32_bf16 a[252:255], a[120:123], a[72:75], a[252:255]\n"
    "v_mfma_f32_16x16x32_bf16 a[252:255], a[124:127], a[76:79], a[252:255]\n"
    "s_waitcnt vmcnt(12)\n"
    "s_waitcnt lgkmcnt(8)\n"
    "s_cmp_lg_u32 s67, 0\n"
    "v_mfma_f32_16x16x32_bf16 v[208:211], a[148:151], a[40:43], v[208:211]\n"
    "v_mfma_f32_16x16x32_bf16 v[208:211], a[144:147], a[36:39], v[208:211]\n"
    "v_mfma_f32_16x16x32_bf16 v[208:211], a[140:143], a[32:35], v[208:211]\n"
    "v_mfma_f32_16x16x32_bf16 v[208:211], a[136:139], a[8:11], v[208:211]\n"
    "v_mfma_f32_16x16x32_bf16 v[208:211], a[132:135], a[4:7], v[208:211]\n"
    "v_mfma_f32_16x16x32_bf16 v[208:211], a[128:131], a[0:3], v[208:211]\n"
    "v_mfma_f32_16x16x32_bf16 v[212:215], a[152:155], a[0:3], v[212:215]\n"
    "v_mfma_f32_16x16x32_bf16 v[212:215], a[156:159], a[4:7], v[212:215]\n"
    "v_mfma_f32_16x16x32_bf16 v[212:215], a[160:163], a[8:11], v[212:215]\n"
    "v_mfma_f32_16x16x32_bf16 v[212:215], a[164:167], a[32:35], v[212:215]\n"
    "v_mfma_f32_16x16x32_bf16 v[212:215], a[168:171], a[36:39], v[212:215]\n"
    "v_mfma_f32_16x16x32_bf16 v[212:215], a[172:175], a[40:43], v[212:215]\n"
    "v_mfma_f32_16x16x32_bf16 v[216:219], a[148:151], a[44:47], v[216:219]\n"
    "ds_read_b128 a[0:3], %[rd0] offset:49152\n"
    "v_mfma_f32_16x16x32_bf16 v[216:219], a[144:147], a[40:43], v[216:219]\n"
    "v_mfma_f32_16x16x32_bf16 v[216:219], a[140:143], a[36:39], v[216:219]\n"
    "buffer_load_dwordx4 a[80:83], %[l16], %[srdW], s54 offen offset:0\n"
    "v_mfma_f32_16x16x32_bf16 v[216:219], a[136:139], a[12:15], v[216:219]\n"
    "v_mfma_f32_16x16x32_bf16 v[216:219], a[132:135], a[8:11], v[216:219]\n"
    "ds_read_b128 a[32:35], %[rd1] offset:49152\n"
    "v_mfma_f32_16x16x32_bf16 v[216:219], a[128:131], a[4:7], v[216:219]\n"
    "v_mfma_f32_16x16x32_bf16 v[220:223], a[152:155], a[4:7], v[220:223]\n"
    "ds_read_b128 a[4:7], %[rd0] offset:51200\n"
    "v_mfma_f32_16x16x32_bf16 v[220:223], a[156:159], a[8:11], v[220:223]\n"
    "v_mfma_f32_16x16x32_bf16 v[220:223], a[160:163], a[12:15], v[220:223]\n"
    "v_mfma_f32_16x16x32_bf16 v[220:223], a[164:167], a[36:39], v[220:223]\n"
    "ds_read_b128 a[36:39], %[rd1] offset:51200\n"
    "v_mfma_f32_16x16x32_bf16 v[220:223], a[168:171], a[40:43], v[220:223]\n"
    "buffer_load_dwordx4 a[84:87], %[l16], %[srdW], s54 offen offset:1024\n"
    "v_mfma_f32_16x16x32_bf16 v[220:223], a[172:175], a[44:47], v[220:223]\n"
    "s_waitcnt lgkmcnt(8)\n"
    "v_mfma_f32_16x16x32_bf16 v[224:227], a[148:151], a[48:51], v[224:227]\n"
    "v_mfma_f32_16x16x32_bf16 v[224:227], a[144:147], a[44:47], v[224:227]\n"
    "ds_read_b128 a[64:67], %[re0] offset:32768\n"
    "v_mfma_f32_16x16x32_bf16 v[224:227], a[140:143], a[40:43], v[224:227]\n"
    "buffer_load_dwordx4 a[88:91], %[l16], %[srdW], s54 offen offset:2048\n"
    "v_mfma_f32_16x16x32_bf16 v[224:227], a[136:139], a[16:19], v[224:227]\n"
    "v_mfma_f32_16x16x32_bf16 v[224:227], a[132:135], a[12:15], v[224:227]\n"
    "v_mfma_f32_16x16x32_bf16 v[224:227], a[128:131], a[8:11], v[224:227]\n"
    "v_mfma_f32_16x16x32_bf16 v[228:231], a[152:155], a[8:11], v[228:231]\n"
    "ds_read_b128 a[8:11], %[rd0] offset:53248\n"
    "v_mfma_f32_16x16x32_bf16 v[228:231], a[156:159], a[12:15], v[228:231]\n"
    "v_mfma_f32_16x16x32_bf16 v[228:231], a[160:163], a[16:19], v[228:231]\n"
    "v_mfma_f32_16x16x32_bf16 v[228:231], a[164:167], a[40:43], v[228:231]\n"
    "ds_read_b128 a[40:43], %[rd1] offset:53248\n"
    "v_mfma_f32_16x16x32_bf16 v[228:231], a[168:171], a[44:47], v[228:231]\n"
    "buffer_load_dwordx4 a[92:95], %[l16], %[srdW], s54 offen offset:3072\n"
    "v_mfma_f32_16x16x32_bf16 v[228:231], a[172:175], a[48:51], v[228:231]\n"
    "v_mfma_f32_16x16x32_bf16 v[232:235], a[148:151], a[52:55], v[232:235]\n"
    "s_add_u32 s54, s54, 4096\n"
    "v_mfma_f32_16x16x32_bf16 v[232:235], a[144:147], a[48:51], v[232:235]\n"
    "ds_read_b128 a[72:75], %[re1] offset:32768\n"
    "v_mfma_f32_16x16x32_bf16 v[232:235], a[140:143], a[44:47], v[232:235]\n"
    "buffer_load_dwordx4 a[96:99], %[l16], %[srdW], s54 offen offset:0\n"
    "v_mfma_f32_16x16x32_bf16 v[232:235], a[136:139], a[20:23], v[232:235]\n"
    "v_mfma_f32_16x16x32_bf16 v[232:235], a[132:135], a[16:19], v[232:235]\n"
    "v_mfma_f32_16x16x32_bf16 v[232:235], a[128:131], a[12:15], v[232:235]\n"
    "v_mfma_f32_16x16x32_bf16 v[236:239], a[152:155], a[12:15], v[236:239]\n"
    "ds_read_b128 a[12:15], %[rd0] offset:55296\n"
    "v_mfma_f32_16x16x32_bf16 v[236:239], a[156:159], a[16:19], v[236:239]\n"
    "v_mfma_f32_16x16x32_bf16 v[236:239], a[160:163], a[20:23], v[236:239]\n"
    "v_mfma_f32_16x16x32_bf16 v[236:239], a[164:167], a[44:47], v[236:239]\n"
    "ds_read_b128 a[44:47], %[rd1] offset:55296\n"
    "v_mfma_f32_16x16x32_bf16 v[236:239], a[168:171], a[48:51], v[236:239]\n"
    "buffer_load_dwordx4 a[100:103], %[l16], %[srdW], s54 offen offset:1024\n"
    "v_mfma_f32_16x16x32_bf16 v[236:239], a[172:175], a[52:55], v[236:239]\n"
    "s_waitcnt lgkmcnt(10)\n"
    "v_mfma_f32_16x16x32_bf16 v[240:243], a[148:151], a[56:59], v[240:243]\n"
    "v_mfma_f32_16x16x32_bf16 v[240:243], a[144:147], a[52:55], v[240:243]\n"
    "ds_read_b128 a[68:71], %[re0] offset:34816\n"
    "v_mfma_f32_16x16x32_bf16 v[240:243], a[140:143], a[48:51], v[240:243]\n"
    "buffer_load_dwordx4 a[104:107], %[l16], %[srdW], s54 offen offset:2048\n"
    "v_mfma_f32_16x16x32_bf16 v[240:243], a[136:139], a[24:27], v[240:243]\n"
    "v_mfma_f32_16x16x32_bf16 v[240:243], a[132:135], a[20:23], v[240:243]\n"
    "v_mfma_f32_16x16x32_bf16 v[240:243], a[128:131], a[16:19], v[240:243]\n"
    "v_mfma_f32_16x16x32_bf16 v[244:247], a[152:155], a[16:19], v[244:247]\n"
    "ds_read_b128 a[16:19], %[rd0] offset:57344\n"
    "v_mfma_f32_16x16x32_bf16 v[244:247], a[156:159], a[20:23], v[244:247]\n"
    "v_mfma_f32_16x16x32_bf16 v[244:247], a[160:163], a[24:27], v[244:247]\n"
    "v_mfma_f32_16x16x32_bf16 v[244:247], a[164:167], a[48:51], v[244:247]\n"
    "ds_read_b128 a[48:51], %[rd1] offset:57344\n"
    "v_mfma_f32_16x16x32_bf16 v[244:247], a[168:171], a[52:55], v[244:247]\n"
    "buffer_load_dwordx4 a[108:111], %[l16], %[srdW], s54 offen offset:3072\n"
    "v_mfma_f32_16x16x32_bf16 v[244:247], a[172:175], a[56:59], v[244:247]\n"
    "v_mfma_f32_16x16x32_bf16 v[248:251], a[148:151], a[60:63], v[248:251]\n"
    "s_add_u32 s54, s54, 4096\n"
    "v_mfma_f32_16x16x32_bf16 v[248:251], a[144:147], a[56:59], v[248:251]\n"
    "ds_read_b128 a[76:79], %[re1] offset:34816\n"
    "v_mfma_f32_16x16x32_bf16 v[248:251], a[140:143], a[52:55], v[248:251]\n"
    "buffer_load_dwordx4 a[112:115], %[l16], %[srdW], s54 offen offset:0\n"
    "v_mfma_f32_16x16x32_bf16 v[248:251], a[136:139], a[28:31], v[248:251]\n"
    "v_mfma_f32_16x16x32_bf16 v[248:251], a[132:135], a[24:27], v[248:251]\n"
    "v_mfma_f32_16x16x32_bf16 v[248:251], a[128:131], a[20:23], v[248:251]\n"
    "v_mfma_f32_16x16x32_bf16 v[252:255], a[152:155], a[20:23], v[252:255]\n"
    "ds_read_b128 a[20:23], %[rd0] offset:59392\n"
    "v_mfma_f32_16x16x32_bf16 v[252:255], a[156:159], a[24:27], v[252:255]\n"
    "v_mfma_f32_16x16x32_bf16 v[252:255], a[160:163], a[28:31], v[252:255]\n"
    "v_mfma_f32_16x16x32_bf16 v[252:255], a[164:167], a[52:55], v[252:255]\n"
    "ds_read_b128 a[52:55], %[rd1] offset:59392\n"
    "v_mfma_f32_16x16x32_bf16 v[252:255], a[168:171], a[56:59], v[252:255]\n"
    "buffer_load_dwordx4 a[116:119], %[l16], %[srdW], s54 offen offset:1024\n"
    "v_mfma_f32_16x16x32_bf16 v[252:255], a[172:175], a[60:63], v[252:255]\n"
    "s_waitcnt lgkmcnt(12)\n"
    "v_mfma_f32_16x16x32_bf16 a[176:179], a[148:151], a[32:35], a[176:179]\n"
    "v_mfma_f32_16x16x32_bf16 a[176:179], a[144:147], a[60:63], a[176:179]\n"
    "v_mfma_f32_16x16x32_bf16 a[176:179], a[140:143], a[56:59], a[176:179]\n"
    "buffer_load_dwordx4 a[120:123], %[l16], %[srdW], s54 offen offset:2048\n"
    "v_mfma_f32_16x16x32_bf16 a[176:179], a[136:139], a[0:3], a[176:179]\n"
    "v_mfma_f32_16x16x32_bf16 a[176:179], a[132:135], a[28:31], a[176:179]\n"
    "v_mfma_f32_16x16x32_bf16 a[176:179], a[128:131], a[24:27], a[176:179]\n"
    "v_mfma_f32_16x16x32_bf16 a[180:183], a[152:155], a[24:27], a[180:183]\n"
    "ds_read_b128 a[24:27], %[rd0] offset:61440\n"
    "v_mfma_f32_16x16x32_bf16 a[180:183], a[156:159], a[28:31], a[180:183]\n"
    "v_mfma_f32_16x16x32_bf16 a[180:183], a[160:163], a[0:3], a[180:183]\n"
    "v_mfma_f32_16x16x32_bf16 a[180:183], a[164:167], a[56:59], a[180:183]\n"
    "ds_read_b128 a[56:59], %[rd1] offset:61440\n"
    "v_mfma_f32_16x16x32_bf16 a[180:183], a[168:171], a[60:63], a[180:183]\n"
    "buffer_load_dwordx4 a[124:127], %[l16], %[srdW], s54 offen offset:3072\n"
    "v_mfma_f32_16x16x32_bf16 a[180:183], a[172:175], a[32:35], a[180:183]\n"
    "v_mfma_f32_16x16x32_bf16 a[184:187], a[148:151], a[36:39], a[184:187]\n"
    "s_add_u32 s54, s54, 4096\n"
    "v_mfma_f32_16x16x32_bf16 a[184:187], a[144:147], a[32:35], a[184:187]\n"
    "s_add_u32 m0, s69, 0\n"
    "s_add_u32 s57, s55, 0\n"
    "v_subrev_u32 v13, 0x2000, %[od1]\n"
    "v_mfma_f32_16x16x32_bf16 a[184:187], a[140:143], a[60:63], a[184:187]\n"
    "buffer_load_dwordx4 v13, %[srdX], s57 offen  lds\n"
    "v_mfma_f32_16x16x32_bf16 a[184:187], a[136:139], a[4:7], a[184:187]\n"
    "s_add_u32 m0, s69, 4096\n"
    "s_add_u32 s57, s55, 0\n"
    "s_waitcnt vmcnt(22)\n"
    "v_mfma_f32_16x16x32_bf16 a[184:187], a[132:135], a[0:3], a[184:187]\n"
    "buffer_load_dwordx4 %[od1], %[srdX], s57 offen  lds\n"
    "v_lshlrev_b32 v60, 16, v32\n"
    "v_mfma_f32_16x16x32_bf16 a[184:187], a[128:131], a[28:31], a[184:187]\n"
    "s_add_u32 s64, s56, 0\n"
    "buffer_load_dwordx4 v[48:51], %[xr1], %[srdX], s64 offen\n"
    "v_mfma_f32_16x16x32_bf16 a[188:191], a[152:155], a[28:31], a[188:191]\n"
    "ds_read_b128 a[28:31], %[rd0] offset:63488\n"
    "v_and_b32 v61, s65, v32\n"
    "v_mfma_f32_16x16x32_bf16 a[188:191], a[156:159], a[0:3], a[188:191]\n"
    "v_lshlrev_b32 v62, 16, v33\n"
    "v_and_b32 v63, s65, v33\n"
    "v_mfma_f32_16x16x32_bf16 a[188:191], a[160:163], a[4:7], a[188:191]\n"
    "v_lshlrev_b32 v64, 16, v34\n"
    "v_and_b32 v65, s65, v34\n"
    "v_mfma_f32_16x16x32_bf16 a[188:191], a[164:167], a[60:63], a[188:191]\n"
    "ds_read_b128 a[60:63], %[rd1] offset:63488\n"
    "v_lshlrev_b32 v66, 16, v35\n"
    "v_mfma_f32_16x16x32_bf16 a[188:191], a[168:171], a[32:35], a[188:191]\n"
    "v_and_b32 v67, s65, v35\n"
    "v_mfma_f32_16x16x32_bf16 a[188:191], a[172:175], a[36:39], a[188:191]\n"
    "s_waitcnt lgkmcnt(10)\n"
    "v_mfma_f32_16x16x32_bf16 a[192:195], a[148:151], a[40:43], a[192:195]\n"
    "v_xor_b32 %[rd0], 0x10000, %[rd0]\n"
    "v_mfma_f32_16x16x32_bf16 a[192:195], a[144:147], a[36:39], a[192:195]\n"
    "v_xor_b32 %[rd1], 0x10000, %[rd1]\n"
    "v_mfma_f32_16x16x32_bf16 a[192:195], a[140:143], a[32:35], a[192:195]\n"
    "s_add_u32 s63, s63, 1\n"
    "v_mfma_f32_16x16x32_bf16 a[192:195], a[136:139], a[8:11], a[192:195]\n"
    "v_mfma_f32_16x16x32_bf16 a[192:195], a[132:135], a[4:7], a[192:195]\n"
    "v_mfma_f32_16x16x32_bf16 a[192:195], a[128:131], a[0:3], a[192:195]\n"
    "v_mfma_f32_16x16x32_bf16 a[196:199], a[152:155], a[0:3], a[196:199]\n"
    "s_waitcnt lgkmcnt(0)\n"
    "v_mov_b32 v13, s63\n"
    "v_mfma_f32_16x16x32_bf16 a[196:199], a[156:159], a[4:7], a[196:199]\n"
    "ds_write_b32 v14, v13\n"
    "v_mfma_f32_16x16x32_bf16 a[196:199], a[160:163], a[8:11], a[196:199]\n"
    "v_mfma_f32_16x16x32_bf16 a[196:199], a[164:167], a[32:35], a[196:199]\n"
    "v_mfma_f32_16x16x32_bf16 a[196:199], a[168:171], a[36:39], a[196:199]\n"
    "v_mfma_f32_16x16x32_bf16 a[196:199], a[172:175], a[40:43], a[196:199]\n"
    "v_mfma_f32_16x16x32_bf16 a[200:203], a[148:151], a[44:47], a[200:203]\n"
    "v_xor_b32 %[re0], 0x10000, %[re0]\n"
    "v_mfma_f32_16x16x32_bf16 a[200:203], a[144:147], a[40:43], a[200:203]\n"
    "v_xor_b32 %[re1], 0x10000, %[re1]\n"
    "v_mfma_f32_16x16x32_bf16 a[200:203], a[140:143], a[36:39], a[200:203]\n"
    "v_mfma_f32_16x16x32_bf16 a[200:203], a[136:139], a[12:15], a[200:203]\n"
    "v_mfma_f32_16x16x32_bf16 a[200:203], a[132:135], a[8:11], a[200:203]\n"
    "ds_read_b32 v13, v12\n"
    "v_mfma_f32_16x16x32_bf16 a[200:203], a[128:131], a[4:7], a[200:203]\n"
    "v_mfma_f32_16x16x32_bf16 a[204:207], a[152:155], a[4:7], a[204:207]\n"
    "v_mfma_f32_16x16x32_bf16 a[204:207], a[156:159], a[8:11], a[204:207]\n"
    "v_mfma_f32_16x16x32_bf16 a[204:207], a[160:163], a[12:15], a[204:207]\n"
    "v_mfma_f32_16x16x32_bf16 a[204:207], a[164:167], a[36:39], a[204:207]\n"
    "v_mfma_f32_16x16x32_bf16 a[204:207], a[168:171], a[40:43], a[204:207]\n"
    "s_waitcnt lgkmcnt(0)\n"
    "v_cmp_gt_u32 vcc, s63, v13\n"
    "v_mfma_f32_16x16x32_bf16 a[204:207], a[172:175], a[44:47], a[204:207]\n"
    "s_cbranch_vccnz Ls42%=\n"
    "Lb42%=:\n"
    "v_mfma_f32_16x16x32_bf16 a[208:211], a[148:151], a[48:51], a[208:211]\n"
    "ds_read_b128 a[0:3], %[rd0] offset:0\n"
    "v_mfma_f32_16x16x32_bf16 a[208:211], a[144:147], a[44:47], a[208:211]\n"
    "ds_read_b128 a[32:35], %[rd1] offset:0\n"
    "v_mfma_f32_16x16x32_bf16 a[208:211], a[140:143], a[40:43], a[208:211]\n"
    "ds_read_b128 a[4:7], %[rd0] offset:2048\n"
    "s_waitcnt vmcnt(23)\n"
    "v_mfma_f32_16x16x32_bf16 a[208:211], a[136:139], a[16:19], a[208:211]\n"
    "ds_read_b128 a[36:39], %[rd1] offset:2048\n"
    "v_lshlrev_b32 v68, 16, v36\n"
    "v_mfma_f32_16x16x32_bf16 a[208:211], a[132:135], a[12:15], a[208:211]\n"
    "s_add_u32 s64, s56, 2048\n"
    "buffer_load_dwordx4 v[52:55], %[xr1], %[srdX], s64 offen\n"
    "v_mfma_f32_16x16x32_bf16 a[208:211], a[128:131], a[8:11], a[208:211]\n"
    "v_and_b32 v69, s65, v36\n"
    "v_lshlrev_b32 v70, 16, v37\n"
    "v_mfma_f32_16x16x32_bf16 a[212:215], a[152:155], a[8:11], a[212:215]\n"
    "ds_read_b128 a[8:11], %[rd0] offset:4096\n"
    "v_and_b32 v71, s65, v37\n"
    "v_mfma_f32_16x16x32_bf16 a[212:215], a[156:159], a[12:15], a[212:215]\n"
    "s_add_u32 m0, s69, 8192\n"
    "s_add_u32 s57, s55, 8192\n"
    "v_lshlrev_b32 v72, 16, v38\n"
    "v_mfma_f32_16x16x32_bf16 a[212:215], a[160:163], a[16:19], a[212:215]\n"
    "buffer_load_dwordx4 %[od1], %[srdX], s57 offen  lds\n"
    "v_and_b32 v73, s65, v38\n"
    "v_mfma_f32_16x16x32_bf16 a[212:215], a[164:167], a[40:43], a[212:215]\n"
    "ds_read_b128 a[40:43], %[rd1] offset:4096\n"
    "v_lshlrev_b32 v74, 16, v39\n"
    "v_mfma_f32_16x16x32_bf16 a[212:215], a[168:171], a[44:47], a[212:215]\n"
    "v_and_b32 v75, s65, v39\n"
    "v_sub_f32 v60, v60, v68\n"
    "v_mfma_f32_16x16x32_bf16 a[212:215], a[172:175], a[48:51], a[212:215]\n"
    "v_sub_f32 v61, v61, v69\n"
    "v_sub_f32 v62, v62, v70\n"
    "v_mfma_f32_16x16x32_bf16 a[216:219], a[148:151], a[52:55], a[216:219]\n"
    "v_sub_f32 v63, v63, v71\n"
    "v_sub_f32 v64, v64, v72\n"
    "v_mfma_f32_16x16x32_bf16 a[216:219], a[144:147], a[48:51], a[216:219]\n"
    "v_sub_f32 v65, v65, v73\n"
    "v_sub_f32 v66, v66, v74\n"
    "v_mfma_f32_16x16x32_bf16 a[216:219], a[140:143], a[44:47], a[216:219]\n"
    "s_add_u32 m0, s69, 12288\n"
    "s_add_u32 s57, s55, 16384\n"
    "v_sub_f32 v67, v67, v75\n"
    "v_mfma_f32_16x16x32_bf16 a[216:219], a[136:139], a[20:23], a[216:219]\n"
    "buffer_load_dwordx4 %[od1], %[srdX], s57 offen  lds\n"
    "v_cvt_pk_bf16_f32 v36, v60, v61\n"
    "v_mfma_f32_16x16x32_bf16 a[216:219], a[132:135], a[16:19], a[216:219]\n"
    "v_cvt_pk_bf16_f32 v37, v62, v63\n"
    "v_cvt_pk_bf16_f32 v38, v64, v65\n"
    "v_mfma_f32_16x16x32_bf16 a[216:219], a[128:131], a[12:15], a[216:219]\n"
    "v_cvt_pk_bf16_f32 v39, v66, v67\n"
    "v_mfma_f32_16x16x32_bf16 a[220:223], a[152:155], a[12:15], a[220:223]\n"
    "ds_read_b128 a[12:15], %[rd0] offset:6144\n"
    "v_mfma_f32_16x16x32_bf16 a[220:223], a[156:159], a[16:19], a[220:223]\n"
    "s_add_u32 s64, s71, 262144\n"
    "buffer_store_dwordx4 v[36:39], %[vst], %[srdD], s64 offen sc0 sc1\n"
    "v_mfma_f32_16x16x32_bf16 a[220:223], a[160:163], a[20:23], a[220:223]\n"
    "v_mfma_f32_16x16x32_bf16 a[220:223], a[164:167], a[44:47], a[220:223]\n"
    "ds_read_b128 a[44:47], %[rd1] offset:6144\n"
    "v_mfma_f32_16x16x32_bf16 a[220:223], a[168:171], a[48:51], a[220:223]\n"
    "v_mfma_f32_16x16x32_bf16 a[220:223], a[172:175], a[52:55], a[220:223]\n"
    "v_mfma_f32_16x16x32_bf16 a[224:227], a[148:151], a[56:59], a[224:227]\n"
    "v_mfma_f32_16x16x32_bf16 a[224:227], a[144:147], a[52:55], a[224:227]\n"
    "v_mfma_f32_16x16x32_bf16 a[224:227], a[140:143], a[48:51], a[224:227]\n"
    "s_add_u32 m0, s69, 16384\n"
    "s_add_u32 s57, s55, 24576\n"
    "v_mfma_f32_16x16x32_bf16 a[224:227], a[136:139], a[24:27], a[224:227]\n"
    "buffer_load_dwordx4 %[od1], %[srdX], s57 offen  lds\n"
    "v_mfma_f32_16x16x32_bf16 a[224:227], a[132:135], a[20:23], a[224:227]\n"
    "v_mfma_f32_16x16x32_bf16 a[224:227], a[128:131], a[16:19], a[224:227]\n"
    "v_mfma_f32_16x16x32_bf16 a[228:231], a[152:155], a[16:19], a[228:231]\n"
    "ds_read_b128 a[16:19], %[rd0] offset:8192\n"
    "v_mfma_f32_16x16x32_bf16 a[228:231], a[156:159], a[20:23], a[228:231]\n"
    "v_mfma_f32_16x16x32_bf16 a[228:231], a[160:163], a[24:27], a[228:231]\n"
    "s_waitcnt vmcnt(24)\n"
    "v_lshlrev_b32 v60, 16, v40\n"
    "v_and_b32 v61, s65, v40\n"
    "v_mfma_f32_16x16x32_bf16 a[228:231], a[164:167], a[48:51], a[228:231]\n"
    "ds_read_b128 a[48:51], %[rd1] offset:8192\n"
    "v_lshlrev_b32 v62, 16, v41\n"
    "v_mfma_f32_16x16x32_bf16 a[228:231], a[168:171], a[52:55], a[228:231]\n"
    "s_add_u32 s64, s56, 4096\n"
    "buffer_load_dwordx4 v[56:59], %[xr1], %[srdX], s64 offen\n"
    "v_mfma_f32_16x16x32_bf16 a[228:231], a[172:175], a[56:59], a[228:231]\n"
    "v_and_b32 v63, s65, v41\n"
    "v_lshlrev_b32 v64, 16, v42\n"
    "v_mfma_f32_16x16x32_bf16 a[232:235], a[148:151], a[60:63], a[232:235]\n"
    "v_and_b32 v65, s65, v42\n"
    "v_lshlrev_b32 v66, 16, v43\n"
    "v_mfma_f32_16x16x32_bf16 a[232:235], a[144:147], a[56:59], a[232:235]\n"
    "v_and_b32 v67, s65, v43\n"
    "v_sub_f32 v68, v60, v68\n"
    "v_mfma_f32_16x16x32_bf16 a[232:235], a[140:143], a[52:55], a[232:235]\n"
    "s_add_u32 m0, s69, 20480\n"
    "s_add_u32 s57, s55, 32768\n"
    "v_sub_f32 v69, v61, v69\n"
    "v_mfma_f32_16x16x32_bf16 a[232:235], a[136:139], a[28:31], a[232:235]\n"
    "buffer_load_dwordx4 %[od1], %[srdX], s57 offen  lds\n"
    "v_sub_f32 v70, v62, v70\n"
    "v_mfma_f32_16x16x32_bf16 a[232:235], a[132:135], a[24:27], a[232:235]\n"
    "v_sub_f32 v71, v63, v71\n"
    "v_sub_f32 v72, v64, v72\n"
    "v_mfma_f32_16x16x32_bf16 a[232:235], a[128:131], a[20:23], a[232:235]\n"
    "v_sub_f32 v73, v65, v73\n"
    "v_sub_f32 v74, v66, v74\n"
    "v_mfma_f32_16x16x32_bf16 a[236:239], a[152:155], a[20:23], a[236:239]\n"
    "ds_read_b128 a[20:23], %[rd0] offset:10240\n"
    "v_sub_f32 v75, v67, v75\n"
    "v_mfma_f32_16x16x32_bf16 a[236:239], a[156:159], a[24:27], a[236:239]\n"
    "v_cvt_pk_bf16_f32 v40, v68, v69\n"
    "v_cvt_pk_bf16_f32 v41, v70, v71\n"
    "v_mfma_f32_16x16x32_bf16 a[236:239], a[160:163], a[28:31], a[236:239]\n"
    "v_cvt_pk_bf16_f32 v42, v72, v73\n"
    "v_cvt_pk_bf16_f32 v43, v74, v75\n"
    "v_mfma_f32_16x16x32_bf16 a[236:239], a[164:167], a[52:55], a[236:239]\n"
    "ds_read_b128 a[52:55], %[rd1] offset:10240\n"
    "v_mfma_f32_16x16x32_bf16 a[236:239], a[168:171], a[56:59], a[236:239]\n"
    "s_add_u32 s64, s71, 0\n"
    "buffer_store_dwordx4 v[40:43], %[vst], %[srdD], s64 offen sc0 sc1\n"
    "v_mfma_f32_16x16x32_bf16 a[236:239], a[172:175], a[60:63], a[236:239]\n"
    "v_mfma_f32_16x16x32_bf16 a[240:243], a[148:151], a[72:75], a[240:243]\n"
    "v_mfma_f32_16x16x32_bf16 a[240:243], a[144:147], a[60:63], a[240:243]\n"
    "v_mfma_f32_16x16x32_bf16 a[240:243], a[140:143], a[56:59], a[240:243]\n"
    "s_add_u32 m0, s69, 24576\n"
    "s_add_u32 s57, s55, 40960\n"
    "v_mfma_f32_16x16x32_bf16 a[240:243], a[136:139], a[64:67], a[240:243]\n"
    "buffer_load_dwordx4 %[od1], %[srdX], s57 offen  lds\n"
    "v_mfma_f32_16x16x32_bf16 a[240:243], a[132:135], a[28:31], a[240:243]\n"
    "v_mfma_f32_16x16x32_bf16 a[240:243], a[128:131], a[24:27], a[240:243]\n"
    "v_mfma_f32_16x16x32_bf16 a[244:247], a[152:155], a[24:27], a[244:247]\n"
    "ds_read_b128 a[24:27], %[rd0] offset:12288\n"
    "v_mfma_f32_16x16x32_bf16 a[244:247], a[156:159], a[28:31], a[244:247]\n"
    "v_mfma_f32_16x16x32_bf16 a[244:247], a[160:163], a[64:67], a[244:247]\n"
    "v_mfma_f32_16x16x32_bf16 a[244:247], a[164:167], a[56:59], a[244:247]\n"
    "ds_read_b128 a[56:59], %[rd1] offset:12288\n"
    "v_mfma_f32_16x16x32_bf16 a[244:247], a[168:171], a[60:63], a[244:247]\n"
    "v_mfma_f32_16x16x32_bf16 a[244:247], a[172:175], a[72:75], a[244:247]\n"
    "v_mfma_f32_16x16x32_bf16 a[248:251], a[148:151], a[76:79], a[248:251]\n"
    "v_mfma_f32_16x16x32_bf16 a[248:251], a[144:147], a[72:75], a[248:251]\n"
    "v_mfma_f32_16x16x32_bf16 a[248:251], a[140:143], a[60:63], a[248:251]\n"
    "s_add_u32 m0, s69, 28672\n"
    "s_add_u32 s57, s55, 49152\n"
    "s_waitcnt vmcnt(24)\n"
    "v_mfma_f32_16x16x32_bf16 a[248:251], a[136:139], a[68:71], a[248:251]\n"
    "buffer_load_dwordx4 %[od1], %[srdX], s57 offen  lds\n"
    "v_lshlrev_b32 v68, 16, v44\n"
    "v_mfma_f32_16x16x32_bf16 a[248:251], a[132:135], a[64:67], a[248:251]\n"
    "s_add_u32 s64, s56, 6144\n"
    "buffer_load_dwordx4 v[32:35], %[xr1], %[srdX], s64 offen\n"
    "v_mfma_f32_16x16x32_bf16 a[248:251], a[128:131], a[28:31], a[248:251]\n"
    "s_add_u32 s69, s69, 0x8000\n"
    "s_and_b32 s69, s69, 0x1ffff\n"
    "v_and_b32 v69, s65, v44\n"
    "v_mfma_f32_16x16x32_bf16 a[252:255], a[152:155], a[28:31], a[252:255]\n"
    "ds_read_b128 a[28:31], %[rd0] offset:14336\n"
    "v_lshlrev_b32 v70, 16, v45\n"
    "v_mfma_f32_16x16x32_bf16 a[252:255], a[156:159], a[64:67], a[252:255]\n"
    "s_add_u32 s55, s55, 128\n"
    "v_and_b32 v71, s65, v45\n"
    "v_lshlrev_b32 v72, 16, v46\n"
    "v_mfma_f32_16x16x32_bf16 a[252:255], a[160:163], a[68:71], a[252:255]\n"
    "v_and_b32 v73, s65, v46\n"
    "v_lshlrev_b32 v74, 16, v47\n"
    "v_mfma_f32_16x16x32_bf16 a[252:255], a[164:167], a[60:63], a[252:255]\n"
    "ds_read_b128 a[60:63], %[rd1] offset:14336\n"
    "v_and_b32 v75, s65, v47\n"
    "v_mfma_f32_16x16x32_bf16 a[252:255], a[168:171], a[72:75], a[252:255]\n"
    "v_sub_f32 v60, v60, v68\n"
    "v_sub_f32 v61, v61, v69\n"
    "v_mfma_f32_16x16x32_bf16 a[252:255], a[172:175], a[76:79], a[252:255]\n"
    "s_waitcnt vmcnt(14)\n"
    "s_waitcnt lgkmcnt(8)\n"
    "s_cmp_lg_u32 s67, 0\n"
    "v_mfma_f32_16x16x32_bf16 v[208:211], a[100:103], a[40:43], v[208:211]\n"
    "v_mfma_f32_16x16x32_bf16 v[208:211], a[96:99], a[36:39], v[208:211]\n"
    "v_mfma_f32_16x16x32_bf16 v[208:211], a[92:95], a[32:35], v[208:211]\n"
    "v_mfma_f32_16x16x32_bf16 v[208:211], a[88:91], a[8:11], v[208:211]\n"
    "v_mfma_f32_16x16x32_bf16 v[208:211], a[84:87], a[4:7], v[208:211]\n"
    "v_mfma_f32_16x16x32_bf16 v[208:211], a[80:83], a[0:3], v[208:211]\n"
    "v_mfma_f32_16x16x32_bf16 v[212:215], a[104:107], a[0:3], v[212:215]\n"
    "v_mfma_f32_16x16x32_bf16 v[212:215], a[108:111], a[4:7], v[212:215]\n"
    "v_mfma_f32_16x16x32_bf16 v[212:215], a[112:115], a[8:11], v[212:215]\n"
    "v_mfma_f32_16x16x32_bf16 v[212:215], a[116:119], a[32:35], v[212:215]\n"
    "v_mfma_f32_16x16x32_bf16 v[212:215], a[120:123], a[36:39], v[212:215]\n"
    "v_mfma_f32_16x16x32_bf16 v[212:215], a[124:127], a[40:43], v[212:215]\n"
    "v_mfma_f32_16x16x32_bf16 v[216:219], a[100:103], a[44:47], v[216:219]\n"
    "ds_read_b128 a[0:3], %[rd0] offset:16384\n"
    "v_sub_f32 v62, v62, v70\n"
    "v_mfma_f32_16x16x32_bf16 v[216:219], a[96:99], a[40:43], v[216:219]\n"
    "v_sub_f32 v63, v63, v71\n"
    "v_sub_f32 v64, v64, v72\n"
    "v_mfma_f32_16x16x32_bf16 v[216:219], a[92:95], a[36:39], v[216:219]\n"
    "buffer_load_dwordx4 a[128:131], %[l16], %[srdW], s54 offen offset:0\n"
    "v_sub_f32 v65, v65, v73\n"
    "v_mfma_f32_16x16x32_bf16 v[216:219], a[88:91], a[12:15], v[216:219]\n"
    "v_sub_f32 v66, v66, v74\n"
    "v_sub_f32 v67, v67, v75\n"
    "v_mfma_f32_16x16x32_bf16 v[216:219], a[84:87], a[8:11], v[216:219]\n"
    "ds_read_b128 a[32:35], %[rd1] offset:16384\n"
    "v_cvt_pk_bf16_f32 v44, v60, v61\n"
    "v_mfma_f32_16x16x32_bf16 v[216:219], a[80:83], a[4:7], v[216:219]\n"
    "v_cvt_pk_bf16_f32 v45, v62, v63\n"
    "v_cvt_pk_bf16_f32 v46, v64, v65\n"
    "v_mfma_f32_16x16x32_bf16 v[220:223], a[104:107], a[4:7], v[220:223]\n"
    "ds_read_b128 a[4:7], %[rd0] offset:18432\n"
    "v_cvt_pk_bf16_f32 v47, v66, v67\n"
    "v_mfma_f32_16x16x32_bf16 v[220:223], a[108:111], a[8:11], v[220:223]\n"
    "v_mfma_f32_16x16x32_bf16 v[220:223], a[112:115], a[12:15], v[220:223]\n"
    "v_mfma_f32_16x16x32_bf16 v[220:223], a[116:119], a[36:39], v[220:223]\n"
    "ds_read_b128 a[36:39], %[rd1] offset:18432\n"
    "v_mfma_f32_16x16x32_bf16 v[220:223], a[120:123], a[40:43], v[220:223]\n"
    "buffer_load_dwordx4 a[132:135], %[l16], %[srdW], s54 offen offset:1024\n"
    "v_mfma_f32_16x16x32_bf16 v[220:223], a[124:127], a[44:47], v[220:223]\n"
    "s_waitcnt lgkmcnt(8)\n"
    "v_mfma_f32_16x16x32_bf16 v[224:227], a[100:103], a[48:51], v[224:227]\n"
    "v_mfma_f32_16x16x32_bf16 v[224:227], a[96:99], a[44:47], v[224:227]\n"
    "ds_read_b128 a[64:67], %[re0] offset:0\n"
    "v_mfma_f32_16x16x32_bf16 v[224:227], a[92:95], a[40:43], v[224:227]\n"
    "buffer_load_dwordx4 a[136:139], %[l16], %[srdW], s54 offen offset:2048\n"
    "v_mfma_f32_16x16x32_bf16 v[224:227], a[88:91], a[16:19], v[224:227]\n"
    "v_mfma_f32_16x16x32_bf16 v[224:227], a[84:87], a[12:15], v[224:227]\n"
    "v_mfma_f32_16x16x32_bf16 v[224:227], a[80:83], a[8:11], v[224:227]\n"
    "v_mfma_f32_16x16x32_bf16 v[228:231], a[104:107], a[8:11], v[228:231]\n"
    "ds_read_b128 a[8:11], %[rd0] offset:20480\n"
    "v_mfma_f32_16x16x32_bf16 v[228:231], a[108:111], a[12:15], v[228:231]\n"
    "v_mfma_f32_16x16x32_bf16 v[228:231], a[112:115], a[16:19], v[228:231]\n"
    "v_mfma_f32_16x16x32_bf16 v[228:231], a[116:119], a[40:43], v[228:231]\n"
    "ds_read_b128 a[40:43], %[rd1] offset:20480\n"
    "v_mfma_f32_16x16x32_bf16 v[228:231], a[120:123], a[44:47], v[228:231]\n"
    "buffer_load_dwordx4 a[140:143], %[l16], %[srdW], s54 offen offset:3072\n"
    "v_mfma_f32_16x16x32_bf16 v[228:231], a[124:127], a[48:51], v[228:231]\n"
    "v_mfma_f32_16x16x32_bf16 v[232:235], a[100:103], a[52:55], v[232:235]\n"
    "s_add_u32 s54, s54, 4096\n"
    "v_mfma_f32_16x16x32_bf16 v[232:235], a[96:99], a[48:51], v[232:235]\n"
    "ds_read_b128 a[72:75], %[re1] offset:0\n"
    "v_mfma_f32_16x16x32_bf16 v[232:235], a[92:95], a[44:47], v[232:235]\n"
    "buffer_load_dwordx4 a[144:147], %[l16], %[srdW], s54 offen offset:0\n"
    "v_mfma_f32_16x16x32_bf16 v[232:235], a[88:91], a[20:23], v[232:235]\n"
    "v_mfma_f32_16x16x32_bf16 v[232:235], a[84:87], a[16:19], v[232:235]\n"
    "v_mfma_f32_16x16x32_bf16 v[232:235], a[80:83], a[12:15], v[232:235]\n"
    "v_mfma_f32_16x16x32_bf16 v[236:239], a[104:107], a[12:15], v[236:239]\n"
    "ds_read_b128 a[12:15], %[rd0] offset:22528\n"
    "v_mfma_f32_16x16x32_bf16 v[236:239], a[108:111], a[16:19], v[236:239]\n"
    "v_mfma_f32_16x16x32_bf16 v[236:239], a[112:115], a[20:23], v[236:239]\n"
    "v_mfma_f32_16x16x32_bf16 v[236:239], a[116:119], a[44:47], v[236:239]\n"
    "ds_read_b128 a[44:47], %[rd1] offset:22528\n"
    "v_mfma_f32_16x16x32_bf16 v[236:239], a[120:123], a[48:51], v[236:239]\n"
    "buffer_load_dwordx4 a[148:151], %[l16], %[srdW], s54 offen offset:1024\n"
    "v_mfma_f32_16x16x32_bf16 v[236:239], a[124:127], a[52:55], v[236:239]\n"
    "s_waitcnt lgkmcnt(10)\n"
    "v_mfma_f32_16x16x32_bf16 v[240:243], a[100:103], a[56:59], v[240:243]\n"
    "v_mfma_f32_16x16x32_bf16 v[240:243], a[96:99], a[52:55], v[240:243]\n"
    "ds_read_b128 a[68:71], %[re0] offset:2048\n"
    "v_mfma_f32_16x16x32_bf16 v[240:243], a[92:95], a[48:51], v[240:243]\n"
    "buffer_load_dwordx4 a[152:155], %[l16], %[srdW], s54 offen offset:2048\n"
    "v_mfma_f32_16x16x32_bf16 v[240:243], a[88:91], a[24:27], v[240:243]\n"
    "v_mfma_f32_16x16x32_bf16 v[240:243], a[84:87], a[20:23], v[240:243]\n"
    "v_mfma_f32_16x16x32_bf16 v[240:243], a[80:83], a[16:19], v[240:243]\n"
    "v_mfma_f32_16x16x32_bf16 v[244:247], a[104:107], a[16:19], v[244:247]\n"
    "ds_read_b128 a[16:19], %[rd0] offset:24576\n"
    "v_mfma_f32_16x16x32_bf16 v[244:247], a[108:111], a[20:23], v[244:247]\n"
    "v_mfma_f32_16x16x32_bf16 v[244:247], a[112:115], a[24:27], v[244:247]\n"
    "v_mfma_f32_16x16x32_bf16 v[244:247], a[116:119], a[48:51], v[244:247]\n"
    "ds_read_b128 a[48:51], %[rd1] offset:24576\n"
    "v_mfma_f32_16x16x32_bf16 v[244:247], a[120:123], a[52:55], v[244:247]\n"
    "buffer_load_dwordx4 a[156:159], %[l16], %[srdW], s54 offen offset:3072\n"
    "v_mfma_f32_16x16x32_bf16 v[244:247], a[124:127], a[56:59], v[244:247]\n"
    "v_mfma_f32_16x16x32_bf16 v[248:251], a[100:103], a[60:63], v[248:251]\n"
    "s_add_u32 s54, s54, 4096\n"
    "v_mfma_f32_16x16x32_bf16 v[248:251], a[96:99], a[56:59], v[248:251]\n"
    "ds_read_b128 a[76:79], %[re1] offset:2048\n"
    "v_mfma_f32_16x16x32_bf16 v[248:251], a[92:95], a[52:55], v[248:251]\n"
    "buffer_load_dwordx4 a[160:163], %[l16], %[srdW], s54 offen offset:0\n"
    "v_mfma_f32_16x16x32_bf16 v[248:251], a[88:91], a[28:31], v[248:251]\n"
    "v_mfma_f32_16x16x32_bf16 v[248:251], a[84:87], a[24:27], v[248:251]\n"
    "v_mfma_f32_16x16x32_bf16 v[248:251], a[80:83], a[20:23], v[248:251]\n"
    "v_mfma_f32_16x16x32_bf16 v[252:255], a[104:107], a[20:23], v[252:255]\n"
    "ds_read_b128 a[20:23], %[rd0] offset:26624\n"
    "v_mfma_f32_16x16x32_bf16 v[252:255], a[108:111], a[24:27], v[252:255]\n"
    "v_mfma_f32_16x16x32_bf16 v[252:255], a[112:115], a[28:31], v[252:255]\n"
    "v_mfma_f32_16x16x32_bf16 v[252:255], a[116:119], a[52:55], v[252:255]\n"
    "ds_read_b128 a[52:55], %[rd1] offset:26624\n"
    "v_mfma_f32_16x16x32_bf16 v[252:255], a[120:123], a[56:59], v[252:255]\n"
    "buffer_load_dwordx4 a[164:167], %[l16], %[srdW], s54 offen offset:1024\n"
    "v_mfma_f32_16x16x32_bf16 v[252:255], a[124:127], a[60:63], v[252:255]\n"
    "s_waitcnt lgkmcnt(12)\n"
    "v_mfma_f32_16x16x32_bf16 a[176:179], a[100:103], a[32:35], a[176:179]\n"
    "v_mfma_f32_16x16x32_bf16 a[176:179], a[96:99], a[60:63], a[176:179]\n"
    "v_mfma_f32_16x16x32_bf16 a[176:179], a[92:95], a[56:59], a[176:179]\n"
    "buffer_load_dwordx4 a[168:171], %[l16], %[srdW], s54 offen offset:2048\n"
    "v_mfma_f32_16x16x32_bf16 a[176:179], a[88:91], a[0:3], a[176:179]\n"
    "v_mfma_f32_16x16x32_bf16 a[176:179], a[84:87], a[28:31], a[176:179]\n"
    "v_mfma_f32_16x16x32_bf16 a[176:179], a[80:83], a[24:27], a[176:179]\n"
    "v_mfma_f32_16x16x32_bf16 a[180:183], a[104:107], a[24:27], a[180:183]\n"
    "ds_read_b128 a[24:27], %[rd0] offset:28672\n"
    "v_mfma_f32_16x16x32_bf16 a[180:183], a[108:111], a[28:31], a[180:183]\n"
    "v_mfma_f32_16x16x32_bf16 a[180:183], a[112:115], a[0:3], a[180:183]\n"
    "v_mfma_f32_16x16x32_bf16 a[180:183], a[116:119], a[56:59], a[180:183]\n"
    "ds_read_b128 a[56:59], %[rd1] offset:28672\n"
    "v_mfma_f32_16x16x32_bf16 a[180:183], a[120:123], a[60:63], a[180:183]\n"
    "buffer_load_dwordx4 a[172:175], %[l16], %[srdW], s54 offen offset:3072\n"
    "v_mfma_f32_16x16x32_bf16 a[180:183], a[124:127], a[32:35], a[180:183]\n"
    "v_mfma_f32_16x16x32_bf16 a[184:187], a[100:103], a[36:39], a[184:187]\n"
    "s_add_u32 s54, s54, 4096\n"
    "v_mfma_f32_16x16x32_bf16 a[184:187], a[96:99], a[32:35], a[184:187]\n"
    "s_add_u32 m0, s69, 0\n"
    "s_add_u32 s57, s55, 0\n"
    "v_subrev_u32 v13, 0x2000, %[od1]\n"
    "v_mfma_f32_16x16x32_bf16 a[184:187], a[92:95], a[60:63], a[184:187]\n"
    "buffer_load_dwordx4 v13, %[srdX], s57 offen  lds\n"
    "v_mfma_f32_16x16x32_bf16 a[184:187], a[88:91], a[4:7], a[184:187]\n"
    "s_add_u32 m0, s69, 4096\n"
    "s_add_u32 s57, s55, 0\n"
    "v_mfma_f32_16x16x32_bf16 a[184:187], a[84:87], a[0:3], a[184:187]\n"
    "buffer_load_dwordx4 %[od1], %[srdX], s57 offen  lds\n"
    "v_mfma_f32_16x16x32_bf16 a[184:187], a[80:83], a[28:31], a[184:187]\n"
    "s_add_u32 s64, s56, 8192\n"
    "buffer_load_dwordx4 v[36:39], %[xr1], %[srdX], s64 offen\n"
    "v_mfma_f32_16x16x32_bf16 a[188:191], a[104:107], a[28:31], a[188:191]\n"
    "ds_read_b128 a[28:31], %[rd0] offset:30720\n"
    "v_mfma_f32_16x16x32_bf16 a[188:191], a[108:111], a[0:3], a[188:191]\n"
    "s_add_u32 s64, s71, 264192\n"
    "buffer_store_dwordx4 v[44:47], %[vst], %[srdD], s64 offen sc0 sc1\n"
    "v_mfma_f32_16x16x32_bf16 a[188:191], a[112:115], a[4:7], a[188:191]\n"
    "s_waitcnt vmcnt(27)\n"
    "v_lshlrev_b32 v60, 16, v48\n"
    "v_and_b32 v61, s65, v48\n"
    "v_mfma_f32_16x16x32_bf16 a[188:191], a[116:119], a[60:63], a[188:191]\n"
    "ds_read_b128 a[60:63], %[rd1] offset:30720\n"
    "v_lshlrev_b32 v62, 16, v49\n"
    "v_mfma_f32_16x16x32_bf16 a[188:191], a[120:123], a[32:35], a[188:191]\n"
    "v_and_b32 v63, s65, v49\n"
    "v_lshlrev_b32 v64, 16, v50\n"
    "v_mfma_f32_16x16x32_bf16 a[188:191], a[124:127], a[36:39], a[188:191]\n"
    "s_waitcnt lgkmcnt(10)\n"
    "v_and_b32 v65, s65, v50\n"
    "v_lshlrev_b32 v66, 16, v51\n"
    "v_mfma_f32_16x16x32_bf16 a[192:195], a[100:103], a[40:43], a[192:195]\n"
    "v_and_b32 v67, s65, v51\n"
    "v_sub_f32 v68, v60, v68\n"
    "v_mfma_f32_16x16x32_bf16 a[192:195], a[96:99], a[36:39], a[192:195]\n"
    "v_sub_f32 v69, v61, v69\n"
    "v_sub_f32 v70, v62, v70\n"
    "v_mfma_f32_16x16x32_bf16 a[192:195], a[92:95], a[32:35], a[192:195]\n"
    "s_add_u32 s63, s63, 1\n"
    "v_sub_f32 v71, v63, v71\n"
    "v_sub_f32 v72, v64, v72\n"
    "v_mfma_f32_16x16x32_bf16 a[192:195], a[88:91], a[8:11], a[192:195]\n"
    "v_sub_f32 v73, v65, v73\n"
    "v_sub_f32 v74, v66, v74\n"
    "v_mfma_f32_16x16x32_bf16 a[192:195], a[84:87], a[4:7], a[192:195]\n"
    "v_sub_f32 v75, v67, v75\n"
    "v_cvt_pk_bf16_f32 v48, v68, v69\n"
    "v_mfma_f32_16x16x32_bf16 a[192:195], a[80:83], a[0:3], a[192:195]\n"
    "v_cvt_pk_bf16_f32 v49, v70, v71\n"
    "v_cvt_pk_bf16_f32 v50, v72, v73\n"
    "v_mfma_f32_16x16x32_bf16 a[196:199], a[104:107], a[0:3], a[196:199]\n"
    "s_waitcnt lgkmcnt(0)\n"
    "v_mov_b32 v13, s63\n"
    "v_mfma_f32_16x16x32_bf16 a[196:199], a[108:111], a[4:7], a[196:199]\n"
    "ds_write_b32 v14, v13\n"
    "v_mfma_f32_16x16x32_bf16 a[196:199], a[112:115], a[8:11], a[196:199]\n"
    "v_cvt_pk_bf16_f32 v51, v74, v75\n"
    "v_mfma_f32_16x16x32_bf16 a[196:199], a[116:119], a[32:35], a[196:199]\n"
    "s_add_u32 s64, s71, 2048\n"
    "buffer_store_dwordx4 v[48:51], %[vst], %[srdD], s64 offen sc0 sc1\n"
    "v_mfma_f32_16x16x32_bf16 a[196:199], a[120:123], a[36:39], a[196:199]\n"
    "v_mfma_f32_16x16x32_bf16 a[196:199], a[124:127], a[40:43], a[196:199]\n"
    "v_mfma_f32_16x16x32_bf16 a[200:203], a[100:103], a[44:47], a[200:203]\n"
    "v_mfma_f32_16x16x32_bf16 a[200:203], a[96:99], a[40:43], a[200:203]\n"
    "v_mfma_f32_16x16x32_bf16 a[200:203], a[92:95], a[36:39], a[200:203]\n"
    "v_mfma_f32_16x16x32_bf16 a[200:203], a[88:91], a[12:15], a[200:203]\n"
    "v_mfma_f32_16x16x32_bf16 a[200:203], a[84:87], a[8:11], a[200:203]\n"
    "ds_read_b32 v13, v12\n"
    "v_mfma_f32_16x16x32_bf16 a[200:203], a[80:83], a[4:7], a[200:203]\n"
    "v_mfma_f32_16x16x32_bf16 a[204:207], a[104:107], a[4:7], a[204:207]\n"
    "v_mfma_f32_16x16x32_bf16 a[204:207], a[108:111], a[8:11], a[204:207]\n"
    "v_mfma_f32_16x16x32_bf16 a[204:207], a[112:115], a[12:15], a[204:207]\n"
    "v_mfma_f32_16x16x32_bf16 a[204:207], a[116:119], a[36:39], a[204:207]\n"
    "v_mfma_f32_16x16x32_bf16 a[204:207], a[120:123], a[40:43], a[204:207]\n"
    "s_waitcnt lgkmcnt(0)\n"
    "v_cmp_gt_u32 vcc, s63, v13\n"
    "v_mfma_f32_16x16x32_bf16 a[204:207], a[124:127], a[44:47], a[204:207]\n"
    "s_cbranch_vccnz Ls43%=\n"
    "Lb43%=:\n"
    "v_mfma_f32_16x16x32_bf16 a[208:211], a[100:103], a[48:51], a[208:211]\n"
    "ds_read_b128 a[0:3], %[rd0] offset:32768\n"
    "v_mfma_f32_16x16x32_bf16 a[208:211], a[96:99], a[44:47], a[208:211]\n"
    "ds_read_b128 a[32:35], %[rd1] offset:32768\n"
    "v_mfma_f32_16x16x32_bf16 a[208:211], a[92:95], a[40:43], a[208:211]\n"
    "ds_read_b128 a[4:7], %[rd0] offset:34816\n"
    "s_waitcnt vmcnt(27)\n"
    "v_mfma_f32_16x16x32_bf16 a[208:211], a[88:91], a[16:19], a[208:211]\n"
    "ds_read_b128 a[36:39], %[rd1] offset:34816\n"
    "v_lshlrev_b32 v68, 16, v52\n"
    "v_mfma_f32_16x16x32_bf16 a[208:211], a[84:87], a[12:15], a[208:211]\n"
    "v_and_b32 v69, s65, v52\n"
    "v_lshlrev_b32 v70, 16, v53\n"
    "v_mfma_f32_16x16x32_bf16 a[208:211], a[80:83], a[8:11], a[208:211]\n"
    "v_and_b32 v71, s65, v53\n"
    "v_lshlrev_b32 v72, 16, v54\n"
    "v_mfma_f32_16x16x32_bf16 a[212:215], a[104:107], a[8:11], a[212:215]\n"
    "ds_read_b128 a[8:11], %[rd0] offset:36864\n"
    "v_and_b32 v73, s65, v54\n"
    "v_mfma_f32_16x16x32_bf16 a[212:215], a[108:111], a[12:15], a[212:215]\n"
    "s_add_u32 m0, s69, 8192\n"
    "s_add_u32 s57, s55, 8192\n"
    "v_lshlrev_b32 v74, 16, v55\n"
    "v_mfma_f32_16x16x32_bf16 a[212:215], a[112:115], a[16:19], a[212:215]\n"
    "buffer_load_dwordx4 %[od1], %[srdX], s57 offen  lds\n"
    "v_and_b32 v75, s65, v55\n"
    "v_mfma_f32_16x16x32_bf16 a[212:215], a[116:119], a[40:43], a[212:215]\n"
    "ds_read_b128 a[40:43], %[rd1] offset:36864\n"
    "v_sub_f32 v60, v60, v68\n"
    "v_mfma_f32_16x16x32_bf16 a[212:215], a[120:123], a[44:47], a[212:215]\n"
    "v_sub_f32 v61, v61, v69\n"
    "v_sub_f32 v62, v62, v70\n"
    "v_mfma_f32_16x16x32_bf16 a[212:215], a[124:127], a[48:51], a[212:215]\n"
    "v_sub_f32 v63, v63, v71\n"
    "v_sub_f32 v64, v64, v72\n"
    "v_mfma_f32_16x16x32_bf16 a[216:219], a[100:103], a[52:55], a[216:219]\n"
    "v_sub_f32 v65, v65, v73\n"
    "v_sub_f32 v66, v66, v74\n"
    "v_mfma_f32_16x16x32_bf16 a[216:219], a[96:99], a[48:51], a[216:219]\n"
    "v_sub_f32 v67, v67, v75\n"
    "v_cvt_pk_bf16_f32 v52, v60, v61\n"
    "v_mfma_f32_16x16x32_bf16 a[216:219], a[92:95], a[44:47], a[216:219]\n"
    "s_add_u32 m0, s69, 12288\n"
    "s_add_u32 s57, s55, 16384\n"
    "v_cvt_pk_bf16_f32 v53, v62, v63\n"
    "v_mfma_f32_16x16x32_bf16 a[216:219], a[88:91], a[20:23], a[216:219]\n"
    "buffer_load_dwordx4 %[od1], %[srdX], s57 offen  lds\n"
    "v_cvt_pk_bf16_f32 v54, v64, v65\n"
    "v_mfma_f32_16x16x32_bf16 a[216:219], a[84:87], a[16:19], a[216:219]\n"
    "v_cvt_pk_bf16_f32 v55, v66, v67\n"
    "v_mfma_f32_16x16x32_bf16 a[216:219], a[80:83], a[12:15], a[216:219]\n"
    "s_add_u32 s64, s71, 266240\n"
    "buffer_store_dwordx4 v[52:55], %[vst], %[srdD], s64 offen sc0 sc1\n"
    "v_mfma_f32_16x16x32_bf16 a[220:223], a[104:107], a[12:15], a[220:223]\n"
    "ds_read_b128 a[12:15], %[rd0] offset:38912\n"
    "v_mfma_f32_16x16x32_bf16 a[220:223], a[108:111], a[16:19], a[220:223]\n"
    "v_mfma_f32_16x16x32_bf16 a[220:223], a[112:115], a[20:23], a[220:223]\n"
    "v_mfma_f32_16x16x32_bf16 a[220:223], a[116:119], a[44:47], a[220:223]\n"
    "ds_read_b128 a[44:47], %[rd1] offset:38912\n"
    "v_mfma_f32_16x16x32_bf16 a[220:223], a[120:123], a[48:51], a[220:223]\n"
    "v_mfma_f32_16x16x32_bf16 a[220:223], a[124:127], a[52:55], a[220:223]\n"
    "v_mfma_f32_16x16x32_bf16 a[224:227], a[100:103], a[56:59], a[224:227]\n"
    "v_mfma_f32_16x16x32_bf16 a[224:227], a[96:99], a[52:55], a[224:227]\n"
    "v_mfma_f32_16x16x32_bf16 a[224:227], a[92:95], a[48:51], a[224:227]\n"
    "s_add_u32 m0, s69, 16384\n"
    "s_add_u32 s57, s55, 24576\n"
    "v_mfma_f32_16x16x32_bf16 a[224:227], a[88:91], a[24:27], a[224:227]\n"
    "buffer_load_dwordx4 %[od1], %[srdX], s57 offen  lds\n"
    "v_mfma_f32_16x16x32_bf16 a[224:227], a[84:87], a[20:23], a[224:227]\n"
    "v_mfma_f32_16x16x32_bf16 a[224:227], a[80:83], a[16:19], a[224:227]\n"
    "v_mfma_f32_16x16x32_bf16 a[228:231], a[104:107], a[16:19], a[228:231]\n"
    "ds_read_b128 a[16:19], %[rd0] offset:40960\n"
    "v_mfma_f32_16x16x32_bf16 a[228:231], a[108:111], a[20:23], a[228:231]\n"
    "v_mfma_f32_16x16x32_bf16 a[228:231], a[112:115], a[24:27], a[228:231]\n"
    "s_waitcnt vmcnt(26)\n"
    "v_lshlrev_b32 v60, 16, v56\n"
    "v_and_b32 v61, s65, v56\n"
    "v_mfma_f32_16x16x32_bf16 a[228:231], a[116:119], a[48:51], a[228:231]\n"
    "ds_read_b128 a[48:51], %[rd1] offset:40960\n"
    "v_lshlrev_b32 v62, 16, v57\n"
    "v_mfma_f32_16x16x32_bf16 a[228:231], a[120:123], a[52:55], a[228:231]\n"
    "v_and_b32 v63, s65, v57\n"
    "v_lshlrev_b32 v64, 16, v58\n"
    "v_mfma_f32_16x16x32_bf16 a[228:231], a[124:127], a[56:59], a[228:231]\n"
    "v_and_b32 v65, s65, v58\n"
    "v_lshlrev_b32 v66, 16, v59\n"
    "v_mfma_f32_16x16x32_bf16 a[232:235], a[100:103], a[60:63], a[232:235]\n"
    "v_and_b32 v67, s65, v59\n"
    "v_sub_f32 v68, v60, v68\n"
    "v_mfma_f32_16x16x32_bf16 a[232:235], a[96:99], a[56:59], a[232:235]\n"
    "v_sub_f32 v69, v61, v69\n"
    "v_sub_f32 v70, v62, v70\n"
    "v_mfma_f32_16x16x32_bf16 a[232:235], a[92:95], a[52:55], a[232:235]\n"
    "s_add_u32 m0, s69, 20480\n"
    "s_add_u32 s57, s55, 32768\n"
    "v_sub_f32 v71, v63, v71\n"
    "v_mfma_f32_16x16x32_bf16 a[232:235], a[88:91], a[28:31], a[232:235]\n"
    "buffer_load_dwordx4 %[od1], %[srdX], s57 offen  lds\n"
    "v_sub_f32 v72, v64, v72\n"
    "v_mfma_f32_16x16x32_bf16 a[232:235], a[84:87], a[24:27], a[232:235]\n"
    "v_sub_f32 v73, v65, v73\n"
    "v_sub_f32 v74, v66, v74\n"
    "v_mfma_f32_16x16x32_bf16 a[232:235], a[80:83], a[20:23], a[232:235]\n"
    "v_sub_f32 v75, v67, v75\n"
    "v_cvt_pk_bf16_f32 v56, v68, v69\n"
    "v_mfma_f32_16x16x32_bf16 a[236:239], a[104:107], a[20:23], a[236:239]\n"
    "ds_read_b128 a[20:23], %[rd0] offset:43008\n"
    "v_cvt_pk_bf16_f32 v57, v70, v71\n"
    "v_mfma_f32_16x16x32_bf16 a[236:239], a[108:111], a[24:27], a[236:239]\n"
    "v_cvt_pk_bf16_f32 v58, v72, v73\n"
    "v_cvt_pk_bf16_f32 v59, v74, v75\n"
    "v_mfma_f32_16x16x32_bf16 a[236:239], a[112:115], a[28:31], a[236:239]\n"
    "s_add_u32 s64, s71, 4096\n"
    "buffer_store_dwordx4 v[56:59], %[vst], %[srdD], s64 offen sc0 sc1\n"
    "v_mfma_f32_16x16x32_bf16 a[236:239], a[116:119], a[52:55], a[236:239]\n"
    "ds_read_b128 a[52:55], %[rd1] offset:43008\n"
    "v_mfma_f32_16x16x32_bf16 a[236:239], a[120:123], a[56:59], a[236:239]\n"
    "v_mfma_f32_16x16x32_bf16 a[236:239], a[124:127], a[60:63], a[236:239]\n"
    "v_mfma_f32_16x16x32_bf16 a[240:243], a[100:103], a[72:75], a[240:243]\n"
    "v_mfma_f32_16x16x32_bf16 a[240:243], a[96:99], a[60:63], a[240:243]\n"
    "v_mfma_f32_16x16x32_bf16 a[240:243], a[92:95], a[56:59], a[240:243]\n"
    "s_add_u32 m0, s69, 24576\n"
    "s_add_u32 s57, s55, 40960\n"
    "v_mfma_f32_16x16x32_bf16 a[240:243], a[88:91], a[64:67], a[240:243]\n"
    "buffer_load_dwordx4 %[od1], %[srdX], s57 offen  lds\n"
    "v_mfma_f32_16x16x32_bf16 a[240:243], a[84:87], a[28:31], a[240:243]\n"
    "v_mfma_f32_16x16x32_bf16 a[240:243], a[80:83], a[24:27], a[240:243]\n"
    "v_mfma_f32_16x16x32_bf16 a[244:247], a[104:107], a[24:27], a[244:247]\n"
    "ds_read_b128 a[24:27], %[rd0] offset:45056\n"
    "v_mfma_f32_16x16x32_bf16 a[244:247], a[108:111], a[28:31], a[244:247]\n"
    "v_mfma_f32_16x16x32_bf16 a[244:247], a[112:115], a[64:67], a[244:247]\n"
    "v_mfma_f32_16x16x32_bf16 a[244:247], a[116:119], a[56:59], a[244:247]\n"
    "ds_read_b128 a[56:59], %[rd1] offset:45056\n"
    "v_mfma_f32_16x16x32_bf16 a[244:247], a[120:123], a[60:63], a[244:247]\n"
    "v_mfma_f32_16x16x32_bf16 a[244:247], a[124:127], a[72:75], a[244:247]\n"
    "v_mfma_f32_16x16x32_bf16 a[248:251], a[100:103], a[76:79], a[248:251]\n"
    "v_mfma_f32_16x16x32_bf16 a[248:251], a[96:99], a[72:75], a[248:251]\n"
    "v_mfma_f32_16x16x32_bf16 a[248:251], a[92:95], a[60:63], a[248:251]\n"
    "s_add_u32 m0, s69, 28672\n"
    "s_add_u32 s57, s55, 49152\n"
    "s_waitcnt vmcnt(24)\n"
    "v_mfma_f32_16x16x32_bf16 a[248:251], a[88:91], a[68:71], a[248:251]\n"
    "buffer_load_dwordx4 %[od1], %[srdX], s57 offen  lds\n"
    "v_lshlrev_b32 v68, 16, v32\n"
    "v_mfma_f32_16x16x32_bf16 a[248:251], a[84:87], a[64:67], a[248:251]\n"
    "v_and_b32 v69, s65, v32\n"
    "v_lshlrev_b32 v70, 16, v33\n"
    "v_mfma_f32_16x16x32_bf16 a[248:251], a[80:83], a[28:31], a[248:251]\n"
    "s_add_u32 s69, s69, 0x8000\n"
    "s_and_b32 s69, s69, 0x1ffff\n"
    "v_and_b32 v71, s65, v33\n"
    "v_mfma_f32_16x16x32_bf16 a[252:255], a[104:107], a[28:31], a[252:255]\n"
    "ds_read_b128 a[28:31], %[rd0] offset:47104\n"
    "v_lshlrev_b32 v72, 16, v34\n"
    "v_mfma_f32_16x16x32_bf16 a[252:255], a[108:111], a[64:67], a[252:255]\n"
    "s_add_u32 s55, s55, 128\n"
    "v_and_b32 v73, s65, v34\n"
    "v_lshlrev_b32 v74, 16, v35\n"
    "v_mfma_f32_16x16x32_bf16 a[252:255], a[112:115], a[68:71], a[252:255]\n"
    "v_and_b32 v75, s65, v35\n"
    "v_sub_f32 v60, v60, v68\n"
    "v_mfma_f32_16x16x32_bf16 a[252:255], a[116:119], a[60:63], a[252:255]\n"
    "ds_read_b128 a[60:63], %[rd1] offset:47104\n"
    "v_sub_f32 v61, v61, v69\n"
    "v_mfma_f32_16x16x32_bf16 a[252:255], a[120:123], a[72:75], a[252:255]\n"
    "v_sub_f32 v62, v62, v70\n"
    "v_sub_f32 v63, v63, v71\n"
    "v_mfma_f32_16x16x32_bf16 a[252:255], a[124:127], a[76:79], a[252:255]\n"
    "s_waitcnt vmcnt(13)\n"
    "s_waitcnt lgkmcnt(8)\n"
    "s_cmp_lg_u32 s67, 0\n"
    "v_mfma_f32_16x16x32_bf16 v[208:211], a[148:151], a[40:43], v[208:211]\n"
    "v_mfma_f32_16x16x32_bf16 v[208:211], a[144:147], a[36:39], v[208:211]\n"
    "v_mfma_f32_16x16x32_bf16 v[208:211], a[140:143], a[32:35], v[208:211]\n"
    "v_mfma_f32_16x16x32_bf16 v[208:211], a[136:139], a[8:11], v[208:211]\n"
    "v_mfma_f32_16x16x32_bf16 v[208:211], a[132:135], a[4:7], v[208:211]\n"
    "v_mfma_f32_16x16x32_bf16 v[208:211], a[128:131], a[0:3], v[208:211]\n"
    "v_mfma_f32_16x16x32_bf16 v[212:215], a[152:155], a[0:3], v[212:215]\n"
    "v_mfma_f32_16x16x32_bf16 v[212:215], a[156:159], a[4:7], v[212:215]\n"
    "v_mfma_f32_16x16x32_bf16 v[212:215], a[160:163], a[8:11], v[212:215]\n"
    "v_mfma_f32_16x16x32_bf16 v[212:215], a[164:167], a[32:35], v[212:215]\n"
    "v_mfma_f32_16x16x32_bf16 v[212:215], a[168:171], a[36:39], v[212:215]\n"
    "v_mfma_f32_16x16x32_bf16 v[212:215], a[172:175], a[40:43], v[212:215]\n"
    "v_mfma_f32_16x16x32_bf16 v[216:219], a[148:151], a[44:47], v[216:219]\n"
    "ds_read_b128 a[0:3], %[rd0] offset:49152\n"
    "v_sub_f32 v64, v64, v72\n"
    "v_mfma_f32_16x16x32_bf16 v[216:219], a[144:147], a[40:43], v[216:219]\n"
    "v_sub_f32 v65, v65, v73\n"
    "v_sub_f32 v66, v66, v74\n"
    "v_mfma_f32_16x16x32_bf16 v[216:219], a[140:143], a[36:39], v[216:219]\n"
    "buffer_load_dwordx4 a[80:83], %[l16], %[srdW], s54 offen offset:0\n"
    "v_sub_f32 v67, v67, v75\n"
    "v_mfma_f32_16x16x32_bf16 v[216:219], a[136:139], a[12:15], v[216:219]\n"
    "v_cvt_pk_bf16_f32 v32, v60, v61\n"
    "v_cvt_pk_bf16_f32 v33, v62, v63\n"
    "v_mfma_f32_16x16x32_bf16 v[216:219], a[132:135], a[8:11], v[216:219]\n"
    "ds_read_b128 a[32:35], %[rd1] offset:49152\n"
    "v_cvt_pk_bf16_f32 v34, v64, v65\n"
    "v_mfma_f32_16x16x32_bf16 v[216:219], a[128:131], a[4:7], v[216:219]\n"
    "v_cvt_pk_bf16_f32 v35, v66, v67\n"
    "v_mfma_f32_16x16x32_bf16 v[220:223], a[152:155], a[4:7], v[220:223]\n"
    "ds_read_b128 a[4:7], %[rd0] offset:51200\n"
    "v_mfma_f32_16x16x32_bf16 v[220:223], a[156:159], a[8:11], v[220:223]\n"
    "v_mfma_f32_16x16x32_bf16 v[220:223], a[160:163], a[12:15], v[220:223]\n"
    "v_mfma_f32_16x16x32_bf16 v[220:223], a[164:167], a[36:39], v[220:223]\n"
    "ds_read_b128 a[36:39], %[rd1] offset:51200\n"
    "v_mfma_f32_16x16x32_bf16 v[220:223], a[168:171], a[40:43], v[220:223]\n"
    "buffer_load_dwordx4 a[84:87], %[l16], %[srdW], s54 offen offset:1024\n"
    "v_mfma_f32_16x16x32_bf16 v[220:223], a[172:175], a[44:47], v[220:223]\n"
    "s_waitcnt lgkmcnt(8)\n"
    "v_mfma_f32_16x16x32_bf16 v[224:227], a[148:151], a[48:51], v[224:227]\n"
    "v_mfma_f32_16x16x32_bf16 v[224:227], a[144:147], a[44:47], v[224:227]\n"
    "ds_read_b128 a[64:67], %[re0] offset:32768\n"
    "v_mfma_f32_16x16x32_bf16 v[224:227], a[140:143], a[40:43], v[224:227]\n"
    "buffer_load_dwordx4 a[88:91], %[l16], %[srdW], s54 offen offset:2048\n"
    "v_mfma_f32_16x16x32_bf16 v[224:227], a[136:139], a[16:19], v[224:227]\n"
    "v_mfma_f32_16x16x32_bf16 v[224:227], a[132:135], a[12:15], v[224:227]\n"
    "v_mfma_f32_16x16x32_bf16 v[224:227], a[128:131], a[8:11], v[224:227]\n"
    "v_mfma_f32_16x16x32_bf16 v[228:231], a[152:155], a[8:11], v[228:231]\n"
    "ds_read_b128 a[8:11], %[rd0] offset:53248\n"
    "v_mfma_f32_16x16x32_bf16 v[228:231], a[156:159], a[12:15], v[228:231]\n"
    "v_mfma_f32_16x16x32_bf16 v[228:231], a[160:163], a[16:19], v[228:231]\n"
    "v_mfma_f32_16x16x32_bf16 v[228:231], a[164:167], a[40:43], v[228:231]\n"
    "ds_read_b128 a[40:43], %[rd1] offset:53248\n"
    "v_mfma_f32_16x16x32_bf16 v[228:231], a[168:171], a[44:47], v[228:231]\n"
    "buffer_load_dwordx4 a[92:95], %[l16], %[srdW], s54 offen offset:3072\n"
    "v_mfma_f32_16x16x32_bf16 v[228:231], a[172:175], a[48:51], v[228:231]\n"
    "v_mfma_f32_16x16x32_bf16 v[232:235], a[148:151], a[52:55], v[232:235]\n"
    "s_add_u32 s54, s54, 4096\n"
    "v_mfma_f32_16x16x32_bf16 v[232:235], a[144:147], a[48:51], v[232:235]\n"
    "ds_read_b128 a[72:75], %[re1] offset:32768\n"
    "v_mfma_f32_16x16x32_bf16 v[232:235], a[140:143], a[44:47], v[232:235]\n"
    "buffer_load_dwordx4 a[96:99], %[l16], %[srdW], s54 offen offset:0\n"
    "v_mfma_f32_16x16x32_bf16 v[232:235], a[136:139], a[20:23], v[232:235]\n"
    "v_mfma_f32_16x16x32_bf16 v[232:235], a[132:135], a[16:19], v[232:235]\n"
    "v_mfma_f32_16x16x32_bf16 v[232:235], a[128:131], a[12:15], v[232:235]\n"
    "v_mfma_f32_16x16x32_bf16 v[236:239], a[152:155], a[12:15], v[236:239]\n"
    "ds_read_b128 a[12:15], %[rd0] offset:55296\n"
    "v_mfma_f32_16x16x32_bf16 v[236:239], a[156:159], a[16:19], v[236:239]\n"
    "v_mfma_f32_16x16x32_bf16 v[236:239], a[160:163], a[20:23], v[236:239]\n"
    "v_mfma_f32_16x16x32_bf16 v[236:239], a[164:167], a[44:47], v[236:239]\n"
    "ds_read_b128 a[44:47], %[rd1] offset:55296\n"
    "v_mfma_f32_16x16x32_bf16 v[236:239], a[168:171], a[48:51], v[236:239]\n"
    "buffer_load_dwordx4 a[100:103], %[l16], %[srdW], s54 offen offset:1024\n"
    "v_mfma_f32_16x16x32_bf16 v[236:239], a[172:175], a[52:55], v[236:239]\n"
    "s_waitcnt lgkmcnt(10)\n"
    "v_mfma_f32_16x16x32_bf16 v[240:243], a[148:151], a[56:59], v[240:243]\n"
    "v_mfma_f32_16x16x32_bf16 v[240:243], a[144:147], a[52:55], v[240:243]\n"
    "ds_read_b128 a[68:71], %[re0] offset:34816\n"
    "v_mfma_f32_16x16x32_bf16 v[240:243], a[140:143], a[48:51], v[240:243]\n"
    "buffer_load_dwordx4 a[104:107], %[l16], %[srdW], s54 offen offset:2048\n"
    "v_mfma_f32_16x16x32_bf16 v[240:243], a[136:139], a[24:27], v[240:243]\n"
    "v_mfma_f32_16x16x32_bf16 v[240:243], a[132:135], a[20:23], v[240:243]\n"
    "v_mfma_f32_16x16x32_bf16 v[240:243], a[128:131], a[16:19], v[240:243]\n"
    "v_mfma_f32_16x16x32_bf16 v[244:247], a[152:155], a[16:19], v[244:247]\n"
    "ds_read_b128 a[16:19], %[rd0] offset:57344\n"
    "v_mfma_f32_16x16x32_bf16 v[244:247], a[156:159], a[20:23], v[244:247]\n"
    "v_mfma_f32_16x16x32_bf16 v[244:247], a[160:163], a[24:27], v[244:247]\n"
    "v_mfma_f32_16x16x32_bf16 v[244:247], a[164:167], a[48:51], v[244:247]\n"
    "ds_read_b128 a[48:51], %[rd1] offset:57344\n"
    "v_mfma_f32_16x16x32_bf16 v[244:247], a[168:171], a[52:55], v[244:247]\n"
    "buffer_load_dwordx4 a[108:111], %[l16], %[srdW], s54 offen offset:3072\n"
    "v_mfma_f32_16x16x32_bf16 v[244:247], a[172:175], a[56:59], v[244:247]\n"
    "v_mfma_f32_16x16x32_bf16 v[248:251], a[148:151], a[60:63], v[248:251]\n"
    "s_add_u32 s54, s54, 4096\n"
    "v_mfma_f32_16x16x32_bf16 v[248:251], a[144:147], a[56:59], v[248:251]\n"
    "ds_read_b128 a[76:79], %[re1] offset:34816\n"
    "v_mfma_f32_16x16x32_bf16 v[248:251], a[140:143], a[52:55], v[248:251]\n"
    "buffer_load_dwordx4 a[112:115], %[l16], %[srdW], s54 offen offset:0\n"
    "v_mfma_f32_16x16x32_bf16 v[248:251], a[136:139], a[28:31], v[248:251]\n"
    "v_mfma_f32_16x16x32_bf16 v[248:251], a[132:135], a[24:27], v[248:251]\n"
    "v_mfma_f32_16x16x32_bf16 v[248:251], a[128:131], a[20:23], v[248:251]\n"
    "v_mfma_f32_16x16x32_bf16 v[252:255], a[152:155], a[20:23], v[252:255]\n"
    "ds_read_b128 a[20:23], %[rd0] offset:59392\n"
    "v_mfma_f32_16x16x32_bf16 v[252:255], a[156:159], a[24:27], v[252:255]\n"
    "v_mfma_f32_16x16x32_bf16 v[252:255], a[160:163], a[28:31], v[252:255]\n"
    "v_mfma_f32_16x16x32_bf16 v[252:255], a[164:167], a[52:55], v[252:255]\n"
    "ds_read_b128 a[52:55], %[rd1] offset:59392\n"
    "v_mfma_f32_16x16x32_bf16 v[252:255], a[168:171], a[56:59], v[252:255]\n"
    "buffer_load_dwordx4 a[116:119], %[l16], %[srdW], s54 offen offset:1024\n"
    "v_mfma_f32_16x16x32_bf16 v[252:255], a[172:175], a[60:63], v[252:255]\n"
    "s_waitcnt lgkmcnt(12)\n"
    "v_mfma_f32_16x16x32_bf16 a[176:179], a[148:151], a[32:35], a[176:179]\n"
    "v_mfma_f32_16x16x32_bf16 a[176:179], a[144:147], a[60:63], a[176:179]\n"
    "v_mfma_f32_16x16x32_bf16 a[176:179], a[140:143], a[56:59], a[176:179]\n"
    "buffer_load_dwordx4 a[120:123], %[l16], %[srdW], s54 offen offset:2048\n"
    "v_mfma_f32_16x16x32_bf16 a[176:179], a[136:139], a[0:3], a[176:179]\n"
    "v_mfma_f32_16x16x32_bf16 a[176:179], a[132:135], a[28:31], a[176:179]\n"
    "v_mfma_f32_16x16x32_bf16 a[176:179], a[128:131], a[24:27], a[176:179]\n"
    "v_mfma_f32_16x16x32_bf16 a[180:183], a[152:155], a[24:27], a[180:183]\n"
    "ds_read_b128 a[24:27], %[rd0] offset:61440\n"
    "v_mfma_f32_16x16x32_bf16 a[180:183], a[156:159], a[28:31], a[180:183]\n"
    "v_mfma_f32_16x16x32_bf16 a[180:183], a[160:163], a[0:3], a[180:183]\n"
    "v_mfma_f32_16x16x32_bf16 a[180:183], a[164:167], a[56:59], a[180:183]\n"
    "ds_read_b128 a[56:59], %[rd1] offset:61440\n"
    "v_mfma_f32_16x16x32_bf16 a[180:183], a[168:171], a[60:63], a[180:183]\n"
    "buffer_load_dwordx4 a[124:127], %[l16], %[srdW], s54 offen offset:3072\n"
    "v_mfma_f32_16x16x32_bf16 a[180:183], a[172:175], a[32:35], a[180:183]\n"
    "v_mfma_f32_16x16x32_bf16 a[184:187], a[148:151], a[36:39], a[184:187]\n"
    "s_add_u32 s54, s54, 4096\n"
    "v_mfma_f32_16x16x32_bf16 a[184:187], a[144:147], a[32:35], a[184:187]\n"
    "s_add_u32 m0, s69, 0\n"
    "s_add_u32 s57, s55, 0\n"
    "v_subrev_u32 v13, 0x2000, %[od1]\n"
    "v_mfma_f32_16x16x32_bf16 a[184:187], a[140:143], a[60:63], a[184:187]\n"
    "buffer_load_dwordx4 v13, %[srdX], s57 offen  lds\n"
    "v_mfma_f32_16x16x32_bf16 a[184:187], a[136:139], a[4:7], a[184:187]\n"
    "s_add_u32 m0, s69, 4096\n"
    "s_add_u32 s57, s55, 0\n"
    "v_mfma_f32_16x16x32_bf16 a[184:187], a[132:135], a[0:3], a[184:187]\n"
    "buffer_load_dwordx4 %[od1], %[srdX], s57 offen  lds\n"
    "v_mfma_f32_16x16x32_bf16 a[184:187], a[128:131], a[28:31], a[184:187]\n"
    "s_add_u32 s64, s71, 268288\n"
    "buffer_store_dwordx4 v[32:35], %[vst], %[srdD], s64 offen sc0 sc1\n"
    "v_mfma_f32_16x16x32_bf16 a[188:191], a[152:155], a[28:31], a[188:191]\n"
    "ds_read_b128 a[28:31], %[rd0] offset:63488\n"
    "s_waitcnt vmcnt(25)\n"
    "v_mfma_f32_16x16x32_bf16 a[188:191], a[156:159], a[0:3], a[188:191]\n"
    "v_lshlrev_b32 v60, 16, v36\n"
    "v_and_b32 v61, s65, v36\n"
    "v_mfma_f32_16x16x32_bf16 a[188:191], a[160:163], a[4:7], a[188:191]\n"
    "v_lshlrev_b32 v62, 16, v37\n"
    "v_and_b32 v63, s65, v37\n"
    "v_mfma_f32_16x16x32_bf16 a[188:191], a[164:167], a[60:63], a[188:191]\n"
    "ds_read_b128 a[60:63], %[rd1] offset:63488\n"
    "v_lshlrev_b32 v64, 16, v38\n"
    "v_mfma_f32_16x16x32_bf16 a[188:191], a[168:171], a[32:35], a[188:191]\n"
    "v_and_b32 v65, s65, v38\n"
    "v_lshlrev_b32 v66, 16, v39\n"
    "v_mfma_f32_16x16x32_bf16 a[188:191], a[172:175], a[36:39], a[188:191]\n"
    "s_waitcnt lgkmcnt(10)\n"
    "v_and_b32 v67, s65, v39\n"
    "v_sub_f32 v68, v60, v68\n"
    "v_mfma_f32_16x16x32_bf16 a[192:195], a[148:151], a[40:43], a[192:195]\n"
    "v_xor_b32 %[rd0], 0x10000, %[rd0]\n"
    "v_sub_f32 v69, v61, v69\n"
    "v_mfma_f32_16x16x32_bf16 a[192:195], a[144:147], a[36:39], a[192:195]\n"
    "v_xor_b32 %[rd1], 0x10000, %[rd1]\n"
    "v_sub_f32 v70, v62, v70\n"
    "v_mfma_f32_16x16x32_bf16 a[192:195], a[140:143], a[32:35], a[192:195]\n"
    "s_add_u32 s63, s63, 1\n"
    "v_sub_f32 v71, v63, v71\n"
    "v_sub_f32 v72, v64, v72\n"
    "v_mfma_f32_16x16x32_bf16 a[192:195], a[136:139], a[8:11], a[192:195]\n"
    "v_sub_f32 v73, v65, v73\n"
    "v_sub_f32 v74, v66, v74\n"
    "v_mfma_f32_16x16x32_bf16 a[192:195], a[132:135], a[4:7], a[192:195]\n"
    "v_sub_f32 v75, v67, v75\n"
    "v_cvt_pk_bf16_f32 v36, v68, v69\n"
    "v_mfma_f32_16x16x32_bf16 a[192:195], a[128:131], a[0:3], a[192:195]\n"
    "v_cvt_pk_bf16_f32 v37, v70, v71\n"
    "v_cvt_pk_bf16_f32 v38, v72, v73\n"
    "v_mfma_f32_16x16x32_bf16 a[196:199], a[152:155], a[0:3], a[196:199]\n"
    "s_waitcnt lgkmcnt(0)\n"
    "v_mov_b32 v13, s63\n"
    "v_mfma_f32_16x16x32_bf16 a[196:199], a[156:159], a[4:7], a[196:199]\n"
    "ds_write_b32 v14, v13\n"
    "v_mfma_f32_16x16x32_bf16 a[196:199], a[160:163], a[8:11], a[196:199]\n"
    "v_cvt_pk_bf16_f32 v39, v74, v75\n"
    "v_mfma_f32_16x16x32_bf16 a[196:199], a[164:167], a[32:35], a[196:199]\n"
    "s_add_u32 s64, s71, 6144\n"
    "buffer_store_dwordx4 v[36:39], %[vst], %[srdD], s64 offen sc0 sc1\n"
    "v_mfma_f32_16x16x32_bf16 a[196:199], a[168:171], a[36:39], a[196:199]\n"
    "v_mfma_f32_16x16x32_bf16 a[196:199], a[172:175], a[40:43], a[196:199]\n"
    "v_mfma_f32_16x16x32_bf16 a[200:203], a[148:151], a[44:47], a[200:203]\n"
    "v_xor_b32 %[re0], 0x10000, %[re0]\n"
    "v_mfma_f32_16x16x32_bf16 a[200:203], a[144:147], a[40:43], a[200:203]\n"
    "v_xor_b32 %[re1], 0x10000, %[re1]\n"
    "v_mfma_f32_16x16x32_bf16 a[200:203], a[140:143], a[36:39], a[200:203]\n"
    "v_mfma_f32_16x16x32_bf16 a[200:203], a[136:139], a[12:15], a[200:203]\n"
    "v_mfma_f32_16x16x32_bf16 a[200:203], a[132:135], a[8:11], a[200:203]\n"
    "ds_read_b32 v13, v12\n"
    "v_mfma_f32_16x16x32_bf16 a[200:203], a[128:131], a[4:7], a[200:203]\n"
    "v_mfma_f32_16x16x32_bf16 a[204:207], a[152:155], a[4:7], a[204:207]\n"
    "v_mfma_f32_16x16x32_bf16 a[204:207], a[156:159], a[8:11], a[204:207]\n"
    "v_mfma_f32_16x16x32_bf16 a[204:207], a[160:163], a[12:15], a[204:207]\n"
    "v_mfma_f32_16x16x32_bf16 a[204:207], a[164:167], a[36:39], a[204:207]\n"
    "v_mfma_f32_16x16x32_bf16 a[204:207], a[168:171], a[40:43], a[204:207]\n"
    "s_waitcnt lgkmcnt(0)\n"
    "v_cmp_gt_u32 vcc, s63, v13\n"
    "v_mfma_f32_16x16x32_bf16 a[204:207], a[172:175], a[44:47], a[204:207]\n"
    "s_cbranch_vccnz Ls44%=\n"
    "Lb44%=:\n"
    "v_mfma_f32_16x16x32_bf16 a[208:211], a[148:151], a[48:51], a[208:211]\n"
    "ds_read_b128 a[0:3], %[rd0] offset:0\n"
    "v_mfma_f32_16x16x32_bf16 a[208:211], a[144:147], a[44:47], a[208:211]\n"
    "ds_read_b128 a[32:35], %[rd1] offset:0\n"
    "v_mfma_f32_16x16x32_bf16 a[208:211], a[140:143], a[40:43], a[208:211]\n"
    "ds_read_b128 a[4:7], %[rd0] offset:2048\n"
    "v_mfma_f32_16x16x32_bf16 a[208:211], a[136:139], a[16:19], a[208:211]\n"
    "ds_read_b128 a[36:39], %[rd1] offset:2048\n"
    "v_mfma_f32_16x16x32_bf16 a[208:211], a[132:135], a[12:15], a[208:211]\n"
    "v_mfma_f32_16x16x32_bf16 a[208:211], a[128:131], a[8:11], a[208:211]\n"
    "v_mfma_f32_16x16x32_bf16 a[212:215], a[152:155], a[8:11], a[212:215]\n"
    "ds_read_b128 a[8:11], %[rd0] offset:4096\n"
    "v_mfma_f32_16x16x32_bf16 a[212:215], a[156:159], a[12:15], a[212:215]\n"
    "s_add_u32 m0, s69, 8192\n"
    "s_add_u32 s57, s55, 8192\n"
    "v_mfma_f32_16x16x32_bf16 a[212:215], a[160:163], a[16:19], a[212:215]\n"
    "buffer_load_dwordx4 %[od1], %[srdX], s57 offen  lds\n"
    "v_mfma_f32_16x16x32_bf16 a[212:215], a[164:167], a[40:43], a[212:215]\n"
    "ds_read_b128 a[40:43], %[rd1] offset:4096\n"
    "v_mfma_f32_16x16x32_bf16 a[212:215], a[168:171], a[44:47], a[212:215]\n"
    "v_mfma_f32_16x16x32_bf16 a[212:215], a[172:175], a[48:51], a[212:215]\n"
    "v_mfma_f32_16x16x32_bf16 a[216:219], a[148:151], a[52:55], a[216:219]\n"
    "v_mfma_f32_16x16x32_bf16 a[216:219], a[144:147], a[48:51], a[216:219]\n"
    "v_mfma_f32_16x16x32_bf16 a[216:219], a[140:143], a[44:47], a[216:219]\n"
    "s_add_u32 m0, s69, 12288\n"
    "s_add_u32 s57, s55, 16384\n"
    "v_mfma_f32_16x16x32_bf16 a[216:219], a[136:139], a[20:23], a[216:219]\n"
    "buffer_load_dwordx4 %[od1], %[srdX], s57 offen  lds\n"
    "v_mfma_f32_16x16x32_bf16 a[216:219], a[132:135], a[16:19], a[216:219]\n"
    "v_mfma_f32_16x16x32_bf16 a[216:219], a[128:131], a[12:15], a[216:219]\n"
    "v_mfma_f32_16x16x32_bf16 a[220:223], a[152:155], a[12:15], a[220:223]\n"
    "ds_read_b128 a[12:15], %[rd0] offset:6144\n"
    "v_mfma_f32_16x16x32_bf16 a[220:223], a[156:159], a[16:19], a[220:223]\n"
    "v_mfma_f32_16x16x32_bf16 a[220:223], a[160:163], a[20:23], a[220:223]\n"
    "v_mfma_f32_16x16x32_bf16 a[220:223], a[164:167], a[44:47], a[220:223]\n"
    "ds_read_b128 a[44:47], %[rd1] offset:6144\n"
    "v_mfma_f32_16x16x32_bf16 a[220:223], a[168:171], a[48:51], a[220:223]\n"
    "v_mfma_f32_16x16x32_bf16 a[220:223], a[172:175], a[52:55], a[220:223]\n"
    "v_mfma_f32_16x16x32_bf16 a[224:227], a[148:151], a[56:59], a[224:227]\n"
    "v_mfma_f32_16x16x32_bf16 a[224:227], a[144:147], a[52:55], a[224:227]\n"
    "v_mfma_f32_16x16x32_bf16 a[224:227], a[140:143], a[48:51], a[224:227]\n"
    "s_add_u32 m0, s69, 16384\n"
    "s_add_u32 s57, s55, 24576\n"
    "v_mfma_f32_16x16x32_bf16 a[224:227], a[136:139], a[24:27], a[224:227]\n"
    "buffer_load_dwordx4 %[od1], %[srdX], s57 offen  lds\n"
    "v_mfma_f32_16x16x32_bf16 a[224:227], a[132:135], a[20:23], a[224:227]\n"
    "v_mfma_f32_16x16x32_bf16 a[224:227], a[128:131], a[16:19], a[224:227]\n"
    "v_mfma_f32_16x16x32_bf16 a[228:231], a[152:155], a[16:19], a[228:231]\n"
    "ds_read_b128 a[16:19], %[rd0] offset:8192\n"
    "v_mfma_f32_16x16x32_bf16 a[228:231], a[156:159], a[20:23], a[228:231]\n"
    "v_mfma_f32_16x16x32_bf16 a[228:231], a[160:163], a[24:27], a[228:231]\n"
    "v_mfma_f32_16x16x32_bf16 a[228:231], a[164:167], a[48:51], a[228:231]\n"
    "ds_read_b128 a[48:51], %[rd1] offset:8192\n"
    "v_mfma_f32_16x16x32_bf16 a[228:231], a[168:171], a[52:55], a[228:231]\n"
    "v_mfma_f32_16x16x32_bf16 a[228:231], a[172:175], a[56:59], a[228:231]\n"
    "v_mfma_f32_16x16x32_bf16 a[232:235], a[148:151], a[60:63], a[232:235]\n"
    "v_mfma_f32_16x16x32_bf16 a[232:235], a[144:147], a[56:59], a[232:235]\n"
    "v_mfma_f32_16x16x32_bf16 a[232:235], a[140:143], a[52:55], a[232:235]\n"
    "s_add_u32 m0, s69, 20480\n"
    "s_add_u32 s57, s55, 32768\n"
    "v_mfma_f32_16x16x32_bf16 a[232:235], a[136:139], a[28:31], a[232:235]\n"
    "buffer_load_dwordx4 %[od1], %[srdX], s57 offen  lds\n"
    "v_mfma_f32_16x16x32_bf16 a[232:235], a[132:135], a[24:27], a[232:235]\n"
    "v_mfma_f32_16x16x32_bf16 a[232:235], a[128:131], a[20:23], a[232:235]\n"
    "v_mfma_f32_16x16x32_bf16 a[236:239], a[152:155], a[20:23], a[236:239]\n"
    "ds_read_b128 a[20:23], %[rd0] offset:10240\n"
    "v_mfma_f32_16x16x32_bf16 a[236:239], a[156:159], a[24:27], a[236:239]\n"
    "v_mfma_f32_16x16x32_bf16 a[236:239], a[160:163], a[28:31], a[236:239]\n"
    "v_mfma_f32_16x16x32_bf16 a[236:239], a[164:167], a[52:55], a[236:239]\n"
    "ds_read_b128 a[52:55], %[rd1] offset:10240\n"
    "v_mfma_f32_16x16x32_bf16 a[236:239], a[168:171], a[56:59], a[236:239]\n"
    "v_mfma_f32_16x16x32_bf16 a[236:239], a[172:175], a[60:63], a[236:239]\n"
    "v_mfma_f32_16x16x32_bf16 a[240:243], a[148:151], a[72:75], a[240:243]\n"
    "v_mfma_f32_16x16x32_bf16 a[240:243], a[144:147], a[60:63], a[240:243]\n"
    "v_mfma_f32_16x16x32_bf16 a[240:243], a[140:143], a[56:59], a[240:243]\n"
    "s_add_u32 m0, s69, 24576\n"
    "s_add_u32 s57, s55, 40960\n"
    "v_mfma_f32_16x16x32_bf16 a[240:243], a[136:139], a[64:67], a[240:243]\n"
    "buffer_load_dwordx4 %[od1], %[srdX], s57 offen  lds\n"
    "v_mfma_f32_16x16x32_bf16 a[240:243], a[132:135], a[28:31], a[240:243]\n"
    "v_mfma_f32_16x16x32_bf16 a[240:243], a[128:131], a[24:27], a[240:243]\n"
    "v_mfma_f32_16x16x32_bf16 a[244:247], a[152:155], a[24:27], a[244:247]\n"
    "ds_read_b128 a[24:27], %[rd0] offset:12288\n"
    "v_mfma_f32_16x16x32_bf16 a[244:247], a[156:159], a[28:31], a[244:247]\n"
    "v_mfma_f32_16x16x32_bf16 a[244:247], a[160:163], a[64:67], a[244:247]\n"
    "v_mfma_f32_16x16x32_bf16 a[244:247], a[164:167], a[56:59], a[244:247]\n"
    "ds_read_b128 a[56:59], %[rd1] offset:12288\n"
    "v_mfma_f32_16x16x32_bf16 a[244:247], a[168:171], a[60:63], a[244:247]\n"
    "v_mfma_f32_16x16x32_bf16 a[244:247], a[172:175], a[72:75], a[244:247]\n"
    "v_mfma_f32_16x16x32_bf16 a[248:251], a[148:151], a[76:79], a[248:251]\n"
    "v_mfma_f32_16x16x32_bf16 a[248:251], a[144:147], a[72:75], a[248:251]\n"
    "v_mfma_f32_16x16x32_bf16 a[248:251], a[140:143], a[60:63], a[248:251]\n"
    "s_add_u32 m0, s69, 28672\n"
    "s_add_u32 s57, s55, 49152\n"
    "v_mfma_f32_16x16x32_bf16 a[248:251], a[136:139], a[68:71], a[248:251]\n"
    "buffer_load_dwordx4 %[od1], %[srdX], s57 offen  lds\n"
    "v_mfma_f32_16x16x32_bf16 a[248:251], a[132:135], a[64:67], a[248:251]\n"
    "v_mfma_f32_16x16x32_bf16 a[248:251], a[128:131], a[28:31], a[248:251]\n"
    "s_add_u32 s69, s69, 0x8000\n"
    "s_and_b32 s69, s69, 0x1ffff\n"
    "v_mfma_f32_16x16x32_bf16 a[252:255], a[152:155], a[28:31], a[252:255]\n"
    "ds_read_b128 a[28:31], %[rd0] offset:14336\n"
    "v_mfma_f32_16x16x32_bf16 a[252:255], a[156:159], a[64:67], a[252:255]\n"
    "s_add_u32 s55, s55, 128\n"
    "v_mfma_f32_16x16x32_bf16 a[252:255], a[160:163], a[68:71], a[252:255]\n"
    "v_mfma_f32_16x16x32_bf16 a[252:255], a[164:167], a[60:63], a[252:255]\n"
    "ds_read_b128 a[60:63], %[rd1] offset:14336\n"
    "v_mfma_f32_16x16x32_bf16 a[252:255], a[168:171], a[72:75], a[252:255]\n"
    "v_mfma_f32_16x16x32_bf16 a[252:255], a[172:175], a[76:79], a[252:255]\n"
    "s_branch Ljoin%=\n"
    "Ltile%=:\n"
    "s_waitcnt vmcnt(8)\n"
    "s_waitcnt lgkmcnt(8)\n"
    "s_cmp_lg_u32 s67, 0\n"
    "s_cbranch_scc1 Lc1%=\n"
    "v_mfma_f32_16x16x32_bf16 v[208:211], a[100:103], a[40:43], 0\n"
    "v_mfma_f32_16x16x32_bf16 v[208:211], a[96:99], a[36:39], v[208:211]\n"
    "v_mfma_f32_16x16x32_bf16 v[208:211], a[92:95], a[32:35], v[208:211]\n"
    "v_mfma_f32_16x16x32_bf16 v[208:211], a[88:91], a[8:11], v[208:211]\n"
    "v_mfma_f32_16x16x32_bf16 v[208:211], a[84:87], a[4:7], v[208:211]\n"
    "v_mfma_f32_16x16x32_bf16 v[208:211], a[80:83], a[0:3], v[208:211]\n"
    "v_mfma_f32_16x16x32_bf16 v[212:215], a[104:107], a[0:3], 0\n"
    "v_mfma_f32_16x16x32_bf16 v[212:215], a[108:111], a[4:7], v[212:215]\n"
    "v_mfma_f32_16x16x32_bf16 v[212:215], a[112:115], a[8:11], v[212:215]\n"
    "v_mfma_f32_16x16x32_bf16 v[212:215], a[116:119], a[32:35], v[212:215]\n"
    "v_mfma_f32_16x16x32_bf16 v[212:215], a[120:123], a[36:39], v[212:215]\n"
    "v_mfma_f32_16x16x32_bf16 v[212:215], a[124:127], a[40:43], v[212:215]\n"
    "Lc1%=:\n"
    "v_mfma_f32_16x16x32_bf16 v[216:219], a[100:103], a[44:47], 0\n"
    "ds_read_b128 a[0:3], %[rd0] offset:16384\n"
    "v_fma_f32 v113, v17, v113, v113\n"
    "v_mfma_f32_16x16x32_bf16 v[216:219], a[96:99], a[40:43], v[216:219]\n"
    "v_rcp_f32 v111, v111\n"
    "v_mfma_f32_16x16x32_bf16 v[216:219], a[92:95], a[36:39], v[216:219]\n"
    "buffer_load_dwordx4 a[128:131], %[l16], %[srdW], s54 offen offset:0\n"
    "v_fma_f32 v100, -abs(v96), v100, v100\n"
    "v_mfma_f32_16x16x32_bf16 v[216:219], a[88:91], a[12:15], v[216:219]\n"
    "v_fma_f32 v101, -abs(v97), v101, v101\n"
    "v_fma_f32 v102, -abs(v98), v102, v102\n"
    "v_mfma_f32_16x16x32_bf16 v[216:219], a[84:87], a[8:11], v[216:219]\n"
    "ds_read_b128 a[32:35], %[rd1] offset:16384\n"
    "v_fma_f32 v103, -abs(v99), v103, v103\n"
    "v_mfma_f32_16x16x32_bf16 v[216:219], a[80:83], a[4:7], v[216:219]\n"
    "v_fma_f32 v108, -abs(v104), v108, v108\n"
    "v_fma_f32 v109, -abs(v105), v109, v109\n"
    "v_mfma_f32_16x16x32_bf16 v[220:223], a[104:107], a[4:7], 0\n"
    "ds_read_b128 a[4:7], %[rd0] offset:18432\n"
    "v_fma_f32 v110, -abs(v106), v110, v110\n"
    "v_mfma_f32_16x16x32_bf16 v[220:223], a[108:111], a[8:11], v[220:223]\n"
    "v_fma_f32 v111, -abs(v107), v111, v111\n"
    "v_cvt_pk_bf16_f32 v20, v100, v101\n"
    "v_mfma_f32_16x16x32_bf16 v[220:223], a[112:115], a[12:15], v[220:223]\n"
    "v_cvt_pk_bf16_f32 v21, v102, v103\n"
    "v_cvt_pk_bf16_f32 v22, v108, v109\n"
    "v_mfma_f32_16x16x32_bf16 v[220:223], a[116:119], a[36:39], v[220:223]\n"
    "ds_read_b128 a[36:39], %[rd1] offset:18432\n"
    "v_cvt_pk_bf16_f32 v23, v110, v111\n"
    "v_mfma_f32_16x16x32_bf16 v[220:223], a[120:123], a[40:43], v[220:223]\n"
    "buffer_load_dwordx4 a[132:135], %[l16], %[srdW], s54 offen offset:1024\n"
    "v_fma_f32 v114, v18, v114, v114\n"
    "v_mfma_f32_16x16x32_bf16 v[220:223], a[124:127], a[44:47], v[220:223]\n"
    "s_waitcnt lgkmcnt(8)\n"
    "v_fma_f32 v115, v19, v115, v115\n"
    "v_mul_f32 v132, s73, v132\n"
    "v_mfma_f32_16x16x32_bf16 v[224:227], a[100:103], a[48:51], 0\n"
    "ds_write_b64 %[vsw], v[20:21] offset:4096\n"
    "v_mfma_f32_16x16x32_bf16 v[224:227], a[96:99], a[44:47], v[224:227]\n"
    "ds_read_b128 a[64:67], %[re0] offset:0\n"
    "v_mul_f32 v133, s73, v133\n"
    "v_mfma_f32_16x16x32_bf16 v[224:227], a[92:95], a[40:43], v[224:227]\n"
    "buffer_load_dwordx4 a[136:139], %[l16], %[srdW], s54 offen offset:2048\n"
    "v_mul_f32 v134, s73, v134\n"
    "v_mfma_f32_16x16x32_bf16 v[224:227], a[88:91], a[16:19], v[224:227]\n"
    "ds_write_b64 %[vsw], v[22:23] offset:4224\n"
    "v_mfma_f32_16x16x32_bf16 v[224:227], a[84:87], a[12:15], v[224:227]\n"
    "v_exp_f32_e64 v16, -abs(v120)\n"
    "v_mfma_f32_16x16x32_bf16 v[224:227], a[80:83], a[8:11], v[224:227]\n"
    "v_exp_f32_e64 v17, -abs(v121)\n"
    "v_mfma_f32_16x16x32_bf16 v[228:231], a[104:107], a[8:11], 0\n"
    "ds_read_b128 a[8:11], %[rd0] offset:20480\n"
    "v_mul_f32 v135, s73, v135\n"
    "v_mfma_f32_16x16x32_bf16 v[228:231], a[108:111], a[12:15], v[228:231]\n"
    "v_exp_f32_e64 v18, -abs(v122)\n"
    "v_mfma_f32_16x16x32_bf16 v[228:231], a[112:115], a[16:19], v[228:231]\n"
    "v_exp_f32_e64 v19, -abs(v123)\n"
    "v_mfma_f32_16x16x32_bf16 v[228:231], a[116:119], a[40:43], v[228:231]\n"
    "ds_read_b128 a[40:43], %[rd1] offset:20480\n"
    "v_bfi_b32 v120, s61, 0.5, v120\n"
    "v_mfma_f32_16x16x32_bf16 v[228:231], a[120:123], a[44:47], v[228:231]\n"
    "buffer_load_dwordx4 a[140:143], %[l16], %[srdW], s54 offen offset:3072\n"
    "v_bfi_b32 v121, s61, 0.5, v121\n"
    "v_mfma_f32_16x16x32_bf16 v[228:231], a[124:127], a[48:51], v[228:231]\n"
    "v_bfi_b32 v122, s61, 0.5, v122\n"
    "v_bfi_b32 v123, s61, 0.5, v123\n"
    "v_mfma_f32_16x16x32_bf16 v[232:235], a[100:103], a[52:55], 0\n"
    "s_add_u32 s54, s54, 4096\n"
    "v_fma_f32 v120, v16, v120, v120\n"
    "v_fma_f32 v121, v17, v121, v121\n"
    "v_mfma_f32_16x16x32_bf16 v[232:235], a[96:99], a[48:51], v[232:235]\n"
    "ds_read_b128 a[72:75], %[re1] offset:0\n"
    "v_fma_f32 v122, v18, v122, v122\n"
    "v_mfma_f32_16x16x32_bf16 v[232:235], a[92:95], a[44:47], v[232:235]\n"
    "buffer_load_dwordx4 a[144:147], %[l16], %[srdW], s54 offen offset:0\n"
    "v_fma_f32 v123, v19, v123, v123\n"
    "v_mfma_f32_16x16x32_bf16 v[232:235], a[88:91], a[20:23], v[232:235]\n"
    "v_fma_f32 v116, v116, v112, v112\n"
    "v_fma_f32 v117, v117, v113, v113\n"
    "v_mfma_f32_16x16x32_bf16 v[232:235], a[84:87], a[16:19], v[232:235]\n"
    "v_fma_f32 v118, v118, v114, v114\n"
    "v_fma_f32 v119, v119, v115, v115\n"
    "v_mfma_f32_16x16x32_bf16 v[232:235], a[80:83], a[12:15], v[232:235]\n"
    "v_fma_f32 v124, v124, v120, v120\n"
    "v_fma_f32 v125, v125, v121, v121\n"
    "v_mfma_f32_16x16x32_bf16 v[236:239], a[104:107], a[12:15], 0\n"
    "ds_read_b128 a[12:15], %[rd0] offset:22528\n"
    "v_fma_f32 v126, v126, v122, v122\n"
    "v_mfma_f32_16x16x32_bf16 v[236:239], a[108:111], a[16:19], v[236:239]\n"
    "v_fma_f32 v127, v127, v123, v123\n"
    "v_mul_f32 v140, s73, v140\n"
    "v_mfma_f32_16x16x32_bf16 v[236:239], a[112:115], a[20:23], v[236:239]\n"
    "v_rcp_f32 v116, v116\n"
    "v_mfma_f32_16x16x32_bf16 v[236:239], a[116:119], a[44:47], v[236:239]\n"
    "ds_read_b128 a[44:47], %[rd1] offset:22528\n"
    "v_mul_f32 v141, s73, v141\n"
    "v_mfma_f32_16x16x32_bf16 v[236:239], a[120:123], a[48:51], v[236:239]\n"
    "buffer_load_dwordx4 a[148:151], %[l16], %[srdW], s54 offen offset:1024\n"
    "v_mul_f32 v142, s73, v142\n"
    "v_mfma_f32_16x16x32_bf16 v[236:239], a[124:127], a[52:55], v[236:239]\n"
    "s_waitcnt lgkmcnt(12)\n"
    "v_rcp_f32 v117, v117\n"
    "v_mfma_f32_16x16x32_bf16 v[240:243], a[100:103], a[56:59], 0\n"
    "v_rcp_f32 v118, v118\n"
    "v_mfma_f32_16x16x32_bf16 v[240:243], a[96:99], a[52:55], v[240:243]\n"
    "ds_read_b128 a[68:71], %[re0] offset:2048\n"
    "v_mul_f32 v143, s73, v143\n"
    "v_mfma_f32_16x16x32_bf16 v[240:243], a[92:95], a[48:51], v[240:243]\n"
    "buffer_load_dwordx4 a[152:155], %[l16], %[srdW], s54 offen offset:2048\n"
    "v_mul_f32 v128, s72, v128\n"
    "v_mfma_f32_16x16x32_bf16 v[240:243], a[88:91], a[24:27], v[240:243]\n"
    "v_rcp_f32 v119, v119\n"
    "v_mfma_f32_16x16x32_bf16 v[240:243], a[84:87], a[20:23], v[240:243]\n"
    "v_rcp_f32 v124, v124\n"
    "v_mfma_f32_16x16x32_bf16 v[240:243], a[80:83], a[16:19], v[240:243]\n"
    "v_rcp_f32 v125, v125\n"
    "v_mfma_f32_16x16x32_bf16 v[244:247], a[104:107], a[16:19], 0\n"
    "ds_read_b128 a[16:19], %[rd0] offset:24576\n"
    "v_mul_f32 v129, s72, v129\n"
    "v_mfma_f32_16x16x32_bf16 v[244:247], a[108:111], a[20:23], v[244:247]\n"
    "v_rcp_f32 v126, v126\n"
    "v_mfma_f32_16x16x32_bf16 v[244:247], a[112:115], a[24:27], v[244:247]\n"
    "v_rcp_f32 v127, v127\n"
    "v_mfma_f32_16x16x32_bf16 v[244:247], a[116:119], a[48:51], v[244:247]\n"
    "ds_read_b128 a[48:51], %[rd1] offset:24576\n"
    "v_fma_f32 v116, -abs(v112), v116, v116\n"
    "v_mfma_f32_16x16x32_bf16 v[244:247], a[120:123], a[52:55], v[244:247]\n"
    "buffer_load_dwordx4 a[156:159], %[l16], %[srdW], s54 offen offset:3072\n"
    "v_fma_f32 v117, -abs(v113), v117, v117\n"
    "v_mfma_f32_16x16x32_bf16 v[244:247], a[124:127], a[56:59], v[244:247]\n"
    "v_fma_f32 v118, -abs(v114), v118, v118\n"
    "v_fma_f32 v119, -abs(v115), v119, v119\n"
    "v_mfma_f32_16x16x32_bf16 v[248:251], a[100:103], a[60:63], 0\n"
    "s_add_u32 s54, s54, 4096\n"
    "v_fma_f32 v124, -abs(v120), v124, v124\n"
    "v_fma_f32 v125, -abs(v121), v125, v125\n"
    "v_mfma_f32_16x16x32_bf16 v[248:251], a[96:99], a[56:59], v[248:251]\n"
    "ds_read_b128 a[76:79], %[re1] offset:2048\n"
    "v_fma_f32 v126, -abs(v122), v126, v126\n"
    "v_mfma_f32_16x16x32_bf16 v[248:251], a[92:95], a[52:55], v[248:251]\n"
    "buffer_load_dwordx4 a[160:163], %[l16], %[srdW], s54 offen offset:0\n"
    "v_fma_f32 v127, -abs(v123), v127, v127\n"
    "v_mfma_f32_16x16x32_bf16 v[248:251], a[88:91], a[28:31], v[248:251]\n"
    "v_cvt_pk_bf16_f32 v16, v116, v117\n"
    "v_cvt_pk_bf16_f32 v17, v118, v119\n"
    "v_mfma_f32_16x16x32_bf16 v[248:251], a[84:87], a[24:27], v[248:251]\n"
    "v_cvt_pk_bf16_f32 v18, v124, v125\n"
    "v_cvt_pk_bf16_f32 v19, v126, v127\n"
    "v_mfma_f32_16x16x32_bf16 v[248:251], a[80:83], a[20:23], v[248:251]\n"
    "ds_write_b64 %[vsw], v[16:17] offset:8192\n"
    "v_mfma_f32_16x16x32_bf16 v[252:255], a[104:107], a[20:23], 0\n"
    "ds_read_b128 a[20:23], %[rd0] offset:26624\n"
    "v_mul_f32 v130, s72, v130\n"
    "v_mfma_f32_16x16x32_bf16 v[252:255], a[108:111], a[24:27], v[252:255]\n"
    "ds_write_b64 %[vsw], v[18:19] offset:8320\n"
    "v_mfma_f32_16x16x32_bf16 v[252:255], a[112:115], a[28:31], v[252:255]\n"
    "v_mul_f32 v131, s72, v131\n"
    "v_mul_f32 v136, s72, v136\n"
    "v_mfma_f32_16x16x32_bf16 v[252:255], a[116:119], a[52:55], v[252:255]\n"
    "ds_read_b128 a[52:55], %[rd1] offset:26624\n"
    "v_mul_f32 v137, s72, v137\n"
    "v_mfma_f32_16x16x32_bf16 v[252:255], a[120:123], a[56:59], v[252:255]\n"
    "buffer_load_dwordx4 a[164:167], %[l16], %[srdW], s54 offen offset:1024\n"
    "v_mul_f32 v138, s72, v138\n"
    "v_mfma_f32_16x16x32_bf16 v[252:255], a[124:127], a[60:63], v[252:255]\n"
    "s_waitcnt lgkmcnt(14)\n"
    "v_mul_f32 v139, s72, v139\n"
    "v_mul_f32 v148, s73, v148\n"
    "v_mfma_f32_16x16x32_bf16 a[176:179], a[100:103], a[32:35], 0\n"
    "v_exp_f32 v132, v132\n"
    "v_mfma_f32_16x16x32_bf16 a[176:179], a[96:99], a[60:63], a[176:179]\n"
    "v_exp_f32 v133, v133\n"
    "v_mfma_f32_16x16x32_bf16 a[176:179], a[92:95], a[56:59], a[176:179]\n"
    "buffer_load_dwordx4 a[168:171], %[l16], %[srdW], s54 offen offset:2048\n"
    "v_mul_f32 v149, s73, v149\n"
    "v_mfma_f32_16x16x32_bf16 a[176:179], a[88:91], a[0:3], a[176:179]\n"
    "v_exp_f32 v134, v134\n"
    "v_mfma_f32_16x16x32_bf16 a[176:179], a[84:87], a[28:31], a[176:179]\n"
    "v_exp_f32 v135, v135\n"
    "v_mfma_f32_16x16x32_bf16 a[176:179], a[80:83], a[24:27], a[176:179]\n"
    "v_exp_f32 v140, v140\n"
    "v_mfma_f32_16x16x32_bf16 a[180:183], a[104:107], a[24:27], 0\n"
    "ds_read_b128 a[24:27], %[rd0] offset:28672\n"
    "v_mul_f32 v150, s73, v150\n"
    "v_mfma_f32_16x16x32_bf16 a[180:183], a[108:111], a[28:31], a[180:183]\n"
    "v_exp_f32 v141, v141\n"
    "v_mfma_f32_16x16x32_bf16 a[180:183], a[112:115], a[0:3], a[180:183]\n"
    "v_exp_f32 v142, v142\n"
    "v_mfma_f32_16x16x32_bf16 a[180:183], a[116:119], a[56:59], a[180:183]\n"
    "ds_read_b128 a[56:59], %[rd1] offset:28672\n"
    "v_mul_f32 v151, s73, v151\n"
    "v_mfma_f32_16x16x32_bf16 a[180:183], a[120:123], a[60:63], a[180:183]\n"
    "buffer_load_dwordx4 a[172:175], %[l16], %[srdW], s54 offen offset:3072\n"
    "v_mul_f32 v156, s73, v156\n"
    "v_mfma_f32_16x16x32_bf16 a[180:183], a[124:127], a[32:35], a[180:183]\n"
    "v_exp_f32 v143, v143\n"
    "v_mfma_f32_16x16x32_bf16 a[184:187], a[100:103], a[36:39], 0\n"
    "s_add_u32 s54, s54, 4096\n"
    "v_exp_f32_e64 v20, -abs(v128)\n"
    "v_mfma_f32_16x16x32_bf16 a[184:187], a[96:99], a[32:35], a[184:187]\n"
    "s_add_u32 m0, s69, 0\n"
    "s_add_u32 s57, s55, 0\n"
    "v_subrev_u32 v13, 0x2000, %[od1]\n"
    "v_mfma_f32_16x16x32_bf16 a[184:187], a[92:95], a[60:63], a[184:187]\n"
    "buffer_load_dwordx4 v13, %[srdX], s57 offen  lds\n"
    "v_mul_f32 v157, s73, v157\n"
    "v_mfma_f32_16x16x32_bf16 a[184:187], a[88:91], a[4:7], a[184:187]\n"
    "s_add_u32 m0, s69, 4096\n"
    "s_add_u32 s57, s55, 0\n"
    "v_mul_f32 v158, s73, v158\n"
    "v_mfma_f32_16x16x32_bf16 a[184:187], a[84:87], a[0:3], a[184:187]\n"
    "buffer_load_dwordx4 %[od1], %[srdX], s57 offen  lds\n"
    "v_mul_f32 v159, s73, v159\n"
    "v_mfma_f32_16x16x32_bf16 a[184:187], a[80:83], a[28:31], a[184:187]\n"
    "v_exp_f32_e64 v21, -abs(v129)\n"
    "v_mfma_f32_16x16x32_bf16 a[188:191], a[104:107], a[28:31], 0\n"
    "ds_read_b128 a[28:31], %[rd0] offset:30720\n"
    "v_mul_f32 v144, s72, v144\n"
    "v_mfma_f32_16x16x32_bf16 a[188:191], a[108:111], a[0:3], a[188:191]\n"
    "v_exp_f32_e64 v22, -abs(v130)\n"
    "v_mfma_f32_16x16x32_bf16 a[188:191], a[112:115], a[4:7], a[188:191]\n"
    "v_exp_f32_e64 v23, -abs(v131)\n"
    "v_mfma_f32_16x16x32_bf16 a[188:191], a[116:119], a[60:63], a[188:191]\n"
    "ds_read_b128 a[60:63], %[rd1] offset:30720\n"
    "v_bfi_b32 v128, s61, 0.5, v128\n"
    "v_mfma_f32_16x16x32_bf16 a[188:191], a[120:123], a[32:35], a[188:191]\n"
    "v_bfi_b32 v129, s61, 0.5, v129\n"
    "v_bfi_b32 v130, s61, 0.5, v130\n"
    "v_mfma_f32_16x16x32_bf16 a[188:191], a[124:127], a[36:39], a[188:191]\n"
    "s_waitcnt lgkmcnt(12)\n"
    "v_bfi_b32 v131, s61, 0.5, v131\n"
    "v_fma_f32 v128, v20, v128, v128\n"
    "v_mfma_f32_16x16x32_bf16 a[192:195], a[100:103], a[40:43], 0\n"
    "v_fma_f32 v129, v21, v129, v129\n"
    "v_fma_f32 v130, v22, v130, v130\n"
    "v_mfma_f32_16x16x32_bf16 a[192:195], a[96:99], a[36:39], a[192:195]\n"
    "v_fma_f32 v131, v23, v131, v131\n"
    "v_mul_f32 v145, s72, v145\n"
    "v_mfma_f32_16x16x32_bf16 a[192:195], a[92:95], a[32:35], a[192:195]\n"
    "s_add_u32 s63, s63, 1\n"
    "v_exp_f32_e64 v20, -abs(v136)\n"
    "v_mfma_f32_16x16x32_bf16 a[192:195], a[88:91], a[8:11], a[192:195]\n"
    "v_exp_f32_e64 v21, -abs(v137)\n"
    "v_mfma_f32_16x16x32_bf16 a[192:195], a[84:87], a[4:7], a[192:195]\n"
    "v_exp_f32_e64 v22, -abs(v138)\n"
    "v_mfma_f32_16x16x32_bf16 a[192:195], a[80:83], a[0:3], a[192:195]\n"
    "v_exp_f32_e64 v23, -abs(v139)\n"
    "v_mfma_f32_16x16x32_bf16 a[196:199], a[104:107], a[0:3], 0\n"
    "s_waitcnt lgkmcnt(0)\n"
    "v_mov_b32 v13, s63\n"
    "v_mfma_f32_16x16x32_bf16 a[196:199], a[108:111], a[4:7], a[196:199]\n"
    "ds_write_b32 v14, v13\n"
    "v_mfma_f32_16x16x32_bf16 a[196:199], a[112:115], a[8:11], a[196:199]\n"
    "v_bfi_b32 v136, s61, 0.5, v136\n"
    "v_bfi_b32 v137, s61, 0.5, v137\n"
    "v_mfma_f32_16x16x32_bf16 a[196:199], a[116:119], a[32:35], a[196:199]\n"
    "v_bfi_b32 v138, s61, 0.5, v138\n"
    "v_bfi_b32 v139, s61, 0.5, v139\n"
    "v_mfma_f32_16x16x32_bf16 a[196:199], a[120:123], a[36:39], a[196:199]\n"
    "v_fma_f32 v136, v20, v136, v136\n"
    "v_fma_f32 v137, v21, v137, v137\n"
    "v_mfma_f32_16x16x32_bf16 a[196:199], a[124:127], a[40:43], a[196:199]\n"
    "v_fma_f32 v138, v22, v138, v138\n"
    "v_fma_f32 v139, v23, v139, v139\n"
    "v_mfma_f32_16x16x32_bf16 a[200:203], a[100:103], a[44:47], 0\n"
    "v_fma_f32 v132, v132, v128, v128\n"
    "v_fma_f32 v133, v133, v129, v129\n"
    "v_mfma_f32_16x16x32_bf16 a[200:203], a[96:99], a[40:43], a[200:203]\n"
    "v_fma_f32 v134, v134, v130, v130\n"
    "v_fma_f32 v135, v135, v131, v131\n"
    "v_mfma_f32_16x16x32_bf16 a[200:203], a[92:95], a[36:39], a[200:203]\n"
    "v_fma_f32 v140, v140, v136, v136\n"
    "v_fma_f32 v141, v141, v137, v137\n"
    "v_mfma_f32_16x16x32_bf16 a[200:203], a[88:91], a[12:15], a[200:203]\n"
    "v_fma_f32 v142, v142, v138, v138\n"
    "v_fma_f32 v143, v143, v139, v139\n"
    "v_mfma_f32_16x16x32_bf16 a[200:203], a[84:87], a[8:11], a[200:203]\n"
    "ds_read_b32 v13, v12\n"
    "v_mul_f32 v146, s72, v146\n"
    "v_mfma_f32_16x16x32_bf16 a[200:203], a[80:83], a[4:7], a[200:203]\n"
    "v_rcp_f32 v132, v132\n"
    "v_mfma_f32_16x16x32_bf16 a[204:207], a[104:107], a[4:7], 0\n"
    "v_rcp_f32 v133, v133\n"
    "v_mfma_f32_16x16x32_bf16 a[204:207], a[108:111], a[8:11], a[204:207]\n"
    "v_rcp_f32 v134, v134\n"
    "v_mfma_f32_16x16x32_bf16 a[204:207], a[112:115], a[12:15], a[204:207]\n"
    "v_rcp_f32 v135, v135\n"
    "v_mfma_f32_16x16x32_bf16 a[204:207], a[116:119], a[36:39], a[204:207]\n"
    "v_rcp_f32 v140, v140\n"
    "v_mfma_f32_16x16x32_bf16 a[204:207], a[120:123], a[40:43], a[204:207]\n"
    "s_waitcnt lgkmcnt(0)\n"
    "v_cmp_gt_u32 vcc, s63, v13\n"
    "v_mul_f32 v147, s72, v147\n"
    "v_mfma_f32_16x16x32_bf16 a[204:207], a[124:127], a[44:47], a[204:207]\n"
    "s_cbranch_vccnz Ls1%=\n"
    "Lb1%=:\n"
    "v_mul_f32 v152, s72, v152\n"
    "v_mfma_f32_16x16x32_bf16 a[208:211], a[100:103], a[48:51], 0\n"
    "ds_read_b128 a[0:3], %[rd0] offset:32768\n"
    "v_mul_f32 v153, s72, v153\n"
    "v_mfma_f32_16x16x32_bf16 a[208:211], a[96:99], a[44:47], a[208:211]\n"
    "ds_read_b128 a[32:35], %[rd1] offset:32768\n"
    "v_mul_f32 v154, s72, v154\n"
    "v_mfma_f32_16x16x32_bf16 a[208:211], a[92:95], a[40:43], a[208:211]\n"
    "ds_read_b128 a[4:7], %[rd0] offset:34816\n"
    "v_mul_f32 v155, s72, v155\n"
    "v_mfma_f32_16x16x32_bf16 a[208:211], a[88:91], a[16:19], a[208:211]\n"
    "ds_read_b128 a[36:39], %[rd1] offset:34816\n"
    "v_mfma_f32_16x16x32_bf16 a[208:211], a[84:87], a[12:15], a[208:211]\n"
    "ds_read_b128 v[24:27], v14 offset:4096\n"
    "v_mfma_f32_16x16x32_bf16 a[208:211], a[80:83], a[8:11], a[208:211]\n"
    "ds_read_b128 v[28:31], v14 offset:8192\n"
    "v_mfma_f32_16x16x32_bf16 a[212:215], a[104:107], a[8:11], 0\n"
    "ds_read_b128 a[8:11], %[rd0] offset:36864\n"
    "v_mfma_f32_16x16x32_bf16 a[212:215], a[108:111], a[12:15], a[212:215]\n"
    "s_add_u32 m0, s69, 8192\n"
    "s_add_u32 s57, s55, 8192\n"
    "v_mfma_f32_16x16x32_bf16 a[212:215], a[112:115], a[16:19], a[212:215]\n"
    "buffer_load_dwordx4 %[od1], %[srdX], s57 offen  lds\n"
    "v_mfma_f32_16x16x32_bf16 a[212:215], a[116:119], a[40:43], a[212:215]\n"
    "ds_read_b128 a[40:43], %[rd1] offset:36864\n"
    "v_mfma_f32_16x16x32_bf16 a[212:215], a[120:123], a[44:47], a[212:215]\n"
    "v_rcp_f32 v141, v141\n"
    "v_mfma_f32_16x16x32_bf16 a[212:215], a[124:127], a[48:51], a[212:215]\n"
    "v_rcp_f32 v142, v142\n"
    "v_mfma_f32_16x16x32_bf16 a[216:219], a[100:103], a[52:55], 0\n"
    "v_rcp_f32 v143, v143\n"
    "v_mfma_f32_16x16x32_bf16 a[216:219], a[96:99], a[48:51], a[216:219]\n"
    "v_exp_f32 v148, v148\n"
    "v_mfma_f32_16x16x32_bf16 a[216:219], a[92:95], a[44:47], a[216:219]\n"
    "s_add_u32 m0, s69, 12288\n"
    "s_add_u32 s57, s55, 16384\n"
    "s_waitcnt lgkmcnt(3)\n"
    "v_mfma_f32_16x16x32_bf16 a[216:219], a[88:91], a[20:23], a[216:219]\n"
    "buffer_load_dwordx4 %[od1], %[srdX], s57 offen  lds\n"
    "v_fma_f32 v132, -abs(v128), v132, v132\n"
    "v_mfma_f32_16x16x32_bf16 a[216:219], a[84:87], a[16:19], a[216:219]\n"
    "s_mov_b32 s60, 0\n"
    "buffer_store_dwordx4 v[24:27], v15, s[76:79], s60 offen sc0 sc1\n"
    "v_mfma_f32_16x16x32_bf16 a[216:219], a[80:83], a[12:15], a[216:219]\n"
    "ds_read_b128 v[24:27], v14 offset:12288\n"
    "v_fma_f32 v133, -abs(v129), v133, v133\n"
    "v_mfma_f32_16x16x32_bf16 a[220:223], a[104:107], a[12:15], 0\n"
    "ds_read_b128 a[12:15], %[rd0] offset:38912\n"
    "v_fma_f32 v134, -abs(v130), v134, v134\n"
    "v_mfma_f32_16x16x32_bf16 a[220:223], a[108:111], a[16:19], a[220:223]\n"
    "v_exp_f32 v149, v149\n"
    "v_mfma_f32_16x16x32_bf16 a[220:223], a[112:115], a[20:23], a[220:223]\n"
    "v_exp_f32 v150, v150\n"
    "v_mfma_f32_16x16x32_bf16 a[220:223], a[116:119], a[44:47], a[220:223]\n"
    "ds_read_b128 a[44:47], %[rd1] offset:38912\n"
    "v_fma_f32 v135, -abs(v131), v135, v135\n"
    "v_mfma_f32_16x16x32_bf16 a[220:223], a[120:123], a[48:51], a[220:223]\n"
    "v_exp_f32 v151, v151\n"
    "v_mfma_f32_16x16x32_bf16 a[220:223], a[124:127], a[52:55], a[220:223]\n"
    "v_exp_f32 v156, v156\n"
    "v_mfma_f32_16x16x32_bf16 a[224:227], a[100:103], a[56:59], 0\n"
    "v_exp_f32 v157, v157\n"
    "v_mfma_f32_16x16x32_bf16 a[224:227], a[96:99], a[52:55], a[224:227]\n"
    "v_exp_f32 v158, v158\n"
    "v_mfma_f32_16x16x32_bf16 a[224:227], a[92:95], a[48:51], a[224:227]\n"
    "s_add_u32 m0, s69, 16384\n"
    "s_add_u32 s57, s55, 24576\n"
    "v_fma_f32 v140, -abs(v136), v140, v140\n"
    "v_mfma_f32_16x16x32_bf16 a[224:227], a[88:91], a[24:27], a[224:227]\n"
    "buffer_load_dwordx4 %[od1], %[srdX], s57 offen  lds\n"
    "v_fma_f32 v141, -abs(v137), v141, v141\n"
    "v_mfma_f32_16x16x32_bf16 a[224:227], a[84:87], a[20:23], a[224:227]\n"
    "v_exp_f32 v159, v159\n"
    "v_mfma_f32_16x16x32_bf16 a[224:227], a[80:83], a[16:19], a[224:227]\n"
    "v_exp_f32_e64 v16, -abs(v144)\n"
    "v_mfma_f32_16x16x32_bf16 a[228:231], a[104:107], a[16:19], 0\n"
    "ds_read_b128 a[16:19], %[rd0] offset:40960\n"
    "v_fma_f32 v142, -abs(v138), v142, v142\n"
    "v_mfma_f32_16x16x32_bf16 a[228:231], a[108:111], a[20:23], a[228:231]\n"
    "v_exp_f32_e64 v17, -abs(v145)\n"
    "v_mfma_f32_16x16x32_bf16 a[228:231], a[112:115], a[24:27], a[228:231]\n"
    "v_exp_f32_e64 v18, -abs(v146)\n"
    "v_mfma_f32_16x16x32_bf16 a[228:231], a[116:119], a[48:51], a[228:231]\n"
    "ds_read_b128 a[48:51], %[rd1] offset:40960\n"
    "v_fma_f32 v143, -abs(v139), v143, v143\n"
    "v_mfma_f32_16x16x32_bf16 a[228:231], a[120:123], a[52:55], a[228:231]\n"
    "v_exp_f32_e64 v19, -abs(v147)\n"
    "v_mfma_f32_16x16x32_bf16 a[228:231], a[124:127], a[56:59], a[228:231]\n"
    "v_cvt_pk_bf16_f32 v20, v132, v133\n"
    "v_cvt_pk_bf16_f32 v21, v134, v135\n"
    "v_mfma_f32_16x16x32_bf16 a[232:235], a[100:103], a[60:63], 0\n"
    "v_cvt_pk_bf16_f32 v22, v140, v141\n"
    "v_cvt_pk_bf16_f32 v23, v142, v143\n"
    "v_mfma_f32_16x16x32_bf16 a[232:235], a[96:99], a[56:59], a[232:235]\n"
    "ds_write_b64 %[vsw], v[20:21] offset:12288\n"
    "v_mfma_f32_16x16x32_bf16 a[232:235], a[92:95], a[52:55], a[232:235]\n"
    "s_add_u32 m0, s69, 20480\n"
    "s_add_u32 s57, s55, 32768\n"
    "v_bfi_b32 v144, s61, 0.5, v144\n"
    "v_mfma_f32_16x16x32_bf16 a[232:235], a[88:91], a[28:31], a[232:235]\n"
    "buffer_load_dwordx4 %[od1], %[srdX], s57 offen  lds\n"
    "v_bfi_b32 v145, s61, 0.5, v145\n"
    "v_mfma_f32_16x16x32_bf16 a[232:235], a[84:87], a[24:27], a[232:235]\n"
    "ds_write_b64 %[vsw], v[22:23] offset:12416\n"
    "v_mfma_f32_16x16x32_bf16 a[232:235], a[80:83], a[20:23], a[232:235]\n"
    "v_bfi_b32 v146, s61, 0.5, v146\n"
    "v_bfi_b32 v147, s61, 0.5, v147\n"
    "v_mfma_f32_16x16x32_bf16 a[236:239], a[104:107], a[20:23], 0\n"
    "ds_read_b128 a[20:23], %[rd0] offset:43008\n"
    "v_fma_f32 v144, v16, v144, v144\n"
    "v_mfma_f32_16x16x32_bf16 a[236:239], a[108:111], a[24:27], a[236:239]\n"
    "v_fma_f32 v145, v17, v145, v145\n"
    "v_fma_f32 v146, v18, v146, v146\n"
    "v_mfma_f32_16x16x32_bf16 a[236:239], a[112:115], a[28:31], a[236:239]\n"
    "v_fma_f32 v147, v19, v147, v147\n"
    "v_mul_f32 v164, s73, v164\n"
    "v_mfma_f32_16x16x32_bf16 a[236:239], a[116:119], a[52:55], a[236:239]\n"
    "ds_read_b128 a[52:55], %[rd1] offset:43008\n"
    "v_mul_f32 v165, s73, v165\n"
    "v_mfma_f32_16x16x32_bf16 a[236:239], a[120:123], a[56:59], a[236:239]\n"
    "v_exp_f32_e64 v16, -abs(v152)\n"
    "v_mfma_f32_16x16x32_bf16 a[236:239], a[124:127], a[60:63], a[236:239]\n"
    "v_exp_f32_e64 v17, -abs(v153)\n"
    "v_mfma_f32_16x16x32_bf16 a[240:243], a[100:103], a[72:75], 0\n"
    "v_exp_f32_e64 v18, -abs(v154)\n"
    "v_mfma_f32_16x16x32_bf16 a[240:243], a[96:99], a[60:63], a[240:243]\n"
    "v_exp_f32_e64 v19, -abs(v155)\n"
    "v_mfma_f32_16x16x32_bf16 a[240:243], a[92:95], a[56:59], a[240:243]\n"
    "s_add_u32 m0, s69, 24576\n"
    "s_add_u32 s57, s55, 40960\n"
    "v_bfi_b32 v152, s61, 0.5, v152\n"
    "v_mfma_f32_16x16x32_bf16 a[240:243], a[88:91], a[64:67], a[240:243]\n"
    "buffer_load_dwordx4 %[od1], %[srdX], s57 offen  lds\n"
    "v_bfi_b32 v153, s61, 0.5, v153\n"
    "v_mfma_f32_16x16x32_bf16 a[240:243], a[84:87], a[28:31], a[240:243]\n"
    "v_bfi_b32 v154, s61, 0.5, v154\n"
    "v_bfi_b32 v155, s61, 0.5, v155\n"
    "v_mfma_f32_16x16x32_bf16 a[240:243], a[80:83], a[24:27], a[240:243]\n"
    "v_fma_f32 v152, v16, v152, v152\n"
    "v_fma_f32 v153, v17, v153, v153\n"
    "v_mfma_f32_16x16x32_bf16 a[244:247], a[104:107], a[24:27], 0\n"
    "ds_read_b128 a[24:27], %[rd0] offset:45056\n"
    "v_fma_f32 v154, v18, v154, v154\n"
    "v_mfma_f32_16x16x32_bf16 a[244:247], a[108:111], a[28:31], a[244:247]\n"
    "v_fma_f32 v155, v19, v155, v155\n"
    "v_fma_f32 v148, v148, v144, v144\n"
    "v_mfma_f32_16x16x32_bf16 a[244:247], a[112:115], a[64:67], a[244:247]\n"
    "s_waitcnt lgkmcnt(12)\n"
    "v_fma_f32 v149, v149, v145, v145\n"
    "v_fma_f32 v150, v150, v146, v146\n"
    "v_mfma_f32_16x16x32_bf16 a[244:247], a[116:119], a[56:59], a[244:247]\n"
    "ds_read_b128 a[56:59], %[rd1] offset:45056\n"
    "v_fma_f32 v151, v151, v147, v147\n"
    "v_mfma_f32_16x16x32_bf16 a[244:247], a[120:123], a[60:63], a[244:247]\n"
    "s_mov_b32 s60, 8192\n"
    "buffer_store_dwordx4 v[28:31], %[oo], s[76:79], s60 offen sc0 sc1\n"
    "v_mfma_f32_16x16x32_bf16 a[244:247], a[124:127], a[72:75], a[244:247]\n"
    "v_fma_f32 v156, v156, v152, v152\n"
    "v_fma_f32 v157, v157, v153, v153\n"
    "v_mfma_f32_16x16x32_bf16 a[248:251], a[100:103], a[76:79], 0\n"
    "v_fma_f32 v158, v158, v154, v154\n"
    "v_fma_f32 v159, v159, v155, v155\n"
    "v_mfma_f32_16x16x32_bf16 a[248:251], a[96:99], a[72:75], a[248:251]\n"
    "v_rcp_f32 v148, v148\n"
    "v_mfma_f32_16x16x32_bf16 a[248:251], a[92:95], a[60:63], a[248:251]\n"
    "s_add_u32 m0, s69, 28672\n"
    "s_add_u32 s57, s55, 49152\n"
    "v_mul_f32 v166, s73, v166\n"
    "v_mfma_f32_16x16x32_bf16 a[248:251], a[88:91], a[68:71], a[248:251]\n"
    "buffer_load_dwordx4 %[od1], %[srdX], s57 offen  lds\n"
    "v_mul_f32 v167, s73, v167\n"
    "v_mfma_f32_16x16x32_bf16 a[248:251], a[84:87], a[64:67], a[248:251]\n"
    "v_rcp_f32 v149, v149\n"
    "v_mfma_f32_16x16x32_bf16 a[248:251], a[80:83], a[28:31], a[248:251]\n"
    "s_add_u32 s69, s69, 0x8000\n"
    "s_and_b32 s69, s69, 0x1ffff\n"
    "v_mul_f32 v172, s73, v172\n"
    "v_mfma_f32_16x16x32_bf16 a[252:255], a[104:107], a[28:31], 0\n"
    "ds_read_b128 a[28:31], %[rd0] offset:47104\n"
    "v_mul_f32 v173, s73, v173\n"
    "v_mfma_f32_16x16x32_bf16 a[252:255], a[108:111], a[64:67], a[252:255]\n"
    "s_add_u32 s55, s55, 128\n"
    "v_rcp_f32 v150, v150\n"
    "v_mfma_f32_16x16x32_bf16 a[252:255], a[112:115], a[68:71], a[252:255]\n"
    "v_rcp_f32 v151, v151\n"
    "v_mfma_f32_16x16x32_bf16 a[252:255], a[116:119], a[60:63], a[252:255]\n"
    "ds_read_b128 a[60:63], %[rd1] offset:47104\n"
    "v_mul_f32 v174, s73, v174\n"
    "v_mfma_f32_16x16x32_bf16 a[252:255], a[120:123], a[72:75], a[252:255]\n"
    "v_rcp_f32 v156, v156\n"
    "v_mfma_f32_16x16x32_bf16 a[252:255], a[124:127], a[76:79], a[252:255]\n"
    "s_waitcnt vmcnt(10)\n"
    "s_waitcnt lgkmcnt(10)\n"
    "s_cmp_lg_u32 s67, 0\n"
    "s_cbranch_scc1 Lc2%=\n"
    "v_mfma_f32_16x16x32_bf16 v[208:211], a[148:151], a[40:43], v[208:211]\n"
    "v_mfma_f32_16x16x32_bf16 v[208:211], a[144:147], a[36:39], v[208:211]\n"
    "v_mfma_f32_16x16x32_bf16 v[208:211], a[140:143], a[32:35], v[208:211]\n"
    "v_mfma_f32_16x16x32_bf16 v[208:211], a[136:139], a[8:11], v[208:211]\n"
    "v_mfma_f32_16x16x32_bf16 v[208:211], a[132:135], a[4:7], v[208:211]\n"
    "v_mfma_f32_16x16x32_bf16 v[208:211], a[128:131], a[0:3], v[208:211]\n"
    "v_mfma_f32_16x16x32_bf16 v[212:215], a[152:155], a[0:3], v[212:215]\n"
    "v_mfma_f32_16x16x32_bf16 v[212:215], a[156:159], a[4:7], v[212:215]\n"
    "v_mfma_f32_16x16x32_bf16 v[212:215], a[160:163], a[8:11], v[212:215]\n"
    "v_mfma_f32_16x16x32_bf16 v[212:215], a[164:167], a[32:35], v[212:215]\n"
    "v_mfma_f32_16x16x32_bf16 v[212:215], a[168:171], a[36:39], v[212:215]\n"
    "v_mfma_f32_16x16x32_bf16 v[212:215], a[172:175], a[40:43], v[212:215]\n"
    "Lc2%=:\n"
    "v_mfma_f32_16x16x32_bf16 v[216:219], a[148:151], a[44:47], v[216:219]\n"
    "ds_read_b128 a[0:3], %[rd0] offset:49152\n"
    "v_mul_f32 v175, s73, v175\n"
    "v_mfma_f32_16x16x32_bf16 v[216:219], a[144:147], a[40:43], v[216:219]\n"
    "v_rcp_f32 v157, v157\n"
    "v_mfma_f32_16x16x32_bf16 v[216:219], a[140:143], a[36:39], v[216:219]\n"
    "buffer_load_dwordx4 a[80:83], %[l16], %[srdW], s54 offen offset:0\n"
    "v_mul_f32 v160, s72, v160\n"
    "v_mfma_f32_16x16x32_bf16 v[216:219], a[136:139], a[12:15], v[216:219]\n"
    "v_rcp_f32 v158, v158\n"
    "v_mfma_f32_16x16x32_bf16 v[216:219], a[132:135], a[8:11], v[216:219]\n"
    "ds_read_b128 a[32:35], %[rd1] offset:49152\n"
    "v_mul_f32 v161, s72, v161\n"
    "v_mfma_f32_16x16x32_bf16 v[216:219], a[128:131], a[4:7], v[216:219]\n"
    "v_rcp_f32 v159, v159\n"
    "v_mfma_f32_16x16x32_bf16 v[220:223], a[152:155], a[4:7], v[220:223]\n"
    "ds_read_b128 a[4:7], %[rd0] offset:51200\n"
    "v_fma_f32 v148, -abs(v144), v148, v148\n"
    "v_mfma_f32_16x16x32_bf16 v[220:223], a[156:159], a[8:11], v[220:223]\n"
    "v_fma_f32 v149, -abs(v145), v149, v149\n"
    "v_fma_f32 v150, -abs(v146), v150, v150\n"
    "v_mfma_f32_16x16x32_bf16 v[220:223], a[160:163], a[12:15], v[220:223]\n"
    "v_fma_f32 v151, -abs(v147), v151, v151\n"
    "v_fma_f32 v156, -abs(v152), v156, v156\n"
    "v_mfma_f32_16x16x32_bf16 v[220:223], a[164:167], a[36:39], v[220:223]\n"
    "ds_read_b128 a[36:39], %[rd1] offset:51200\n"
    "v_fma_f32 v157, -abs(v153), v157, v157\n"
    "v_mfma_f32_16x16x32_bf16 v[220:223], a[168:171], a[40:43], v[220:223]\n"
    "buffer_load_dwordx4 a[84:87], %[l16], %[srdW], s54 offen offset:1024\n"
    "v_fma_f32 v158, -abs(v154), v158, v158\n"
    "v_mfma_f32_16x16x32_bf16 v[220:223], a[172:175], a[44:47], v[220:223]\n"
    "s_waitcnt lgkmcnt(8)\n"
    "v_fma_f32 v159, -abs(v155), v159, v159\n"
    "v_cvt_pk_bf16_f32 v16, v148, v149\n"
    "v_mfma_f32_16x16x32_bf16 v[224:227], a[148:151], a[48:51], v[224:227]\n"
    "v_cvt_pk_bf16_f32 v17, v150, v151\n"
    "v_cvt_pk_bf16_f32 v18, v156, v157\n"
    "v_mfma_f32_16x16x32_bf16 v[224:227], a[144:147], a[44:47], v[224:227]\n"
    "ds_read_b128 a[64:67], %[re0] offset:32768\n"
    "v_cvt_pk_bf16_f32 v19, v158, v159\n"
    "v_mfma_f32_16x16x32_bf16 v[224:227], a[140:143], a[40:43], v[224:227]\n"
    "buffer_load_dwordx4 a[88:91], %[l16], %[srdW], s54 offen offset:2048\n"
    "v_mul_f32 v162, s72, v162\n"
    "v_mfma_f32_16x16x32_bf16 v[224:227], a[136:139], a[16:19], v[224:227]\n"
    "ds_write_b64 %[vsw], v[16:17] offset:16384\n"
    "v_mfma_f32_16x16x32_bf16 v[224:227], a[132:135], a[12:15], v[224:227]\n"
    "ds_write_b64 %[vsw], v[18:19] offset:16512\n"
    "v_mfma_f32_16x16x32_bf16 v[224:227], a[128:131], a[8:11], v[224:227]\n"
    "v_mul_f32 v163, s72, v163\n"
    "v_mul_f32 v168, s72, v168\n"
    "v_mfma_f32_16x16x32_bf16 v[228:231], a[152:155], a[8:11], v[228:231]\n"
    "ds_read_b128 a[8:11], %[rd0] offset:53248\n"
    "v_mul_f32 v169, s72, v169\n"
    "v_mfma_f32_16x16x32_bf16 v[228:231], a[156:159], a[12:15], v[228:231]\n"
    "v_mul_f32 v170, s72, v170\n"
    "v_mul_f32 v171, s72, v171\n"
    "v_mfma_f32_16x16x32_bf16 v[228:231], a[160:163], a[16:19], v[228:231]\n"
    "v_exp_f32 v164, v164\n"
    "v_mfma_f32_16x16x32_bf16 v[228:231], a[164:167], a[40:43], v[228:231]\n"
    "ds_read_b128 a[40:43], %[rd1] offset:53248\n"
    "v_mul_f32 v180, s73, v180\n"
    "v_mfma_f32_16x16x32_bf16 v[228:231], a[168:171], a[44:47], v[228:231]\n"
    "buffer_load_dwordx4 a[92:95], %[l16], %[srdW], s54 offen offset:3072\n"
    "v_mul_f32 v181, s73, v181\n"
    "v_mfma_f32_16x16x32_bf16 v[228:231], a[172:175], a[48:51], v[228:231]\n"
    "v_exp_f32 v165, v165\n"
    "v_mfma_f32_16x16x32_bf16 v[232:235], a[148:151], a[52:55], v[232:235]\n"
    "s_add_u32 s54, s54, 4096\n"
    "v_exp_f32 v166, v166\n"
    "v_mfma_f32_16x16x32_bf16 v[232:235], a[144:147], a[48:51], v[232:235]\n"
    "ds_read_b128 a[72:75], %[re1] offset:32768\n"
    "v_mul_f32 v182, s73, v182\n"
    "v_mfma_f32_16x16x32_bf16 v[232:235], a[140:143], a[44:47], v[232:235]\n"
    "buffer_load_dwordx4 a[96:99], %[l16], %[srdW], s54 offen offset:0\n"
    "v_mul_f32 v183, s73, v183\n"
    "v_mfma_f32_16x16x32_bf16 v[232:235], a[136:139], a[20:23], v[232:235]\n"
    "v_exp_f32 v167, v167\n"
    "v_mfma_f32_16x16x32_bf16 v[232:235], a[132:135], a[16:19], v[232:235]\n"
    "v_exp_f32 v172, v172\n"
    "v_mfma_f32_16x16x32_bf16 v[232:235], a[128:131], a[12:15], v[232:235]\n"
    "v_exp_f32 v173, v173\n"
    "v_mfma_f32_16x16x32_bf16 v[236:239], a[152:155], a[12:15], v[236:239]\n"
    "ds_read_b128 a[12:15], %[rd0] offset:55296\n"
    "v_mul_f32 v188, s73, v188\n"
    "v_mfma_f32_16x16x32_bf16 v[236:239], a[156:159], a[16:19], v[236:239]\n"
    "v_exp_f32 v174, v174\n"
    "v_mfma_f32_16x16x32_bf16 v[236:239], a[160:163], a[20:23], v[236:239]\n"
    "v_exp_f32 v175, v175\n"
    "v_mfma_f32_16x16x32_bf16 v[236:239], a[164:167], a[44:47], v[236:239]\n"
    "ds_read_b128 a[44:47], %[rd1] offset:55296\n"
    "v_mul_f32 v189, s73, v189\n"
    "v_mfma_f32_16x16x32_bf16 v[236:239], a[168:171], a[48:51], v[236:239]\n"
    "buffer_load_dwordx4 a[100:103], %[l16], %[srdW], s54 offen offset:1024\n"
    "v_mul_f32 v190, s73, v190\n"
    "v_mfma_f32_16x16x32_bf16 v[236:239], a[172:175], a[52:55], v[236:239]\n"
    "s_waitcnt lgkmcnt(12)\n"
    "v_exp_f32_e64 v20, -abs(v160)\n"
    "v_mfma_f32_16x16x32_bf16 v[240:243], a[148:151], a[56:59], v[240:243]\n"
    "v_exp_f32_e64 v21, -abs(v161)\n"
    "v_mfma_f32_16x16x32_bf16 v[240:243], a[144:147], a[52:55], v[240:243]\n"
    "ds_read_b128 a[68:71], %[re0] offset:34816\n"
    "v_mul_f32 v191, s73, v191\n"
    "v_mfma_f32_16x16x32_bf16 v[240:243], a[140:143], a[48:51], v[240:243]\n"
    "buffer_load_dwordx4 a[104:107], %[l16], %[srdW], s54 offen offset:2048\n"
    "v_mul_f32 v176, s72, v176\n"
    "v_mfma_f32_16x16x32_bf16 v[240:243], a[136:139], a[24:27], v[240:243]\n"
    "v_exp_f32_e64 v22, -abs(v162)\n"
    "v_mfma_f32_16x16x32_bf16 v[240:243], a[132:135], a[20:23], v[240:243]\n"
    "v_exp_f32_e64 v23, -abs(v163)\n"
    "v_mfma_f32_16x16x32_bf16 v[240:243], a[128:131], a[16:19], v[240:243]\n"
    "v_bfi_b32 v160, s61, 0.5, v160\n"
    "v_bfi_b32 v161, s61, 0.5, v161\n"
    "v_mfma_f32_16x16x32_bf16 v[244:247], a[152:155], a[16:19], v[244:247]\n"
    "ds_read_b128 a[16:19], %[rd0] offset:57344\n"
    "v_bfi_b32 v162, s61, 0.5, v162\n"
    "v_mfma_f32_16x16x32_bf16 v[244:247], a[156:159], a[20:23], v[244:247]\n"
    "v_bfi_b32 v163, s61, 0.5, v163\n"
    "v_fma_f32 v160, v20, v160, v160\n"
    "v_mfma_f32_16x16x32_bf16 v[244:247], a[160:163], a[24:27], v[244:247]\n"
    "v_fma_f32 v161, v21, v161, v161\n"
    "v_fma_f32 v162, v22, v162, v162\n"
    "v_mfma_f32_16x16x32_bf16 v[244:247], a[164:167], a[48:51], v[244:247]\n"
    "ds_read_b128 a[48:51], %[rd1] offset:57344\n"
    "v_fma_f32 v163, v23, v163, v163\n"
    "v_mfma_f32_16x16x32_bf16 v[244:247], a[168:171], a[52:55], v[244:247]\n"
    "buffer_load_dwordx4 a[108:111], %[l16], %[srdW], s54 offen offset:3072\n"
    "v_mul_f32 v177, s72, v177\n"
    "v_mfma_f32_16x16x32_bf16 v[244:247], a[172:175], a[56:59], v[244:247]\n"
    "v_exp_f32_e64 v20, -abs(v168)\n"
    "v_mfma_f32_16x16x32_bf16 v[248:251], a[148:151], a[60:63], v[248:251]\n"
    "s_add_u32 s54, s54, 4096\n"
    "v_exp_f32_e64 v21, -abs(v169)\n"
    "v_mfma_f32_16x16x32_bf16 v[248:251], a[144:147], a[56:59], v[248:251]\n"
    "ds_read_b128 a[76:79], %[re1] offset:34816\n"
    "v_mul_f32 v178, s72, v178\n"
    "v_mfma_f32_16x16x32_bf16 v[248:251], a[140:143], a[52:55], v[248:251]\n"
    "buffer_load_dwordx4 a[112:115], %[l16], %[srdW], s54 offen offset:0\n"
    "v_mul_f32 v179, s72, v179\n"
    "v_mfma_f32_16x16x32_bf16 v[248:251], a[136:139], a[28:31], v[248:251]\n"
    "v_exp_f32_e64 v22, -abs(v170)\n"
    "v_mfma_f32_16x16x32_bf16 v[248:251], a[132:135], a[24:27], v[248:251]\n"
    "v_exp_f32_e64 v23, -abs(v171)\n"
    "v_mfma_f32_16x16x32_bf16 v[248:251], a[128:131], a[20:23], v[248:251]\n"
    "v_bfi_b32 v168, s61, 0.5, v168\n"
    "v_bfi_b32 v169, s61, 0.5, v169\n"
    "v_mfma_f32_16x16x32_bf16 v[252:255], a[152:155], a[20:23], v[252:255]\n"
    "ds_read_b128 a[20:23], %[rd0] offset:59392\n"
    "v_bfi_b32 v170, s61, 0.5, v170\n"
    "v_mfma_f32_16x16x32_bf16 v[252:255], a[156:159], a[24:27], v[252:255]\n"
    "v_bfi_b32 v171, s61, 0.5, v171\n"
    "v_fma_f32 v168, v20, v168, v168\n"
    "v_mfma_f32_16x16x32_bf16 v[252:255], a[160:163], a[28:31], v[252:255]\n"
    "v_fma_f32 v169, v21, v169, v169\n"
    "v_fma_f32 v170, v22, v170, v170\n"
    "v_mfma_f32_16x16x32_bf16 v[252:255], a[164:167], a[52:55], v[252:255]\n"
    "ds_read_b128 a[52:55], %[rd1] offset:59392\n"
    "v_fma_f32 v171, v23, v171, v171\n"
    "v_mfma_f32_16x16x32_bf16 v[252:255], a[168:171], a[56:59], v[252:255]\n"
    "buffer_load_dwordx4 a[116:119], %[l16], %[srdW], s54 offen offset:1024\n"
    "v_fma_f32 v164, v164, v160, v160\n"
    "v_mfma_f32_16x16x32_bf16 v[252:255], a[172:175], a[60:63], v[252:255]\n"
    "s_waitcnt lgkmcnt(14)\n"
    "v_fma_f32 v165, v165, v161, v161\n"
    "v_fma_f32 v166, v166, v162, v162\n"
    "v_mfma_f32_16x16x32_bf16 a[176:179], a[148:151], a[32:35], a[176:179]\n"
    "v_fma_f32 v167, v167, v163, v163\n"
    "v_fma_f32 v172, v172, v168, v168\n"
    "v_mfma_f32_16x16x32_bf16 a[176:179], a[144:147], a[60:63], a[176:179]\n"
    "v_fma_f32 v173, v173, v169, v169\n"
    "v_fma_f32 v174, v174, v170, v170\n"
    "v_mfma_f32_16x16x32_bf16 a[176:179], a[140:143], a[56:59], a[176:179]\n"
    "buffer_load_dwordx4 a[120:123], %[l16], %[srdW], s54 offen offset:2048\n"
    "v_fma_f32 v175, v175, v171, v171\n"
    "v_mfma_f32_16x16x32_bf16 a[176:179], a[136:139], a[0:3], a[176:179]\n"
    "v_rcp_f32 v164, v164\n"
    "v_mfma_f32_16x16x32_bf16 a[176:179], a[132:135], a[28:31], a[176:179]\n"
    "v_rcp_f32 v165, v165\n"
    "v_mfma_f32_16x16x32_bf16 a[176:179], a[128:131], a[24:27], a[176:179]\n"
    "v_rcp_f32 v166, v166\n"
    "v_mfma_f32_16x16x32_bf16 a[180:183], a[152:155], a[24:27], a[180:183]\n"
    "ds_read_b128 a[24:27], %[rd0] offset:61440\n"
    "v_mul_f32 v184, s72, v184\n"
    "v_mfma_f32_16x16x32_bf16 a[180:183], a[156:159], a[28:31], a[180:183]\n"
    "v_rcp_f32 v167, v167\n"
    "v_mfma_f32_16x16x32_bf16 a[180:183], a[160:163], a[0:3], a[180:183]\n"
    "v_rcp_f32 v172, v172\n"
    "v_mfma_f32_16x16x32_bf16 a[180:183], a[164:167], a[56:59], a[180:183]\n"
    "ds_read_b128 a[56:59], %[rd1] offset:61440\n"
    "v_mul_f32 v185, s72, v185\n"
    "v_mfma_f32_16x16x32_bf16 a[180:183], a[168:171], a[60:63], a[180:183]\n"
    "buffer_load_dwordx4 a[124:127], %[l16], %[srdW], s54 offen offset:3072\n"
    "v_mul_f32 v186, s72, v186\n"
    "v_mfma_f32_16x16x32_bf16 a[180:183], a[172:175], a[32:35], a[180:183]\n"
    "v_rcp_f32 v173, v173\n"
    "v_mfma_f32_16x16x32_bf16 a[184:187], a[148:151], a[36:39], a[184:187]\n"
    "s_add_u32 s54, s54, 4096\n"
    "v_mul_f32 v187, s72, v187\n"
    "v_mfma_f32_16x16x32_bf16 a[184:187], a[144:147], a[32:35], a[184:187]\n"
    "s_add_u32 m0, s69, 0\n"
    "s_add_u32 s57, s55, 0\n"
    "v_subrev_u32 v13, 0x2000, %[od1]\n"
    "v_mfma_f32_16x16x32_bf16 a[184:187], a[140:143], a[60:63], a[184:187]\n"
    "buffer_load_dwordx4 v13, %[srdX], s57 offen  lds\n"
    "v_mfma_f32_16x16x32_bf16 a[184:187], a[136:139], a[4:7], a[184:187]\n"
    "s_add_u32 m0, s69, 4096\n"
    "s_add_u32 s57, s55, 0\n"
    "v_mfma_f32_16x16x32_bf16 a[184:187], a[132:135], a[0:3], a[184:187]\n"
    "buffer_load_dwordx4 %[od1], %[srdX], s57 offen  lds\n"
    "v_mfma_f32_16x16x32_bf16 a[184:187], a[128:131], a[28:31], a[184:187]\n"
    "s_mov_b32 s60, 16384\n"
    "buffer_store_dwordx4 v[24:27], %[oo], s[76:79], s60 offen sc0 sc1\n"
    "v_mfma_f32_16x16x32_bf16 a[188:191], a[152:155], a[28:31], a[188:191]\n"
    "ds_read_b128 a[28:31], %[rd0] offset:63488\n"
    "v_mfma_f32_16x16x32_bf16 a[188:191], a[156:159], a[0:3], a[188:191]\n"
    "v_rcp_f32 v174, v174\n"
    "v_mfma_f32_16x16x32_bf16 a[188:191], a[160:163], a[4:7], a[188:191]\n"
    "v_rcp_f32 v175, v175\n"
    "v_mfma_f32_16x16x32_bf16 a[188:191], a[164:167], a[60:63], a[188:191]\n"
    "ds_read_b128 a[60:63], %[rd1] offset:63488\n"
    "v_fma_f32 v164, -abs(v160), v164, v164\n"
    "v_mfma_f32_16x16x32_bf16 a[188:191], a[168:171], a[32:35], a[188:191]\n"
    "v_exp_f32 v180, v180\n"
    "v_mfma_f32_16x16x32_bf16 a[188:191], a[172:175], a[36:39], a[188:191]\n"
    "s_waitcnt lgkmcnt(10)\n"
    "v_exp_f32 v181, v181\n"
    "v_mfma_f32_16x16x32_bf16 a[192:195], a[148:151], a[40:43], a[192:195]\n"
    "v_xor_b32 %[rd0], 0x10000, %[rd0]\n"
    "v_fma_f32 v165, -abs(v161), v165, v165\n"
    "v_mfma_f32_16x16x32_bf16 a[192:195], a[144:147], a[36:39], a[192:195]\n"
    "v_xor_b32 %[rd1], 0x10000, %[rd1]\n"
    "v_fma_f32 v166, -abs(v162), v166, v166\n"
    "v_mfma_f32_16x16x32_bf16 a[192:195], a[140:143], a[32:35], a[192:195]\n"
    "s_add_u32 s63, s63, 1\n"
    "v_exp_f32 v182, v182\n"
    "v_mfma_f32_16x16x32_bf16 a[192:195], a[136:139], a[8:11], a[192:195]\n"
    "v_exp_f32 v183, v183\n"
    "v_mfma_f32_16x16x32_bf16 a[192:195], a[132:135], a[4:7], a[192:195]\n"
    "v_exp_f32 v188, v188\n"
    "v_mfma_f32_16x16x32_bf16 a[192:195], a[128:131], a[0:3], a[192:195]\n"
    "v_exp_f32 v189, v189\n"
    "v_mfma_f32_16x16x32_bf16 a[196:199], a[152:155], a[0:3], a[196:199]\n"
    "s_waitcnt lgkmcnt(0)\n"
    "v_mov_b32 v13, s63\n"
    "v_mfma_f32_16x16x32_bf16 a[196:199], a[156:159], a[4:7], a[196:199]\n"
    "ds_write_b32 v14, v13\n"
    "v_mfma_f32_16x16x32_bf16 a[196:199], a[160:163], a[8:11], a[196:199]\n"
    "v_exp_f32 v190, v190\n"
    "v_mfma_f32_16x16x32_bf16 a[196:199], a[164:167], a[32:35], a[196:199]\n"
    "v_exp_f32 v191, v191\n"
    "v_mfma_f32_16x16x32_bf16 a[196:199], a[168:171], a[36:39], a[196:199]\n"
    "v_exp_f32_e64 v16, -abs(v176)\n"
    "v_mfma_f32_16x16x32_bf16 a[196:199], a[172:175], a[40:43], a[196:199]\n"
    "v_exp_f32_e64 v17, -abs(v177)\n"
    "v_mfma_f32_16x16x32_bf16 a[200:203], a[148:151], a[44:47], a[200:203]\n"
    "v_xor_b32 %[re0], 0x10000, %[re0]\n"
    "v_fma_f32 v167, -abs(v163), v167, v167\n"
    "v_mfma_f32_16x16x32_bf16 a[200:203], a[144:147], a[40:43], a[200:203]\n"
    "v_xor_b32 %[re1], 0x10000, %[re1]\n"
    "v_fma_f32 v172, -abs(v168), v172, v172\n"
    "v_mfma_f32_16x16x32_bf16 a[200:203], a[140:143], a[36:39], a[200:203]\n"
    "v_exp_f32_e64 v18, -abs(v178)\n"
    "v_mfma_f32_16x16x32_bf16 a[200:203], a[136:139], a[12:15], a[200:203]\n"
    "v_exp_f32_e64 v19, -abs(v179)\n"
    "v_mfma_f32_16x16x32_bf16 a[200:203], a[132:135], a[8:11], a[200:203]\n"
    "ds_read_b32 v13, v12\n"
    "v_fma_f32 v173, -abs(v169), v173, v173\n"
    "v_mfma_f32_16x16x32_bf16 a[200:203], a[128:131], a[4:7], a[200:203]\n"
    "v_fma_f32 v174, -abs(v170), v174, v174\n"
    "v_fma_f32 v175, -abs(v171), v175, v175\n"
    "v_mfma_f32_16x16x32_bf16 a[204:207], a[152:155], a[4:7], a[204:207]\n"
    "v_cvt_pk_bf16_f32 v20, v164, v165\n"
    "v_cvt_pk_bf16_f32 v21, v166, v167\n"
    "v_mfma_f32_16x16x32_bf16 a[204:207], a[156:159], a[8:11], a[204:207]\n"
    "v_cvt_pk_bf16_f32 v22, v172, v173\n"
    "v_cvt_pk_bf16_f32 v23, v174, v175\n"
    "v_mfma_f32_16x16x32_bf16 a[204:207], a[160:163], a[12:15], a[204:207]\n"
    "ds_write_b64 %[vsw], v[20:21] offset:20480\n"
    "v_mfma_f32_16x16x32_bf16 a[204:207], a[164:167], a[36:39], a[204:207]\n"
    "ds_write_b64 %[vsw], v[22:23] offset:20608\n"
    "v_mfma_f32_16x16x32_bf16 a[204:207], a[168:171], a[40:43], a[204:207]\n"
    "s_waitcnt lgkmcnt(2)\n"
    "v_cmp_gt_u32 vcc, s63, v13\n"
    "v_bfi_b32 v176, s61, 0.5, v176\n"
    "v_mfma_f32_16x16x32_bf16 a[204:207], a[172:175], a[44:47], a[204:207]\n"
    "s_cbranch_vccnz Ls2%=\n"
    "Lb2%=:\n"
    "v_bfi_b32 v177, s61, 0.5, v177\n"
    "v_mfma_f32_16x16x32_bf16 a[208:211], a[148:151], a[48:51], a[208:211]\n"
    "ds_read_b128 a[0:3], %[rd0] offset:0\n"
    "v_bfi_b32 v178, s61, 0.5, v178\n"
    "v_mfma_f32_16x16x32_bf16 a[208:211], a[144:147], a[44:47], a[208:211]\n"
    "ds_read_b128 a[32:35], %[rd1] offset:0\n"
    "v_bfi_b32 v179, s61, 0.5, v179\n"
    "v_mfma_f32_16x16x32_bf16 a[208:211], a[140:143], a[40:43], a[208:211]\n"
    "ds_read_b128 a[4:7], %[rd0] offset:2048\n"
    "v_fma_f32 v176, v16, v176, v176\n"
    "v_mfma_f32_16x16x32_bf16 a[208:211], a[136:139], a[16:19], a[208:211]\n"
    "ds_read_b128 a[36:39], %[rd1] offset:2048\n"
    "v_fma_f32 v177, v17, v177, v177\n"
    "v_mfma_f32_16x16x32_bf16 a[208:211], a[132:135], a[12:15], a[208:211]\n"
    "ds_read_b128 v[28:31], v14 offset:16384\n"
    "v_fma_f32 v178, v18, v178, v178\n"
    "v_mfma_f32_16x16x32_bf16 a[208:211], a[128:131], a[8:11], a[208:211]\n"
    "ds_read_b128 v[24:27], v14 offset:20480\n"
    "v_fma_f32 v179, v19, v179, v179\n"
    "v_mfma_f32_16x16x32_bf16 a[212:215], a[152:155], a[8:11], a[212:215]\n"
    "ds_read_b128 a[8:11], %[rd0] offset:4096\n"
    "v_mul_f32 v196, s73, v196\n"
    "v_mfma_f32_16x16x32_bf16 a[212:215], a[156:159], a[12:15], a[212:215]\n"
    "s_add_u32 m0, s69, 8192\n"
    "s_add_u32 s57, s55, 8192\n"
    "v_mul_f32 v197, s73, v197\n"
    "v_mfma_f32_16x16x32_bf16 a[212:215], a[160:163], a[16:19], a[212:215]\n"
    "buffer_load_dwordx4 %[od1], %[srdX], s57 offen  lds\n"
    "v_mul_f32 v198, s73, v198\n"
    "v_mfma_f32_16x16x32_bf16 a[212:215], a[164:167], a[40:43], a[212:215]\n"
    "ds_read_b128 a[40:43], %[rd1] offset:4096\n"
    "v_mul_f32 v199, s73, v199\n"
    "v_mfma_f32_16x16x32_bf16 a[212:215], a[168:171], a[44:47], a[212:215]\n"
    "v_exp_f32_e64 v16, -abs(v184)\n"
    "v_mfma_f32_16x16x32_bf16 a[212:215], a[172:175], a[48:51], a[212:215]\n"
    "v_exp_f32_e64 v17, -abs(v185)\n"
    "v_mfma_f32_16x16x32_bf16 a[216:219], a[148:151], a[52:55], a[216:219]\n"
    "v_exp_f32_e64 v18, -abs(v186)\n"
    "v_mfma_f32_16x16x32_bf16 a[216:219], a[144:147], a[48:51], a[216:219]\n"
    "v_exp_f32_e64 v19, -abs(v187)\n"
    "v_mfma_f32_16x16x32_bf16 a[216:219], a[140:143], a[44:47], a[216:219]\n"
    "s_add_u32 m0, s69, 12288\n"
    "s_add_u32 s57, s55, 16384\n"
    "s_waitcnt lgkmcnt(3)\n"
    "v_mfma_f32_16x16x32_bf16 a[216:219], a[136:139], a[20:23], a[216:219]\n"
    "buffer_load_dwordx4 %[od1], %[srdX], s57 offen  lds\n"
    "v_bfi_b32 v184, s61, 0.5, v184\n"
    "v_mfma_f32_16x16x32_bf16 a[216:219], a[132:135], a[16:19], a[216:219]\n"
    "s_mov_b32 s60, 24576\n"
    "buffer_store_dwordx4 v[28:31], %[oo], s[76:79], s60 offen sc0 sc1\n"
    "v_mfma_f32_16x16x32_bf16 a[216:219], a[128:131], a[12:15], a[216:219]\n"
    "v_bfi_b32 v185, s61, 0.5, v185\n"
    "v_bfi_b32 v186, s61, 0.5, v186\n"
    "v_mfma_f32_16x16x32_bf16 a[220:223], a[152:155], a[12:15], a[220:223]\n"
    "ds_read_b128 a[12:15], %[rd0] offset:6144\n"
    "v_bfi_b32 v187, s61, 0.5, v187\n"
    "v_mfma_f32_16x16x32_bf16 a[220:223], a[156:159], a[16:19], a[220:223]\n"
    "v_fma_f32 v184, v16, v184, v184\n"
    "v_fma_f32 v185, v17, v185, v185\n"
    "v_mfma_f32_16x16x32_bf16 a[220:223], a[160:163], a[20:23], a[220:223]\n"
    "v_fma_f32 v186, v18, v186, v186\n"
    "v_fma_f32 v187, v19, v187, v187\n"
    "v_mfma_f32_16x16x32_bf16 a[220:223], a[164:167], a[44:47], a[220:223]\n"
    "ds_read_b128 a[44:47], %[rd1] offset:6144\n"
    "v_fma_f32 v180, v180, v176, v176\n"
    "v_mfma_f32_16x16x32_bf16 a[220:223], a[168:171], a[48:51], a[220:223]\n"
    "v_fma_f32 v181, v181, v177, v177\n"
    "v_fma_f32 v182, v182, v178, v178\n"
    "v_mfma_f32_16x16x32_bf16 a[220:223], a[172:175], a[52:55], a[220:223]\n"
    "v_fma_f32 v183, v183, v179, v179\n"
    "v_fma_f32 v188, v188, v184, v184\n"
    "v_mfma_f32_16x16x32_bf16 a[224:227], a[148:151], a[56:59], a[224:227]\n"
    "v_fma_f32 v189, v189, v185, v185\n"
    "v_fma_f32 v190, v190, v186, v186\n"
    "v_mfma_f32_16x16x32_bf16 a[224:227], a[144:147], a[52:55], a[224:227]\n"
    "v_fma_f32 v191, v191, v187, v187\n"
    "v_mul_f32 v204, s73, v204\n"
    "v_mfma_f32_16x16x32_bf16 a[224:227], a[140:143], a[48:51], a[224:227]\n"
    "s_add_u32 m0, s69, 16384\n"
    "s_add_u32 s57, s55, 24576\n"
    "v_mul_f32 v205, s73, v205\n"
    "v_mfma_f32_16x16x32_bf16 a[224:227], a[136:139], a[24:27], a[224:227]\n"
    "buffer_load_dwordx4 %[od1], %[srdX], s57 offen  lds\n"
    "v_mul_f32 v206, s73, v206\n"
    "v_mfma_f32_16x16x32_bf16 a[224:227], a[132:135], a[20:23], a[224:227]\n"
    "v_rcp_f32 v180, v180\n"
    "v_mfma_f32_16x16x32_bf16 a[224:227], a[128:131], a[16:19], a[224:227]\n"
    "v_rcp_f32 v181, v181\n"
    "v_mfma_f32_16x16x32_bf16 a[228:231], a[152:155], a[16:19], a[228:231]\n"
    "ds_read_b128 a[16:19], %[rd0] offset:8192\n"
    "v_mul_f32 v207, s73, v207\n"
    "v_mfma_f32_16x16x32_bf16 a[228:231], a[156:159], a[20:23], a[228:231]\n"
    "v_rcp_f32 v182, v182\n"
    "v_mfma_f32_16x16x32_bf16 a[228:231], a[160:163], a[24:27], a[228:231]\n"
    "v_rcp_f32 v183, v183\n"
    "v_mfma_f32_16x16x32_bf16 a[228:231], a[164:167], a[48:51], a[228:231]\n"
    "ds_read_b128 a[48:51], %[rd1] offset:8192\n"
    "v_mul_f32 v192, s72, v192\n"
    "v_mfma_f32_16x16x32_bf16 a[228:231], a[168:171], a[52:55], a[228:231]\n"
    "v_rcp_f32 v188, v188\n"
    "v_mfma_f32_16x16x32_bf16 a[228:231], a[172:175], a[56:59], a[228:231]\n"
    "v_rcp_f32 v189, v189\n"
    "v_mfma_f32_16x16x32_bf16 a[232:235], a[148:151], a[60:63], a[232:235]\n"
    "v_rcp_f32 v190, v190\n"
    "v_mfma_f32_16x16x32_bf16 a[232:235], a[144:147], a[56:59], a[232:235]\n"
    "v_rcp_f32 v191, v191\n"
    "v_mfma_f32_16x16x32_bf16 a[232:235], a[140:143], a[52:55], a[232:235]\n"
    "s_add_u32 m0, s69, 20480\n"
    "s_add_u32 s57, s55, 32768\n"
    "v_fma_f32 v180, -abs(v176), v180, v180\n"
    "v_mfma_f32_16x16x32_bf16 a[232:235], a[136:139], a[28:31], a[232:235]\n"
    "buffer_load_dwordx4 %[od1], %[srdX], s57 offen  lds\n"
    "v_fma_f32 v181, -abs(v177), v181, v181\n"
    "v_mfma_f32_16x16x32_bf16 a[232:235], a[132:135], a[24:27], a[232:235]\n"
    "v_fma_f32 v182, -abs(v178), v182, v182\n"
    "v_fma_f32 v183, -abs(v179), v183, v183\n"
    "v_mfma_f32_16x16x32_bf16 a[232:235], a[128:131], a[20:23], a[232:235]\n"
    "v_fma_f32 v188, -abs(v184), v188, v188\n"
    "v_fma_f32 v189, -abs(v185), v189, v189\n"
    "v_mfma_f32_16x16x32_bf16 a[236:239], a[152:155], a[20:23], a[236:239]\n"
    "ds_read_b128 a[20:23], %[rd0] offset:10240\n"
    "v_fma_f32 v190, -abs(v186), v190, v190\n"
    "v_mfma_f32_16x16x32_bf16 a[236:239], a[156:159], a[24:27], a[236:239]\n"
    "v_fma_f32 v191, -abs(v187), v191, v191\n"
    "v_cvt_pk_bf16_f32 v16, v180, v181\n"
    "v_mfma_f32_16x16x32_bf16 a[236:239], a[160:163], a[28:31], a[236:239]\n"
    "v_cvt_pk_bf16_f32 v17, v182, v183\n"
    "v_cvt_pk_bf16_f32 v18, v188, v189\n"
    "v_mfma_f32_16x16x32_bf16 a[236:239], a[164:167], a[52:55], a[236:239]\n"
    "ds_read_b128 a[52:55], %[rd1] offset:10240\n"
    "v_cvt_pk_bf16_f32 v19, v190, v191\n"
    "v_mfma_f32_16x16x32_bf16 a[236:239], a[168:171], a[56:59], a[236:239]\n"
    "ds_write_b64 %[vsw], v[16:17] offset:24576\n"
    "v_mfma_f32_16x16x32_bf16 a[236:239], a[172:175], a[60:63], a[236:239]\n"
    "v_mul_f32 v193, s72, v193\n"
    "v_mul_f32 v194, s72, v194\n"
    "v_mfma_f32_16x16x32_bf16 a[240:243], a[148:151], a[72:75], a[240:243]\n"
    "ds_write_b64 %[vsw], v[18:19] offset:24704\n"
    "v_mfma_f32_16x16x32_bf16 a[240:243], a[144:147], a[60:63], a[240:243]\n"
    "v_mul_f32 v195, s72, v195\n"
    "v_mul_f32 v200, s72, v200\n"
    "v_mfma_f32_16x16x32_bf16 a[240:243], a[140:143], a[56:59], a[240:243]\n"
    "s_add_u32 m0, s69, 24576\n"
    "s_add_u32 s57, s55, 40960\n"
    "v_mul_f32 v201, s72, v201\n"
    "v_mfma_f32_16x16x32_bf16 a[240:243], a[136:139], a[64:67], a[240:243]\n"
    "buffer_load_dwordx4 %[od1], %[srdX], s57 offen  lds\n"
    "v_mul_f32 v202, s72, v202\n"
    "v_mfma_f32_16x16x32_bf16 a[240:243], a[132:135], a[28:31], a[240:243]\n"
    "v_mul_f32 v203, s72, v203\n"
    "v_mfma_f32_16x16x32_bf16 a[240:243], a[128:131], a[24:27], a[240:243]\n"
    "v_exp_f32 v196, v196\n"
    "v_mfma_f32_16x16x32_bf16 a[244:247], a[152:155], a[24:27], a[244:247]\n"
    "ds_read_b128 a[24:27], %[rd0] offset:12288\n"
    "v_mfma_f32_16x16x32_bf16 a[244:247], a[156:159], a[28:31], a[244:247]\n"
    "v_exp_f32 v197, v197\n"
    "v_mfma_f32_16x16x32_bf16 a[244:247], a[160:163], a[64:67], a[244:247]\n"
    "s_waitcnt lgkmcnt(11)\n"
    "v_exp_f32 v198, v198\n"
    "v_mfma_f32_16x16x32_bf16 a[244:247], a[164:167], a[56:59], a[244:247]\n"
    "ds_read_b128 a[56:59], %[rd1] offset:12288\n"
    "v_mfma_f32_16x16x32_bf16 a[244:247], a[168:171], a[60:63], a[244:247]\n"
    "s_mov_b32 s60, 32768\n"
    "buffer_store_dwordx4 v[24:27], %[oo], s[76:79], s60 offen sc0 sc1\n"
    "v_mfma_f32_16x16x32_bf16 a[244:247], a[172:175], a[72:75], a[244:247]\n"
    "v_exp_f32 v199, v199\n"
    "v_mfma_f32_16x16x32_bf16 a[248:251], a[148:151], a[76:79], a[248:251]\n"
    "v_exp_f32 v204, v204\n"
    "v_mfma_f32_16x16x32_bf16 a[248:251], a[144:147], a[72:75], a[248:251]\n"
    "v_exp_f32 v205, v205\n"
    "v_mfma_f32_16x16x32_bf16 a[248:251], a[140:143], a[60:63], a[248:251]\n"
    "s_add_u32 m0, s69, 28672\n"
    "s_add_u32 s57, s55, 49152\n"
    "v_mfma_f32_16x16x32_bf16 a[248:251], a[136:139], a[68:71], a[248:251]\n"
    "buffer_load_dwordx4 %[od1], %[srdX], s57 offen  lds\n"
    "v_mfma_f32_16x16x32_bf16 a[248:251], a[132:135], a[64:67], a[248:251]\n"
    "v_exp_f32 v206, v206\n"
    "v_mfma_f32_16x16x32_bf16 a[248:251], a[128:131], a[28:31], a[248:251]\n"
    "s_add_u32 s69, s69, 0x8000\n"
    "s_and_b32 s69, s69, 0x1ffff\n"
    "v_mfma_f32_16x16x32_bf16 a[252:255], a[152:155], a[28:31], a[252:255]\n"
    "ds_read_b128 a[28:31], %[rd0] offset:14336\n"
    "v_mfma_f32_16x16x32_bf16 a[252:255], a[156:159], a[64:67], a[252:255]\n"
    "s_add_u32 s55, s55, 128\n"
    "v_exp_f32 v207, v207\n"
    "v_mfma_f32_16x16x32_bf16 a[252:255], a[160:163], a[68:71], a[252:255]\n"
    "v_exp_f32_e64 v20, -abs(v192)\n"
    "v_mfma_f32_16x16x32_bf16 a[252:255], a[164:167], a[60:63], a[252:255]\n"
    "ds_read_b128 a[60:63], %[rd1] offset:14336\n"
    "v_mfma_f32_16x16x32_bf16 a[252:255], a[168:171], a[72:75], a[252:255]\n"
    "v_exp_f32_e64 v21, -abs(v193)\n"
    "v_mfma_f32_16x16x32_bf16 a[252:255], a[172:175], a[76:79], a[252:255]\n"
    "s_waitcnt vmcnt(11)\n"
    "s_waitcnt lgkmcnt(10)\n"
    "s_cmp_lg_u32 s67, 0\n"
    "s_cbranch_scc1 Lc3%=\n"
    "v_mfma_f32_16x16x32_bf16 v[208:211], a[100:103], a[40:43], v[208:211]\n"
    "v_mfma_f32_16x16x32_bf16 v[208:211], a[96:99], a[36:39], v[208:211]\n"
    "v_mfma_f32_16x16x32_bf16 v[208:211], a[92:95], a[32:35], v[208:211]\n"
    "v_mfma_f32_16x16x32_bf16 v[208:211], a[88:91], a[8:11], v[208:211]\n"
    "v_mfma_f32_16x16x32_bf16 v[208:211], a[84:87], a[4:7], v[208:211]\n"
    "v_mfma_f32_16x16x32_bf16 v[208:211], a[80:83], a[0:3], v[208:211]\n"
    "v_mfma_f32_16x16x32_bf16 v[212:215], a[104:107], a[0:3], v[212:215]\n"
    "v_mfma_f32_16x16x32_bf16 v[212:215], a[108:111], a[4:7], v[212:215]\n"
    "v_mfma_f32_16x16x32_bf16 v[212:215], a[112:115], a[8:11], v[212:215]\n"
    "v_mfma_f32_16x16x32_bf16 v[212:215], a[116:119], a[32:35], v[212:215]\n"
    "v_mfma_f32_16x16x32_bf16 v[212:215], a[120:123], a[36:39], v[212:215]\n"
    "v_mfma_f32_16x16x32_bf16 v[212:215], a[124:127], a[40:43], v[212:215]\n"
    "Lc3%=:\n"
    "v_mfma_f32_16x16x32_bf16 v[216:219], a[100:103], a[44:47], v[216:219]\n"
    "ds_read_b128 a[0:3], %[rd0] offset:16384\n"
    "v_mfma_f32_16x16x32_bf16 v[216:219], a[96:99], a[40:43], v[216:219]\n"
    "v_exp_f32_e64 v22, -abs(v194)\n"
    "v_mfma_f32_16x16x32_bf16 v[216:219], a[92:95], a[36:39], v[216:219]\n"
    "buffer_load_dwordx4 a[128:131], %[l16], %[srdW], s54 offen offset:0\n"
    "v_mfma_f32_16x16x32_bf16 v[216:219], a[88:91], a[12:15], v[216:219]\n"
    "v_exp_f32_e64 v23, -abs(v195)\n"
    "v_mfma_f32_16x16x32_bf16 v[216:219], a[84:87], a[8:11], v[216:219]\n"
    "ds_read_b128 a[32:35], %[rd1] offset:16384\n"
    "v_bfi_b32 v192, s61, 0.5, v192\n"
    "v_mfma_f32_16x16x32_bf16 v[216:219], a[80:83], a[4:7], v[216:219]\n"
    "v_bfi_b32 v193, s61, 0.5, v193\n"
    "v_bfi_b32 v194, s61, 0.5, v194\n"
    "v_mfma_f32_16x16x32_bf16 v[220:223], a[104:107], a[4:7], v[220:223]\n"
    "ds_read_b128 a[4:7], %[rd0] offset:18432\n"
    "v_bfi_b32 v195, s61, 0.5, v195\n"
    "v_mfma_f32_16x16x32_bf16 v[220:223], a[108:111], a[8:11], v[220:223]\n"
    "v_fma_f32 v192, v20, v192, v192\n"
    "v_fma_f32 v193, v21, v193, v193\n"
    "v_mfma_f32_16x16x32_bf16 v[220:223], a[112:115], a[12:15], v[220:223]\n"
    "v_fma_f32 v194, v22, v194, v194\n"
    "v_fma_f32 v195, v23, v195, v195\n"
    "v_mfma_f32_16x16x32_bf16 v[220:223], a[116:119], a[36:39], v[220:223]\n"
    "ds_read_b128 a[36:39], %[rd1] offset:18432\n"
    "v_mfma_f32_16x16x32_bf16 v[220:223], a[120:123], a[40:43], v[220:223]\n"
    "buffer_load_dwordx4 a[132:135], %[l16], %[srdW], s54 offen offset:1024\n"
    "v_mfma_f32_16x16x32_bf16 v[220:223], a[124:127], a[44:47], v[220:223]\n"
    "s_waitcnt lgkmcnt(10)\n"
    "v_exp_f32_e64 v20, -abs(v200)\n"
    "v_mfma_f32_16x16x32_bf16 v[224:227], a[100:103], a[48:51], v[224:227]\n"
    "v_exp_f32_e64 v21, -abs(v201)\n"
    "v_mfma_f32_16x16x32_bf16 v[224:227], a[96:99], a[44:47], v[224:227]\n"
    "ds_read_b128 a[64:67], %[re0] offset:0\n"
    "v_mfma_f32_16x16x32_bf16 v[224:227], a[92:95], a[40:43], v[224:227]\n"
    "buffer_load_dwordx4 a[136:139], %[l16], %[srdW], s54 offen offset:2048\n"
    "v_mfma_f32_16x16x32_bf16 v[224:227], a[88:91], a[16:19], v[224:227]\n"
    "v_exp_f32_e64 v22, -abs(v202)\n"
    "v_mfma_f32_16x16x32_bf16 v[224:227], a[84:87], a[12:15], v[224:227]\n"
    "v_exp_f32_e64 v23, -abs(v203)\n"
    "v_mfma_f32_16x16x32_bf16 v[224:227], a[80:83], a[8:11], v[224:227]\n"
    "v_bfi_b32 v200, s61, 0.5, v200\n"
    "v_bfi_b32 v201, s61, 0.5, v201\n"
    "v_mfma_f32_16x16x32_bf16 v[228:231], a[104:107], a[8:11], v[228:231]\n"
    "ds_read_b128 a[8:11], %[rd0] offset:20480\n"
    "v_bfi_b32 v202, s61, 0.5, v202\n"
    "v_mfma_f32_16x16x32_bf16 v[228:231], a[108:111], a[12:15], v[228:231]\n"
    "v_bfi_b32 v203, s61, 0.5, v203\n"
    "v_fma_f32 v200, v20, v200, v200\n"
    "v_mfma_f32_16x16x32_bf16 v[228:231], a[112:115], a[16:19], v[228:231]\n"
    "v_fma_f32 v201, v21, v201, v201\n"
    "v_fma_f32 v202, v22, v202, v202\n"
    "v_mfma_f32_16x16x32_bf16 v[228:231], a[116:119], a[40:43], v[228:231]\n"
    "ds_read_b128 a[40:43], %[rd1] offset:20480\n"
    "v_fma_f32 v203, v23, v203, v203\n"
    "v_mfma_f32_16x16x32_bf16 v[228:231], a[120:123], a[44:47], v[228:231]\n"
    "buffer_load_dwordx4 a[140:143], %[l16], %[srdW], s54 offen offset:3072\n"
    "v_fma_f32 v196, v196, v192, v192\n"
    "v_mfma_f32_16x16x32_bf16 v[228:231], a[124:127], a[48:51], v[228:231]\n"
    "v_fma_f32 v197, v197, v193, v193\n"
    "v_fma_f32 v198, v198, v194, v194\n"
    "v_mfma_f32_16x16x32_bf16 v[232:235], a[100:103], a[52:55], v[232:235]\n"
    "s_add_u32 s54, s54, 4096\n"
    "v_fma_f32 v199, v199, v195, v195\n"
    "v_fma_f32 v204, v204, v200, v200\n"
    "v_mfma_f32_16x16x32_bf16 v[232:235], a[96:99], a[48:51], v[232:235]\n"
    "ds_read_b128 a[72:75], %[re1] offset:0\n"
    "v_fma_f32 v205, v205, v201, v201\n"
    "v_mfma_f32_16x16x32_bf16 v[232:235], a[92:95], a[44:47], v[232:235]\n"
    "buffer_load_dwordx4 a[144:147], %[l16], %[srdW], s54 offen offset:0\n"
    "v_fma_f32 v206, v206, v202, v202\n"
    "v_mfma_f32_16x16x32_bf16 v[232:235], a[88:91], a[20:23], v[232:235]\n"
    "v_fma_f32 v207, v207, v203, v203\n"
    "v_mfma_f32_16x16x32_bf16 v[232:235], a[84:87], a[16:19], v[232:235]\n"
    "v_rcp_f32 v196, v196\n"
    "v_mfma_f32_16x16x32_bf16 v[232:235], a[80:83], a[12:15], v[232:235]\n"
    "v_rcp_f32 v197, v197\n"
    "v_mfma_f32_16x16x32_bf16 v[236:239], a[104:107], a[12:15], v[236:239]\n"
    "ds_read_b128 a[12:15], %[rd0] offset:22528\n"
    "v_mfma_f32_16x16x32_bf16 v[236:239], a[108:111], a[16:19], v[236:239]\n"
    "v_rcp_f32 v198, v198\n"
    "v_mfma_f32_16x16x32_bf16 v[236:239], a[112:115], a[20:23], v[236:239]\n"
    "v_rcp_f32 v199, v199\n"
    "v_mfma_f32_16x16x32_bf16 v[236:239], a[116:119], a[44:47], v[236:239]\n"
    "ds_read_b128 a[44:47], %[rd1] offset:22528\n"
    "v_mfma_f32_16x16x32_bf16 v[236:239], a[120:123], a[48:51], v[236:239]\n"
    "buffer_load_dwordx4 a[148:151], %[l16], %[srdW], s54 offen offset:1024\n"
    "v_mfma_f32_16x16x32_bf16 v[236:239], a[124:127], a[52:55], v[236:239]\n"
    "s_waitcnt lgkmcnt(10)\n"
    "v_rcp_f32 v204, v204\n"
    "v_mfma_f32_16x16x32_bf16 v[240:243], a[100:103], a[56:59], v[240:243]\n"
    "v_rcp_f32 v205, v205\n"
    "v_mfma_f32_16x16x32_bf16 v[240:243], a[96:99], a[52:55], v[240:243]\n"
    "ds_read_b128 a[68:71], %[re0] offset:2048\n"
    "v_mfma_f32_16x16x32_bf16 v[240:243], a[92:95], a[48:51], v[240:243]\n"
    "buffer_load_dwordx4 a[152:155], %[l16], %[srdW], s54 offen offset:2048\n"
    "v_mfma_f32_16x16x32_bf16 v[240:243], a[88:91], a[24:27], v[240:243]\n"
    "v_rcp_f32 v206, v206\n"
    "v_mfma_f32_16x16x32_bf16 v[240:243], a[84:87], a[20:23], v[240:243]\n"
    "v_rcp_f32 v207, v207\n"
    "v_mfma_f32_16x16x32_bf16 v[240:243], a[80:83], a[16:19], v[240:243]\n"
    "v_fma_f32 v196, -abs(v192), v196, v196\n"
    "v_fma_f32 v197, -abs(v193), v197, v197\n"
    "v_mfma_f32_16x16x32_bf16 v[244:247], a[104:107], a[16:19], v[244:247]\n"
    "ds_read_b128 a[16:19], %[rd0] offset:24576\n"
    "v_fma_f32 v198, -abs(v194), v198, v198\n"
    "v_mfma_f32_16x16x32_bf16 v[244:247], a[108:111], a[20:23], v[244:247]\n"
    "v_fma_f32 v199, -abs(v195), v199, v199\n"
    "v_fma_f32 v204, -abs(v200), v204, v204\n"
    "v_mfma_f32_16x16x32_bf16 v[244:247], a[112:115], a[24:27], v[244:247]\n"
    "v_fma_f32 v205, -abs(v201), v205, v205\n"
    "v_fma_f32 v206, -abs(v202), v206, v206\n"
    "v_mfma_f32_16x16x32_bf16 v[244:247], a[116:119], a[48:51], v[244:247]\n"
    "ds_read_b128 a[48:51], %[rd1] offset:24576\n"
    "v_fma_f32 v207, -abs(v203), v207, v207\n"
    "v_mfma_f32_16x16x32_bf16 v[244:247], a[120:123], a[52:55], v[244:247]\n"
    "buffer_load_dwordx4 a[156:159], %[l16], %[srdW], s54 offen offset:3072\n"
    "v_cvt_pk_bf16_f32 v20, v196, v197\n"
    "v_mfma_f32_16x16x32_bf16 v[244:247], a[124:127], a[56:59], v[244:247]\n"
    "v_cvt_pk_bf16_f32 v21, v198, v199\n"
    "v_cvt_pk_bf16_f32 v22, v204, v205\n"
    "v_mfma_f32_16x16x32_bf16 v[248:251], a[100:103], a[60:63], v[248:251]\n"
    "s_add_u32 s54, s54, 4096\n"
    "v_cvt_pk_bf16_f32 v23, v206, v207\n"
    "v_mfma_f32_16x16x32_bf16 v[248:251], a[96:99], a[56:59], v[248:251]\n"
    "ds_read_b128 a[76:79], %[re1] offset:2048\n"
    "v_mfma_f32_16x16x32_bf16 v[248:251], a[92:95], a[52:55], v[248:251]\n"
    "buffer_load_dwordx4 a[160:163], %[l16], %[srdW], s54 offen offset:0\n"
    "v_mfma_f32_16x16x32_bf16 v[248:251], a[88:91], a[28:31], v[248:251]\n"
    "ds_write_b64 %[vsw], v[20:21] offset:0\n"
    "v_mfma_f32_16x16x32_bf16 v[248:251], a[84:87], a[24:27], v[248:251]\n"
    "ds_write_b64 %[vsw], v[22:23] offset:128\n"
    "v_mfma_f32_16x16x32_bf16 v[248:251], a[80:83], a[20:23], v[248:251]\n"
    "v_mfma_f32_16x16x32_bf16 v[252:255], a[104:107], a[20:23], v[252:255]\n"
    "ds_read_b128 a[20:23], %[rd0] offset:26624\n"
    "v_mfma_f32_16x16x32_bf16 v[252:255], a[108:111], a[24:27], v[252:255]\n"
    "v_mfma_f32_16x16x32_bf16 v[252:255], a[112:115], a[28:31], v[252:255]\n"
    "v_mfma_f32_16x16x32_bf16 v[252:255], a[116:119], a[52:55], v[252:255]\n"
    "ds_read_b128 a[52:55], %[rd1] offset:26624\n"
    "v_mfma_f32_16x16x32_bf16 v[252:255], a[120:123], a[56:59], v[252:255]\n"
    "buffer_load_dwordx4 a[164:167], %[l16], %[srdW], s54 offen offset:1024\n"
    "v_mfma_f32_16x16x32_bf16 v[252:255], a[124:127], a[60:63], v[252:255]\n"
    "s_waitcnt lgkmcnt(14)\n"
    "v_mfma_f32_16x16x32_bf16 a[176:179], a[100:103], a[32:35], a[176:179]\n"
    "v_mfma_f32_16x16x32_bf16 a[176:179], a[96:99], a[60:63], a[176:179]\n"
    "v_mfma_f32_16x16x32_bf16 a[176:179], a[92:95], a[56:59], a[176:179]\n"
    "buffer_load_dwordx4 a[168:171], %[l16], %[srdW], s54 offen offset:2048\n"
    "v_mfma_f32_16x16x32_bf16 a[176:179], a[88:91], a[0:3], a[176:179]\n"
    "v_mfma_f32_16x16x32_bf16 a[176:179], a[84:87], a[28:31], a[176:179]\n"
    "v_mfma_f32_16x16x32_bf16 a[176:179], a[80:83], a[24:27], a[176:179]\n"
    "v_mfma_f32_16x16x32_bf16 a[180:183], a[104:107], a[24:27], a[180:183]\n"
    "ds_read_b128 a[24:27], %[rd0] offset:28672\n"
    "v_mfma_f32_16x16x32_bf16 a[180:183], a[108:111], a[28:31], a[180:183]\n"
    "v_mfma_f32_16x16x32_bf16 a[180:183], a[112:115], a[0:3], a[180:183]\n"
    "v_mfma_f32_16x16x32_bf16 a[180:183], a[116:119], a[56:59], a[180:183]\n"
    "ds_read_b128 a[56:59], %[rd1] offset:28672\n"
    "v_mfma_f32_16x16x32_bf16 a[180:183], a[120:123], a[60:63], a[180:183]\n"
    "buffer_load_dwordx4 a[172:175], %[l16], %[srdW], s54 offen offset:3072\n"
    "v_mfma_f32_16x16x32_bf16 a[180:183], a[124:127], a[32:35], a[180:183]\n"
    "v_mfma_f32_16x16x32_bf16 a[184:187], a[100:103], a[36:39], a[184:187]\n"
    "s_add_u32 s54, s54, 4096\n"
    "v_mfma_f32_16x16x32_bf16 a[184:187], a[96:99], a[32:35], a[184:187]\n"
    "s_add_u32 m0, s69, 0\n"
    "s_add_u32 s57, s55, 0\n"
    "v_subrev_u32 v13, 0x2000, %[od1]\n"
    "v_mfma_f32_16x16x32_bf16 a[184:187], a[92:95], a[60:63], a[184:187]\n"
    "buffer_load_dwordx4 v13, %[srdX], s57 offen  lds\n"
    "v_mfma_f32_16x16x32_bf16 a[184:187], a[88:91], a[4:7], a[184:187]\n"
    "s_add_u32 m0, s69, 4096\n"
    "s_add_u32 s57, s55, 0\n"
    "v_mfma_f32_16x16x32_bf16 a[184:187], a[84:87], a[0:3], a[184:187]\n"
    "buffer_load_dwordx4 %[od1], %[srdX], s57 offen  lds\n"
    "v_mfma_f32_16x16x32_bf16 a[184:187], a[80:83], a[28:31], a[184:187]\n"
    "v_mfma_f32_16x16x32_bf16 a[188:191], a[104:107], a[28:31], a[188:191]\n"
    "ds_read_b128 a[28:31], %[rd0] offset:30720\n"
    "v_mfma_f32_16x16x32_bf16 a[188:191], a[108:111], a[0:3], a[188:191]\n"
    "v_mfma_f32_16x16x32_bf16 a[188:191], a[112:115], a[4:7], a[188:191]\n"
    "v_mfma_f32_16x16x32_bf16 a[188:191], a[116:119], a[60:63], a[188:191]\n"
    "ds_read_b128 a[60:63], %[rd1] offset:30720\n"
    "v_mfma_f32_16x16x32_bf16 a[188:191], a[120:123], a[32:35], a[188:191]\n"
    "v_mfma_f32_16x16x32_bf16 a[188:191], a[124:127], a[36:39], a[188:191]\n"
    "s_waitcnt lgkmcnt(12)\n"
    "v_mfma_f32_16x16x32_bf16 a[192:195], a[100:103], a[40:43], a[192:195]\n"
    "v_mfma_f32_16x16x32_bf16 a[192:195], a[96:99], a[36:39], a[192:195]\n"
    "v_mfma_f32_16x16x32_bf16 a[192:195], a[92:95], a[32:35], a[192:195]\n"
    "s_add_u32 s63, s63, 1\n"
    "v_mfma_f32_16x16x32_bf16 a[192:195], a[88:91], a[8:11], a[192:195]\n"
    "v_mfma_f32_16x16x32_bf16 a[192:195], a[84:87], a[4:7], a[192:195]\n"
    "v_mfma_f32_16x16x32_bf16 a[192:195], a[80:83], a[0:3], a[192:195]\n"
    "v_mfma_f32_16x16x32_bf16 a[196:199], a[104:107], a[0:3], a[196:199]\n"
    "s_waitcnt lgkmcnt(0)\n"
    "v_mov_b32 v13, s63\n"
    "v_mfma_f32_16x16x32_bf16 a[196:199], a[108:111], a[4:7], a[196:199]\n"
    "ds_write_b32 v14, v13\n"
    "v_mfma_f32_16x16x32_bf16 a[196:199], a[112:115], a[8:11], a[196:199]\n"
    "v_mfma_f32_16x16x32_bf16 a[196:199], a[116:119], a[32:35], a[196:199]\n"
    "v_mfma_f32_16x16x32_bf16 a[196:199], a[120:123], a[36:39], a[196:199]\n"
    "v_mfma_f32_16x16x32_bf16 a[196:199], a[124:127], a[40:43], a[196:199]\n"
    "v_mfma_f32_16x16x32_bf16 a[200:203], a[100:103], a[44:47], a[200:203]\n"
    "v_mfma_f32_16x16x32_bf16 a[200:203], a[96:99], a[40:43], a[200:203]\n"
    "v_mfma_f32_16x16x32_bf16 a[200:203], a[92:95], a[36:39], a[200:203]\n"
    "v_mfma_f32_16x16x32_bf16 a[200:203], a[88:91], a[12:15], a[200:203]\n"
    "v_mfma_f32_16x16x32_bf16 a[200:203], a[84:87], a[8:11], a[200:203]\n"
    "ds_read_b32 v13, v12\n"
    "v_mfma_f32_16x16x32_bf16 a[200:203], a[80:83], a[4:7], a[200:203]\n"
    "v_mfma_f32_16x16x32_bf16 a[204:207], a[104:107], a[4:7], a[204:207]\n"
    "v_mfma_f32_16x16x32_bf16 a[204:207], a[108:111], a[8:11], a[204:207]\n"
    "v_mfma_f32_16x16x32_bf16 a[204:207], a[112:115], a[12:15], a[204:207]\n"
    "v_mfma_f32_16x16x32_bf16 a[204:207], a[116:119], a[36:39], a[204:207]\n"
    "v_mfma_f32_16x16x32_bf16 a[204:207], a[120:123], a[40:43], a[204:207]\n"
    "s_waitcnt lgkmcnt(0)\n"
    "v_cmp_gt_u32 vcc, s63, v13\n"
    "v_mfma_f32_16x16x32_bf16 a[204:207], a[124:127], a[44:47], a[204:207]\n"
    "s_cbranch_vccnz Ls3%=\n"
    "Lb3%=:\n"
    "v_mfma_f32_16x16x32_bf16 a[208:211], a[100:103], a[48:51], a[208:211]\n"
    "ds_read_b128 a[0:3], %[rd0] offset:32768\n"
    "v_mfma_f32_16x16x32_bf16 a[208:211], a[96:99], a[44:47], a[208:211]\n"
    "ds_read_b128 a[32:35], %[rd1] offset:32768\n"
    "v_mfma_f32_16x16x32_bf16 a[208:211], a[92:95], a[40:43], a[208:211]\n"
    "ds_read_b128 a[4:7], %[rd0] offset:34816\n"
    "v_mfma_f32_16x16x32_bf16 a[208:211], a[88:91], a[16:19], a[208:211]\n"
    "ds_read_b128 a[36:39], %[rd1] offset:34816\n"
    "v_mfma_f32_16x16x32_bf16 a[208:211], a[84:87], a[12:15], a[208:211]\n"
    "ds_read_b128 v[28:31], v14 offset:24576\n"
    "v_mfma_f32_16x16x32_bf16 a[208:211], a[80:83], a[8:11], a[208:211]\n"
    "ds_read_b128 v[24:27], v14 offset:28672\n"
    "v_mfma_f32_16x16x32_bf16 a[212:215], a[104:107], a[8:11], a[212:215]\n"
    "ds_read_b128 a[8:11], %[rd0] offset:36864\n"
    "v_mfma_f32_16x16x32_bf16 a[212:215], a[108:111], a[12:15], a[212:215]\n"
    "s_add_u32 m0, s69, 8192\n"
    "s_add_u32 s57, s55, 8192\n"
    "v_mfma_f32_16x16x32_bf16 a[212:215], a[112:115], a[16:19], a[212:215]\n"
    "buffer_load_dwordx4 %[od1], %[srdX], s57 offen  lds\n"
    "v_mfma_f32_16x16x32_bf16 a[212:215], a[116:119], a[40:43], a[212:215]\n"
    "ds_read_b128 a[40:43], %[rd1] offset:36864\n"
    "v_mfma_f32_16x16x32_bf16 a[212:215], a[120:123], a[44:47], a[212:215]\n"
    "v_mfma_f32_16x16x32_bf16 a[212:215], a[124:127], a[48:51], a[212:215]\n"
    "v_mfma_f32_16x16x32_bf16 a[216:219], a[100:103], a[52:55], a[216:219]\n"
    "v_mfma_f32_16x16x32_bf16 a[216:219], a[96:99], a[48:51], a[216:219]\n"
    "v_mfma_f32_16x16x32_bf16 a[216:219], a[92:95], a[44:47], a[216:219]\n"
    "s_add_u32 m0, s69, 12288\n"
    "s_add_u32 s57, s55, 16384\n"
    "s_waitcnt lgkmcnt(3)\n"
    "v_mfma_f32_16x16x32_bf16 a[216:219], a[88:91], a[20:23], a[216:219]\n"
    "buffer_load_dwordx4 %[od1], %[srdX], s57 offen  lds\n"
    "v_mfma_f32_16x16x32_bf16 a[216:219], a[84:87], a[16:19], a[216:219]\n"
    "s_mov_b32 s60, 40960\n"
    "buffer_store_dwordx4 v[28:31], %[oo], s[76:79], s60 offen sc0 sc1\n"
    "v_mfma_f32_16x16x32_bf16 a[216:219], a[80:83], a[12:15], a[216:219]\n"
    "ds_read_b128 v[28:31], v14 offset:4096\n"
    "v_mfma_f32_16x16x32_bf16 a[220:223], a[104:107], a[12:15], a[220:223]\n"
    "ds_read_b128 a[12:15], %[rd0] offset:38912\n"
    "v_mfma_f32_16x16x32_bf16 a[220:223], a[108:111], a[16:19], a[220:223]\n"
    "v_mfma_f32_16x16x32_bf16 a[220:223], a[112:115], a[20:23], a[220:223]\n"
    "v_mfma_f32_16x16x32_bf16 a[220:223], a[116:119], a[44:47], a[220:223]\n"
    "ds_read_b128 a[44:47], %[rd1] offset:38912\n"
    "v_mfma_f32_16x16x32_bf16 a[220:223], a[120:123], a[48:51], a[220:223]\n"
    "v_mfma_f32_16x16x32_bf16 a[220:223], a[124:127], a[52:55], a[220:223]\n"
    "v_mfma_f32_16x16x32_bf16 a[224:227], a[100:103], a[56:59], a[224:227]\n"
    "v_mfma_f32_16x16x32_bf16 a[224:227], a[96:99], a[52:55], a[224:227]\n"
    "v_mfma_f32_16x16x32_bf16 a[224:227], a[92:95], a[48:51], a[224:227]\n"
    "s_add_u32 m0, s69, 16384\n"
    "s_add_u32 s57, s55, 24576\n"
    "v_mfma_f32_16x16x32_bf16 a[224:227], a[88:91], a[24:27], a[224:227]\n"
    "buffer_load_dwordx4 %[od1], %[srdX], s57 offen  lds\n"
    "v_mfma_f32_16x16x32_bf16 a[224:227], a[84:87], a[20:23], a[224:227]\n"
    "v_mfma_f32_16x16x32_bf16 a[224:227], a[80:83], a[16:19], a[224:227]\n"
    "v_mfma_f32_16x16x32_bf16 a[228:231], a[104:107], a[16:19], a[228:231]\n"
    "ds_read_b128 a[16:19], %[rd0] offset:40960\n"
    "v_mfma_f32_16x16x32_bf16 a[228:231], a[108:111], a[20:23], a[228:231]\n"
    "v_mfma_f32_16x16x32_bf16 a[228:231], a[112:115], a[24:27], a[228:231]\n"
    "v_mfma_f32_16x16x32_bf16 a[228:231], a[116:119], a[48:51], a[228:231]\n"
    "ds_read_b128 a[48:51], %[rd1] offset:40960\n"
    "v_mfma_f32_16x16x32_bf16 a[228:231], a[120:123], a[52:55], a[228:231]\n"
    "v_mfma_f32_16x16x32_bf16 a[228:231], a[124:127], a[56:59], a[228:231]\n"
    "v_mfma_f32_16x16x32_bf16 a[232:235], a[100:103], a[60:63], a[232:235]\n"
    "v_mfma_f32_16x16x32_bf16 a[232:235], a[96:99], a[56:59], a[232:235]\n"
    "v_mfma_f32_16x16x32_bf16 a[232:235], a[92:95], a[52:55], a[232:235]\n"
    "s_add_u32 m0, s69, 20480\n"
    "s_add_u32 s57, s55, 32768\n"
    "v_mfma_f32_16x16x32_bf16 a[232:235], a[88:91], a[28:31], a[232:235]\n"
    "buffer_load_dwordx4 %[od1], %[srdX], s57 offen  lds\n"
    "v_mfma_f32_16x16x32_bf16 a[232:235], a[84:87], a[24:27], a[232:235]\n"
    "v_mfma_f32_16x16x32_bf16 a[232:235], a[80:83], a[20:23], a[232:235]\n"
    "v_mfma_f32_16x16x32_bf16 a[236:239], a[104:107], a[20:23], a[236:239]\n"
    "ds_read_b128 a[20:23], %[rd0] offset:43008\n"
    "v_mfma_f32_16x16x32_bf16 a[236:239], a[108:111], a[24:27], a[236:239]\n"
    "v_mfma_f32_16x16x32_bf16 a[236:239], a[112:115], a[28:31], a[236:239]\n"
    "v_mfma_f32_16x16x32_bf16 a[236:239], a[116:119], a[52:55], a[236:239]\n"
    "ds_read_b128 a[52:55], %[rd1] offset:43008\n"
    "v_mfma_f32_16x16x32_bf16 a[236:239], a[120:123], a[56:59], a[236:239]\n"
    "v_mfma_f32_16x16x32_bf16 a[236:239], a[124:127], a[60:63], a[236:239]\n"
    "v_mfma_f32_16x16x32_bf16 a[240:243], a[100:103], a[72:75], a[240:243]\n"
    "v_mfma_f32_16x16x32_bf16 a[240:243], a[96:99], a[60:63], a[240:243]\n"
    "v_mfma_f32_16x16x32_bf16 a[240:243], a[92:95], a[56:59], a[240:243]\n"
    "s_add_u32 m0, s69, 24576\n"
    "s_add_u32 s57, s55, 40960\n"
    "v_mfma_f32_16x16x32_bf16 a[240:243], a[88:91], a[64:67], a[240:243]\n"
    "buffer_load_dwordx4 %[od1], %[srdX], s57 offen  lds\n"
    "v_mfma_f32_16x16x32_bf16 a[240:243], a[84:87], a[28:31], a[240:243]\n"
    "v_mfma_f32_16x16x32_bf16 a[240:243], a[80:83], a[24:27], a[240:243]\n"
    "v_mfma_f32_16x16x32_bf16 a[244:247], a[104:107], a[24:27], a[244:247]\n"
    "ds_read_b128 a[24:27], %[rd0] offset:45056\n"
    "v_mfma_f32_16x16x32_bf16 a[244:247], a[108:111], a[28:31], a[244:247]\n"
    "v_mfma_f32_16x16x32_bf16 a[244:247], a[112:115], a[64:67], a[244:247]\n"
    "s_waitcnt lgkmcnt(10)\n"
    "v_mfma_f32_16x16x32_bf16 a[244:247], a[116:119], a[56:59], a[244:247]\n"
    "ds_read_b128 a[56:59], %[rd1] offset:45056\n"
    "v_mfma_f32_16x16x32_bf16 a[244:247], a[120:123], a[60:63], a[244:247]\n"
    "s_mov_b32 s60, 49152\n"
    "buffer_store_dwordx4 v[24:27], %[oo], s[76:79], s60 offen sc0 sc1\n"
    "v_mfma_f32_16x16x32_bf16 a[244:247], a[124:127], a[72:75], a[244:247]\n"
    "v_mfma_f32_16x16x32_bf16 a[248:251], a[100:103], a[76:79], a[248:251]\n"
    "v_mfma_f32_16x16x32_bf16 a[248:251], a[96:99], a[72:75], a[248:251]\n"
    "v_mfma_f32_16x16x32_bf16 a[248:251], a[92:95], a[60:63], a[248:251]\n"
    "s_add_u32 m0, s69, 28672\n"
    "s_add_u32 s57, s55, 49152\n"
    "v_mfma_f32_16x16x32_bf16 a[248:251], a[88:91], a[68:71], a[248:251]\n"
    "buffer_load_dwordx4 %[od1], %[srdX], s57 offen  lds\n"
    "v_mfma_f32_16x16x32_bf16 a[248:251], a[84:87], a[64:67], a[248:251]\n"
    "v_mfma_f32_16x16x32_bf16 a[248:251], a[80:83], a[28:31], a[248:251]\n"
    "s_add_u32 s69, s69, 0x8000\n"
    "s_and_b32 s69, s69, 0x1ffff\n"
    "v_mfma_f32_16x16x32_bf16 a[252:255], a[104:107], a[28:31], a[252:255]\n"
    "ds_read_b128 a[28:31], %[rd0] offset:47104\n"
    "v_mfma_f32_16x16x32_bf16 a[252:255], a[108:111], a[64:67], a[252:255]\n"
    "s_add_u32 s55, s55, 128\n"
    "v_mfma_f32_16x16x32_bf16 a[252:255], a[112:115], a[68:71], a[252:255]\n"
    "v_mfma_f32_16x16x32_bf16 a[252:255], a[116:119], a[60:63], a[252:255]\n"
    "ds_read_b128 a[60:63], %[rd1] offset:47104\n"
    "v_mfma_f32_16x16x32_bf16 a[252:255], a[120:123], a[72:75], a[252:255]\n"
    "v_mfma_f32_16x16x32_bf16 a[252:255], a[124:127], a[76:79], a[252:255]\n"
    "s_waitcnt vmcnt(10)\n"
    "s_waitcnt lgkmcnt(8)\n"
    "s_cmp_lg_u32 s67, 0\n"
    "s_cbranch_scc1 Lc4%=\n"
    "v_mfma_f32_16x16x32_bf16 v[208:211], a[148:151], a[40:43], v[208:211]\n"
    "v_mfma_f32_16x16x32_bf16 v[208:211], a[144:147], a[36:39], v[208:211]\n"
    "v_mfma_f32_16x16x32_bf16 v[208:211], a[140:143], a[32:35], v[208:211]\n"
    "v_mfma_f32_16x16x32_bf16 v[208:211], a[136:139], a[8:11], v[208:211]\n"
    "v_mfma_f32_16x16x32_bf16 v[208:211], a[132:135], a[4:7], v[208:211]\n"
    "v_mfma_f32_16x16x32_bf16 v[208:211], a[128:131], a[0:3], v[208:211]\n"
    "v_mfma_f32_16x16x32_bf16 v[212:215], a[152:155], a[0:3], v[212:215]\n"
    "v_mfma_f32_16x16x32_bf16 v[212:215], a[156:159], a[4:7], v[212:215]\n"
    "v_mfma_f32_16x16x32_bf16 v[212:215], a[160:163], a[8:11], v[212:215]\n"
    "v_mfma_f32_16x16x32_bf16 v[212:215], a[164:167], a[32:35], v[212:215]\n"
    "v_mfma_f32_16x16x32_bf16 v[212:215], a[168:171], a[36:39], v[212:215]\n"
    "v_mfma_f32_16x16x32_bf16 v[212:215], a[172:175], a[40:43], v[212:215]\n"
    "Lc4%=:\n"
    "v_mfma_f32_16x16x32_bf16 v[216:219], a[148:151], a[44:47], v[216:219]\n"
    "ds_read_b128 a[0:3], %[rd0] offset:49152\n"
    "v_mfma_f32_16x16x32_bf16 v[216:219], a[144:147], a[40:43], v[216:219]\n"
    "v_mfma_f32_16x16x32_bf16 v[216:219], a[140:143], a[36:39], v[216:219]\n"
    "buffer_load_dwordx4 a[80:83], %[l16], %[srdW], s54 offen offset:0\n"
    "v_mfma_f32_16x16x32_bf16 v[216:219], a[136:139], a[12:15], v[216:219]\n"
    "v_mfma_f32_16x16x32_bf16 v[216:219], a[132:135], a[8:11], v[216:219]\n"
    "ds_read_b128 a[32:35], %[rd1] offset:49152\n"
    "v_mfma_f32_16x16x32_bf16 v[216:219], a[128:131], a[4:7], v[216:219]\n"
    "v_mfma_f32_16x16x32_bf16 v[220:223], a[152:155], a[4:7], v[220:223]\n"
    "ds_read_b128 a[4:7], %[rd0] offset:51200\n"
    "v_mfma_f32_16x16x32_bf16 v[220:223], a[156:159], a[8:11], v[220:223]\n"
    "v_mfma_f32_16x16x32_bf16 v[220:223], a[160:163], a[12:15], v[220:223]\n"
    "v_mfma_f32_16x16x32_bf16 v[220:223], a[164:167], a[36:39], v[220:223]\n"
    "ds_read_b128 a[36:39], %[rd1] offset:51200\n"
    "v_mfma_f32_16x16x32_bf16 v[220:223], a[168:171], a[40:43], v[220:223]\n"
    "buffer_load_dwordx4 a[84:87], %[l16], %[srdW], s54 offen offset:1024\n"
    "v_mfma_f32_16x16x32_bf16 v[220:223], a[172:175], a[44:47], v[220:223]\n"
    "s_waitcnt lgkmcnt(8)\n"
    "v_mfma_f32_16x16x32_bf16 v[224:227], a[148:151], a[48:51], v[224:227]\n"
    "v_mfma_f32_16x16x32_bf16 v[224:227], a[144:147], a[44:47], v[224:227]\n"
    "ds_read_b128 a[64:67], %[re0] offset:32768\n"
    "v_mfma_f32_16x16x32_bf16 v[224:227], a[140:143], a[40:43], v[224:227]\n"
    "buffer_load_dwordx4 a[88:91], %[l16], %[srdW], s54 offen offset:2048\n"
    "v_mfma_f32_16x16x32_bf16 v[224:227], a[136:139], a[16:19], v[224:227]\n"
    "v_mfma_f32_16x16x32_bf16 v[224:227], a[132:135], a[12:15], v[224:227]\n"
    "v_mfma_f32_16x16x32_bf16 v[224:227], a[128:131], a[8:11], v[224:227]\n"
    "v_mfma_f32_16x16x32_bf16 v[228:231], a[152:155], a[8:11], v[228:231]\n"
    "ds_read_b128 a[8:11], %[rd0] offset:53248\n"
    "v_mfma_f32_16x16x32_bf16 v[228:231], a[156:159], a[12:15], v[228:231]\n"
    "v_mfma_f32_16x16x32_bf16 v[228:231], a[160:163], a[16:19], v[228:231]\n"
    "v_mfma_f32_16x16x32_bf16 v[228:231], a[164:167], a[40:43], v[228:231]\n"
    "ds_read_b128 a[40:43], %[rd1] offset:53248\n"
    "v_mfma_f32_16x16x32_bf16 v[228:231], a[168:171], a[44:47], v[228:231]\n"
    "buffer_load_dwordx4 a[92:95], %[l16], %[srdW], s54 offen offset:3072\n"
    "v_mfma_f32_16x16x32_bf16 v[228:231], a[172:175], a[48:51], v[228:231]\n"
    "v_mfma_f32_16x16x32_bf16 v[232:235], a[148:151], a[52:55], v[232:235]\n"
    "s_add_u32 s54, s54, 4096\n"
    "v_mfma_f32_16x16x32_bf16 v[232:235], a[144:147], a[48:51], v[232:235]\n"
    "ds_read_b128 a[72:75], %[re1] offset:32768\n"
    "v_mfma_f32_16x16x32_bf16 v[232:235], a[140:143], a[44:47], v[232:235]\n"
    "buffer_load_dwordx4 a[96:99], %[l16], %[srdW], s54 offen offset:0\n"
    "v_mfma_f32_16x16x32_bf16 v[232:235], a[136:139], a[20:23], v[232:235]\n"
    "v_mfma_f32_16x16x32_bf16 v[232:235], a[132:135], a[16:19], v[232:235]\n"
    "v_mfma_f32_16x16x32_bf16 v[232:235], a[128:131], a[12:15], v[232:235]\n"
    "v_mfma_f32_16x16x32_bf16 v[236:239], a[152:155], a[12:15], v[236:239]\n"
    "ds_read_b128 a[12:15], %[rd0] offset:55296\n"
    "v_mfma_f32_16x16x32_bf16 v[236:239], a[156:159], a[16:19], v[236:239]\n"
    "v_mfma_f32_16x16x32_bf16 v[236:239], a[160:163], a[20:23], v[236:239]\n"
    "v_mfma_f32_16x16x32_bf16 v[236:239], a[164:167], a[44:47], v[236:239]\n"
    "ds_read_b128 a[44:47], %[rd1] offset:55296\n"
    "v_mfma_f32_16x16x32_bf16 v[236:239], a[168:171], a[48:51], v[236:239]\n"
    "buffer_load_dwordx4 a[100:103], %[l16], %[srdW], s54 offen offset:1024\n"
    "v_mfma_f32_16x16x32_bf16 v[236:239], a[172:175], a[52:55], v[236:239]\n"
    "s_waitcnt lgkmcnt(10)\n"
    "v_mfma_f32_16x16x32_bf16 v[240:243], a[148:151], a[56:59], v[240:243]\n"
    "v_mfma_f32_16x16x32_bf16 v[240:243], a[144:147], a[52:55], v[240:243]\n"
    "ds_read_b128 a[68:71], %[re0] offset:34816\n"
    "v_mfma_f32_16x16x32_bf16 v[240:243], a[140:143], a[48:51], v[240:243]\n"
    "buffer_load_dwordx4 a[104:107], %[l16], %[srdW], s54 offen offset:2048\n"
    "v_mfma_f32_16x16x32_bf16 v[240:243], a[136:139], a[24:27], v[240:243]\n"
    "v_mfma_f32_16x16x32_bf16 v[240:243], a[132:135], a[20:23], v[240:243]\n"
    "v_mfma_f32_16x16x32_bf16 v[240:243], a[128:131], a[16:19], v[240:243]\n"
    "v_mfma_f32_16x16x32_bf16 v[244:247], a[152:155], a[16:19], v[244:247]\n"
    "ds_read_b128 a[16:19], %[rd0] offset:57344\n"
    "v_mfma_f32_16x16x32_bf16 v[244:247], a[156:159], a[20:23], v[244:247]\n"
    "v_mfma_f32_16x16x32_bf16 v[244:247], a[160:163], a[24:27], v[244:247]\n"
    "v_mfma_f32_16x16x32_bf16 v[244:247], a[164:167], a[48:51], v[244:247]\n"
    "ds_read_b128 a[48:51], %[rd1] offset:57344\n"
    "v_mfma_f32_16x16x32_bf16 v[244:247], a[168:171], a[52:55], v[244:247]\n"
    "buffer_load_dwordx4 a[108:111], %[l16], %[srdW], s54 offen offset:3072\n"
    "v_mfma_f32_16x16x32_bf16 v[244:247], a[172:175], a[56:59], v[244:247]\n"
    "v_mfma_f32_16x16x32_bf16 v[248:251], a[148:151], a[60:63], v[248:251]\n"
    "s_add_u32 s54, s54, 4096\n"
    "v_mfma_f32_16x16x32_bf16 v[248:251], a[144:147], a[56:59], v[248:251]\n"
    "ds_read_b128 a[76:79], %[re1] offset:34816\n"
    "v_mfma_f32_16x16x32_bf16 v[248:251], a[140:143], a[52:55], v[248:251]\n"
    "buffer_load_dwordx4 a[112:115], %[l16], %[srdW], s54 offen offset:0\n"
    "v_mfma_f32_16x16x32_bf16 v[248:251], a[136:139], a[28:31], v[248:251]\n"
    "v_mfma_f32_16x16x32_bf16 v[248:251], a[132:135], a[24:27], v[248:251]\n"
    "v_mfma_f32_16x16x32_bf16 v[248:251], a[128:131], a[20:23], v[248:251]\n"
    "v_mfma_f32_16x16x32_bf16 v[252:255], a[152:155], a[20:23], v[252:255]\n"
    "ds_read_b128 a[20:23], %[rd0] offset:59392\n"
    "v_mfma_f32_16x16x32_bf16 v[252:255], a[156:159], a[24:27], v[252:255]\n"
    "v_mfma_f32_16x16x32_bf16 v[252:255], a[160:163], a[28:31], v[252:255]\n"
    "v_mfma_f32_16x16x32_bf16 v[252:255], a[164:167], a[52:55], v[252:255]\n"
    "ds_read_b128 a[52:55], %[rd1] offset:59392\n"
    "v_mfma_f32_16x16x32_bf16 v[252:255], a[168:171], a[56:59], v[252:255]\n"
    "buffer_load_dwordx4 a[116:119], %[l16], %[srdW], s54 offen offset:1024\n"
    "v_mfma_f32_16x16x32_bf16 v[252:255], a[172:175], a[60:63], v[252:255]\n"
    "s_waitcnt lgkmcnt(12)\n"
    "v_mfma_f32_16x16x32_bf16 a[176:179], a[148:151], a[32:35], a[176:179]\n"
    "v_mfma_f32_16x16x32_bf16 a[176:179], a[144:147], a[60:63], a[176:179]\n"
    "v_mfma_f32_16x16x32_bf16 a[176:179], a[140:143], a[56:59], a[176:179]\n"
    "buffer_load_dwordx4 a[120:123], %[l16], %[srdW], s54 offen offset:2048\n"
    "v_mfma_f32_16x16x32_bf16 a[176:179], a[136:139], a[0:3], a[176:179]\n"
    "v_mfma_f32_16x16x32_bf16 a[176:179], a[132:135], a[28:31], a[176:179]\n"
    "v_mfma_f32_16x16x32_bf16 a[176:179], a[128:131], a[24:27], a[176:179]\n"
    "v_mfma_f32_16x16x32_bf16 a[180:183], a[152:155], a[24:27], a[180:183]\n"
    "ds_read_b128 a[24:27], %[rd0] offset:61440\n"
    "v_mfma_f32_16x16x32_bf16 a[180:183], a[156:159], a[28:31], a[180:183]\n"
    "v_mfma_f32_16x16x32_bf16 a[180:183], a[160:163], a[0:3], a[180:183]\n"
    "v_mfma_f32_16x16x32_bf16 a[180:183], a[164:167], a[56:59], a[180:183]\n"
    "ds_read_b128 a[56:59], %[rd1] offset:61440\n"
    "v_mfma_f32_16x16x32_bf16 a[180:183], a[168:171], a[60:63], a[180:183]\n"
    "buffer_load_dwordx4 a[124:127], %[l16], %[srdW], s54 offen offset:3072\n"
    "v_mfma_f32_16x16x32_bf16 a[180:183], a[172:175], a[32:35], a[180:183]\n"
    "v_mfma_f32_16x16x32_bf16 a[184:187], a[148:151], a[36:39], a[184:187]\n"
    "s_add_u32 s54, s54, 4096\n"
    "v_mfma_f32_16x16x32_bf16 a[184:187], a[144:147], a[32:35], a[184:187]\n"
    "s_add_u32 m0, s69, 0\n"
    "s_add_u32 s57, s55, 0\n"
    "v_subrev_u32 v13, 0x2000, %[od1]\n"
    "v_mfma_f32_16x16x32_bf16 a[184:187], a[140:143], a[60:63], a[184:187]\n"
    "buffer_load_dwordx4 v13, %[srdX], s57 offen  lds\n"
    "v_mfma_f32_16x16x32_bf16 a[184:187], a[136:139], a[4:7], a[184:187]\n"
    "s_add_u32 m0, s69, 4096\n"
    "s_add_u32 s57, s55, 0\n"
    "v_mfma_f32_16x16x32_bf16 a[184:187], a[132:135], a[0:3], a[184:187]\n"
    "buffer_load_dwordx4 %[od1], %[srdX], s57 offen  lds\n"
    "v_mfma_f32_16x16x32_bf16 a[184:187], a[128:131], a[28:31], a[184:187]\n"
    "s_mov_b32 s60, 57344\n"
    "buffer_store_dwordx4 v[28:31], %[oo], s[76:79], s60 offen sc0 sc1\n"
    "v_mfma_f32_16x16x32_bf16 a[188:191], a[152:155], a[28:31], a[188:191]\n"
    "ds_read_b128 a[28:31], %[rd0] offset:63488\n"
    "v_mfma_f32_16x16x32_bf16 a[188:191], a[156:159], a[0:3], a[188:191]\n"
    "v_mfma_f32_16x16x32_bf16 a[188:191], a[160:163], a[4:7], a[188:191]\n"
    "v_mfma_f32_16x16x32_bf16 a[188:191], a[164:167], a[60:63], a[188:191]\n"
    "ds_read_b128 a[60:63], %[rd1] offset:63488\n"
    "v_mfma_f32_16x16x32_bf16 a[188:191], a[168:171], a[32:35], a[188:191]\n"
    "v_mfma_f32_16x16x32_bf16 a[188:191], a[172:175], a[36:39], a[188:191]\n"
    "s_waitcnt lgkmcnt(10)\n"
    "v_mfma_f32_16x16x32_bf16 a[192:195], a[148:151], a[40:43], a[192:195]\n"
    "v_xor_b32 %[rd0], 0x10000, %[rd0]\n"
    "v_mfma_f32_16x16x32_bf16 a[192:195], a[144:147], a[36:39], a[192:195]\n"
    "v_xor_b32 %[rd1], 0x10000, %[rd1]\n"
    "v_mfma_f32_16x16x32_bf16 a[192:195], a[140:143], a[32:35], a[192:195]\n"
    "s_add_u32 s63, s63, 1\n"
    "v_mfma_f32_16x16x32_bf16 a[192:195], a[136:139], a[8:11], a[192:195]\n"
    "v_mfma_f32_16x16x32_bf16 a[192:195], a[132:135], a[4:7], a[192:195]\n"
    "v_mfma_f32_16x16x32_bf16 a[192:195], a[128:131], a[0:3], a[192:195]\n"
    "v_mfma_f32_16x16x32_bf16 a[196:199], a[152:155], a[0:3], a[196:199]\n"
    "s_waitcnt lgkmcnt(0)\n"
    "v_mov_b32 v13, s63\n"
    "v_mfma_f32_16x16x32_bf16 a[196:199], a[156:159], a[4:7], a[196:199]\n"
    "ds_write_b32 v14, v13\n"
    "v_mfma_f32_16x16x32_bf16 a[196:199], a[160:163], a[8:11], a[196:199]\n"
    "v_mfma_f32_16x16x32_bf16 a[196:199], a[164:167], a[32:35], a[196:199]\n"
    "v_mfma_f32_16x16x32_bf16 a[196:199], a[168:171], a[36:39], a[196:199]\n"
    "v_mfma_f32_16x16x32_bf16 a[196:199], a[172:175], a[40:43], a[196:199]\n"
    "v_mfma_f32_16x16x32_bf16 a[200:203], a[148:151], a[44:47], a[200:203]\n"
    "v_xor_b32 %[re0], 0x10000, %[re0]\n"
    "v_mfma_f32_16x16x32_bf16 a[200:203], a[144:147], a[40:43], a[200:203]\n"
    "v_xor_b32 %[re1], 0x10000, %[re1]\n"
    "v_mfma_f32_16x16x32_bf16 a[200:203], a[140:143], a[36:39], a[200:203]\n"
    "v_mfma_f32_16x16x32_bf16 a[200:203], a[136:139], a[12:15], a[200:203]\n"
    "v_mfma_f32_16x16x32_bf16 a[200:203], a[132:135], a[8:11], a[200:203]\n"
    "ds_read_b32 v13, v12\n"
    "v_mfma_f32_16x16x32_bf16 a[200:203], a[128:131], a[4:7], a[200:203]\n"
    "v_mfma_f32_16x16x32_bf16 a[204:207], a[152:155], a[4:7], a[204:207]\n"
    "v_mfma_f32_16x16x32_bf16 a[204:207], a[156:159], a[8:11], a[204:207]\n"
    "v_mfma_f32_16x16x32_bf16 a[204:207], a[160:163], a[12:15], a[204:207]\n"
    "v_mfma_f32_16x16x32_bf16 a[204:207], a[164:167], a[36:39], a[204:207]\n"
    "v_mfma_f32_16x16x32_bf16 a[204:207], a[168:171], a[40:43], a[204:207]\n"
    "s_waitcnt lgkmcnt(0)\n"
    "v_cmp_gt_u32 vcc, s63, v13\n"
    "v_mfma_f32_16x16x32_bf16 a[204:207], a[172:175], a[44:47], a[204:207]\n"
    "s_cbranch_vccnz Ls4%=\n"
    "Lb4%=:\n"
    "v_mfma_f32_16x16x32_bf16 a[208:211], a[148:151], a[48:51], a[208:211]\n"
    "ds_read_b128 a[0:3], %[rd0] offset:0\n"
    "v_mfma_f32_16x16x32_bf16 a[208:211], a[144:147], a[44:47], a[208:211]\n"
    "ds_read_b128 a[32:35], %[rd1] offset:0\n"
    "v_mfma_f32_16x16x32_bf16 a[208:211], a[140:143], a[40:43], a[208:211]\n"
    "ds_read_b128 a[4:7], %[rd0] offset:2048\n"
    "v_mfma_f32_16x16x32_bf16 a[208:211], a[136:139], a[16:19], a[208:211]\n"
    "ds_read_b128 a[36:39], %[rd1] offset:2048\n"
    "v_mfma_f32_16x16x32_bf16 a[208:211], a[132:135], a[12:15], a[208:211]\n"
    "v_mfma_f32_16x16x32_bf16 a[208:211], a[128:131], a[8:11], a[208:211]\n"
    "v_mfma_f32_16x16x32_bf16 a[212:215], a[152:155], a[8:11], a[212:215]\n"
    "ds_read_b128 a[8:11], %[rd0] offset:4096\n"
    "v_mfma_f32_16x16x32_bf16 a[212:215], a[156:159], a[12:15], a[212:215]\n"
    "s_add_u32 m0, s69, 8192\n"
    "s_add_u32 s57, s55, 8192\n"
    "v_mfma_f32_16x16x32_bf16 a[212:215], a[160:163], a[16:19], a[212:215]\n"
    "buffer_load_dwordx4 %[od1], %[srdX], s57 offen  lds\n"
    "v_mfma_f32_16x16x32_bf16 a[212:215], a[164:167], a[40:43], a[212:215]\n"
    "ds_read_b128 a[40:43], %[rd1] offset:4096\n"
    "v_mfma_f32_16x16x32_bf16 a[212:215], a[168:171], a[44:47], a[212:215]\n"
    "v_mfma_f32_16x16x32_bf16 a[212:215], a[172:175], a[48:51], a[212:215]\n"
    "v_mfma_f32_16x16x32_bf16 a[216:219], a[148:151], a[52:55], a[216:219]\n"
    "v_mfma_f32_16x16x32_bf16 a[216:219], a[144:147], a[48:51], a[216:219]\n"
    "v_mfma_f32_16x16x32_bf16 a[216:219], a[140:143], a[44:47], a[216:219]\n"
    "s_add_u32 m0, s69, 12288\n"
    "s_add_u32 s57, s55, 16384\n"
    "v_mfma_f32_16x16x32_bf16 a[216:219], a[136:139], a[20:23], a[216:219]\n"
    "buffer_load_dwordx4 %[od1], %[srdX], s57 offen  lds\n"
    "v_mfma_f32_16x16x32_bf16 a[216:219], a[132:135], a[16:19], a[216:219]\n"
    "v_mfma_f32_16x16x32_bf16 a[216:219], a[128:131], a[12:15], a[216:219]\n"
    "v_mfma_f32_16x16x32_bf16 a[220:223], a[152:155], a[12:15], a[220:223]\n"
    "ds_read_b128 a[12:15], %[rd0] offset:6144\n"
    "v_mfma_f32_16x16x32_bf16 a[220:223], a[156:159], a[16:19], a[220:223]\n"
    "v_mfma_f32_16x16x32_bf16 a[220:223], a[160:163], a[20:23], a[220:223]\n"
    "v_mfma_f32_16x16x32_bf16 a[220:223], a[164:167], a[44:47], a[220:223]\n"
    "ds_read_b128 a[44:47], %[rd1] offset:6144\n"
    "v_mfma_f32_16x16x32_bf16 a[220:223], a[168:171], a[48:51], a[220:223]\n"
    "v_mfma_f32_16x16x32_bf16 a[220:223], a[172:175], a[52:55], a[220:223]\n"
    "v_mfma_f32_16x16x32_bf16 a[224:227], a[148:151], a[56:59], a[224:227]\n"
    "v_mfma_f32_16x16x32_bf16 a[224:227], a[144:147], a[52:55], a[224:227]\n"
    "v_mfma_f32_16x16x32_bf16 a[224:227], a[140:143], a[48:51], a[224:227]\n"
    "s_add_u32 m0, s69, 16384\n"
    "s_add_u32 s57, s55, 24576\n"
    "v_mfma_f32_16x16x32_bf16 a[224:227], a[136:139], a[24:27], a[224:227]\n"
    "buffer_load_dwordx4 %[od1], %[srdX], s57 offen  lds\n"
    "v_mfma_f32_16x16x32_bf16 a[224:227], a[132:135], a[20:23], a[224:227]\n"
    "v_mfma_f32_16x16x32_bf16 a[224:227], a[128:131], a[16:19], a[224:227]\n"
    "v_mfma_f32_16x16x32_bf16 a[228:231], a[152:155], a[16:19], a[228:231]\n"
    "ds_read_b128 a[16:19], %[rd0] offset:8192\n"
    "v_mfma_f32_16x16x32_bf16 a[228:231], a[156:159], a[20:23], a[228:231]\n"
    "v_mfma_f32_16x16x32_bf16 a[228:231], a[160:163], a[24:27], a[228:231]\n"
    "v_mfma_f32_16x16x32_bf16 a[228:231], a[164:167], a[48:51], a[228:231]\n"
    "ds_read_b128 a[48:51], %[rd1] offset:8192\n"
    "v_mfma_f32_16x16x32_bf16 a[228:231], a[168:171], a[52:55], a[228:231]\n"
    "v_mfma_f32_16x16x32_bf16 a[228:231], a[172:175], a[56:59], a[228:231]\n"
    "v_mfma_f32_16x16x32_bf16 a[232:235], a[148:151], a[60:63], a[232:235]\n"
    "v_mfma_f32_16x16x32_bf16 a[232:235], a[144:147], a[56:59], a[232:235]\n"
    "v_mfma_f32_16x16x32_bf16 a[232:235], a[140:143], a[52:55], a[232:235]\n"
    "s_add_u32 m0, s69, 20480\n"
    "s_add_u32 s57, s55, 32768\n"
    "v_mfma_f32_16x16x32_bf16 a[232:235], a[136:139], a[28:31], a[232:235]\n"
    "buffer_load_dwordx4 %[od1], %[srdX], s57 offen  lds\n"
    "v_mfma_f32_16x16x32_bf16 a[232:235], a[132:135], a[24:27], a[232:235]\n"
    "v_mfma_f32_16x16x32_bf16 a[232:235], a[128:131], a[20:23], a[232:235]\n"
    "v_mfma_f32_16x16x32_bf16 a[236:239], a[152:155], a[20:23], a[236:239]\n"
    "ds_read_b128 a[20:23], %[rd0] offset:10240\n"
    "v_mfma_f32_16x16x32_bf16 a[236:239], a[156:159], a[24:27], a[236:239]\n"
    "v_mfma_f32_16x16x32_bf16 a[236:239], a[160:163], a[28:31], a[236:239]\n"
    "v_mfma_f32_16x16x32_bf16 a[236:239], a[164:167], a[52:55], a[236:239]\n"
    "ds_read_b128 a[52:55], %[rd1] offset:10240\n"
    "v_mfma_f32_16x16x32_bf16 a[236:239], a[168:171], a[56:59], a[236:239]\n"
    "v_mfma_f32_16x16x32_bf16 a[236:239], a[172:175], a[60:63], a[236:239]\n"
    "v_mfma_f32_16x16x32_bf16 a[240:243], a[148:151], a[72:75], a[240:243]\n"
    "v_mfma_f32_16x16x32_bf16 a[240:243], a[144:147], a[60:63], a[240:243]\n"
    "v_mfma_f32_16x16x32_bf16 a[240:243], a[140:143], a[56:59], a[240:243]\n"
    "s_add_u32 m0, s69, 24576\n"
    "s_add_u32 s57, s55, 40960\n"
    "v_mfma_f32_16x16x32_bf16 a[240:243], a[136:139], a[64:67], a[240:243]\n"
    "buffer_load_dwordx4 %[od1], %[srdX], s57 offen  lds\n"
    "v_mfma_f32_16x16x32_bf16 a[240:243], a[132:135], a[28:31], a[240:243]\n"
    "v_mfma_f32_16x16x32_bf16 a[240:243], a[128:131], a[24:27], a[240:243]\n"
    "v_mfma_f32_16x16x32_bf16 a[244:247], a[152:155], a[24:27], a[244:247]\n"
    "ds_read_b128 a[24:27], %[rd0] offset:12288\n"
    "v_mfma_f32_16x16x32_bf16 a[244:247], a[156:159], a[28:31], a[244:247]\n"
    "v_mfma_f32_16x16x32_bf16 a[244:247], a[160:163], a[64:67], a[244:247]\n"
    "v_mfma_f32_16x16x32_bf16 a[244:247], a[164:167], a[56:59], a[244:247]\n"
    "ds_read_b128 a[56:59], %[rd1] offset:12288\n"
    "v_mfma_f32_16x16x32_bf16 a[244:247], a[168:171], a[60:63], a[244:247]\n"
    "v_mfma_f32_16x16x32_bf16 a[244:247], a[172:175], a[72:75], a[244:247]\n"
    "v_mfma_f32_16x16x32_bf16 a[248:251], a[148:151], a[76:79], a[248:251]\n"
    "v_mfma_f32_16x16x32_bf16 a[248:251], a[144:147], a[72:75], a[248:251]\n"
    "v_mfma_f32_16x16x32_bf16 a[248:251], a[140:143], a[60:63], a[248:251]\n"
    "s_add_u32 m0, s69, 28672\n"
    "s_add_u32 s57, s55, 49152\n"
    "v_mfma_f32_16x16x32_bf16 a[248:251], a[136:139], a[68:71], a[248:251]\n"
    "buffer_load_dwordx4 %[od1], %[srdX], s57 offen  lds\n"
    "v_mfma_f32_16x16x32_bf16 a[248:251], a[132:135], a[64:67], a[248:251]\n"
    "v_mfma_f32_16x16x32_bf16 a[248:251], a[128:131], a[28:31], a[248:251]\n"
    "s_add_u32 s69, s69, 0x8000\n"
    "s_and_b32 s69, s69, 0x1ffff\n"
    "v_mfma_f32_16x16x32_bf16 a[252:255], a[152:155], a[28:31], a[252:255]\n"
    "ds_read_b128 a[28:31], %[rd0] offset:14336\n"
    "v_mfma_f32_16x16x32_bf16 a[252:255], a[156:159], a[64:67], a[252:255]\n"
    "s_add_u32 s55, s55, 128\n"
    "v_mfma_f32_16x16x32_bf16 a[252:255], a[160:163], a[68:71], a[252:255]\n"
    "v_mfma_f32_16x16x32_bf16 a[252:255], a[164:167], a[60:63], a[252:255]\n"
    "ds_read_b128 a[60:63], %[rd1] offset:14336\n"
    "v_mfma_f32_16x16x32_bf16 a[252:255], a[168:171], a[72:75], a[252:255]\n"
    "v_mfma_f32_16x16x32_bf16 a[252:255], a[172:175], a[76:79], a[252:255]\n"
    "Ljoin%=:\n"
    "s_waitcnt vmcnt(9)\n"
    "s_waitcnt lgkmcnt(8)\n"
    "s_cmp_lg_u32 s67, 0\n"
    "s_cbranch_scc1 Lc5%=\n"
    "v_mfma_f32_16x16x32_bf16 v[208:211], a[100:103], a[40:43], v[208:211]\n"
    "v_mfma_f32_16x16x32_bf16 v[208:211], a[96:99], a[36:39], v[208:211]\n"
    "v_mfma_f32_16x16x32_bf16 v[208:211], a[92:95], a[32:35], v[208:211]\n"
    "v_mfma_f32_16x16x32_bf16 v[208:211], a[88:91], a[8:11], v[208:211]\n"
    "v_mfma_f32_16x16x32_bf16 v[208:211], a[84:87], a[4:7], v[208:211]\n"
    "v_mfma_f32_16x16x32_bf16 v[208:211], a[80:83], a[0:3], v[208:211]\n"
    "v_mfma_f32_16x16x32_bf16 v[212:215], a[104:107], a[0:3], v[212:215]\n"
    "v_mfma_f32_16x16x32_bf16 v[212:215], a[108:111], a[4:7], v[212:215]\n"
    "v_mfma_f32_16x16x32_bf16 v[212:215], a[112:115], a[8:11], v[212:215]\n"
    "v_mfma_f32_16x16x32_bf16 v[212:215], a[116:119], a[32:35], v[212:215]\n"
    "v_mfma_f32_16x16x32_bf16 v[212:215], a[120:123], a[36:39], v[212:215]\n"
    "v_mfma_f32_16x16x32_bf16 v[212:215], a[124:127], a[40:43], v[212:215]\n"
    "Lc5%=:\n"
    "v_mfma_f32_16x16x32_bf16 v[216:219], a[100:103], a[44:47], v[216:219]\n"
    "ds_read_b128 a[0:3], %[rd0] offset:16384\n"
    "v_mfma_f32_16x16x32_bf16 v[216:219], a[96:99], a[40:43], v[216:219]\n"
    "v_mfma_f32_16x16x32_bf16 v[216:219], a[92:95], a[36:39], v[216:219]\n"
    "buffer_load_dwordx4 a[128:131], %[l16], %[srdW], s54 offen offset:0\n"
    "v_mfma_f32_16x16x32_bf16 v[216:219], a[88:91], a[12:15], v[216:219]\n"
    "v_mfma_f32_16x16x32_bf16 v[216:219], a[84:87], a[8:11], v[216:219]\n"
    "ds_read_b128 a[32:35], %[rd1] offset:16384\n"
    "v_mfma_f32_16x16x32_bf16 v[216:219], a[80:83], a[4:7], v[216:219]\n"
    "v_mfma_f32_16x16x32_bf16 v[220:223], a[104:107], a[4:7], v[220:223]\n"
    "ds_read_b128 a[4:7], %[rd0] offset:18432\n"
    "v_mfma_f32_16x16x32_bf16 v[220:223], a[108:111], a[8:11], v[220:223]\n"
    "v_mfma_f32_16x16x32_bf16 v[220:223], a[112:115], a[12:15], v[220:223]\n"
    "v_mfma_f32_16x16x32_bf16 v[220:223], a[116:119], a[36:39], v[220:223]\n"
    "ds_read_b128 a[36:39], %[rd1] offset:18432\n"
    "v_mfma_f32_16x16x32_bf16 v[220:223], a[120:123], a[40:43], v[220:223]\n"
    "buffer_load_dwordx4 a[132:135], %[l16], %[srdW], s54 offen offset:1024\n"
    "v_mfma_f32_16x16x32_bf16 v[220:223], a[124:127], a[44:47], v[220:223]\n"
    "s_waitcnt lgkmcnt(8)\n"
    "v_mfma_f32_16x16x32_bf16 v[224:227], a[100:103], a[48:51], v[224:227]\n"
    "v_mfma_f32_16x16x32_bf16 v[224:227], a[96:99], a[44:47], v[224:227]\n"
    "ds_read_b128 a[64:67], %[re0] offset:0\n"
    "v_mfma_f32_16x16x32_bf16 v[224:227], a[92:95], a[40:43], v[224:227]\n"
    "buffer_load_dwordx4 a[136:139], %[l16], %[srdW], s54 offen offset:2048\n"
    "v_mfma_f32_16x16x32_bf16 v[224:227], a[88:91], a[16:19], v[224:227]\n"
    "v_mfma_f32_16x16x32_bf16 v[224:227], a[84:87], a[12:15], v[224:227]\n"
    "v_mfma_f32_16x16x32_bf16 v[224:227], a[80:83], a[8:11], v[224:227]\n"
    "v_mfma_f32_16x16x32_bf16 v[228:231], a[104:107], a[8:11], v[228:231]\n"
    "ds_read_b128 a[8:11], %[rd0] offset:20480\n"
    "v_mfma_f32_16x16x32_bf16 v[228:231], a[108:111], a[12:15], v[228:231]\n"
    "v_mfma_f32_16x16x32_bf16 v[228:231], a[112:115], a[16:19], v[228:231]\n"
    "v_mfma_f32_16x16x32_bf16 v[228:231], a[116:119], a[40:43], v[228:231]\n"
    "ds_read_b128 a[40:43], %[rd1] offset:20480\n"
    "v_mfma_f32_16x16x32_bf16 v[228:231], a[120:123], a[44:47], v[228:231]\n"
    "buffer_load_dwordx4 a[140:143], %[l16], %[srdW], s54 offen offset:3072\n"
    "v_mfma_f32_16x16x32_bf16 v[228:231], a[124:127], a[48:51], v[228:231]\n"
    "v_mfma_f32_16x16x32_bf16 v[232:235], a[100:103], a[52:55], v[232:235]\n"
    "s_add_u32 s54, s54, 4096\n"
    "v_mfma_f32_16x16x32_bf16 v[232:235], a[96:99], a[48:51], v[232:235]\n"
    "ds_read_b128 a[72:75], %[re1] offset:0\n"
    "v_mfma_f32_16x16x32_bf16 v[232:235], a[92:95], a[44:47], v[232:235]\n"
    "buffer_load_dwordx4 a[144:147], %[l16], %[srdW], s54 offen offset:0\n"
    "v_mfma_f32_16x16x32_bf16 v[232:235], a[88:91], a[20:23], v[232:235]\n"
    "v_mfma_f32_16x16x32_bf16 v[232:235], a[84:87], a[16:19], v[232:235]\n"
    "v_mfma_f32_16x16x32_bf16 v[232:235], a[80:83], a[12:15], v[232:235]\n"
    "v_mfma_f32_16x16x32_bf16 v[236:239], a[104:107], a[12:15], v[236:239]\n"
    "ds_read_b128 a[12:15], %[rd0] offset:22528\n"
    "v_mfma_f32_16x16x32_bf16 v[236:239], a[108:111], a[16:19], v[236:239]\n"
    "v_mfma_f32_16x16x32_bf16 v[236:239], a[112:115], a[20:23], v[236:239]\n"
    "v_mfma_f32_16x16x32_bf16 v[236:239], a[116:119], a[44:47], v[236:239]\n"
    "ds_read_b128 a[44:47], %[rd1] offset:22528\n"
    "v_mfma_f32_16x16x32_bf16 v[236:239], a[120:123], a[48:51], v[236:239]\n"
    "buffer_load_dwordx4 a[148:151], %[l16], %[srdW], s54 offen offset:1024\n"
    "v_mfma_f32_16x16x32_bf16 v[236:239], a[124:127], a[52:55], v[236:239]\n"
    "s_waitcnt lgkmcnt(10)\n"
    "v_mfma_f32_16x16x32_bf16 v[240:243], a[100:103], a[56:59], v[240:243]\n"
    "v_mfma_f32_16x16x32_bf16 v[240:243], a[96:99], a[52:55], v[240:243]\n"
    "ds_read_b128 a[68:71], %[re0] offset:2048\n"
    "v_mfma_f32_16x16x32_bf16 v[240:243], a[92:95], a[48:51], v[240:243]\n"
    "buffer_load_dwordx4 a[152:155], %[l16], %[srdW], s54 offen offset:2048\n"
    "v_mfma_f32_16x16x32_bf16 v[240:243], a[88:91], a[24:27], v[240:243]\n"
    "v_mfma_f32_16x16x32_bf16 v[240:243], a[84:87], a[20:23], v[240:243]\n"
    "s_lshl_b32 s58, s62, 6\n"
    "v_mov_b32 v77, 1\n"
    "v_mov_b32 v78, %[pcnt]\n"
    "v_mfma_f32_16x16x32_bf16 v[240:243], a[80:83], a[16:19], v[240:243]\n"
    "v_mfma_f32_16x16x32_bf16 v[244:247], a[104:107], a[16:19], v[244:247]\n"
    "ds_read_b128 a[16:19], %[rd0] offset:24576\n"
    "v_mfma_f32_16x16x32_bf16 v[244:247], a[108:111], a[20:23], v[244:247]\n"
    "s_waitcnt vmcnt(13)\n"
    "s_mov_b64 exec, 1\n"
    "buffer_atomic_add v77, v78, %[srdW], s58 offen offset:0 sc1\n"
    "s_mov_b64 exec, -1\n"
    "v_mfma_f32_16x16x32_bf16 v[244:247], a[112:115], a[24:27], v[244:247]\n"
    "v_mfma_f32_16x16x32_bf16 v[244:247], a[116:119], a[48:51], v[244:247]\n"
    "ds_read_b128 a[48:51], %[rd1] offset:24576\n"
    "v_mfma_f32_16x16x32_bf16 v[244:247], a[120:123], a[52:55], v[244:247]\n"
    "buffer_load_dwordx4 a[156:159], %[l16], %[srdW], s54 offen offset:3072\n"
    "v_mfma_f32_16x16x32_bf16 v[244:247], a[124:127], a[56:59], v[244:247]\n"
    "v_mfma_f32_16x16x32_bf16 v[248:251], a[100:103], a[60:63], v[248:251]\n"
    "s_add_u32 s54, s54, 4096\n"
    "v_mfma_f32_16x16x32_bf16 v[248:251], a[96:99], a[56:59], v[248:251]\n"
    "ds_read_b128 a[76:79], %[re1] offset:2048\n"
    "v_mfma_f32_16x16x32_bf16 v[248:251], a[92:95], a[52:55], v[248:251]\n"
    "buffer_load_dwordx4 a[160:163], %[l16], %[srdW], s54 offen offset:0\n"
    "v_mfma_f32_16x16x32_bf16 v[248:251], a[88:91], a[28:31], v[248:251]\n"
    "v_mfma_f32_16x16x32_bf16 v[248:251], a[84:87], a[24:27], v[248:251]\n"
    "s_add_u32 s56, s56, 512\n"
    "s_add_u32 s71, s71, 0x20000\n"
    "v_subrev_u32 v76, 0x2000, %[xr1]\n"
    "v_mfma_f32_16x16x32_bf16 v[248:251], a[80:83], a[20:23], v[248:251]\n"
    "v_mfma_f32_16x16x32_bf16 v[252:255], a[104:107], a[20:23], v[252:255]\n"
    "ds_read_b128 a[20:23], %[rd0] offset:26624\n"
    "v_mfma_f32_16x16x32_bf16 v[252:255], a[108:111], a[24:27], v[252:255]\n"
    "v_mfma_f32_16x16x32_bf16 v[252:255], a[112:115], a[28:31], v[252:255]\n"
    "v_mfma_f32_16x16x32_bf16 v[252:255], a[116:119], a[52:55], v[252:255]\n"
    "ds_read_b128 a[52:55], %[rd1] offset:26624\n"
    "v_mfma_f32_16x16x32_bf16 v[252:255], a[120:123], a[56:59], v[252:255]\n"
    "buffer_load_dwordx4 a[164:167], %[l16], %[srdW], s54 offen offset:1024\n"
    "v_mfma_f32_16x16x32_bf16 v[252:255], a[124:127], a[60:63], v[252:255]\n"
    "s_waitcnt lgkmcnt(12)\n"
    "v_mfma_f32_16x16x32_bf16 a[176:179], a[100:103], a[32:35], a[176:179]\n"
    "v_mfma_f32_16x16x32_bf16 a[176:179], a[96:99], a[60:63], a[176:179]\n"
    "v_mfma_f32_16x16x32_bf16 a[176:179], a[92:95], a[56:59], a[176:179]\n"
    "buffer_load_dwordx4 a[168:171], %[l16], %[srdW], s54 offen offset:2048\n"
    "v_mfma_f32_16x16x32_bf16 a[176:179], a[88:91], a[0:3], a[176:179]\n"
    "v_mfma_f32_16x16x32_bf16 a[176:179], a[84:87], a[28:31], a[176:179]\n"
    "v_mfma_f32_16x16x32_bf16 a[176:179], a[80:83], a[24:27], a[176:179]\n"
    "v_mfma_f32_16x16x32_bf16 a[180:183], a[104:107], a[24:27], a[180:183]\n"
    "ds_read_b128 a[24:27], %[rd0] offset:28672\n"
    "v_mfma_f32_16x16x32_bf16 a[180:183], a[108:111], a[28:31], a[180:183]\n"
    "v_mfma_f32_16x16x32_bf16 a[180:183], a[112:115], a[0:3], a[180:183]\n"
    "v_mfma_f32_16x16x32_bf16 a[180:183], a[116:119], a[56:59], a[180:183]\n"
    "ds_read_b128 a[56:59], %[rd1] offset:28672\n"
    "v_mfma_f32_16x16x32_bf16 a[180:183], a[120:123], a[60:63], a[180:183]\n"
    "buffer_load_dwordx4 a[172:175], %[l16], %[srdW], s54 offen offset:3072\n"
    "v_mfma_f32_16x16x32_bf16 a[180:183], a[124:127], a[32:35], a[180:183]\n"
    "v_mfma_f32_16x16x32_bf16 a[184:187], a[100:103], a[36:39], a[184:187]\n"
    "s_add_u32 s54, s54, 4096\n"
    "v_mfma_f32_16x16x32_bf16 a[184:187], a[96:99], a[32:35], a[184:187]\n"
    "s_add_u32 m0, s69, 0\n"
    "s_add_u32 s57, s55, 0\n"
    "v_subrev_u32 v13, 0x2000, %[od1]\n"
    "v_mfma_f32_16x16x32_bf16 a[184:187], a[92:95], a[60:63], a[184:187]\n"
    "buffer_load_dwordx4 v13, %[srdX], s57 offen  lds\n"
    "v_mfma_f32_16x16x32_bf16 a[184:187], a[88:91], a[4:7], a[184:187]\n"
    "s_add_u32 m0, s69, 4096\n"
    "s_add_u32 s57, s55, 0\n"
    "v_mfma_f32_16x16x32_bf16 a[184:187], a[84:87], a[0:3], a[184:187]\n"
    "buffer_load_dwordx4 %[od1], %[srdX], s57 offen  lds\n"
    "v_mfma_f32_16x16x32_bf16 a[184:187], a[80:83], a[28:31], a[184:187]\n"
    "s_add_u32 s64, s56, 0\n"
    "buffer_load_dwordx4 v[32:35], v76, %[srdX], s64 offen\n"
    "v_mfma_f32_16x16x32_bf16 a[188:191], a[104:107], a[28:31], a[188:191]\n"
    "ds_read_b128 a[28:31], %[rd0] offset:30720\n"
    "v_mfma_f32_16x16x32_bf16 a[188:191], a[108:111], a[0:3], a[188:191]\n"
    "v_mfma_f32_16x16x32_bf16 a[188:191], a[112:115], a[4:7], a[188:191]\n"
    "v_mfma_f32_16x16x32_bf16 a[188:191], a[116:119], a[60:63], a[188:191]\n"
    "ds_read_b128 a[60:63], %[rd1] offset:30720\n"
    "v_mfma_f32_16x16x32_bf16 a[188:191], a[120:123], a[32:35], a[188:191]\n"
    "v_mfma_f32_16x16x32_bf16 a[188:191], a[124:127], a[36:39], a[188:191]\n"
    "s_waitcnt lgkmcnt(10)\n"
    "v_mfma_f32_16x16x32_bf16 a[192:195], a[100:103], a[40:43], a[192:195]\n"
    "v_mfma_f32_16x16x32_bf16 a[192:195], a[96:99], a[36:39], a[192:195]\n"
    "v_mfma_f32_16x16x32_bf16 a[192:195], a[92:95], a[32:35], a[192:195]\n"
    "s_add_u32 s63, s63, 1\n"
    "v_mfma_f32_16x16x32_bf16 a[192:195], a[88:91], a[8:11], a[192:195]\n"
    "v_mfma_f32_16x16x32_bf16 a[192:195], a[84:87], a[4:7], a[192:195]\n"
    "v_mfma_f32_16x16x32_bf16 a[192:195], a[80:83], a[0:3], a[192:195]\n"
    "v_mfma_f32_16x16x32_bf16 a[196:199], a[104:107], a[0:3], a[196:199]\n"
    "s_waitcnt lgkmcnt(0)\n"
    "v_mov_b32 v13, s63\n"
    "v_mfma_f32_16x16x32_bf16 a[196:199], a[108:111], a[4:7], a[196:199]\n"
    "ds_write_b32 v14, v13\n"
    "v_mfma_f32_16x16x32_bf16 a[196:199], a[112:115], a[8:11], a[196:199]\n"
    "v_mfma_f32_16x16x32_bf16 a[196:199], a[116:119], a[32:35], a[196:199]\n"
    "v_mfma_f32_16x16x32_bf16 a[196:199], a[120:123], a[36:39], a[196:199]\n"
    "v_mfma_f32_16x16x32_bf16 a[196:199], a[124:127], a[40:43], a[196:199]\n"
    "v_mfma_f32_16x16x32_bf16 a[200:203], a[100:103], a[44:47], a[200:203]\n"
    "v_mfma_f32_16x16x32_bf16 a[200:203], a[96:99], a[40:43], a[200:203]\n"
    "v_mfma_f32_16x16x32_bf16 a[200:203], a[92:95], a[36:39], a[200:203]\n"
    "v_mfma_f32_16x16x32_bf16 a[200:203], a[88:91], a[12:15], a[200:203]\n"
    "v_mfma_f32_16x16x32_bf16 a[200:203], a[84:87], a[8:11], a[200:203]\n"
    "ds_read_b32 v13, v12\n"
    "v_mfma_f32_16x16x32_bf16 a[200:203], a[80:83], a[4:7], a[200:203]\n"
    "v_mfma_f32_16x16x32_bf16 a[204:207], a[104:107], a[4:7], a[204:207]\n"
    "v_mfma_f32_16x16x32_bf16 a[204:207], a[108:111], a[8:11], a[204:207]\n"
    "v_mfma_f32_16x16x32_bf16 a[204:207], a[112:115], a[12:15], a[204:207]\n"
    "v_mfma_f32_16x16x32_bf16 a[204:207], a[116:119], a[36:39], a[204:207]\n"
    "v_mfma_f32_16x16x32_bf16 a[204:207], a[120:123], a[40:43], a[204:207]\n"
    "s_waitcnt lgkmcnt(0)\n"
    "v_cmp_gt_u32 vcc, s63, v13\n"
    "v_mfma_f32_16x16x32_bf16 a[204:207], a[124:127], a[44:47], a[204:207]\n"
    "s_cbranch_vccnz Ls5%=\n"
    "Lb5%=:\n"
    "v_mfma_f32_16x16x32_bf16 a[208:211], a[100:103], a[48:51], a[208:211]\n"
    "ds_read_b128 a[0:3], %[rd0] offset:32768\n"
    "v_mfma_f32_16x16x32_bf16 a[208:211], a[96:99], a[44:47], a[208:211]\n"
    "ds_read_b128 a[32:35], %[rd1] offset:32768\n"
    "v_mfma_f32_16x16x32_bf16 a[208:211], a[92:95], a[40:43], a[208:211]\n"
    "ds_read_b128 a[4:7], %[rd0] offset:34816\n"
    "v_mfma_f32_16x16x32_bf16 a[208:211], a[88:91], a[16:19], a[208:211]\n"
    "ds_read_b128 a[36:39], %[rd1] offset:34816\n"
    "v_mfma_f32_16x16x32_bf16 a[208:211], a[84:87], a[12:15], a[208:211]\n"
    "s_add_u32 s64, s56, 2048\n"
    "buffer_load_dwordx4 v[36:39], v76, %[srdX], s64 offen\n"
    "v_mfma_f32_16x16x32_bf16 a[208:211], a[80:83], a[8:11], a[208:211]\n"
    "v_mfma_f32_16x16x32_bf16 a[212:215], a[104:107], a[8:11], a[212:215]\n"
    "ds_read_b128 a[8:11], %[rd0] offset:36864\n"
    "v_mfma_f32_16x16x32_bf16 a[212:215], a[108:111], a[12:15], a[212:215]\n"
    "s_add_u32 m0, s69, 8192\n"
    "s_add_u32 s57, s55, 8192\n"
    "v_mfma_f32_16x16x32_bf16 a[212:215], a[112:115], a[16:19], a[212:215]\n"
    "buffer_load_dwordx4 %[od1], %[srdX], s57 offen  lds\n"
    "v_mfma_f32_16x16x32_bf16 a[212:215], a[116:119], a[40:43], a[212:215]\n"
    "ds_read_b128 a[40:43], %[rd1] offset:36864\n"
    "v_mfma_f32_16x16x32_bf16 a[212:215], a[120:123], a[44:47], a[212:215]\n"
    "v_mfma_f32_16x16x32_bf16 a[212:215], a[124:127], a[48:51], a[212:215]\n"
    "v_mfma_f32_16x16x32_bf16 a[216:219], a[100:103], a[52:55], a[216:219]\n"
    "v_mfma_f32_16x16x32_bf16 a[216:219], a[96:99], a[48:51], a[216:219]\n"
    "v_mfma_f32_16x16x32_bf16 a[216:219], a[92:95], a[44:47], a[216:219]\n"
    "s_add_u32 m0, s69, 12288\n"
    "s_add_u32 s57, s55, 16384\n"
    "v_mfma_f32_16x16x32_bf16 a[216:219], a[88:91], a[20:23], a[216:219]\n"
    "buffer_load_dwordx4 %[od1], %[srdX], s57 offen  lds\n"
    "v_mfma_f32_16x16x32_bf16 a[216:219], a[84:87], a[16:19], a[216:219]\n"
    "v_mfma_f32_16x16x32_bf16 a[216:219], a[80:83], a[12:15], a[216:219]\n"
    "v_mfma_f32_16x16x32_bf16 a[220:223], a[104:107], a[12:15], a[220:223]\n"
    "ds_read_b128 a[12:15], %[rd0] offset:38912\n"
    "v_mfma_f32_16x16x32_bf16 a[220:223], a[108:111], a[16:19], a[220:223]\n"
    "v_mfma_f32_16x16x32_bf16 a[220:223], a[112:115], a[20:23], a[220:223]\n"
    "v_mfma_f32_16x16x32_bf16 a[220:223], a[116:119], a[44:47], a[220:223]\n"
    "ds_read_b128 a[44:47], %[rd1] offset:38912\n"
    "v_mfma_f32_16x16x32_bf16 a[220:223], a[120:123], a[48:51], a[220:223]\n"
    "v_mfma_f32_16x16x32_bf16 a[220:223], a[124:127], a[52:55], a[220:223]\n"
    "v_mfma_f32_16x16x32_bf16 a[224:227], a[100:103], a[56:59], a[224:227]\n"
    "v_mfma_f32_16x16x32_bf16 a[224:227], a[96:99], a[52:55], a[224:227]\n"
    "v_mfma_f32_16x16x32_bf16 a[224:227], a[92:95], a[48:51], a[224:227]\n"
    "s_add_u32 m0, s69, 16384\n"
    "s_add_u32 s57, s55, 24576\n"
    "v_mfma_f32_16x16x32_bf16 a[224:227], a[88:91], a[24:27], a[224:227]\n"
    "buffer_load_dwordx4 %[od1], %[srdX], s57 offen  lds\n"
    "v_mfma_f32_16x16x32_bf16 a[224:227], a[84:87], a[20:23], a[224:227]\n"
    "v_mfma_f32_16x16x32_bf16 a[224:227], a[80:83], a[16:19], a[224:227]\n"
    "v_mfma_f32_16x16x32_bf16 a[228:231], a[104:107], a[16:19], a[228:231]\n"
    "ds_read_b128 a[16:19], %[rd0] offset:40960\n"
    "v_mfma_f32_16x16x32_bf16 a[228:231], a[108:111], a[20:23], a[228:231]\n"
    "v_mfma_f32_16x16x32_bf16 a[228:231], a[112:115], a[24:27], a[228:231]\n"
    "v_mfma_f32_16x16x32_bf16 a[228:231], a[116:119], a[48:51], a[228:231]\n"
    "ds_read_b128 a[48:51], %[rd1] offset:40960\n"
    "v_mfma_f32_16x16x32_bf16 a[228:231], a[120:123], a[52:55], a[228:231]\n"
    "s_add_u32 s64, s56, 4096\n"
    "buffer_load_dwordx4 v[40:43], v76, %[srdX], s64 offen\n"
    "v_mfma_f32_16x16x32_bf16 a[228:231], a[124:127], a[56:59], a[228:231]\n"
    "v_mfma_f32_16x16x32_bf16 a[232:235], a[100:103], a[60:63], a[232:235]\n"
    "v_mfma_f32_16x16x32_bf16 a[232:235], a[96:99], a[56:59], a[232:235]\n"
    "v_mfma_f32_16x16x32_bf16 a[232:235], a[92:95], a[52:55], a[232:235]\n"
    "s_add_u32 m0, s69, 20480\n"
    "s_add_u32 s57, s55, 32768\n"
    "v_mfma_f32_16x16x32_bf16 a[232:235], a[88:91], a[28:31], a[232:235]\n"
    "buffer_load_dwordx4 %[od1], %[srdX], s57 offen  lds\n"
    "v_mfma_f32_16x16x32_bf16 a[232:235], a[84:87], a[24:27], a[232:235]\n"
    "v_mfma_f32_16x16x32_bf16 a[232:235], a[80:83], a[20:23], a[232:235]\n"
    "v_mfma_f32_16x16x32_bf16 a[236:239], a[104:107], a[20:23], a[236:239]\n"
    "ds_read_b128 a[20:23], %[rd0] offset:43008\n"
    "v_mfma_f32_16x16x32_bf16 a[236:239], a[108:111], a[24:27], a[236:239]\n"
    "v_mfma_f32_16x16x32_bf16 a[236:239], a[112:115], a[28:31], a[236:239]\n"
    "v_mfma_f32_16x16x32_bf16 a[236:239], a[116:119], a[52:55], a[236:239]\n"
    "ds_read_b128 a[52:55], %[rd1] offset:43008\n"
    "v_mfma_f32_16x16x32_bf16 a[236:239], a[120:123], a[56:59], a[236:239]\n"
    "v_mfma_f32_16x16x32_bf16 a[236:239], a[124:127], a[60:63], a[236:239]\n"
    "v_mfma_f32_16x16x32_bf16 a[240:243], a[100:103], a[72:75], a[240:243]\n"
    "v_mfma_f32_16x16x32_bf16 a[240:243], a[96:99], a[60:63], a[240:243]\n"
    "v_mfma_f32_16x16x32_bf16 a[240:243], a[92:95], a[56:59], a[240:243]\n"
    "s_add_u32 m0, s69, 24576\n"
    "s_add_u32 s57, s55, 40960\n"
    "v_mfma_f32_16x16x32_bf16 a[240:243], a[88:91], a[64:67], a[240:243]\n"
    "buffer_load_dwordx4 %[od1], %[srdX], s57 offen  lds\n"
    "v_mfma_f32_16x16x32_bf16 a[240:243], a[84:87], a[28:31], a[240:243]\n"
    "v_mfma_f32_16x16x32_bf16 a[240:243], a[80:83], a[24:27], a[240:243]\n"
    "v_mfma_f32_16x16x32_bf16 a[244:247], a[104:107], a[24:27], a[244:247]\n"
    "ds_read_b128 a[24:27], %[rd0] offset:45056\n"
    "v_mfma_f32_16x16x32_bf16 a[244:247], a[108:111], a[28:31], a[244:247]\n"
    "v_mfma_f32_16x16x32_bf16 a[244:247], a[112:115], a[64:67], a[244:247]\n"
    "v_mfma_f32_16x16x32_bf16 a[244:247], a[116:119], a[56:59], a[244:247]\n"
    "ds_read_b128 a[56:59], %[rd1] offset:45056\n"
    "v_mfma_f32_16x16x32_bf16 a[244:247], a[120:123], a[60:63], a[244:247]\n"
    "v_mfma_f32_16x16x32_bf16 a[244:247], a[124:127], a[72:75], a[244:247]\n"
    "v_mfma_f32_16x16x32_bf16 a[248:251], a[100:103], a[76:79], a[248:251]\n"
    "v_mfma_f32_16x16x32_bf16 a[248:251], a[96:99], a[72:75], a[248:251]\n"
    "v_mfma_f32_16x16x32_bf16 a[248:251], a[92:95], a[60:63], a[248:251]\n"
    "s_add_u32 m0, s69, 28672\n"
    "s_add_u32 s57, s55, 49152\n"
    "v_mfma_f32_16x16x32_bf16 a[248:251], a[88:91], a[68:71], a[248:251]\n"
    "buffer_load_dwordx4 %[od1], %[srdX], s57 offen  lds\n"
    "v_mfma_f32_16x16x32_bf16 a[248:251], a[84:87], a[64:67], a[248:251]\n"
    "s_add_u32 s64, s56, 6144\n"
    "buffer_load_dwordx4 v[44:47], v76, %[srdX], s64 offen\n"
    "v_mfma_f32_16x16x32_bf16 a[248:251], a[80:83], a[28:31], a[248:251]\n"
    "s_add_u32 s69, s69, 0x8000\n"
    "s_and_b32 s69, s69, 0x1ffff\n"
    "v_mfma_f32_16x16x32_bf16 a[252:255], a[104:107], a[28:31], a[252:255]\n"
    "ds_read_b128 a[28:31], %[rd0] offset:47104\n"
    "v_mfma_f32_16x16x32_bf16 a[252:255], a[108:111], a[64:67], a[252:255]\n"
    "s_add_u32 s55, s55, 128\n"
    "v_mfma_f32_16x16x32_bf16 a[252:255], a[112:115], a[68:71], a[252:255]\n"
    "v_mfma_f32_16x16x32_bf16 a[252:255], a[116:119], a[60:63], a[252:255]\n"
    "ds_read_b128 a[60:63], %[rd1] offset:47104\n"
    "v_mfma_f32_16x16x32_bf16 a[252:255], a[120:123], a[72:75], a[252:255]\n"
    "v_mfma_f32_16x16x32_bf16 a[252:255], a[124:127], a[76:79], a[252:255]\n"
    "s_waitcnt vmcnt(12)\n"
    "s_waitcnt lgkmcnt(8)\n"
    "s_cmp_lg_u32 s67, 0\n"
    "s_cbranch_scc1 Lc6%=\n"
    "v_mfma_f32_16x16x32_bf16 v[208:211], a[148:151], a[40:43], v[208:211]\n"
    "v_mfma_f32_16x16x32_bf16 v[208:211], a[144:147], a[36:39], v[208:211]\n"
    "v_mfma_f32_16x16x32_bf16 v[208:211], a[140:143], a[32:35], v[208:211]\n"
    "v_mfma_f32_16x16x32_bf16 v[208:211], a[136:139], a[8:11], v[208:211]\n"
    "v_mfma_f32_16x16x32_bf16 v[208:211], a[132:135], a[4:7], v[208:211]\n"
    "v_mfma_f32_16x16x32_bf16 v[208:211], a[128:131], a[0:3], v[208:211]\n"
    "v_mfma_f32_16x16x32_bf16 v[212:215], a[152:155], a[0:3], v[212:215]\n"
    "v_mfma_f32_16x16x32_bf16 v[212:215], a[156:159], a[4:7], v[212:215]\n"
    "v_mfma_f32_16x16x32_bf16 v[212:215], a[160:163], a[8:11], v[212:215]\n"
    "v_mfma_f32_16x16x32_bf16 v[212:215], a[164:167], a[32:35], v[212:215]\n"
    "v_mfma_f32_16x16x32_bf16 v[212:215], a[168:171], a[36:39], v[212:215]\n"
    "v_mfma_f32_16x16x32_bf16 v[212:215], a[172:175], a[40:43], v[212:215]\n"
    "Lc6%=:\n"
    "v_mfma_f32_16x16x32_bf16 v[216:219], a[148:151], a[44:47], v[216:219]\n"
    "ds_read_b128 a[0:3], %[rd0] offset:49152\n"
    "v_mfma_f32_16x16x32_bf16 v[216:219], a[144:147], a[40:43], v[216:219]\n"
    "v_mfma_f32_16x16x32_bf16 v[216:219], a[140:143], a[36:39], v[216:219]\n"
    "buffer_load_dwordx4 a[80:83], %[l16], %[srdW], s54 offen offset:0\n"
    "v_mfma_f32_16x16x32_bf16 v[216:219], a[136:139], a[12:15], v[216:219]\n"
    "v_mfma_f32_16x16x32_bf16 v[216:219], a[132:135], a[8:11], v[216:219]\n"
    "ds_read_b128 a[32:35], %[rd1] offset:49152\n"
    "v_mfma_f32_16x16x32_bf16 v[216:219], a[128:131], a[4:7], v[216:219]\n"
    "v_mfma_f32_16x16x32_bf16 v[220:223], a[152:155], a[4:7], v[220:223]\n"
    "ds_read_b128 a[4:7], %[rd0] offset:51200\n"
    "v_mfma_f32_16x16x32_bf16 v[220:223], a[156:159], a[8:11], v[220:223]\n"
    "v_mfma_f32_16x16x32_bf16 v[220:223], a[160:163], a[12:15], v[220:223]\n"
    "v_mfma_f32_16x16x32_bf16 v[220:223], a[164:167], a[36:39], v[220:223]\n"
    "ds_read_b128 a[36:39], %[rd1] offset:51200\n"
    "v_mfma_f32_16x16x32_bf16 v[220:223], a[168:171], a[40:43], v[220:223]\n"
    "buffer_load_dwordx4 a[84:87], %[l16], %[srdW], s54 offen offset:1024\n"
    "v_mfma_f32_16x16x32_bf16 v[220:223], a[172:175], a[44:47], v[220:223]\n"
    "s_waitcnt lgkmcnt(8)\n"
    "v_mfma_f32_16x16x32_bf16 v[224:227], a[148:151], a[48:51], v[224:227]\n"
    "v_mfma_f32_16x16x32_bf16 v[224:227], a[144:147], a[44:47], v[224:227]\n"
    "ds_read_b128 a[64:67], %[re0] offset:32768\n"
    "v_mfma_f32_16x16x32_bf16 v[224:227], a[140:143], a[40:43], v[224:227]\n"
    "buffer_load_dwordx4 a[88:91], %[l16], %[srdW], s54 offen offset:2048\n"
    "v_mfma_f32_16x16x32_bf16 v[224:227], a[136:139], a[16:19], v[224:227]\n"
    "v_mfma_f32_16x16x32_bf16 v[224:227], a[132:135], a[12:15], v[224:227]\n"
    "s_lshl_b32 s58, s62, 6\n"
    "v_mfma_f32_16x16x32_bf16 v[224:227], a[128:131], a[8:11], v[224:227]\n"
    "buffer_load_dword v79, v78, %[srdW], s58 offen sc0 sc1\n"
    "v_mfma_f32_16x16x32_bf16 v[228:231], a[152:155], a[8:11], v[228:231]\n"
    "ds_read_b128 a[8:11], %[rd0] offset:53248\n"
    "v_mfma_f32_16x16x32_bf16 v[228:231], a[156:159], a[12:15], v[228:231]\n"
    "v_mfma_f32_16x16x32_bf16 v[228:231], a[160:163], a[16:19], v[228:231]\n"
    "v_mfma_f32_16x16x32_bf16 v[228:231], a[164:167], a[40:43], v[228:231]\n"
    "ds_read_b128 a[40:43], %[rd1] offset:53248\n"
    "v_mfma_f32_16x16x32_bf16 v[228:231], a[168:171], a[44:47], v[228:231]\n"
    "buffer_load_dwordx4 a[92:95], %[l16], %[srdW], s54 offen offset:3072\n"
    "v_mfma_f32_16x16x32_bf16 v[228:231], a[172:175], a[48:51], v[228:231]\n"
    "v_mfma_f32_16x16x32_bf16 v[232:235], a[148:151], a[52:55], v[232:235]\n"
    "s_add_u32 s54, s54, 4096\n"
    "v_mfma_f32_16x16x32_bf16 v[232:235], a[144:147], a[48:51], v[232:235]\n"
    "ds_read_b128 a[72:75], %[re1] offset:32768\n"
    "v_mfma_f32_16x16x32_bf16 v[232:235], a[140:143], a[44:47], v[232:235]\n"
    "buffer_load_dwordx4 a[96:99], %[l16], %[srdW], s54 offen offset:0\n"
    "v_mfma_f32_16x16x32_bf16 v[232:235], a[136:139], a[20:23], v[232:235]\n"
    "v_mfma_f32_16x16x32_bf16 v[232:235], a[132:135], a[16:19], v[232:235]\n"
    "v_mfma_f32_16x16x32_bf16 v[232:235], a[128:131], a[12:15], v[232:235]\n"
    "v_mfma_f32_16x16x32_bf16 v[236:239], a[152:155], a[12:15], v[236:239]\n"
    "ds_read_b128 a[12:15], %[rd0] offset:55296\n"
    "v_mfma_f32_16x16x32_bf16 v[236:239], a[156:159], a[16:19], v[236:239]\n"
    "v_mfma_f32_16x16x32_bf16 v[236:239], a[160:163], a[20:23], v[236:239]\n"
    "v_mfma_f32_16x16x32_bf16 v[236:239], a[164:167], a[44:47], v[236:239]\n"
    "ds_read_b128 a[44:47], %[rd1] offset:55296\n"
    "v_mfma_f32_16x16x32_bf16 v[236:239], a[168:171], a[48:51], v[236:239]\n"
    "buffer_load_dwordx4 a[100:103], %[l16], %[srdW], s54 offen offset:1024\n"
    "v_mfma_f32_16x16x32_bf16 v[236:239], a[172:175], a[52:55], v[236:239]\n"
    "s_waitcnt lgkmcnt(10)\n"
    "v_mfma_f32_16x16x32_bf16 v[240:243], a[148:151], a[56:59], v[240:243]\n"
    "v_mfma_f32_16x16x32_bf16 v[240:243], a[144:147], a[52:55], v[240:243]\n"
    "ds_read_b128 a[68:71], %[re0] offset:34816\n"
    "v_mfma_f32_16x16x32_bf16 v[240:243], a[140:143], a[48:51], v[240:243]\n"
    "buffer_load_dwordx4 a[104:107], %[l16], %[srdW], s54 offen offset:2048\n"
    "v_mfma_f32_16x16x32_bf16 v[240:243], a[136:139], a[24:27], v[240:243]\n"
    "v_mfma_f32_16x16x32_bf16 v[240:243], a[132:135], a[20:23], v[240:243]\n"
    "v_mfma_f32_16x16x32_bf16 v[240:243], a[128:131], a[16:19], v[240:243]\n"
    "v_mfma_f32_16x16x32_bf16 v[244:247], a[152:155], a[16:19], v[244:247]\n"
    "ds_read_b128 a[16:19], %[rd0] offset:57344\n"
    "v_mfma_f32_16x16x32_bf16 v[244:247], a[156:159], a[20:23], v[244:247]\n"
    "v_mfma_f32_16x16x32_bf16 v[244:247], a[160:163], a[24:27], v[244:247]\n"
    "v_mfma_f32_16x16x32_bf16 v[244:247], a[164:167], a[48:51], v[244:247]\n"
    "ds_read_b128 a[48:51], %[rd1] offset:57344\n"
    "v_mfma_f32_16x16x32_bf16 v[244:247], a[168:171], a[52:55], v[244:247]\n"
    "buffer_load_dwordx4 a[108:111], %[l16], %[srdW], s54 offen offset:3072\n"
    "v_mfma_f32_16x16x32_bf16 v[244:247], a[172:175], a[56:59], v[244:247]\n"
    "v_mfma_f32_16x16x32_bf16 v[248:251], a[148:151], a[60:63], v[248:251]\n"
    "s_add_u32 s54, s54, 4096\n"
    "v_mfma_f32_16x16x32_bf16 v[248:251], a[144:147], a[56:59], v[248:251]\n"
    "ds_read_b128 a[76:79], %[re1] offset:34816\n"
    "v_mfma_f32_16x16x32_bf16 v[248:251], a[140:143], a[52:55], v[248:251]\n"
    "buffer_load_dwordx4 a[112:115], %[l16], %[srdW], s54 offen offset:0\n"
    "v_mfma_f32_16x16x32_bf16 v[248:251], a[136:139], a[28:31], v[248:251]\n"
    "v_mfma_f32_16x16x32_bf16 v[248:251], a[132:135], a[24:27], v[248:251]\n"
    "v_mfma_f32_16x16x32_bf16 v[248:251], a[128:131], a[20:23], v[248:251]\n"
    "v_mfma_f32_16x16x32_bf16 v[252:255], a[152:155], a[20:23], v[252:255]\n"
    "ds_read_b128 a[20:23], %[rd0] offset:59392\n"
    "v_mfma_f32_16x16x32_bf16 v[252:255], a[156:159], a[24:27], v[252:255]\n"
    "v_mfma_f32_16x16x32_bf16 v[252:255], a[160:163], a[28:31], v[252:255]\n"
    "v_mfma_f32_16x16x32_bf16 v[252:255], a[164:167], a[52:55], v[252:255]\n"
    "ds_read_b128 a[52:55], %[rd1] offset:59392\n"
    "v_mfma_f32_16x16x32_bf16 v[252:255], a[168:171], a[56:59], v[252:255]\n"
    "buffer_load_dwordx4 a[116:119], %[l16], %[srdW], s54 offen offset:1024\n"
    "v_mfma_f32_16x16x32_bf16 v[252:255], a[172:175], a[60:63], v[252:255]\n"
    "s_waitcnt lgkmcnt(12)\n"
    "v_mfma_f32_16x16x32_bf16 a[176:179], a[148:151], a[32:35], a[176:179]\n"
    "v_mfma_f32_16x16x32_bf16 a[176:179], a[144:147], a[60:63], a[176:179]\n"
    "v_mfma_f32_16x16x32_bf16 a[176:179], a[140:143], a[56:59], a[176:179]\n"
    "buffer_load_dwordx4 a[120:123], %[l16], %[srdW], s54 offen offset:2048\n"
    "v_mfma_f32_16x16x32_bf16 a[176:179], a[136:139], a[0:3], a[176:179]\n"
    "v_mfma_f32_16x16x32_bf16 a[176:179], a[132:135], a[28:31], a[176:179]\n"
    "v_mfma_f32_16x16x32_bf16 a[176:179], a[128:131], a[24:27], a[176:179]\n"
    "v_mfma_f32_16x16x32_bf16 a[180:183], a[152:155], a[24:27], a[180:183]\n"
    "ds_read_b128 a[24:27], %[rd0] offset:61440\n"
    "v_mfma_f32_16x16x32_bf16 a[180:183], a[156:159], a[28:31], a[180:183]\n"
    "v_mfma_f32_16x16x32_bf16 a[180:183], a[160:163], a[0:3], a[180:183]\n"
    "v_mfma_f32_16x16x32_bf16 a[180:183], a[164:167], a[56:59], a[180:183]\n"
    "ds_read_b128 a[56:59], %[rd1] offset:61440\n"
    "v_mfma_f32_16x16x32_bf16 a[180:183], a[168:171], a[60:63], a[180:183]\n"
    "buffer_load_dwordx4 a[124:127], %[l16], %[srdW], s54 offen offset:3072\n"
    "v_mfma_f32_16x16x32_bf16 a[180:183], a[172:175], a[32:35], a[180:183]\n"
    "v_mfma_f32_16x16x32_bf16 a[184:187], a[148:151], a[36:39], a[184:187]\n"
    "s_add_u32 s54, s54, 4096\n"
    "v_mfma_f32_16x16x32_bf16 a[184:187], a[144:147], a[32:35], a[184:187]\n"
    "s_add_u32 s64, s56, 0\n"
    "buffer_load_dwordx4 v[48:51], %[xr1], %[srdX], s64 offen\n"
    "v_mfma_f32_16x16x32_bf16 a[184:187], a[140:143], a[60:63], a[184:187]\n"
    "v_mfma_f32_16x16x32_bf16 a[184:187], a[136:139], a[4:7], a[184:187]\n"
    "s_waitcnt vmcnt(23)\n"
    "v_lshlrev_b32 v60, 16, v32\n"
    "v_and_b32 v61, s65, v32\n"
    "v_mfma_f32_16x16x32_bf16 a[184:187], a[132:135], a[0:3], a[184:187]\n"
    "v_lshlrev_b32 v62, 16, v33\n"
    "v_and_b32 v63, s65, v33\n"
    "v_mfma_f32_16x16x32_bf16 a[184:187], a[128:131], a[28:31], a[184:187]\n"
    "v_lshlrev_b32 v64, 16, v34\n"
    "v_and_b32 v65, s65, v34\n"
    "v_mfma_f32_16x16x32_bf16 a[188:191], a[152:155], a[28:31], a[188:191]\n"
    "ds_read_b128 a[28:31], %[rd0] offset:63488\n"
    "v_lshlrev_b32 v66, 16, v35\n"
    "v_mfma_f32_16x16x32_bf16 a[188:191], a[156:159], a[0:3], a[188:191]\n"
    "v_and_b32 v67, s65, v35\n"
    "v_mfma_f32_16x16x32_bf16 a[188:191], a[160:163], a[4:7], a[188:191]\n"
    "v_mfma_f32_16x16x32_bf16 a[188:191], a[164:167], a[60:63], a[188:191]\n"
    "ds_read_b128 a[60:63], %[rd1] offset:63488\n"
    "v_mfma_f32_16x16x32_bf16 a[188:191], a[168:171], a[32:35], a[188:191]\n"
    "v_mfma_f32_16x16x32_bf16 a[188:191], a[172:175], a[36:39], a[188:191]\n"
    "s_waitcnt lgkmcnt(10)\n"
    "v_mfma_f32_16x16x32_bf16 a[192:195], a[148:151], a[40:43], a[192:195]\n"
    "v_xor_b32 %[rd0], 0x10000, %[rd0]\n"
    "v_mfma_f32_16x16x32_bf16 a[192:195], a[144:147], a[36:39], a[192:195]\n"
    "v_xor_b32 %[rd1], 0x10000, %[rd1]\n"
    "v_mfma_f32_16x16x32_bf16 a[192:195], a[140:143], a[32:35], a[192:195]\n"
    "s_add_u32 s63, s63, 1\n"
    "v_mfma_f32_16x16x32_bf16 a[192:195], a[136:139], a[8:11], a[192:195]\n"
    "v_mfma_f32_16x16x32_bf16 a[192:195], a[132:135], a[4:7], a[192:195]\n"
    "v_mfma_f32_16x16x32_bf16 a[192:195], a[128:131], a[0:3], a[192:195]\n"
    "v_mfma_f32_16x16x32_bf16 a[196:199], a[152:155], a[0:3], a[196:199]\n"
    "s_waitcnt lgkmcnt(0)\n"
    "v_mov_b32 v13, s63\n"
    "v_mfma_f32_16x16x32_bf16 a[196:199], a[156:159], a[4:7], a[196:199]\n"
    "ds_write_b32 v14, v13\n"
    "v_mfma_f32_16x16x32_bf16 a[196:199], a[160:163], a[8:11], a[196:199]\n"
    "v_mfma_f32_16x16x32_bf16 a[196:199], a[164:167], a[32:35], a[196:199]\n"
    "v_mfma_f32_16x16x32_bf16 a[196:199], a[168:171], a[36:39], a[196:199]\n"
    "v_mfma_f32_16x16x32_bf16 a[196:199], a[172:175], a[40:43], a[196:199]\n"
    "v_mfma_f32_16x16x32_bf16 a[200:203], a[148:151], a[44:47], a[200:203]\n"
    "v_xor_b32 %[re0], 0x10000, %[re0]\n"
    "v_mfma_f32_16x16x32_bf16 a[200:203], a[144:147], a[40:43], a[200:203]\n"
    "v_xor_b32 %[re1], 0x10000, %[re1]\n"
    "v_mfma_f32_16x16x32_bf16 a[200:203], a[140:143], a[36:39], a[200:203]\n"
    "v_mfma_f32_16x16x32_bf16 a[200:203], a[136:139], a[12:15], a[200:203]\n"
    "v_mfma_f32_16x16x32_bf16 a[200:203], a[132:135], a[8:11], a[200:203]\n"
    "ds_read_b32 v13, v12\n"
    "v_mfma_f32_16x16x32_bf16 a[200:203], a[128:131], a[4:7], a[200:203]\n"
    "v_mfma_f32_16x16x32_bf16 a[204:207], a[152:155], a[4:7], a[204:207]\n"
    "v_mfma_f32_16x16x32_bf16 a[204:207], a[156:159], a[8:11], a[204:207]\n"
    "s_add_u32 s64, s56, 2048\n"
    "buffer_load_dwordx4 v[52:55], %[xr1], %[srdX], s64 offen\n"
    "v_mfma_f32_16x16x32_bf16 a[204:207], a[160:163], a[12:15], a[204:207]\n"
    "v_mfma_f32_16x16x32_bf16 a[204:207], a[164:167], a[36:39], a[204:207]\n"
    "v_mfma_f32_16x16x32_bf16 a[204:207], a[168:171], a[40:43], a[204:207]\n"
    "s_waitcnt lgkmcnt(0)\n"
    "v_cmp_gt_u32 vcc, s63, v13\n"
    "v_mfma_f32_16x16x32_bf16 a[204:207], a[172:175], a[44:47], a[204:207]\n"
    "s_cbranch_vccnz Ls6%=\n"
    "Lb6%=:\n"
    "v_mfma_f32_16x16x32_bf16 a[208:211], a[148:151], a[48:51], a[208:211]\n"
    "ds_read_b128 a[0:3], %[rd0] offset:0\n"
    "v_mfma_f32_16x16x32_bf16 a[208:211], a[144:147], a[44:47], a[208:211]\n"
    "ds_read_b128 a[32:35], %[rd1] offset:0\n"
    "v_mfma_f32_16x16x32_bf16 a[208:211], a[140:143], a[40:43], a[208:211]\n"
    "ds_read_b128 a[4:7], %[rd0] offset:2048\n"
    "s_waitcnt vmcnt(23)\n"
    "v_mfma_f32_16x16x32_bf16 a[208:211], a[136:139], a[16:19], a[208:211]\n"
    "ds_read_b128 a[36:39], %[rd1] offset:2048\n"
    "v_lshlrev_b32 v68, 16, v36\n"
    "v_mfma_f32_16x16x32_bf16 a[208:211], a[132:135], a[12:15], a[208:211]\n"
    "s_waitcnt vmcnt(11)\n"
    "v_cmp_gt_u32 vcc, 32, v79\n"
    "v_and_b32 v69, s65, v36\n"
    "v_mfma_f32_16x16x32_bf16 a[208:211], a[128:131], a[8:11], a[208:211]\n"
    "s_cbranch_vccnz Lps%=\n"
    "Lpb%=:\n"
    "v_lshlrev_b32 v70, 16, v37\n"
    "v_and_b32 v71, s65, v37\n"
    "v_mfma_f32_16x16x32_bf16 a[212:215], a[152:155], a[8:11], a[212:215]\n"
    "ds_read_b128 a[8:11], %[rd0] offset:4096\n"
    "v_lshlrev_b32 v72, 16, v38\n"
    "v_mfma_f32_16x16x32_bf16 a[212:215], a[156:159], a[12:15], a[212:215]\n"
    "s_add_u32 m0, s69, 0\n"
    "s_add_u32 s57, s70, 0\n"
    "v_and_b32 v73, s65, v38\n"
    "v_mfma_f32_16x16x32_bf16 a[212:215], a[160:163], a[16:19], a[212:215]\n"
    "buffer_load_dwordx4 %[l16], %[srdD], s57 offen sc1 lds\n"
    "v_lshlrev_b32 v74, 16, v39\n"
    "v_mfma_f32_16x16x32_bf16 a[212:215], a[164:167], a[40:43], a[212:215]\n"
    "ds_read_b128 a[40:43], %[rd1] offset:4096\n"
    "v_and_b32 v75, s65, v39\n"
    "v_mfma_f32_16x16x32_bf16 a[212:215], a[168:171], a[44:47], a[212:215]\n"
    "v_sub_f32 v60, v60, v68\n"
    "v_sub_f32 v61, v61, v69\n"
    "v_mfma_f32_16x16x32_bf16 a[212:215], a[172:175], a[48:51], a[212:215]\n"
    "v_sub_f32 v62, v62, v70\n"
    "v_sub_f32 v63, v63, v71\n"
    "v_mfma_f32_16x16x32_bf16 a[216:219], a[148:151], a[52:55], a[216:219]\n"
    "v_sub_f32 v64, v64, v72\n"
    "v_sub_f32 v65, v65, v73\n"
    "v_mfma_f32_16x16x32_bf16 a[216:219], a[144:147], a[48:51], a[216:219]\n"
    "v_sub_f32 v66, v66, v74\n"
    "v_sub_f32 v67, v67, v75\n"
    "v_mfma_f32_16x16x32_bf16 a[216:219], a[140:143], a[44:47], a[216:219]\n"
    "s_add_u32 m0, s69, 4096\n"
    "s_add_u32 s57, s70, 4096\n"
    "v_cvt_pk_bf16_f32 v36, v60, v61\n"
    "v_mfma_f32_16x16x32_bf16 a[216:219], a[136:139], a[20:23], a[216:219]\n"
    "buffer_load_dwordx4 %[l16], %[srdD], s57 offen sc1 lds\n"
    "v_cvt_pk_bf16_f32 v37, v62, v63\n"
    "v_mfma_f32_16x16x32_bf16 a[216:219], a[132:135], a[16:19], a[216:219]\n"
    "s_add_u32 m0, s69, 8192\n"
    "s_add_u32 s57, s70, 8192\n"
    "v_cvt_pk_bf16_f32 v38, v64, v65\n"
    "v_mfma_f32_16x16x32_bf16 a[216:219], a[128:131], a[12:15], a[216:219]\n"
    "buffer_load_dwordx4 %[l16], %[srdD], s57 offen sc1 lds\n"
    "v_cvt_pk_bf16_f32 v39, v66, v67\n"
    "v_mfma_f32_16x16x32_bf16 a[220:223], a[152:155], a[12:15], a[220:223]\n"
    "ds_read_b128 a[12:15], %[rd0] offset:6144\n"
    "v_mfma_f32_16x16x32_bf16 a[220:223], a[156:159], a[16:19], a[220:223]\n"
    "s_add_u32 s64, s71, 262144\n"
    "buffer_store_dwordx4 v[36:39], %[vst], %[srdD], s64 offen sc0 sc1\n"
    "v_mfma_f32_16x16x32_bf16 a[220:223], a[160:163], a[20:23], a[220:223]\n"
    "v_mfma_f32_16x16x32_bf16 a[220:223], a[164:167], a[44:47], a[220:223]\n"
    "ds_read_b128 a[44:47], %[rd1] offset:6144\n"
    "v_mfma_f32_16x16x32_bf16 a[220:223], a[168:171], a[48:51], a[220:223]\n"
    "v_mfma_f32_16x16x32_bf16 a[220:223], a[172:175], a[52:55], a[220:223]\n"
    "v_mfma_f32_16x16x32_bf16 a[224:227], a[148:151], a[56:59], a[224:227]\n"
    "v_mfma_f32_16x16x32_bf16 a[224:227], a[144:147], a[52:55], a[224:227]\n"
    "s_add_u32 s64, s56, 4096\n"
    "buffer_load_dwordx4 v[56:59], %[xr1], %[srdX], s64 offen\n"
    "v_mfma_f32_16x16x32_bf16 a[224:227], a[140:143], a[48:51], a[224:227]\n"
    "s_add_u32 m0, s69, 12288\n"
    "s_add_u32 s57, s70, 12288\n"
    "v_mfma_f32_16x16x32_bf16 a[224:227], a[136:139], a[24:27], a[224:227]\n"
    "buffer_load_dwordx4 %[l16], %[srdD], s57 offen sc1 lds\n"
    "v_mfma_f32_16x16x32_bf16 a[224:227], a[132:135], a[20:23], a[224:227]\n"
    "v_mfma_f32_16x16x32_bf16 a[224:227], a[128:131], a[16:19], a[224:227]\n"
    "v_mfma_f32_16x16x32_bf16 a[228:231], a[152:155], a[16:19], a[228:231]\n"
    "ds_read_b128 a[16:19], %[rd0] offset:8192\n"
    "v_mfma_f32_16x16x32_bf16 a[228:231], a[156:159], a[20:23], a[228:231]\n"
    "v_mfma_f32_16x16x32_bf16 a[228:231], a[160:163], a[24:27], a[228:231]\n"
    "v_lshlrev_b32 v60, 16, v40\n"
    "v_and_b32 v61, s65, v40\n"
    "v_mfma_f32_16x16x32_bf16 a[228:231], a[164:167], a[48:51], a[228:231]\n"
    "ds_read_b128 a[48:51], %[rd1] offset:8192\n"
    "v_lshlrev_b32 v62, 16, v41\n"
    "v_mfma_f32_16x16x32_bf16 a[228:231], a[168:171], a[52:55], a[228:231]\n"
    "v_and_b32 v63, s65, v41\n"
    "v_lshlrev_b32 v64, 16, v42\n"
    "v_mfma_f32_16x16x32_bf16 a[228:231], a[172:175], a[56:59], a[228:231]\n"
    "v_and_b32 v65, s65, v42\n"
    "v_lshlrev_b32 v66, 16, v43\n"
    "v_mfma_f32_16x16x32_bf16 a[232:235], a[148:151], a[60:63], a[232:235]\n"
    "v_and_b32 v67, s65, v43\n"
    "v_sub_f32 v68, v60, v68\n"
    "v_mfma_f32_16x16x32_bf16 a[232:235], a[144:147], a[56:59], a[232:235]\n"
    "v_sub_f32 v69, v61, v69\n"
    "v_sub_f32 v70, v62, v70\n"
    "v_mfma_f32_16x16x32_bf16 a[232:235], a[140:143], a[52:55], a[232:235]\n"
    "s_add_u32 m0, s69, 16384\n"
    "s_add_u32 s57, s70, 16384\n"
    "v_sub_f32 v71, v63, v71\n"
    "v_mfma_f32_16x16x32_bf16 a[232:235], a[136:139], a[28:31], a[232:235]\n"
    "buffer_load_dwordx4 %[l16], %[srdD], s57 offen sc1 lds\n"
    "v_sub_f32 v72, v64, v72\n"
    "v_mfma_f32_16x16x32_bf16 a[232:235], a[132:135], a[24:27], a[232:235]\n"
    "s_add_u32 m0, s69, 20480\n"
    "s_add_u32 s57, s70, 20480\n"
    "v_sub_f32 v73, v65, v73\n"
    "v_mfma_f32_16x16x32_bf16 a[232:235], a[128:131], a[20:23], a[232:235]\n"
    "buffer_load_dwordx4 %[l16], %[srdD], s57 offen sc1 lds\n"
    "v_sub_f32 v74, v66, v74\n"
    "v_mfma_f32_16x16x32_bf16 a[236:239], a[152:155], a[20:23], a[236:239]\n"
    "ds_read_b128 a[20:23], %[rd0] offset:10240\n"
    "v_sub_f32 v75, v67, v75\n"
    "v_mfma_f32_16x16x32_bf16 a[236:239], a[156:159], a[24:27], a[236:239]\n"
    "v_cvt_pk_bf16_f32 v40, v68, v69\n"
    "v_cvt_pk_bf16_f32 v41, v70, v71\n"
    "v_mfma_f32_16x16x32_bf16 a[236:239], a[160:163], a[28:31], a[236:239]\n"
    "v_cvt_pk_bf16_f32 v42, v72, v73\n"
    "v_cvt_pk_bf16_f32 v43, v74, v75\n"
    "v_mfma_f32_16x16x32_bf16 a[236:239], a[164:167], a[52:55], a[236:239]\n"
    "ds_read_b128 a[52:55], %[rd1] offset:10240\n"
    "v_mfma_f32_16x16x32_bf16 a[236:239], a[168:171], a[56:59], a[236:239]\n"
    "s_add_u32 s64, s71, 0\n"
    "buffer_store_dwordx4 v[40:43], %[vst], %[srdD], s64 offen sc0 sc1\n"
    "v_mfma_f32_16x16x32_bf16 a[236:239], a[172:175], a[60:63], a[236:239]\n"
    "v_mfma_f32_16x16x32_bf16 a[240:243], a[148:151], a[72:75], a[240:243]\n"
    "v_mfma_f32_16x16x32_bf16 a[240:243], a[144:147], a[60:63], a[240:243]\n"
    "v_mfma_f32_16x16x32_bf16 a[240:243], a[140:143], a[56:59], a[240:243]\n"
    "s_add_u32 m0, s69, 24576\n"
    "s_add_u32 s57, s70, 24576\n"
    "v_mfma_f32_16x16x32_bf16 a[240:243], a[136:139], a[64:67], a[240:243]\n"
    "buffer_load_dwordx4 %[l16], %[srdD], s57 offen sc1 lds\n"
    "v_mfma_f32_16x16x32_bf16 a[240:243], a[132:135], a[28:31], a[240:243]\n"
    "v_mfma_f32_16x16x32_bf16 a[240:243], a[128:131], a[24:27], a[240:243]\n"
    "v_mfma_f32_16x16x32_bf16 a[244:247], a[152:155], a[24:27], a[244:247]\n"
    "ds_read_b128 a[24:27], %[rd0] offset:12288\n"
    "v_mfma_f32_16x16x32_bf16 a[244:247], a[156:159], a[28:31], a[244:247]\n"
    "s_add_u32 s64, s56, 6144\n"
    "buffer_load_dwordx4 v[32:35], %[xr1], %[srdX], s64 offen\n"
    "v_mfma_f32_16x16x32_bf16 a[244:247], a[160:163], a[64:67], a[244:247]\n"
    "v_mfma_f32_16x16x32_bf16 a[244:247], a[164:167], a[56:59], a[244:247]\n"
    "ds_read_b128 a[56:59], %[rd1] offset:12288\n"
    "v_mfma_f32_16x16x32_bf16 a[244:247], a[168:171], a[60:63], a[244:247]\n"
    "v_mfma_f32_16x16x32_bf16 a[244:247], a[172:175], a[72:75], a[244:247]\n"
    "v_mfma_f32_16x16x32_bf16 a[248:251], a[148:151], a[76:79], a[248:251]\n"
    "v_mfma_f32_16x16x32_bf16 a[248:251], a[144:147], a[72:75], a[248:251]\n"
    "v_mfma_f32_16x16x32_bf16 a[248:251], a[140:143], a[60:63], a[248:251]\n"
    "s_add_u32 m0, s69, 28672\n"
    "s_add_u32 s57, s70, 28672\n"
    "v_mfma_f32_16x16x32_bf16 a[248:251], a[136:139], a[68:71], a[248:251]\n"
    "buffer_load_dwordx4 %[l16], %[srdD], s57 offen sc1 lds\n"
    "v_lshlrev_b32 v68, 16, v44\n"
    "v_mfma_f32_16x16x32_bf16 a[248:251], a[132:135], a[64:67], a[248:251]\n"
    "v_and_b32 v69, s65, v44\n"
    "v_lshlrev_b32 v70, 16, v45\n"
    "v_mfma_f32_16x16x32_bf16 a[248:251], a[128:131], a[28:31], a[248:251]\n"
    "s_add_u32 s69, s69, 0x8000\n"
    "s_and_b32 s69, s69, 0x1ffff\n"
    "v_and_b32 v71, s65, v45\n"
    "v_mfma_f32_16x16x32_bf16 a[252:255], a[152:155], a[28:31], a[252:255]\n"
    "ds_read_b128 a[28:31], %[rd0] offset:14336\n"
    "v_lshlrev_b32 v72, 16, v46\n"
    "v_mfma_f32_16x16x32_bf16 a[252:255], a[156:159], a[64:67], a[252:255]\n"
    "s_add_u32 s70, s70, 0x8000\n"
    "v_and_b32 v73, s65, v46\n"
    "v_lshlrev_b32 v74, 16, v47\n"
    "v_mfma_f32_16x16x32_bf16 a[252:255], a[160:163], a[68:71], a[252:255]\n"
    "v_and_b32 v75, s65, v47\n"
    "v_sub_f32 v60, v60, v68\n"
    "v_mfma_f32_16x16x32_bf16 a[252:255], a[164:167], a[60:63], a[252:255]\n"
    "ds_read_b128 a[60:63], %[rd1] offset:14336\n"
    "v_sub_f32 v61, v61, v69\n"
    "v_mfma_f32_16x16x32_bf16 a[252:255], a[168:171], a[72:75], a[252:255]\n"
    "v_sub_f32 v62, v62, v70\n"
    "v_sub_f32 v63, v63, v71\n"
    "v_mfma_f32_16x16x32_bf16 a[252:255], a[172:175], a[76:79], a[252:255]\n"
    "s_waitcnt vmcnt(14)\n"
    "s_waitcnt lgkmcnt(8)\n"
    "s_cmp_lg_u32 s67, 0\n"
    "s_cbranch_scc1 Lc7%=\n"
    "v_mfma_f32_16x16x32_bf16 v[208:211], a[100:103], a[40:43], v[208:211]\n"
    "v_mfma_f32_16x16x32_bf16 v[208:211], a[96:99], a[36:39], v[208:211]\n"
    "v_mfma_f32_16x16x32_bf16 v[208:211], a[92:95], a[32:35], v[208:211]\n"
    "v_mfma_f32_16x16x32_bf16 v[208:211], a[88:91], a[8:11], v[208:211]\n"
    "v_mfma_f32_16x16x32_bf16 v[208:211], a[84:87], a[4:7], v[208:211]\n"
    "v_mfma_f32_16x16x32_bf16 v[208:211], a[80:83], a[0:3], v[208:211]\n"
    "v_mfma_f32_16x16x32_bf16 v[212:215], a[104:107], a[0:3], v[212:215]\n"
    "v_mfma_f32_16x16x32_bf16 v[212:215], a[108:111], a[4:7], v[212:215]\n"
    "v_mfma_f32_16x16x32_bf16 v[212:215], a[112:115], a[8:11], v[212:215]\n"
    "v_mfma_f32_16x16x32_bf16 v[212:215], a[116:119], a[32:35], v[212:215]\n"
    "v_mfma_f32_16x16x32_bf16 v[212:215], a[120:123], a[36:39], v[212:215]\n"
    "v_mfma_f32_16x16x32_bf16 v[212:215], a[124:127], a[40:43], v[212:215]\n"
    "Lc7%=:\n"
    "v_mfma_f32_16x16x32_bf16 v[216:219], a[100:103], a[44:47], v[216:219]\n"
    "ds_read_b128 a[0:3], %[rd0] offset:16384\n"
    "v_sub_f32 v64, v64, v72\n"
    "v_mfma_f32_16x16x32_bf16 v[216:219], a[96:99], a[40:43], v[216:219]\n"
    "v_sub_f32 v65, v65, v73\n"
    "v_sub_f32 v66, v66, v74\n"
    "v_mfma_f32_16x16x32_bf16 v[216:219], a[92:95], a[36:39], v[216:219]\n"
    "buffer_load_dwordx4 a[128:131], %[l16], %[srdW], s54 offen offset:0\n"
    "v_sub_f32 v67, v67, v75\n"
    "v_mfma_f32_16x16x32_bf16 v[216:219], a[88:91], a[12:15], v[216:219]\n"
    "v_cvt_pk_bf16_f32 v44, v60, v61\n"
    "v_cvt_pk_bf16_f32 v45, v62, v63\n"
    "v_mfma_f32_16x16x32_bf16 v[216:219], a[84:87], a[8:11], v[216:219]\n"
    "ds_read_b128 a[32:35], %[rd1] offset:16384\n"
    "v_cvt_pk_bf16_f32 v46, v64, v65\n"
    "v_mfma_f32_16x16x32_bf16 v[216:219], a[80:83], a[4:7], v[216:219]\n"
    "v_cvt_pk_bf16_f32 v47, v66, v67\n"
    "v_mfma_f32_16x16x32_bf16 v[220:223], a[104:107], a[4:7], v[220:223]\n"
    "ds_read_b128 a[4:7], %[rd0] offset:18432\n"
    "v_mfma_f32_16x16x32_bf16 v[220:223], a[108:111], a[8:11], v[220:223]\n"
    "v_mfma_f32_16x16x32_bf16 v[220:223], a[112:115], a[12:15], v[220:223]\n"
    "v_mfma_f32_16x16x32_bf16 v[220:223], a[116:119], a[36:39], v[220:223]\n"
    "ds_read_b128 a[36:39], %[rd1] offset:18432\n"
    "v_mfma_f32_16x16x32_bf16 v[220:223], a[120:123], a[40:43], v[220:223]\n"
    "buffer_load_dwordx4 a[132:135], %[l16], %[srdW], s54 offen offset:1024\n"
    "v_mfma_f32_16x16x32_bf16 v[220:223], a[124:127], a[44:47], v[220:223]\n"
    "s_waitcnt lgkmcnt(8)\n"
    "v_mfma_f32_16x16x32_bf16 v[224:227], a[100:103], a[48:51], v[224:227]\n"
    "v_mfma_f32_16x16x32_bf16 v[224:227], a[96:99], a[44:47], v[224:227]\n"
    "ds_read_b128 a[64:67], %[re0] offset:0\n"
    "v_mfma_f32_16x16x32_bf16 v[224:227], a[92:95], a[40:43], v[224:227]\n"
    "buffer_load_dwordx4 a[136:139], %[l16], %[srdW], s54 offen offset:2048\n"
    "v_mfma_f32_16x16x32_bf16 v[224:227], a[88:91], a[16:19], v[224:227]\n"
    "v_mfma_f32_16x16x32_bf16 v[224:227], a[84:87], a[12:15], v[224:227]\n"
    "s_mul_i32 s58, s62, s68\n"
    "s_lshl_b32 s60, s67, 12\n"
    "v_mfma_f32_16x16x32_bf16 v[224:227], a[80:83], a[8:11], v[224:227]\n"
    "s_sub_u32 s58, s58, s60\n"
    "s_ashr_i32 s60, s58, 31\n"
    "v_mfma_f32_16x16x32_bf16 v[228:231], a[104:107], a[8:11], v[228:231]\n"
    "ds_read_b128 a[8:11], %[rd0] offset:20480\n"
    "v_mfma_f32_16x16x32_bf16 v[228:231], a[108:111], a[12:15], v[228:231]\n"
    "s_add_u32 s76, %[so0], s58\n"
    "s_addc_u32 s77, %[so1], s60\n"
    "v_mfma_f32_16x16x32_bf16 v[228:231], a[112:115], a[16:19], v[228:231]\n"
    "s_mul_i32 s58, s67, 61440\n"
    "s_sub_u32 s78, 1039360, s58\n"
    "v_mfma_f32_16x16x32_bf16 v[228:231], a[116:119], a[40:43], v[228:231]\n"
    "ds_read_b128 a[40:43], %[rd1] offset:20480\n"
    "v_mfma_f32_16x16x32_bf16 v[228:231], a[120:123], a[44:47], v[228:231]\n"
    "buffer_load_dwordx4 a[140:143], %[l16], %[srdW], s54 offen offset:3072\n"
    "v_mfma_f32_16x16x32_bf16 v[228:231], a[124:127], a[48:51], v[228:231]\n"
    "v_mfma_f32_16x16x32_bf16 v[232:235], a[100:103], a[52:55], v[232:235]\n"
    "s_add_u32 s54, s54, 4096\n"
    "v_mfma_f32_16x16x32_bf16 v[232:235], a[96:99], a[48:51], v[232:235]\n"
    "ds_read_b128 a[72:75], %[re1] offset:0\n"
    "v_mfma_f32_16x16x32_bf16 v[232:235], a[92:95], a[44:47], v[232:235]\n"
    "buffer_load_dwordx4 a[144:147], %[l16], %[srdW], s54 offen offset:0\n"
    "v_mfma_f32_16x16x32_bf16 v[232:235], a[88:91], a[20:23], v[232:235]\n"
    "v_mfma_f32_16x16x32_bf16 v[232:235], a[84:87], a[16:19], v[232:235]\n"
    "s_cmp_eq_u32 s62, 0\n"
    "s_cselect_b32 s55, 0, 0x40000000\n"
    "v_mfma_f32_16x16x32_bf16 v[232:235], a[80:83], a[12:15], v[232:235]\n"
    "v_lshrrev_b32 v16, 16, %[oo]\n"
    "v_mul_u32_u24 v16, s67, v16\n"
    "v_mfma_f32_16x16x32_bf16 v[236:239], a[104:107], a[12:15], v[236:239]\n"
    "ds_read_b128 a[12:15], %[rd0] offset:22528\n"
    "v_lshlrev_b32 v16, 12, v16\n"
    "v_mfma_f32_16x16x32_bf16 v[236:239], a[108:111], a[16:19], v[236:239]\n"
    "v_sub_u32 %[oo], %[oo], v16\n"
    "v_bfe_u32 v16, %[l16], 7, 1\n"
    "v_mfma_f32_16x16x32_bf16 v[236:239], a[112:115], a[20:23], v[236:239]\n"
    "v_xor_b32 v16, 1, v16\n"
    "v_mul_u32_u24 v16, s67, v16\n"
    "v_mfma_f32_16x16x32_bf16 v[236:239], a[116:119], a[44:47], v[236:239]\n"
    "ds_read_b128 a[44:47], %[rd1] offset:22528\n"
    "v_lshl_or_b32 v15, v16, 30, %[oo]\n"
    "v_mfma_f32_16x16x32_bf16 v[236:239], a[120:123], a[48:51], v[236:239]\n"
    "buffer_load_dwordx4 a[148:151], %[l16], %[srdW], s54 offen offset:1024\n"
    "v_lshrrev_b32 v16, 7, %[l16]\n"
    "v_mfma_f32_16x16x32_bf16 v[236:239], a[124:127], a[52:55], v[236:239]\n"
    "s_waitcnt lgkmcnt(10)\n"
    "v_add_u32 v16, %[wv8], v16\n"
    "v_and_b32 v16, 15, v16\n"
    "v_mfma_f32_16x16x32_bf16 v[240:243], a[100:103], a[56:59], v[240:243]\n"
    "v_add_u32 v16, 1, v16\n"
    "v_mul_u32_u24 v16, s66, v16\n"
    "v_mfma_f32_16x16x32_bf16 v[240:243], a[96:99], a[52:55], v[240:243]\n"
    "ds_read_b128 a[68:71], %[re0] offset:2048\n"
    "v_lshlrev_b32 v16, 12, v16\n"
    "v_mfma_f32_16x16x32_bf16 v[240:243], a[92:95], a[48:51], v[240:243]\n"
    "buffer_load_dwordx4 a[152:155], %[l16], %[srdW], s54 offen offset:2048\n"
    "v_sub_u32 v16, s68, v16\n"
    "v_mfma_f32_16x16x32_bf16 v[240:243], a[88:91], a[24:27], v[240:243]\n"
    "v_add_u32 %[od1], v16, %[od1]\n"
    "v_mfma_f32_16x16x32_bf16 v[240:243], a[84:87], a[20:23], v[240:243]\n"
    "v_mfma_f32_16x16x32_bf16 v[240:243], a[80:83], a[16:19], v[240:243]\n"
    "v_mfma_f32_16x16x32_bf16 v[244:247], a[104:107], a[16:19], v[244:247]\n"
    "ds_read_b128 a[16:19], %[rd0] offset:24576\n"
    "v_mfma_f32_16x16x32_bf16 v[244:247], a[108:111], a[20:23], v[244:247]\n"
    "v_mfma_f32_16x16x32_bf16 v[244:247], a[112:115], a[24:27], v[244:247]\n"
    "v_mfma_f32_16x16x32_bf16 v[244:247], a[116:119], a[48:51], v[244:247]\n"
    "ds_read_b128 a[48:51], %[rd1] offset:24576\n"
    "v_mfma_f32_16x16x32_bf16 v[244:247], a[120:123], a[52:55], v[244:247]\n"
    "buffer_load_dwordx4 a[156:159], %[l16], %[srdW], s54 offen offset:3072\n"
    "v_mfma_f32_16x16x32_bf16 v[244:247], a[124:127], a[56:59], v[244:247]\n"
    "v_mfma_f32_16x16x32_bf16 v[248:251], a[100:103], a[60:63], v[248:251]\n"
    "s_add_u32 s54, s54, 4096\n"
    "v_mfma_f32_16x16x32_bf16 v[248:251], a[96:99], a[56:59], v[248:251]\n"
    "ds_read_b128 a[76:79], %[re1] offset:2048\n"
    "v_mfma_f32_16x16x32_bf16 v[248:251], a[92:95], a[52:55], v[248:251]\n"
    "buffer_load_dwordx4 a[160:163], %[l16], %[srdW], s54 offen offset:0\n"
    "v_mfma_f32_16x16x32_bf16 v[248:251], a[88:91], a[28:31], v[248:251]\n"
    "v_mfma_f32_16x16x32_bf16 v[248:251], a[84:87], a[24:27], v[248:251]\n"
    "v_mfma_f32_16x16x32_bf16 v[248:251], a[80:83], a[20:23], v[248:251]\n"
    "v_mfma_f32_16x16x32_bf16 v[252:255], a[104:107], a[20:23], v[252:255]\n"
    "ds_read_b128 a[20:23], %[rd0] offset:26624\n"
    "v_mfma_f32_16x16x32_bf16 v[252:255], a[108:111], a[24:27], v[252:255]\n"
    "v_mfma_f32_16x16x32_bf16 v[252:255], a[112:115], a[28:31], v[252:255]\n"
    "v_mfma_f32_16x16x32_bf16 v[252:255], a[116:119], a[52:55], v[252:255]\n"
    "ds_read_b128 a[52:55], %[rd1] offset:26624\n"
    "v_mfma_f32_16x16x32_bf16 v[252:255], a[120:123], a[56:59], v[252:255]\n"
    "buffer_load_dwordx4 a[164:167], %[l16], %[srdW], s54 offen offset:1024\n"
    "v_mfma_f32_16x16x32_bf16 v[252:255], a[124:127], a[60:63], v[252:255]\n"
    "s_waitcnt lgkmcnt(12)\n"
    "v_mfma_f32_16x16x32_bf16 a[176:179], a[100:103], a[32:35], a[176:179]\n"
    "v_mfma_f32_16x16x32_bf16 a[176:179], a[96:99], a[60:63], a[176:179]\n"
    "v_mfma_f32_16x16x32_bf16 a[176:179], a[92:95], a[56:59], a[176:179]\n"
    "buffer_load_dwordx4 a[168:171], %[l16], %[srdW], s54 offen offset:2048\n"
    "v_mfma_f32_16x16x32_bf16 a[176:179], a[88:91], a[0:3], a[176:179]\n"
    "v_mfma_f32_16x16x32_bf16 a[176:179], a[84:87], a[28:31], a[176:179]\n"
    "v_mfma_f32_16x16x32_bf16 a[176:179], a[80:83], a[24:27], a[176:179]\n"
    "v_mfma_f32_16x16x32_bf16 a[180:183], a[104:107], a[24:27], a[180:183]\n"
    "ds_read_b128 a[24:27], %[rd0] offset:28672\n"
    "v_mfma_f32_16x16x32_bf16 a[180:183], a[108:111], a[28:31], a[180:183]\n"
    "v_mfma_f32_16x16x32_bf16 a[180:183], a[112:115], a[0:3], a[180:183]\n"
    "v_mfma_f32_16x16x32_bf16 a[180:183], a[116:119], a[56:59], a[180:183]\n"
    "ds_read_b128 a[56:59], %[rd1] offset:28672\n"
    "v_mfma_f32_16x16x32_bf16 a[180:183], a[120:123], a[60:63], a[180:183]\n"
    "buffer_load_dwordx4 a[172:175], %[l16], %[srdW], s54 offen offset:3072\n"
    "v_mfma_f32_16x16x32_bf16 a[180:183], a[124:127], a[32:35], a[180:183]\n"
    "v_mfma_f32_16x16x32_bf16 a[184:187], a[100:103], a[36:39], a[184:187]\n"
    "s_add_u32 s54, s54, 4096\n"
    "v_mfma_f32_16x16x32_bf16 a[184:187], a[96:99], a[32:35], a[184:187]\n"
    "s_add_u32 m0, s69, 0\n"
    "s_add_u32 s57, s70, 0\n"
    "v_mfma_f32_16x16x32_bf16 a[184:187], a[92:95], a[60:63], a[184:187]\n"
    "buffer_load_dwordx4 %[l16], %[srdD], s57 offen sc1 lds\n"
    "v_mfma_f32_16x16x32_bf16 a[184:187], a[88:91], a[4:7], a[184:187]\n"
    "s_add_u32 m0, s69, 4096\n"
    "s_add_u32 s57, s70, 4096\n"
    "v_mfma_f32_16x16x32_bf16 a[184:187], a[84:87], a[0:3], a[184:187]\n"
    "buffer_load_dwordx4 %[l16], %[srdD], s57 offen sc1 lds\n"
    "v_mfma_f32_16x16x32_bf16 a[184:187], a[80:83], a[28:31], a[184:187]\n"
    "s_add_u32 s64, s56, 8192\n"
    "buffer_load_dwordx4 v[36:39], %[xr1], %[srdX], s64 offen\n"
    "v_mfma_f32_16x16x32_bf16 a[188:191], a[104:107], a[28:31], a[188:191]\n"
    "ds_read_b128 a[28:31], %[rd0] offset:30720\n"
    "v_mfma_f32_16x16x32_bf16 a[188:191], a[108:111], a[0:3], a[188:191]\n"
    "s_add_u32 s64, s71, 264192\n"
    "buffer_store_dwordx4 v[44:47], %[vst], %[srdD], s64 offen sc0 sc1\n"
    "v_mfma_f32_16x16x32_bf16 a[188:191], a[112:115], a[4:7], a[188:191]\n"
    "s_waitcnt vmcnt(29)\n"
    "v_lshlrev_b32 v60, 16, v48\n"
    "v_and_b32 v61, s65, v48\n"
    "v_mfma_f32_16x16x32_bf16 a[188:191], a[116:119], a[60:63], a[188:191]\n"
    "ds_read_b128 a[60:63], %[rd1] offset:30720\n"
    "v_lshlrev_b32 v62, 16, v49\n"
    "v_mfma_f32_16x16x32_bf16 a[188:191], a[120:123], a[32:35], a[188:191]\n"
    "v_and_b32 v63, s65, v49\n"
    "v_lshlrev_b32 v64, 16, v50\n"
    "v_mfma_f32_16x16x32_bf16 a[188:191], a[124:127], a[36:39], a[188:191]\n"
    "s_waitcnt lgkmcnt(10)\n"
    "v_and_b32 v65, s65, v50\n"
    "v_lshlrev_b32 v66, 16, v51\n"
    "v_mfma_f32_16x16x32_bf16 a[192:195], a[100:103], a[40:43], a[192:195]\n"
    "v_and_b32 v67, s65, v51\n"
    "v_sub_f32 v68, v60, v68\n"
    "v_mfma_f32_16x16x32_bf16 a[192:195], a[96:99], a[36:39], a[192:195]\n"
    "v_sub_f32 v69, v61, v69\n"
    "v_sub_f32 v70, v62, v70\n"
    "v_mfma_f32_16x16x32_bf16 a[192:195], a[92:95], a[32:35], a[192:195]\n"
    "s_add_u32 s63, s63, 1\n"
    "v_sub_f32 v71, v63, v71\n"
    "v_sub_f32 v72, v64, v72\n"
    "v_mfma_f32_16x16x32_bf16 a[192:195], a[88:91], a[8:11], a[192:195]\n"
    "v_sub_f32 v73, v65, v73\n"
    "v_sub_f32 v74, v66, v74\n"
    "v_mfma_f32_16x16x32_bf16 a[192:195], a[84:87], a[4:7], a[192:195]\n"
    "v_sub_f32 v75, v67, v75\n"
    "v_cvt_pk_bf16_f32 v48, v68, v69\n"
    "v_mfma_f32_16x16x32_bf16 a[192:195], a[80:83], a[0:3], a[192:195]\n"
    "v_cvt_pk_bf16_f32 v49, v70, v71\n"
    "v_cvt_pk_bf16_f32 v50, v72, v73\n"
    "v_mfma_f32_16x16x32_bf16 a[196:199], a[104:107], a[0:3], a[196:199]\n"
    "s_waitcnt lgkmcnt(0)\n"
    "v_mov_b32 v13, s63\n"
    "v_mfma_f32_16x16x32_bf16 a[196:199], a[108:111], a[4:7], a[196:199]\n"
    "ds_write_b32 v14, v13\n"
    "v_mfma_f32_16x16x32_bf16 a[196:199], a[112:115], a[8:11], a[196:199]\n"
    "v_cvt_pk_bf16_f32 v51, v74, v75\n"
    "v_mfma_f32_16x16x32_bf16 a[196:199], a[116:119], a[32:35], a[196:199]\n"
    "s_add_u32 s64, s71, 2048\n"
    "buffer_store_dwordx4 v[48:51], %[vst], %[srdD], s64 offen sc0 sc1\n"
    "v_mfma_f32_16x16x32_bf16 a[196:199], a[120:123], a[36:39], a[196:199]\n"
    "v_mfma_f32_16x16x32_bf16 a[196:199], a[124:127], a[40:43], a[196:199]\n"
    "v_mfma_f32_16x16x32_bf16 a[200:203], a[100:103], a[44:47], a[200:203]\n"
    "v_mfma_f32_16x16x32_bf16 a[200:203], a[96:99], a[40:43], a[200:203]\n"
    "v_mfma_f32_16x16x32_bf16 a[200:203], a[92:95], a[36:39], a[200:203]\n"
    "v_mfma_f32_16x16x32_bf16 a[200:203], a[88:91], a[12:15], a[200:203]\n"
    "v_mfma_f32_16x16x32_bf16 a[200:203], a[84:87], a[8:11], a[200:203]\n"
    "ds_read_b32 v13, v12\n"
    "v_mfma_f32_16x16x32_bf16 a[200:203], a[80:83], a[4:7], a[200:203]\n"
    "s_waitcnt vmcnt(29)\n"
    "v_lshlrev_b32 v68, 16, v52\n"
    "v_and_b32 v69, s65, v52\n"
    "v_mfma_f32_16x16x32_bf16 a[204:207], a[104:107], a[4:7], a[204:207]\n"
    "v_lshlrev_b32 v70, 16, v53\n"
    "v_and_b32 v71, s65, v53\n"
    "v_mfma_f32_16x16x32_bf16 a[204:207], a[108:111], a[8:11], a[204:207]\n"
    "v_lshlrev_b32 v72, 16, v54\n"
    "v_and_b32 v73, s65, v54\n"
    "v_mfma_f32_16x16x32_bf16 a[204:207], a[112:115], a[12:15], a[204:207]\n"
    "v_lshlrev_b32 v74, 16, v55\n"
    "v_and_b32 v75, s65, v55\n"
    "v_mfma_f32_16x16x32_bf16 a[204:207], a[116:119], a[36:39], a[204:207]\n"
    "v_sub_f32 v60, v60, v68\n"
    "v_sub_f32 v61, v61, v69\n"
    "v_mfma_f32_16x16x32_bf16 a[204:207], a[120:123], a[40:43], a[204:207]\n"
    "s_waitcnt lgkmcnt(0)\n"
    "v_cmp_gt_u32 vcc, s63, v13\n"
    "v_sub_f32 v62, v62, v70\n"
    "v_mfma_f32_16x16x32_bf16 a[204:207], a[124:127], a[44:47], a[204:207]\n"
    "s_cbranch_vccnz Ls7%=\n"
    "Lb7%=:\n"
    "v_sub_f32 v63, v63, v71\n"
    "v_mfma_f32_16x16x32_bf16 a[208:211], a[100:103], a[48:51], a[208:211]\n"
    "ds_read_b128 a[0:3], %[rd0] offset:32768\n"
    "v_sub_f32 v64, v64, v72\n"
    "v_mfma_f32_16x16x32_bf16 a[208:211], a[96:99], a[44:47], a[208:211]\n"
    "ds_read_b128 a[32:35], %[rd1] offset:32768\n"
    "v_sub_f32 v65, v65, v73\n"
    "v_mfma_f32_16x16x32_bf16 a[208:211], a[92:95], a[40:43], a[208:211]\n"
    "ds_read_b128 a[4:7], %[rd0] offset:34816\n"
    "v_sub_f32 v66, v66, v74\n"
    "v_mfma_f32_16x16x32_bf16 a[208:211], a[88:91], a[16:19], a[208:211]\n"
    "ds_read_b128 a[36:39], %[rd1] offset:34816\n"
    "v_sub_f32 v67, v67, v75\n"
    "v_mfma_f32_16x16x32_bf16 a[208:211], a[84:87], a[12:15], a[208:211]\n"
    "v_cvt_pk_bf16_f32 v52, v60, v61\n"
    "v_cvt_pk_bf16_f32 v53, v62, v63\n"
    "v_mfma_f32_16x16x32_bf16 a[208:211], a[80:83], a[8:11], a[208:211]\n"
    "v_cvt_pk_bf16_f32 v54, v64, v65\n"
    "v_cvt_pk_bf16_f32 v55, v66, v67\n"
    "v_mfma_f32_16x16x32_bf16 a[212:215], a[104:107], a[8:11], a[212:215]\n"
    "ds_read_b128 a[8:11], %[rd0] offset:36864\n"
    "v_mfma_f32_16x16x32_bf16 a[212:215], a[108:111], a[12:15], a[212:215]\n"
    "s_add_u32 m0, s69, 8192\n"
    "s_add_u32 s57, s70, 8192\n"
    "v_mfma_f32_16x16x32_bf16 a[212:215], a[112:115], a[16:19], a[212:215]\n"
    "buffer_load_dwordx4 %[l16], %[srdD], s57 offen sc1 lds\n"
    "v_mfma_f32_16x16x32_bf16 a[212:215], a[116:119], a[40:43], a[212:215]\n"
    "ds_read_b128 a[40:43], %[rd1] offset:36864\n"
    "v_mfma_f32_16x16x32_bf16 a[212:215], a[120:123], a[44:47], a[212:215]\n"
    "s_add_u32 s64, s71, 266240\n"
    "buffer_store_dwordx4 v[52:55], %[vst], %[srdD], s64 offen sc0 sc1\n"
    "v_mfma_f32_16x16x32_bf16 a[212:215], a[124:127], a[48:51], a[212:215]\n"
    "v_mfma_f32_16x16x32_bf16 a[216:219], a[100:103], a[52:55], a[216:219]\n"
    "v_mfma_f32_16x16x32_bf16 a[216:219], a[96:99], a[48:51], a[216:219]\n"
    "v_mfma_f32_16x16x32_bf16 a[216:219], a[92:95], a[44:47], a[216:219]\n"
    "s_add_u32 m0, s69, 12288\n"
    "s_add_u32 s57, s70, 12288\n"
    "v_mfma_f32_16x16x32_bf16 a[216:219], a[88:91], a[20:23], a[216:219]\n"
    "buffer_load_dwordx4 %[l16], %[srdD], s57 offen sc1 lds\n"
    "v_mfma_f32_16x16x32_bf16 a[216:219], a[84:87], a[16:19], a[216:219]\n"
    "v_mfma_f32_16x16x32_bf16 a[216:219], a[80:83], a[12:15], a[216:219]\n"
    "v_mfma_f32_16x16x32_bf16 a[220:223], a[104:107], a[12:15], a[220:223]\n"
    "ds_read_b128 a[12:15], %[rd0] offset:38912\n"
    "v_mfma_f32_16x16x32_bf16 a[220:223], a[108:111], a[16:19], a[220:223]\n"
    "v_mfma_f32_16x16x32_bf16 a[220:223], a[112:115], a[20:23], a[220:223]\n"
    "v_mfma_f32_16x16x32_bf16 a[220:223], a[116:119], a[44:47], a[220:223]\n"
    "ds_read_b128 a[44:47], %[rd1] offset:38912\n"
    "v_mfma_f32_16x16x32_bf16 a[220:223], a[120:123], a[48:51], a[220:223]\n"
    "v_mfma_f32_16x16x32_bf16 a[220:223], a[124:127], a[52:55], a[220:223]\n"
    "s_waitcnt vmcnt(27)\n"
    "v_lshlrev_b32 v60, 16, v56\n"
    "v_mfma_f32_16x16x32_bf16 a[224:227], a[100:103], a[56:59], a[224:227]\n"
    "v_and_b32 v61, s65, v56\n"
    "v_lshlrev_b32 v62, 16, v57\n"
    "v_mfma_f32_16x16x32_bf16 a[224:227], a[96:99], a[52:55], a[224:227]\n"
    "v_and_b32 v63, s65, v57\n"
    "v_lshlrev_b32 v64, 16, v58\n"
    "v_mfma_f32_16x16x32_bf16 a[224:227], a[92:95], a[48:51], a[224:227]\n"
    "s_add_u32 m0, s69, 16384\n"
    "s_add_u32 s57, s70, 16384\n"
    "v_and_b32 v65, s65, v58\n"
    "v_mfma_f32_16x16x32_bf16 a[224:227], a[88:91], a[24:27], a[224:227]\n"
    "buffer_load_dwordx4 %[l16], %[srdD], s57 offen sc1 lds\n"
    "v_lshlrev_b32 v66, 16, v59\n"
    "v_mfma_f32_16x16x32_bf16 a[224:227], a[84:87], a[20:23], a[224:227]\n"
    "v_and_b32 v67, s65, v59\n"
    "v_sub_f32 v68, v60, v68\n"
    "v_mfma_f32_16x16x32_bf16 a[224:227], a[80:83], a[16:19], a[224:227]\n"
    "v_sub_f32 v69, v61, v69\n"
    "v_sub_f32 v70, v62, v70\n"
    "v_mfma_f32_16x16x32_bf16 a[228:231], a[104:107], a[16:19], a[228:231]\n"
    "ds_read_b128 a[16:19], %[rd0] offset:40960\n"
    "v_sub_f32 v71, v63, v71\n"
    "v_mfma_f32_16x16x32_bf16 a[228:231], a[108:111], a[20:23], a[228:231]\n"
    "v_sub_f32 v72, v64, v72\n"
    "v_sub_f32 v73, v65, v73\n"
    "v_mfma_f32_16x16x32_bf16 a[228:231], a[112:115], a[24:27], a[228:231]\n"
    "v_sub_f32 v74, v66, v74\n"
    "v_sub_f32 v75, v67, v75\n"
    "v_mfma_f32_16x16x32_bf16 a[228:231], a[116:119], a[48:51], a[228:231]\n"
    "ds_read_b128 a[48:51], %[rd1] offset:40960\n"
    "v_cvt_pk_bf16_f32 v56, v68, v69\n"
    "v_mfma_f32_16x16x32_bf16 a[228:231], a[120:123], a[52:55], a[228:231]\n"
    "v_cvt_pk_bf16_f32 v57, v70, v71\n"
    "v_cvt_pk_bf16_f32 v58, v72, v73\n"
    "v_mfma_f32_16x16x32_bf16 a[228:231], a[124:127], a[56:59], a[228:231]\n"
    "v_cvt_pk_bf16_f32 v59, v74, v75\n"
    "v_mfma_f32_16x16x32_bf16 a[232:235], a[100:103], a[60:63], a[232:235]\n"
    "s_add_u32 s64, s71, 4096\n"
    "buffer_store_dwordx4 v[56:59], %[vst], %[srdD], s64 offen sc0 sc1\n"
    "v_mfma_f32_16x16x32_bf16 a[232:235], a[96:99], a[56:59], a[232:235]\n"
    "v_mfma_f32_16x16x32_bf16 a[232:235], a[92:95], a[52:55], a[232:235]\n"
    "s_add_u32 m0, s69, 20480\n"
    "s_add_u32 s57, s70, 20480\n"
    "v_mfma_f32_16x16x32_bf16 a[232:235], a[88:91], a[28:31], a[232:235]\n"
    "buffer_load_dwordx4 %[l16], %[srdD], s57 offen sc1 lds\n"
    "v_mfma_f32_16x16x32_bf16 a[232:235], a[84:87], a[24:27], a[232:235]\n"
    "v_mfma_f32_16x16x32_bf16 a[232:235], a[80:83], a[20:23], a[232:235]\n"
    "v_mfma_f32_16x16x32_bf16 a[236:239], a[104:107], a[20:23], a[236:239]\n"
    "ds_read_b128 a[20:23], %[rd0] offset:43008\n"
    "v_mfma_f32_16x16x32_bf16 a[236:239], a[108:111], a[24:27], a[236:239]\n"
    "v_mfma_f32_16x16x32_bf16 a[236:239], a[112:115], a[28:31], a[236:239]\n"
    "v_mfma_f32_16x16x32_bf16 a[236:239], a[116:119], a[52:55], a[236:239]\n"
    "ds_read_b128 a[52:55], %[rd1] offset:43008\n"
    "v_mfma_f32_16x16x32_bf16 a[236:239], a[120:123], a[56:59], a[236:239]\n"
    "v_mfma_f32_16x16x32_bf16 a[236:239], a[124:127], a[60:63], a[236:239]\n"
    "v_mfma_f32_16x16x32_bf16 a[240:243], a[100:103], a[72:75], a[240:243]\n"
    "v_mfma_f32_16x16x32_bf16 a[240:243], a[96:99], a[60:63], a[240:243]\n"
    "v_mfma_f32_16x16x32_bf16 a[240:243], a[92:95], a[56:59], a[240:243]\n"
    "s_add_u32 m0, s69, 24576\n"
    "s_add_u32 s57, s70, 24576\n"
    "v_mfma_f32_16x16x32_bf16 a[240:243], a[88:91], a[64:67], a[240:243]\n"
    "buffer_load_dwordx4 %[l16], %[srdD], s57 offen sc1 lds\n"
    "v_mfma_f32_16x16x32_bf16 a[240:243], a[84:87], a[28:31], a[240:243]\n"
    "v_mfma_f32_16x16x32_bf16 a[240:243], a[80:83], a[24:27], a[240:243]\n"
    "s_waitcnt vmcnt(25)\n"
    "v_lshlrev_b32 v68, 16, v32\n"
    "v_and_b32 v69, s65, v32\n"
    "v_mfma_f32_16x16x32_bf16 a[244:247], a[104:107], a[24:27], a[244:247]\n"
    "ds_read_b128 a[24:27], %[rd0] offset:45056\n"
    "v_lshlrev_b32 v70, 16, v33\n"
    "v_mfma_f32_16x16x32_bf16 a[244:247], a[108:111], a[28:31], a[244:247]\n"
    "v_and_b32 v71, s65, v33\n"
    "v_lshlrev_b32 v72, 16, v34\n"
    "v_mfma_f32_16x16x32_bf16 a[244:247], a[112:115], a[64:67], a[244:247]\n"
    "v_and_b32 v73, s65, v34\n"
    "v_lshlrev_b32 v74, 16, v35\n"
    "v_mfma_f32_16x16x32_bf16 a[244:247], a[116:119], a[56:59], a[244:247]\n"
    "ds_read_b128 a[56:59], %[rd1] offset:45056\n"
    "v_and_b32 v75, s65, v35\n"
    "v_mfma_f32_16x16x32_bf16 a[244:247], a[120:123], a[60:63], a[244:247]\n"
    "v_sub_f32 v60, v60, v68\n"
    "v_sub_f32 v61, v61, v69\n"
    "v_mfma_f32_16x16x32_bf16 a[244:247], a[124:127], a[72:75], a[244:247]\n"
    "v_sub_f32 v62, v62, v70\n"
    "v_sub_f32 v63, v63, v71\n"
    "v_mfma_f32_16x16x32_bf16 a[248:251], a[100:103], a[76:79], a[248:251]\n"
    "v_sub_f32 v64, v64, v72\n"
    "v_sub_f32 v65, v65, v73\n"
    "v_mfma_f32_16x16x32_bf16 a[248:251], a[96:99], a[72:75], a[248:251]\n"
    "v_sub_f32 v66, v66, v74\n"
    "v_sub_f32 v67, v67, v75\n"
    "v_mfma_f32_16x16x32_bf16 a[248:251], a[92:95], a[60:63], a[248:251]\n"
    "s_add_u32 m0, s69, 28672\n"
    "s_add_u32 s57, s70, 28672\n"
    "v_cvt_pk_bf16_f32 v32, v60, v61\n"
    "v_mfma_f32_16x16x32_bf16 a[248:251], a[88:91], a[68:71], a[248:251]\n"
    "buffer_load_dwordx4 %[l16], %[srdD], s57 offen sc1 lds\n"
    "v_cvt_pk_bf16_f32 v33, v62, v63\n"
    "v_mfma_f32_16x16x32_bf16 a[248:251], a[84:87], a[64:67], a[248:251]\n"
    "v_cvt_pk_bf16_f32 v34, v64, v65\n"
    "v_cvt_pk_bf16_f32 v35, v66, v67\n"
    "v_mfma_f32_16x16x32_bf16 a[248:251], a[80:83], a[28:31], a[248:251]\n"
    "s_add_u32 s69, s69, 0x8000\n"
    "s_and_b32 s69, s69, 0x1ffff\n"
    "v_mfma_f32_16x16x32_bf16 a[252:255], a[104:107], a[28:31], a[252:255]\n"
    "ds_read_b128 a[28:31], %[rd0] offset:47104\n"
    "v_mfma_f32_16x16x32_bf16 a[252:255], a[108:111], a[64:67], a[252:255]\n"
    "s_add_u32 s70, s70, 0x8000\n"
    "v_mfma_f32_16x16x32_bf16 a[252:255], a[112:115], a[68:71], a[252:255]\n"
    "s_add_u32 s64, s71, 268288\n"
    "buffer_store_dwordx4 v[32:35], %[vst], %[srdD], s64 offen sc0 sc1\n"
    "s_waitcnt vmcnt(11)\n"
    "v_lshlrev_b32 v60, 16, v36\n"
    "v_and_b32 v61, s65, v36\n"
    "v_lshlrev_b32 v62, 16, v37\n"
    "v_and_b32 v63, s65, v37\n"
    "v_lshlrev_b32 v64, 16, v38\n"
    "v_and_b32 v65, s65, v38\n"
    "v_lshlrev_b32 v66, 16, v39\n"
    "v_and_b32 v67, s65, v39\n"
    "v_sub_f32 v68, v60, v68\n"
    "v_sub_f32 v69, v61, v69\n"
    "v_sub_f32 v70, v62, v70\n"
    "v_sub_f32 v71, v63, v71\n"
    "v_sub_f32 v72, v64, v72\n"
    "v_sub_f32 v73, v65, v73\n"
    "v_sub_f32 v74, v66, v74\n"
    "v_sub_f32 v75, v67, v75\n"
    "v_cvt_pk_bf16_f32 v36, v68, v69\n"
    "v_cvt_pk_bf16_f32 v37, v70, v71\n"
    "v_cvt_pk_bf16_f32 v38, v72, v73\n"
    "v_cvt_pk_bf16_f32 v39, v74, v75\n"
    "s_add_u32 s64, s71, 6144\n"
    "buffer_store_dwordx4 v[36:39], %[vst], %[srdD], s64 offen sc0 sc1\n"
    "v_mfma_f32_16x16x32_bf16 a[252:255], a[116:119], a[60:63], a[252:255]\n"
    "ds_read_b128 a[60:63], %[rd1] offset:47104\n"
    "v_mfma_f32_16x16x32_bf16 a[252:255], a[120:123], a[72:75], a[252:255]\n"
    "v_mfma_f32_16x16x32_bf16 a[252:255], a[124:127], a[76:79], a[252:255]\n"
    "s_waitcnt lgkmcnt(8)\n"
    "s_cmp_lg_u32 s67, 0\n"
    "s_cbranch_scc1 Lc8%=\n"
    "v_mfma_f32_16x16x32_bf16 v[208:211], a[148:151], a[40:43], v[208:211]\n"
    "v_mfma_f32_16x16x32_bf16 v[208:211], a[144:147], a[36:39], v[208:211]\n"
    "v_mfma_f32_16x16x32_bf16 v[208:211], a[140:143], a[32:35], v[208:211]\n"
    "v_mfma_f32_16x16x32_bf16 v[208:211], a[136:139], a[8:11], v[208:211]\n"
    "v_mfma_f32_16x16x32_bf16 v[208:211], a[132:135], a[4:7], v[208:211]\n"
    "v_mfma_f32_16x16x32_bf16 v[208:211], a[128:131], a[0:3], v[208:211]\n"
    "v_mfma_f32_16x16x32_bf16 v[212:215], a[152:155], a[0:3], v[212:215]\n"
    "v_mfma_f32_16x16x32_bf16 v[212:215], a[156:159], a[4:7], v[212:215]\n"
    "v_mfma_f32_16x16x32_bf16 v[212:215], a[160:163], a[8:11], v[212:215]\n"
    "v_mfma_f32_16x16x32_bf16 v[212:215], a[164:167], a[32:35], v[212:215]\n"
    "v_mfma_f32_16x16x32_bf16 v[212:215], a[168:171], a[36:39], v[212:215]\n"
    "v_mfma_f32_16x16x32_bf16 v[212:215], a[172:175], a[40:43], v[212:215]\n"
    "Lc8%=:\n"
    "v_mfma_f32_16x16x32_bf16 v[216:219], a[148:151], a[44:47], v[216:219]\n"
    "ds_read_b128 a[0:3], %[rd0] offset:49152\n"
    "v_mfma_f32_16x16x32_bf16 v[216:219], a[144:147], a[40:43], v[216:219]\n"
    "v_mfma_f32_16x16x32_bf16 v[216:219], a[140:143], a[36:39], v[216:219]\n"
    "buffer_load_dwordx4 a[80:83], %[l16], %[srdW], s54 offen offset:0\n"
    "v_mfma_f32_16x16x32_bf16 v[216:219], a[136:139], a[12:15], v[216:219]\n"
    "v_mfma_f32_16x16x32_bf16 v[216:219], a[132:135], a[8:11], v[216:219]\n"
    "ds_read_b128 a[32:35], %[rd1] offset:49152\n"
    "v_mfma_f32_16x16x32_bf16 v[216:219], a[128:131], a[4:7], v[216:219]\n"
    "v_mfma_f32_16x16x32_bf16 v[220:223], a[152:155], a[4:7], v[220:223]\n"
    "ds_read_b128 a[4:7], %[rd0] offset:51200\n"
    "v_mfma_f32_16x16x32_bf16 v[220:223], a[156:159], a[8:11], v[220:223]\n"
    "v_mfma_f32_16x16x32_bf16 v[220:223], a[160:163], a[12:15], v[220:223]\n"
    "v_mfma_f32_16x16x32_bf16 v[220:223], a[164:167], a[36:39], v[220:223]\n"
    "ds_read_b128 a[36:39], %[rd1] offset:51200\n"
    "v_mfma_f32_16x16x32_bf16 v[220:223], a[168:171], a[40:43], v[220:223]\n"
    "buffer_load_dwordx4 a[84:87], %[l16], %[srdW], s54 offen offset:1024\n"
    "v_mfma_f32_16x16x32_bf16 v[220:223], a[172:175], a[44:47], v[220:223]\n"
    "s_waitcnt lgkmcnt(8)\n"
    "v_mfma_f32_16x16x32_bf16 v[224:227], a[148:151], a[48:51], v[224:227]\n"
    "v_mov_b32 v80, v208\n"
    "v_mov_b32 v81, v209\n"
    "v_mfma_f32_16x16x32_bf16 v[224:227], a[144:147], a[44:47], v[224:227]\n"
    "ds_read_b128 a[64:67], %[re0] offset:32768\n"
    "v_mov_b32 v82, v210\n"
    "v_mfma_f32_16x16x32_bf16 v[224:227], a[140:143], a[40:43], v[224:227]\n"
    "buffer_load_dwordx4 a[88:91], %[l16], %[srdW], s54 offen offset:2048\n"
    "v_mov_b32 v83, v211\n"
    "v_mfma_f32_16x16x32_bf16 v[224:227], a[136:139], a[16:19], v[224:227]\n"
    "v_mov_b32 v84, v212\n"
    "v_mov_b32 v85, v213\n"
    "v_mfma_f32_16x16x32_bf16 v[224:227], a[132:135], a[12:15], v[224:227]\n"
    "v_mov_b32 v86, v214\n"
    "v_mov_b32 v87, v215\n"
    "v_mfma_f32_16x16x32_bf16 v[224:227], a[128:131], a[8:11], v[224:227]\n"
    "v_mfma_f32_16x16x32_bf16 v[228:231], a[152:155], a[8:11], v[228:231]\n"
    "ds_read_b128 a[8:11], %[rd0] offset:53248\n"
    "v_mfma_f32_16x16x32_bf16 v[228:231], a[156:159], a[12:15], v[228:231]\n"
    "v_mfma_f32_16x16x32_bf16 v[228:231], a[160:163], a[16:19], v[228:231]\n"
    "v_mfma_f32_16x16x32_bf16 v[228:231], a[164:167], a[40:43], v[228:231]\n"
    "ds_read_b128 a[40:43], %[rd1] offset:53248\n"
    "v_mfma_f32_16x16x32_bf16 v[228:231], a[168:171], a[44:47], v[228:231]\n"
    "buffer_load_dwordx4 a[92:95], %[l16], %[srdW], s54 offen offset:3072\n"
    "v_mfma_f32_16x16x32_bf16 v[228:231], a[172:175], a[48:51], v[228:231]\n"
    "v_mfma_f32_16x16x32_bf16 v[232:235], a[148:151], a[52:55], v[232:235]\n"
    "s_add_u32 s54, s54, 4096\n"
    "v_mov_b32 v88, v216\n"
    "v_mov_b32 v89, v217\n"
    "v_mfma_f32_16x16x32_bf16 v[232:235], a[144:147], a[48:51], v[232:235]\n"
    "ds_read_b128 a[72:75], %[re1] offset:32768\n"
    "v_mov_b32 v90, v218\n"
    "v_mfma_f32_16x16x32_bf16 v[232:235], a[140:143], a[44:47], v[232:235]\n"
    "buffer_load_dwordx4 a[96:99], %[l16], %[srdW], s54 offen offset:0\n"
    "v_mov_b32 v91, v219\n"
    "v_mfma_f32_16x16x32_bf16 v[232:235], a[136:139], a[20:23], v[232:235]\n"
    "v_mov_b32 v92, v220\n"
    "v_mov_b32 v93, v221\n"
    "v_mfma_f32_16x16x32_bf16 v[232:235], a[132:135], a[16:19], v[232:235]\n"
    "v_mov_b32 v94, v222\n"
    "v_mov_b32 v95, v223\n"
    "v_mfma_f32_16x16x32_bf16 v[232:235], a[128:131], a[12:15], v[232:235]\n"
    "v_mfma_f32_16x16x32_bf16 v[236:239], a[152:155], a[12:15], v[236:239]\n"
    "ds_read_b128 a[12:15], %[rd0] offset:55296\n"
    "v_mfma_f32_16x16x32_bf16 v[236:239], a[156:159], a[16:19], v[236:239]\n"
    "v_mfma_f32_16x16x32_bf16 v[236:239], a[160:163], a[20:23], v[236:239]\n"
    "v_mfma_f32_16x16x32_bf16 v[236:239], a[164:167], a[44:47], v[236:239]\n"
    "ds_read_b128 a[44:47], %[rd1] offset:55296\n"
    "v_mfma_f32_16x16x32_bf16 v[236:239], a[168:171], a[48:51], v[236:239]\n"
    "buffer_load_dwordx4 a[100:103], %[l16], %[srdW], s54 offen offset:1024\n"
    "v_mfma_f32_16x16x32_bf16 v[236:239], a[172:175], a[52:55], v[236:239]\n"
    "s_waitcnt lgkmcnt(10)\n"
    "v_mfma_f32_16x16x32_bf16 v[240:243], a[148:151], a[56:59], v[240:243]\n"
    "v_mov_b32 v96, v224\n"
    "v_mov_b32 v97, v225\n"
    "v_mfma_f32_16x16x32_bf16 v[240:243], a[144:147], a[52:55], v[240:243]\n"
    "ds_read_b128 a[68:71], %[re0] offset:34816\n"
    "v_mov_b32 v98, v226\n"
    "v_mfma_f32_16x16x32_bf16 v[240:243], a[140:143], a[48:51], v[240:243]\n"
    "buffer_load_dwordx4 a[104:107], %[l16], %[srdW], s54 offen offset:2048\n"
    "v_mov_b32 v99, v227\n"
    "v_mfma_f32_16x16x32_bf16 v[240:243], a[136:139], a[24:27], v[240:243]\n"
    "v_mov_b32 v100, v228\n"
    "v_mov_b32 v101, v229\n"
    "v_mfma_f32_16x16x32_bf16 v[240:243], a[132:135], a[20:23], v[240:243]\n"
    "v_mov_b32 v102, v230\n"
    "v_mov_b32 v103, v231\n"
    "v_mfma_f32_16x16x32_bf16 v[240:243], a[128:131], a[16:19], v[240:243]\n"
    "v_mfma_f32_16x16x32_bf16 v[244:247], a[152:155], a[16:19], v[244:247]\n"
    "ds_read_b128 a[16:19], %[rd0] offset:57344\n"
    "v_mfma_f32_16x16x32_bf16 v[244:247], a[156:159], a[20:23], v[244:247]\n"
    "v_mfma_f32_16x16x32_bf16 v[244:247], a[160:163], a[24:27], v[244:247]\n"
    "v_mfma_f32_16x16x32_bf16 v[244:247], a[164:167], a[48:51], v[244:247]\n"
    "ds_read_b128 a[48:51], %[rd1] offset:57344\n"
    "v_mfma_f32_16x16x32_bf16 v[244:247], a[168:171], a[52:55], v[244:247]\n"
    "buffer_load_dwordx4 a[108:111], %[l16], %[srdW], s54 offen offset:3072\n"
    "v_mfma_f32_16x16x32_bf16 v[244:247], a[172:175], a[56:59], v[244:247]\n"
    "v_mfma_f32_16x16x32_bf16 v[248:251], a[148:151], a[60:63], v[248:251]\n"
    "s_add_u32 s54, s54, 4096\n"
    "v_mov_b32 v104, v232\n"
    "v_mov_b32 v105, v233\n"
    "v_mfma_f32_16x16x32_bf16 v[248:251], a[144:147], a[56:59], v[248:251]\n"
    "ds_read_b128 a[76:79], %[re1] offset:34816\n"
    "v_mov_b32 v106, v234\n"
    "v_mfma_f32_16x16x32_bf16 v[248:251], a[140:143], a[52:55], v[248:251]\n"
    "v_mov_b32 v107, v235\n"
    "v_mov_b32 v108, v236\n"
    "v_mfma_f32_16x16x32_bf16 v[248:251], a[136:139], a[28:31], v[248:251]\n"
    "v_mov_b32 v109, v237\n"
    "v_mov_b32 v110, v238\n"
    "v_mfma_f32_16x16x32_bf16 v[248:251], a[132:135], a[24:27], v[248:251]\n"
    "v_mov_b32 v111, v239\n"
    "v_mfma_f32_16x16x32_bf16 v[248:251], a[128:131], a[20:23], v[248:251]\n"
    "v_mfma_f32_16x16x32_bf16 v[252:255], a[152:155], a[20:23], v[252:255]\n"
    "ds_read_b128 a[20:23], %[rd0] offset:59392\n"
    "v_mfma_f32_16x16x32_bf16 v[252:255], a[156:159], a[24:27], v[252:255]\n"
    "v_mfma_f32_16x16x32_bf16 v[252:255], a[160:163], a[28:31], v[252:255]\n"
    "v_mfma_f32_16x16x32_bf16 v[252:255], a[164:167], a[52:55], v[252:255]\n"
    "ds_read_b128 a[52:55], %[rd1] offset:59392\n"
    "v_mfma_f32_16x16x32_bf16 v[252:255], a[168:171], a[56:59], v[252:255]\n"
    "v_mfma_f32_16x16x32_bf16 v[252:255], a[172:175], a[60:63], v[252:255]\n"
    "s_waitcnt lgkmcnt(12)\n"
    "v_mfma_f32_16x16x32_bf16 a[176:179], a[148:151], a[32:35], a[176:179]\n"
    "v_mov_b32 v112, v240\n"
    "v_mov_b32 v113, v241\n"
    "v_mfma_f32_16x16x32_bf16 a[176:179], a[144:147], a[60:63], a[176:179]\n"
    "v_mov_b32 v114, v242\n"
    "v_mov_b32 v115, v243\n"
    "v_mfma_f32_16x16x32_bf16 a[176:179], a[140:143], a[56:59], a[176:179]\n"
    "v_mov_b32 v116, v244\n"
    "v_mov_b32 v117, v245\n"
    "v_mfma_f32_16x16x32_bf16 a[176:179], a[136:139], a[0:3], a[176:179]\n"
    "v_mov_b32 v118, v246\n"
    "v_mov_b32 v119, v247\n"
    "v_mfma_f32_16x16x32_bf16 a[176:179], a[132:135], a[28:31], a[176:179]\n"
    "s_lshl_b32 s58, s62, 6\n"
    "v_mfma_f32_16x16x32_bf16 a[176:179], a[128:131], a[24:27], a[176:179]\n"
    "v_mfma_f32_16x16x32_bf16 a[180:183], a[152:155], a[24:27], a[180:183]\n"
    "ds_read_b128 a[24:27], %[rd0] offset:61440\n"
    "v_mfma_f32_16x16x32_bf16 a[180:183], a[156:159], a[28:31], a[180:183]\n"
    "s_waitcnt vmcnt(8)\n"
    "s_mov_b64 exec, 1\n"
    "buffer_atomic_add v77, v78, %[srdW], s58 offen offset:128 sc1\n"
    "s_mov_b64 exec, -1\n"
    "v_mfma_f32_16x16x32_bf16 a[180:183], a[160:163], a[0:3], a[180:183]\n"
    "v_mfma_f32_16x16x32_bf16 a[180:183], a[164:167], a[56:59], a[180:183]\n"
    "ds_read_b128 a[56:59], %[rd1] offset:61440\n"
    "v_mfma_f32_16x16x32_bf16 a[180:183], a[168:171], a[60:63], a[180:183]\n"
    "v_mfma_f32_16x16x32_bf16 a[180:183], a[172:175], a[32:35], a[180:183]\n"
    "v_mfma_f32_16x16x32_bf16 a[184:187], a[148:151], a[36:39], a[184:187]\n"
    "v_mov_b32 v120, v248\n"
    "v_mov_b32 v121, v249\n"
    "v_mfma_f32_16x16x32_bf16 a[184:187], a[144:147], a[32:35], a[184:187]\n"
    "s_add_u32 m0, s69, 0\n"
    "s_add_u32 s57, s70, 0\n"
    "v_mov_b32 v122, v250\n"
    "v_mfma_f32_16x16x32_bf16 a[184:187], a[140:143], a[60:63], a[184:187]\n"
    "buffer_load_dwordx4 %[l16], %[srdD], s57 offen sc1 lds\n"
    "v_mov_b32 v123, v251\n"
    "v_mfma_f32_16x16x32_bf16 a[184:187], a[136:139], a[4:7], a[184:187]\n"
    "s_add_u32 m0, s69, 4096\n"
    "s_add_u32 s57, s70, 4096\n"
    "v_mov_b32 v124, v252\n"
    "v_mfma_f32_16x16x32_bf16 a[184:187], a[132:135], a[0:3], a[184:187]\n"
    "buffer_load_dwordx4 %[l16], %[srdD], s57 offen sc1 lds\n"
    "v_mov_b32 v125, v253\n"
    "v_mfma_f32_16x16x32_bf16 a[184:187], a[128:131], a[28:31], a[184:187]\n"
    "v_mov_b32 v126, v254\n"
    "v_mov_b32 v127, v255\n"
    "v_mfma_f32_16x16x32_bf16 a[188:191], a[152:155], a[28:31], a[188:191]\n"
    "ds_read_b128 a[28:31], %[rd0] offset:63488\n"
    "v_mfma_f32_16x16x32_bf16 a[188:191], a[156:159], a[0:3], a[188:191]\n"
    "v_mfma_f32_16x16x32_bf16 a[188:191], a[160:163], a[4:7], a[188:191]\n"
    "v_mfma_f32_16x16x32_bf16 a[188:191], a[164:167], a[60:63], a[188:191]\n"
    "ds_read_b128 a[60:63], %[rd1] offset:63488\n"
    "v_mfma_f32_16x16x32_bf16 a[188:191], a[168:171], a[32:35], a[188:191]\n"
    "v_mfma_f32_16x16x32_bf16 a[188:191], a[172:175], a[36:39], a[188:191]\n"
    "s_waitcnt lgkmcnt(10)\n"
    "v_mfma_f32_16x16x32_bf16 a[192:195], a[148:151], a[40:43], a[192:195]\n"
    "v_xor_b32 %[rd0], 0x10000, %[rd0]\n"
    "v_accvgpr_read_b32 v128, a176\n"
    "v_mfma_f32_16x16x32_bf16 a[192:195], a[144:147], a[36:39], a[192:195]\n"
    "v_xor_b32 %[rd1], 0x10000, %[rd1]\n"
    "v_accvgpr_read_b32 v129, a177\n"
    "v_mfma_f32_16x16x32_bf16 a[192:195], a[140:143], a[32:35], a[192:195]\n"
    "s_add_u32 s63, s63, 1\n"
    "v_accvgpr_read_b32 v130, a178\n"
    "v_accvgpr_read_b32 v131, a179\n"
    "v_mfma_f32_16x16x32_bf16 a[192:195], a[136:139], a[8:11], a[192:195]\n"
    "v_accvgpr_read_b32 v132, a180\n"
    "v_accvgpr_read_b32 v133, a181\n"
    "v_mfma_f32_16x16x32_bf16 a[192:195], a[132:135], a[4:7], a[192:195]\n"
    "v_accvgpr_read_b32 v134, a182\n"
    "v_accvgpr_read_b32 v135, a183\n"
    "v_mfma_f32_16x16x32_bf16 a[192:195], a[128:131], a[0:3], a[192:195]\n"
    "v_mfma_f32_16x16x32_bf16 a[196:199], a[152:155], a[0:3], a[196:199]\n"
    "s_waitcnt lgkmcnt(0)\n"
    "v_mov_b32 v13, s63\n"
    "v_mfma_f32_16x16x32_bf16 a[196:199], a[156:159], a[4:7], a[196:199]\n"
    "ds_write_b32 v14, v13\n"
    "v_mfma_f32_16x16x32_bf16 a[196:199], a[160:163], a[8:11], a[196:199]\n"
    "v_mfma_f32_16x16x32_bf16 a[196:199], a[164:167], a[32:35], a[196:199]\n"
    "v_mfma_f32_16x16x32_bf16 a[196:199], a[168:171], a[36:39], a[196:199]\n"
    "v_mfma_f32_16x16x32_bf16 a[196:199], a[172:175], a[40:43], a[196:199]\n"
    "v_mfma_f32_16x16x32_bf16 a[200:203], a[148:151], a[44:47], a[200:203]\n"
    "v_xor_b32 %[re0], 0x10000, %[re0]\n"
    "v_accvgpr_read_b32 v136, a184\n"
    "v_mfma_f32_16x16x32_bf16 a[200:203], a[144:147], a[40:43], a[200:203]\n"
    "v_xor_b32 %[re1], 0x10000, %[re1]\n"
    "v_accvgpr_read_b32 v137, a185\n"
    "v_mfma_f32_16x16x32_bf16 a[200:203], a[140:143], a[36:39], a[200:203]\n"
    "v_accvgpr_read_b32 v138, a186\n"
    "v_accvgpr_read_b32 v139, a187\n"
    "v_mfma_f32_16x16x32_bf16 a[200:203], a[136:139], a[12:15], a[200:203]\n"
    "v_accvgpr_read_b32 v140, a188\n"
    "v_accvgpr_read_b32 v141, a189\n"
    "v_mfma_f32_16x16x32_bf16 a[200:203], a[132:135], a[8:11], a[200:203]\n"
    "ds_read_b32 v13, v12\n"
    "v_accvgpr_read_b32 v142, a190\n"
    "v_mfma_f32_16x16x32_bf16 a[200:203], a[128:131], a[4:7], a[200:203]\n"
    "v_accvgpr_read_b32 v143, a191\n"
    "v_mfma_f32_16x16x32_bf16 a[204:207], a[152:155], a[4:7], a[204:207]\n"
    "v_mfma_f32_16x16x32_bf16 a[204:207], a[156:159], a[8:11], a[204:207]\n"
    "v_mfma_f32_16x16x32_bf16 a[204:207], a[160:163], a[12:15], a[204:207]\n"
    "v_mfma_f32_16x16x32_bf16 a[204:207], a[164:167], a[36:39], a[204:207]\n"
    "v_mfma_f32_16x16x32_bf16 a[204:207], a[168:171], a[40:43], a[204:207]\n"
    "s_waitcnt lgkmcnt(0)\n"
    "v_cmp_gt_u32 vcc, s63, v13\n"
    "v_mfma_f32_16x16x32_bf16 a[204:207], a[172:175], a[44:47], a[204:207]\n"
    "s_cbranch_vccnz Ls8%=\n"
    "Lb8%=:\n"
    "v_mfma_f32_16x16x32_bf16 a[208:211], a[148:151], a[48:51], a[208:211]\n"
    "ds_read_b128 a[0:3], %[rd0] offset:0\n"
    "v_accvgpr_read_b32 v144, a192\n"
    "v_mfma_f32_16x16x32_bf16 a[208:211], a[144:147], a[44:47], a[208:211]\n"
    "ds_read_b128 a[32:35], %[rd1] offset:0\n"
    "v_accvgpr_read_b32 v145, a193\n"
    "v_mfma_f32_16x16x32_bf16 a[208:211], a[140:143], a[40:43], a[208:211]\n"
    "ds_read_b128 a[4:7], %[rd0] offset:2048\n"
    "v_accvgpr_read_b32 v146, a194\n"
    "v_mfma_f32_16x16x32_bf16 a[208:211], a[136:139], a[16:19], a[208:211]\n"
    "ds_read_b128 a[36:39], %[rd1] offset:2048\n"
    "v_accvgpr_read_b32 v147, a195\n"
    "v_mfma_f32_16x16x32_bf16 a[208:211], a[132:135], a[12:15], a[208:211]\n"
    "v_accvgpr_read_b32 v148, a196\n"
    "v_accvgpr_read_b32 v149, a197\n"
    "v_mfma_f32_16x16x32_bf16 a[208:211], a[128:131], a[8:11], a[208:211]\n"
    "v_accvgpr_read_b32 v150, a198\n"
    "v_accvgpr_read_b32 v151, a199\n"
    "v_mfma_f32_16x16x32_bf16 a[212:215], a[152:155], a[8:11], a[212:215]\n"
    "ds_read_b128 a[8:11], %[rd0] offset:4096\n"
    "v_mfma_f32_16x16x32_bf16 a[212:215], a[156:159], a[12:15], a[212:215]\n"
    "s_add_u32 m0, s69, 8192\n"
    "s_add_u32 s57, s70, 8192\n"
    "v_mfma_f32_16x16x32_bf16 a[212:215], a[160:163], a[16:19], a[212:215]\n"
    "buffer_load_dwordx4 %[l16], %[srdD], s57 offen sc1 lds\n"
    "v_mfma_f32_16x16x32_bf16 a[212:215], a[164:167], a[40:43], a[212:215]\n"
    "ds_read_b128 a[40:43], %[rd1] offset:4096\n"
    "v_mfma_f32_16x16x32_bf16 a[212:215], a[168:171], a[44:47], a[212:215]\n"
    "v_mfma_f32_16x16x32_bf16 a[212:215], a[172:175], a[48:51], a[212:215]\n"
    "v_mfma_f32_16x16x32_bf16 a[216:219], a[148:151], a[52:55], a[216:219]\n"
    "v_accvgpr_read_b32 v152, a200\n"
    "v_accvgpr_read_b32 v153, a201\n"
    "v_mfma_f32_16x16x32_bf16 a[216:219], a[144:147], a[48:51], a[216:219]\n"
    "v_accvgpr_read_b32 v154, a202\n"
    "v_accvgpr_read_b32 v155, a203\n"
    "v_mfma_f32_16x16x32_bf16 a[216:219], a[140:143], a[44:47], a[216:219]\n"
    "s_add_u32 m0, s69, 12288\n"
    "s_add_u32 s57, s70, 12288\n"
    "v_accvgpr_read_b32 v156, a204\n"
    "v_mfma_f32_16x16x32_bf16 a[216:219], a[136:139], a[20:23], a[216:219]\n"
    "buffer_load_dwordx4 %[l16], %[srdD], s57 offen sc1 lds\n"
    "v_accvgpr_read_b32 v157, a205\n"
    "v_mfma_f32_16x16x32_bf16 a[216:219], a[132:135], a[16:19], a[216:219]\n"
    "v_accvgpr_read_b32 v158, a206\n"
    "v_accvgpr_read_b32 v159, a207\n"
    "v_mfma_f32_16x16x32_bf16 a[216:219], a[128:131], a[12:15], a[216:219]\n"
    "v_mfma_f32_16x16x32_bf16 a[220:223], a[152:155], a[12:15], a[220:223]\n"
    "ds_read_b128 a[12:15], %[rd0] offset:6144\n"
    "v_mfma_f32_16x16x32_bf16 a[220:223], a[156:159], a[16:19], a[220:223]\n"
    "v_mfma_f32_16x16x32_bf16 a[220:223], a[160:163], a[20:23], a[220:223]\n"
    "v_mfma_f32_16x16x32_bf16 a[220:223], a[164:167], a[44:47], a[220:223]\n"
    "ds_read_b128 a[44:47], %[rd1] offset:6144\n"
    "v_mfma_f32_16x16x32_bf16 a[220:223], a[168:171], a[48:51], a[220:223]\n"
    "v_mfma_f32_16x16x32_bf16 a[220:223], a[172:175], a[52:55], a[220:223]\n"
    "v_mfma_f32_16x16x32_bf16 a[224:227], a[148:151], a[56:59], a[224:227]\n"
    "v_accvgpr_read_b32 v160, a208\n"
    "v_accvgpr_read_b32 v161, a209\n"
    "v_mfma_f32_16x16x32_bf16 a[224:227], a[144:147], a[52:55], a[224:227]\n"
    "v_accvgpr_read_b32 v162, a210\n"
    "v_accvgpr_read_b32 v163, a211\n"
    "v_mfma_f32_16x16x32_bf16 a[224:227], a[140:143], a[48:51], a[224:227]\n"
    "s_add_u32 m0, s69, 16384\n"
    "s_add_u32 s57, s70, 16384\n"
    "v_accvgpr_read_b32 v164, a212\n"
    "v_mfma_f32_16x16x32_bf16 a[224:227], a[136:139], a[24:27], a[224:227]\n"
    "buffer_load_dwordx4 %[l16], %[srdD], s57 offen sc1 lds\n"
    "v_accvgpr_read_b32 v165, a213\n"
    "v_mfma_f32_16x16x32_bf16 a[224:227], a[132:135], a[20:23], a[224:227]\n"
    "v_accvgpr_read_b32 v166, a214\n"
    "v_accvgpr_read_b32 v167, a215\n"
    "v_mfma_f32_16x16x32_bf16 a[224:227], a[128:131], a[16:19], a[224:227]\n"
    "v_mfma_f32_16x16x32_bf16 a[228:231], a[152:155], a[16:19], a[228:231]\n"
    "ds_read_b128 a[16:19], %[rd0] offset:8192\n"
    "v_mfma_f32_16x16x32_bf16 a[228:231], a[156:159], a[20:23], a[228:231]\n"
    "v_mfma_f32_16x16x32_bf16 a[228:231], a[160:163], a[24:27], a[228:231]\n"
    "v_mfma_f32_16x16x32_bf16 a[228:231], a[164:167], a[48:51], a[228:231]\n"
    "ds_read_b128 a[48:51], %[rd1] offset:8192\n"
    "v_mfma_f32_16x16x32_bf16 a[228:231], a[168:171], a[52:55], a[228:231]\n"
    "v_mfma_f32_16x16x32_bf16 a[228:231], a[172:175], a[56:59], a[228:231]\n"
    "v_mfma_f32_16x16x32_bf16 a[232:235], a[148:151], a[60:63], a[232:235]\n"
    "v_accvgpr_read_b32 v168, a216\n"
    "v_accvgpr_read_b32 v169, a217\n"
    "v_mfma_f32_16x16x32_bf16 a[232:235], a[144:147], a[56:59], a[232:235]\n"
    "v_accvgpr_read_b32 v170, a218\n"
    "v_accvgpr_read_b32 v171, a219\n"
    "v_mfma_f32_16x16x32_bf16 a[232:235], a[140:143], a[52:55], a[232:235]\n"
    "s_add_u32 m0, s69, 20480\n"
    "s_add_u32 s57, s70, 20480\n"
    "v_accvgpr_read_b32 v172, a220\n"
    "v_mfma_f32_16x16x32_bf16 a[232:235], a[136:139], a[28:31], a[232:235]\n"
    "buffer_load_dwordx4 %[l16], %[srdD], s57 offen sc1 lds\n"
    "v_accvgpr_read_b32 v173, a221\n"
    "v_mfma_f32_16x16x32_bf16 a[232:235], a[132:135], a[24:27], a[232:235]\n"
    "v_accvgpr_read_b32 v174, a222\n"
    "v_accvgpr_read_b32 v175, a223\n"
    "v_mfma_f32_16x16x32_bf16 a[232:235], a[128:131], a[20:23], a[232:235]\n"
    "v_mfma_f32_16x16x32_bf16 a[236:239], a[152:155], a[20:23], a[236:239]\n"
    "ds_read_b128 a[20:23], %[rd0] offset:10240\n"
    "v_mfma_f32_16x16x32_bf16 a[236:239], a[156:159], a[24:27], a[236:239]\n"
    "v_mfma_f32_16x16x32_bf16 a[236:239], a[160:163], a[28:31], a[236:239]\n"
    "v_mfma_f32_16x16x32_bf16 a[236:239], a[164:167], a[52:55], a[236:239]\n"
    "ds_read_b128 a[52:55], %[rd1] offset:10240\n"
    "v_mfma_f32_16x16x32_bf16 a[236:239], a[168:171], a[56:59], a[236:239]\n"
    "v_mfma_f32_16x16x32_bf16 a[236:239], a[172:175], a[60:63], a[236:239]\n"
    "v_mfma_f32_16x16x32_bf16 a[240:243], a[148:151], a[72:75], a[240:243]\n"
    "v_accvgpr_read_b32 v176, a224\n"
    "v_accvgpr_read_b32 v177, a225\n"
    "v_mfma_f32_16x16x32_bf16 a[240:243], a[144:147], a[60:63], a[240:243]\n"
    "v_accvgpr_read_b32 v178, a226\n"
    "v_accvgpr_read_b32 v179, a227\n"
    "v_mfma_f32_16x16x32_bf16 a[240:243], a[140:143], a[56:59], a[240:243]\n"
    "s_add_u32 m0, s69, 24576\n"
    "s_add_u32 s57, s70, 24576\n"
    "v_accvgpr_read_b32 v180, a228\n"
    "v_mfma_f32_16x16x32_bf16 a[240:243], a[136:139], a[64:67], a[240:243]\n"
    "buffer_load_dwordx4 %[l16], %[srdD], s57 offen sc1 lds\n"
    "v_accvgpr_read_b32 v181, a229\n"
    "v_mfma_f32_16x16x32_bf16 a[240:243], a[132:135], a[28:31], a[240:243]\n"
    "v_accvgpr_read_b32 v182, a230\n"
    "v_accvgpr_read_b32 v183, a231\n"
    "v_mfma_f32_16x16x32_bf16 a[240:243], a[128:131], a[24:27], a[240:243]\n"
    "v_mfma_f32_16x16x32_bf16 a[244:247], a[152:155], a[24:27], a[244:247]\n"
    "ds_read_b128 a[24:27], %[rd0] offset:12288\n"
    "v_mfma_f32_16x16x32_bf16 a[244:247], a[156:159], a[28:31], a[244:247]\n"
    "v_mfma_f32_16x16x32_bf16 a[244:247], a[160:163], a[64:67], a[244:247]\n"
    "v_mfma_f32_16x16x32_bf16 a[244:247], a[164:167], a[56:59], a[244:247]\n"
    "ds_read_b128 a[56:59], %[rd1] offset:12288\n"
    "v_mfma_f32_16x16x32_bf16 a[244:247], a[168:171], a[60:63], a[244:247]\n"
    "v_mfma_f32_16x16x32_bf16 a[244:247], a[172:175], a[72:75], a[244:247]\n"
    "v_mfma_f32_16x16x32_bf16 a[248:251], a[148:151], a[76:79], a[248:251]\n"
    "v_accvgpr_read_b32 v184, a232\n"
    "v_accvgpr_read_b32 v185, a233\n"
    "v_mfma_f32_16x16x32_bf16 a[248:251], a[144:147], a[72:75], a[248:251]\n"
    "v_accvgpr_read_b32 v186, a234\n"
    "v_accvgpr_read_b32 v187, a235\n"
    "v_mfma_f32_16x16x32_bf16 a[248:251], a[140:143], a[60:63], a[248:251]\n"
    "s_add_u32 m0, s69, 28672\n"
    "s_add_u32 s57, s70, 28672\n"
    "v_accvgpr_read_b32 v188, a236\n"
    "v_mfma_f32_16x16x32_bf16 a[248:251], a[136:139], a[68:71], a[248:251]\n"
    "buffer_load_dwordx4 %[l16], %[srdD], s57 offen sc1 lds\n"
    "v_accvgpr_read_b32 v189, a237\n"
    "v_mfma_f32_16x16x32_bf16 a[248:251], a[132:135], a[64:67], a[248:251]\n"
    "v_accvgpr_read_b32 v190, a238\n"
    "v_accvgpr_read_b32 v191, a239\n"
    "v_mfma_f32_16x16x32_bf16 a[248:251], a[128:131], a[28:31], a[248:251]\n"
    "s_add_u32 s69, s69, 0x8000\n"
    "s_and_b32 s69, s69, 0x1ffff\n"
    "v_mfma_f32_16x16x32_bf16 a[252:255], a[152:155], a[28:31], a[252:255]\n"
    "ds_read_b128 a[28:31], %[rd0] offset:14336\n"
    "v_mfma_f32_16x16x32_bf16 a[252:255], a[156:159], a[64:67], a[252:255]\n"
    "s_add_u32 s70, s70, 0x8000\n"
    "v_mfma_f32_16x16x32_bf16 a[252:255], a[160:163], a[68:71], a[252:255]\n"
    "v_mfma_f32_16x16x32_bf16 a[252:255], a[164:167], a[60:63], a[252:255]\n"
    "ds_read_b128 a[60:63], %[rd1] offset:14336\n"
    "v_mfma_f32_16x16x32_bf16 a[252:255], a[168:171], a[72:75], a[252:255]\n"
    "v_mfma_f32_16x16x32_bf16 a[252:255], a[172:175], a[76:79], a[252:255]\n"
    "s_nop 7\n"
    "v_accvgpr_read_b32 v192, a240\n"
    "v_accvgpr_read_b32 v193, a241\n"
    "v_accvgpr_read_b32 v194, a242\n"
    "v_accvgpr_read_b32 v195, a243\n"
    "v_accvgpr_read_b32 v196, a244\n"
    "v_accvgpr_read_b32 v197, a245\n"
    "v_accvgpr_read_b32 v198, a246\n"
    "v_accvgpr_read_b32 v199, a247\n"
    "v_accvgpr_read_b32 v200, a248\n"
    "v_accvgpr_read_b32 v201, a249\n"
    "v_accvgpr_read_b32 v202, a250\n"
    "v_accvgpr_read_b32 v203, a251\n"
    "v_accvgpr_read_b32 v204, a252\n"
    "v_accvgpr_read_b32 v205, a253\n"
    "v_accvgpr_read_b32 v206, a254\n"
    "v_accvgpr_read_b32 v207, a255\n"
    "s_mov_b32 s59, 3\n"
    "Lloop%=:\n"
    "s_waitcnt vmcnt(8)\n"
    "s_waitcnt lgkmcnt(10)\n"
    "s_cmp_lg_u32 s67, 0\n"
    "s_cbranch_scc1 Lc9%=\n"
    "v_mfma_f32_16x16x32_bf16 v[208:211], a[92:95], a[36:39], v[208:211]\n"
    "v_mfma_f32_16x16x32_bf16 v[208:211], a[88:91], a[32:35], v[208:211]\n"
    "v_mfma_f32_16x16x32_bf16 v[208:211], a[84:87], a[4:7], v[208:211]\n"
    "v_mfma_f32_16x16x32_bf16 v[208:211], a[80:83], a[0:3], v[208:211]\n"
    "v_mfma_f32_16x16x32_bf16 v[212:215], a[96:99], a[0:3], v[212:215]\n"
    "v_mfma_f32_16x16x32_bf16 v[212:215], a[100:103], a[4:7], v[212:215]\n"
    "v_mfma_f32_16x16x32_bf16 v[212:215], a[104:107], a[32:35], v[212:215]\n"
    "v_mfma_f32_16x16x32_bf16 v[212:215], a[108:111], a[36:39], v[212:215]\n"
    "Lc9%=:\n"
    "v_mfma_f32_16x16x32_bf16 v[216:219], a[92:95], a[40:43], v[216:219]\n"
    "ds_read_b128 a[0:3], %[rd0] offset:16384\n"
    "v_mfma_f32_16x16x32_bf16 v[216:219], a[88:91], a[36:39], v[216:219]\n"
    "v_mfma_f32_16x16x32_bf16 v[216:219], a[84:87], a[8:11], v[216:219]\n"
    "buffer_load_dwordx4 a[128:131], %[l16], %[srdW], s54 offen offset:0\n"
    "v_mfma_f32_16x16x32_bf16 v[216:219], a[80:83], a[4:7], v[216:219]\n"
    "ds_read_b128 a[32:35], %[rd1] offset:16384\n"
    "v_mfma_f32_16x16x32_bf16 v[220:223], a[96:99], a[4:7], v[220:223]\n"
    "ds_read_b128 a[4:7], %[rd0] offset:18432\n"
    "v_mfma_f32_16x16x32_bf16 v[220:223], a[100:103], a[8:11], v[220:223]\n"
    "buffer_load_dwordx4 a[132:135], %[l16], %[srdW], s54 offen offset:1024\n"
    "v_mfma_f32_16x16x32_bf16 v[220:223], a[104:107], a[36:39], v[220:223]\n"
    "ds_read_b128 a[36:39], %[rd1] offset:18432\n"
    "v_mfma_f32_16x16x32_bf16 v[220:223], a[108:111], a[40:43], v[220:223]\n"
    "s_waitcnt lgkmcnt(10)\n"
    "v_mfma_f32_16x16x32_bf16 v[224:227], a[92:95], a[44:47], v[224:227]\n"
    "v_mfma_f32_16x16x32_bf16 v[224:227], a[88:91], a[40:43], v[224:227]\n"
    "ds_read_b128 a[64:67], %[re0] offset:0\n"
    "v_mfma_f32_16x16x32_bf16 v[224:227], a[84:87], a[12:15], v[224:227]\n"
    "buffer_load_dwordx4 a[136:139], %[l16], %[srdW], s54 offen offset:2048\n"
    "v_mfma_f32_16x16x32_bf16 v[224:227], a[80:83], a[8:11], v[224:227]\n"
    "v_mfma_f32_16x16x32_bf16 v[228:231], a[96:99], a[8:11], v[228:231]\n"
    "ds_read_b128 a[8:11], %[rd0] offset:20480\n"
    "v_mfma_f32_16x16x32_bf16 v[228:231], a[100:103], a[12:15], v[228:231]\n"
    "buffer_load_dwordx4 a[140:143], %[l16], %[srdW], s54 offen offset:3072\n"
    "v_mfma_f32_16x16x32_bf16 v[228:231], a[104:107], a[40:43], v[228:231]\n"
    "ds_read_b128 a[40:43], %[rd1] offset:20480\n"
    "v_mfma_f32_16x16x32_bf16 v[228:231], a[108:111], a[44:47], v[228:231]\n"
    "v_mfma_f32_16x16x32_bf16 v[232:235], a[92:95], a[48:51], v[232:235]\n"
    "s_add_u32 s54, s54, 4096\n"
    "v_mfma_f32_16x16x32_bf16 v[232:235], a[88:91], a[44:47], v[232:235]\n"
    "ds_read_b128 a[72:75], %[re1] offset:0\n"
    "v_mfma_f32_16x16x32_bf16 v[232:235], a[84:87], a[16:19], v[232:235]\n"
    "buffer_load_dwordx4 a[144:147], %[l16], %[srdW], s54 offen offset:0\n"
    "v_mfma_f32_16x16x32_bf16 v[232:235], a[80:83], a[12:15], v[232:235]\n"
    "v_mfma_f32_16x16x32_bf16 v[236:239], a[96:99], a[12:15], v[236:239]\n"
    "ds_read_b128 a[12:15], %[rd0] offset:22528\n"
    "v_mfma_f32_16x16x32_bf16 v[236:239], a[100:103], a[16:19], v[236:239]\n"
    "buffer_load_dwordx4 a[148:151], %[l16], %[srdW], s54 offen offset:1024\n"
    "v_mfma_f32_16x16x32_bf16 v[236:239], a[104:107], a[44:47], v[236:239]\n"
    "ds_read_b128 a[44:47], %[rd1] offset:22528\n"
    "v_mfma_f32_16x16x32_bf16 v[236:239], a[108:111], a[48:51], v[236:239]\n"
    "s_waitcnt lgkmcnt(12)\n"
    "v_mfma_f32_16x16x32_bf16 v[240:243], a[92:95], a[52:55], v[240:243]\n"
    "v_mfma_f32_16x16x32_bf16 v[240:243], a[88:91], a[48:51], v[240:243]\n"
    "v_mfma_f32_16x16x32_bf16 v[240:243], a[84:87], a[20:23], v[240:243]\n"
    "buffer_load_dwordx4 a[152:155], %[l16], %[srdW], s54 offen offset:2048\n"
    "v_mfma_f32_16x16x32_bf16 v[240:243], a[80:83], a[16:19], v[240:243]\n"
    "v_mfma_f32_16x16x32_bf16 v[244:247], a[96:99], a[16:19], v[244:247]\n"
    "ds_read_b128 a[16:19], %[rd0] offset:24576\n"
    "v_mfma_f32_16x16x32_bf16 v[244:247], a[100:103], a[20:23], v[244:247]\n"
    "buffer_load_dwordx4 a[156:159], %[l16], %[srdW], s54 offen offset:3072\n"
    "v_mfma_f32_16x16x32_bf16 v[244:247], a[104:107], a[48:51], v[244:247]\n"
    "ds_read_b128 a[48:51], %[rd1] offset:24576\n"
    "v_mfma_f32_16x16x32_bf16 v[244:247], a[108:111], a[52:55], v[244:247]\n"
    "v_mfma_f32_16x16x32_bf16 v[248:251], a[92:95], a[56:59], v[248:251]\n"
    "s_add_u32 s54, s54, 4096\n"
    "v_mfma_f32_16x16x32_bf16 v[248:251], a[88:91], a[52:55], v[248:251]\n"
    "v_mfma_f32_16x16x32_bf16 v[248:251], a[84:87], a[24:27], v[248:251]\n"
    "v_mfma_f32_16x16x32_bf16 v[248:251], a[80:83], a[20:23], v[248:251]\n"
    "v_mfma_f32_16x16x32_bf16 v[252:255], a[96:99], a[20:23], v[252:255]\n"
    "ds_read_b128 a[20:23], %[rd0] offset:26624\n"
    "v_mfma_f32_16x16x32_bf16 v[252:255], a[100:103], a[24:27], v[252:255]\n"
    "v_mfma_f32_16x16x32_bf16 v[252:255], a[104:107], a[52:55], v[252:255]\n"
    "ds_read_b128 a[52:55], %[rd1] offset:26624\n"
    "v_mfma_f32_16x16x32_bf16 v[252:255], a[108:111], a[56:59], v[252:255]\n"
    "s_waitcnt lgkmcnt(12)\n"
    "v_mfma_f32_16x16x32_bf16 a[176:179], a[92:95], a[60:63], a[176:179]\n"
    "v_mfma_f32_16x16x32_bf16 a[176:179], a[88:91], a[56:59], a[176:179]\n"
    "v_mfma_f32_16x16x32_bf16 a[176:179], a[84:87], a[28:31], a[176:179]\n"
    "v_mfma_f32_16x16x32_bf16 a[176:179], a[80:83], a[24:27], a[176:179]\n"
    "v_mfma_f32_16x16x32_bf16 a[180:183], a[96:99], a[24:27], a[180:183]\n"
    "ds_read_b128 a[24:27], %[rd0] offset:28672\n"
    "v_mfma_f32_16x16x32_bf16 a[180:183], a[100:103], a[28:31], a[180:183]\n"
    "v_mfma_f32_16x16x32_bf16 a[180:183], a[104:107], a[56:59], a[180:183]\n"
    "ds_read_b128 a[56:59], %[rd1] offset:28672\n"
    "v_mfma_f32_16x16x32_bf16 a[180:183], a[108:111], a[60:63], a[180:183]\n"
    "v_mfma_f32_16x16x32_bf16 a[184:187], a[92:95], a[32:35], a[184:187]\n"
    "v_mfma_f32_16x16x32_bf16 a[184:187], a[88:91], a[60:63], a[184:187]\n"
    "s_add_u32 m0, s69, 0\n"
    "s_add_u32 s57, s70, 0\n"
    "v_mfma_f32_16x16x32_bf16 a[184:187], a[84:87], a[0:3], a[184:187]\n"
    "buffer_load_dwordx4 %[l16], %[srdD], s57 offen sc1 lds\n"
    "v_mfma_f32_16x16x32_bf16 a[184:187], a[80:83], a[28:31], a[184:187]\n"
    "v_mfma_f32_16x16x32_bf16 a[188:191], a[96:99], a[28:31], a[188:191]\n"
    "ds_read_b128 a[28:31], %[rd0] offset:30720\n"
    "v_mfma_f32_16x16x32_bf16 a[188:191], a[100:103], a[0:3], a[188:191]\n"
    "v_mfma_f32_16x16x32_bf16 a[188:191], a[104:107], a[60:63], a[188:191]\n"
    "ds_read_b128 a[60:63], %[rd1] offset:30720\n"
    "v_mfma_f32_16x16x32_bf16 a[188:191], a[108:111], a[32:35], a[188:191]\n"
    "s_waitcnt lgkmcnt(11)\n"
    "v_mfma_f32_16x16x32_bf16 a[192:195], a[92:95], a[36:39], a[192:195]\n"
    "v_mfma_f32_16x16x32_bf16 a[192:195], a[88:91], a[32:35], a[192:195]\n"
    "v_mfma_f32_16x16x32_bf16 a[192:195], a[84:87], a[4:7], a[192:195]\n"
    "s_add_u32 s63, s63, 1\n"
    "v_mfma_f32_16x16x32_bf16 a[192:195], a[80:83], a[0:3], a[192:195]\n"
    "v_mfma_f32_16x16x32_bf16 a[196:199], a[96:99], a[0:3], a[196:199]\n"
    "s_waitcnt lgkmcnt(0)\n"
    "v_mov_b32 v13, s63\n"
    "v_mfma_f32_16x16x32_bf16 a[196:199], a[100:103], a[4:7], a[196:199]\n"
    "ds_write_b32 v14, v13\n"
    "v_mfma_f32_16x16x32_bf16 a[196:199], a[104:107], a[32:35], a[196:199]\n"
    "v_mfma_f32_16x16x32_bf16 a[196:199], a[108:111], a[36:39], a[196:199]\n"
    "v_mfma_f32_16x16x32_bf16 a[200:203], a[92:95], a[40:43], a[200:203]\n"
    "v_mfma_f32_16x16x32_bf16 a[200:203], a[88:91], a[36:39], a[200:203]\n"
    "v_mfma_f32_16x16x32_bf16 a[200:203], a[84:87], a[8:11], a[200:203]\n"
    "ds_read_b32 v13, v12\n"
    "v_mfma_f32_16x16x32_bf16 a[200:203], a[80:83], a[4:7], a[200:203]\n"
    "v_mfma_f32_16x16x32_bf16 a[204:207], a[96:99], a[4:7], a[204:207]\n"
    "v_mfma_f32_16x16x32_bf16 a[204:207], a[100:103], a[8:11], a[204:207]\n"
    "v_mfma_f32_16x16x32_bf16 a[204:207], a[104:107], a[36:39], a[204:207]\n"
    "s_waitcnt lgkmcnt(0)\n"
    "v_cmp_gt_u32 vcc, s63, v13\n"
    "v_mfma_f32_16x16x32_bf16 a[204:207], a[108:111], a[40:43], a[204:207]\n"
    "s_cbranch_vccnz Ls9%=\n"
    "Lb9%=:\n"
    "v_mfma_f32_16x16x32_bf16 a[208:211], a[92:95], a[44:47], a[208:211]\n"
    "ds_read_b128 a[0:3], %[rd0] offset:32768\n"
    "v_mfma_f32_16x16x32_bf16 a[208:211], a[88:91], a[40:43], a[208:211]\n"
    "ds_read_b128 a[32:35], %[rd1] offset:32768\n"
    "v_mfma_f32_16x16x32_bf16 a[208:211], a[84:87], a[12:15], a[208:211]\n"
    "ds_read_b128 a[4:7], %[rd0] offset:34816\n"
    "v_mfma_f32_16x16x32_bf16 a[208:211], a[80:83], a[8:11], a[208:211]\n"
    "ds_read_b128 a[36:39], %[rd1] offset:34816\n"
    "v_mfma_f32_16x16x32_bf16 a[212:215], a[96:99], a[8:11], a[212:215]\n"
    "ds_read_b128 a[8:11], %[rd0] offset:36864\n"
    "v_mfma_f32_16x16x32_bf16 a[212:215], a[100:103], a[12:15], a[212:215]\n"
    "v_mfma_f32_16x16x32_bf16 a[212:215], a[104:107], a[40:43], a[212:215]\n"
    "ds_read_b128 a[40:43], %[rd1] offset:36864\n"
    "v_mfma_f32_16x16x32_bf16 a[212:215], a[108:111], a[44:47], a[212:215]\n"
    "v_mfma_f32_16x16x32_bf16 a[216:219], a[92:95], a[48:51], a[216:219]\n"
    "s_add_u32 m0, s69, 4096\n"
    "s_add_u32 s57, s70, 4096\n"
    "v_mfma_f32_16x16x32_bf16 a[216:219], a[88:91], a[44:47], a[216:219]\n"
    "buffer_load_dwordx4 %[l16], %[srdD], s57 offen sc1 lds\n"
    "v_mfma_f32_16x16x32_bf16 a[216:219], a[84:87], a[16:19], a[216:219]\n"
    "s_add_u32 m0, s69, 8192\n"
    "s_add_u32 s57, s70, 8192\n"
    "v_mfma_f32_16x16x32_bf16 a[216:219], a[80:83], a[12:15], a[216:219]\n"
    "buffer_load_dwordx4 %[l16], %[srdD], s57 offen sc1 lds\n"
    "v_mfma_f32_16x16x32_bf16 a[220:223], a[96:99], a[12:15], a[220:223]\n"
    "ds_read_b128 a[12:15], %[rd0] offset:38912\n"
    "v_mfma_f32_16x16x32_bf16 a[220:223], a[100:103], a[16:19], a[220:223]\n"
    "v_mfma_f32_16x16x32_bf16 a[220:223], a[104:107], a[44:47], a[220:223]\n"
    "ds_read_b128 a[44:47], %[rd1] offset:38912\n"
    "v_mfma_f32_16x16x32_bf16 a[220:223], a[108:111], a[48:51], a[220:223]\n"
    "v_mfma_f32_16x16x32_bf16 a[224:227], a[92:95], a[52:55], a[224:227]\n"
    "s_lshl_b32 s58, s62, 6\n"
    "v_mfma_f32_16x16x32_bf16 a[224:227], a[88:91], a[48:51], a[224:227]\n"
    "s_add_u32 m0, s69, 12288\n"
    "s_add_u32 s57, s70, 12288\n"
    "v_mfma_f32_16x16x32_bf16 a[224:227], a[84:87], a[20:23], a[224:227]\n"
    "buffer_load_dwordx4 %[l16], %[srdD], s57 offen sc1 lds\n"
    "v_mfma_f32_16x16x32_bf16 a[224:227], a[80:83], a[16:19], a[224:227]\n"
    "buffer_load_dword v79, v78, %[srdW], s58 offen offset:128 sc0 sc1\n"
    "v_mfma_f32_16x16x32_bf16 a[228:231], a[96:99], a[16:19], a[228:231]\n"
    "ds_read_b128 a[16:19], %[rd0] offset:40960\n"
    "v_mfma_f32_16x16x32_bf16 a[228:231], a[100:103], a[20:23], a[228:231]\n"
    "v_mfma_f32_16x16x32_bf16 a[228:231], a[104:107], a[48:51], a[228:231]\n"
    "ds_read_b128 a[48:51], %[rd1] offset:40960\n"
    "v_mfma_f32_16x16x32_bf16 a[228:231], a[108:111], a[52:55], a[228:231]\n"
    "v_mfma_f32_16x16x32_bf16 a[232:235], a[92:95], a[56:59], a[232:235]\n"
    "s_add_u32 m0, s69, 16384\n"
    "s_add_u32 s57, s70, 16384\n"
    "v_mfma_f32_16x16x32_bf16 a[232:235], a[88:91], a[52:55], a[232:235]\n"
    "buffer_load_dwordx4 %[l16], %[srdD], s57 offen sc1 lds\n"
    "v_mfma_f32_16x16x32_bf16 a[232:235], a[84:87], a[24:27], a[232:235]\n"
    "s_add_u32 m0, s69, 20480\n"
    "s_add_u32 s57, s70, 20480\n"
    "v_mfma_f32_16x16x32_bf16 a[232:235], a[80:83], a[20:23], a[232:235]\n"
    "buffer_load_dwordx4 %[l16], %[srdD], s57 offen sc1 lds\n"
    "v_mfma_f32_16x16x32_bf16 a[236:239], a[96:99], a[20:23], a[236:239]\n"
    "ds_read_b128 a[20:23], %[rd0] offset:43008\n"
    "v_mfma_f32_16x16x32_bf16 a[236:239], a[100:103], a[24:27], a[236:239]\n"
    "v_mfma_f32_16x16x32_bf16 a[236:239], a[104:107], a[52:55], a[236:239]\n"
    "ds_read_b128 a[52:55], %[rd1] offset:43008\n"
    "v_mfma_f32_16x16x32_bf16 a[236:239], a[108:111], a[56:59], a[236:239]\n"
    "v_mfma_f32_16x16x32_bf16 a[240:243], a[92:95], a[60:63], a[240:243]\n"
    "v_mfma_f32_16x16x32_bf16 a[240:243], a[88:91], a[56:59], a[240:243]\n"
    "s_add_u32 m0, s69, 24576\n"
    "s_add_u32 s57, s70, 24576\n"
    "v_mfma_f32_16x16x32_bf16 a[240:243], a[84:87], a[28:31], a[240:243]\n"
    "buffer_load_dwordx4 %[l16], %[srdD], s57 offen sc1 lds\n"
    "v_mfma_f32_16x16x32_bf16 a[240:243], a[80:83], a[24:27], a[240:243]\n"
    "v_mfma_f32_16x16x32_bf16 a[244:247], a[96:99], a[24:27], a[244:247]\n"
    "ds_read_b128 a[24:27], %[rd0] offset:45056\n"
    "v_mfma_f32_16x16x32_bf16 a[244:247], a[100:103], a[28:31], a[244:247]\n"
    "v_mfma_f32_16x16x32_bf16 a[244:247], a[104:107], a[56:59], a[244:247]\n"
    "ds_read_b128 a[56:59], %[rd1] offset:45056\n"
    "v_mfma_f32_16x16x32_bf16 a[244:247], a[108:111], a[60:63], a[244:247]\n"
    "v_mfma_f32_16x16x32_bf16 a[248:251], a[92:95], a[72:75], a[248:251]\n"
    "v_mfma_f32_16x16x32_bf16 a[248:251], a[88:91], a[60:63], a[248:251]\n"
    "s_add_u32 m0, s69, 28672\n"
    "s_add_u32 s57, s70, 28672\n"
    "v_mfma_f32_16x16x32_bf16 a[248:251], a[84:87], a[64:67], a[248:251]\n"
    "buffer_load_dwordx4 %[l16], %[srdD], s57 offen sc1 lds\n"
    "v_mfma_f32_16x16x32_bf16 a[248:251], a[80:83], a[28:31], a[248:251]\n"
    "s_add_u32 s70, s70, 0x8000\n"
    "v_mfma_f32_16x16x32_bf16 a[252:255], a[96:99], a[28:31], a[252:255]\n"
    "ds_read_b128 a[28:31], %[rd0] offset:47104\n"
    "v_mfma_f32_16x16x32_bf16 a[252:255], a[100:103], a[64:67], a[252:255]\n"
    "s_add_u32 s69, s69, 0x8000\n"
    "s_and_b32 s69, s69, 0x1ffff\n"
    "v_mfma_f32_16x16x32_bf16 a[252:255], a[104:107], a[60:63], a[252:255]\n"
    "ds_read_b128 a[60:63], %[rd1] offset:47104\n"
    "v_mfma_f32_16x16x32_bf16 a[252:255], a[108:111], a[72:75], a[252:255]\n"
    "s_waitcnt vmcnt(9)\n"
    "s_waitcnt lgkmcnt(10)\n"
    "s_cmp_lg_u32 s67, 0\n"
    "s_cbranch_scc1 Lc10%=\n"
    "v_mfma_f32_16x16x32_bf16 v[208:211], a[140:143], a[36:39], v[208:211]\n"
    "v_mfma_f32_16x16x32_bf16 v[208:211], a[136:139], a[32:35], v[208:211]\n"
    "v_mfma_f32_16x16x32_bf16 v[208:211], a[132:135], a[4:7], v[208:211]\n"
    "v_mfma_f32_16x16x32_bf16 v[208:211], a[128:131], a[0:3], v[208:211]\n"
    "v_mfma_f32_16x16x32_bf16 v[212:215], a[144:147], a[0:3], v[212:215]\n"
    "v_mfma_f32_16x16x32_bf16 v[212:215], a[148:151], a[4:7], v[212:215]\n"
    "v_mfma_f32_16x16x32_bf16 v[212:215], a[152:155], a[32:35], v[212:215]\n"
    "v_mfma_f32_16x16x32_bf16 v[212:215], a[156:159], a[36:39], v[212:215]\n"
    "Lc10%=:\n"
    "v_mfma_f32_16x16x32_bf16 v[216:219], a[140:143], a[40:43], v[216:219]\n"
    "ds_read_b128 a[0:3], %[rd0] offset:49152\n"
    "v_mfma_f32_16x16x32_bf16 v[216:219], a[136:139], a[36:39], v[216:219]\n"
    "v_mfma_f32_16x16x32_bf16 v[216:219], a[132:135], a[8:11], v[216:219]\n"
    "buffer_load_dwordx4 a[80:83], %[l16], %[srdW], s54 offen offset:0\n"
    "v_mfma_f32_16x16x32_bf16 v[216:219], a[128:131], a[4:7], v[216:219]\n"
    "ds_read_b128 a[32:35], %[rd1] offset:49152\n"
    "v_mfma_f32_16x16x32_bf16 v[220:223], a[144:147], a[4:7], v[220:223]\n"
    "ds_read_b128 a[4:7], %[rd0] offset:51200\n"
    "v_mfma_f32_16x16x32_bf16 v[220:223], a[148:151], a[8:11], v[220:223]\n"
    "buffer_load_dwordx4 a[84:87], %[l16], %[srdW], s54 offen offset:1024\n"
    "v_mfma_f32_16x16x32_bf16 v[220:223], a[152:155], a[36:39], v[220:223]\n"
    "ds_read_b128 a[36:39], %[rd1] offset:51200\n"
    "v_mfma_f32_16x16x32_bf16 v[220:223], a[156:159], a[40:43], v[220:223]\n"
    "s_waitcnt lgkmcnt(10)\n"
    "v_mfma_f32_16x16x32_bf16 v[224:227], a[140:143], a[44:47], v[224:227]\n"
    "v_mfma_f32_16x16x32_bf16 v[224:227], a[136:139], a[40:43], v[224:227]\n"
    "ds_read_b128 a[64:67], %[re0] offset:32768\n"
    "v_mfma_f32_16x16x32_bf16 v[224:227], a[132:135], a[12:15], v[224:227]\n"
    "buffer_load_dwordx4 a[88:91], %[l16], %[srdW], s54 offen offset:2048\n"
    "v_mfma_f32_16x16x32_bf16 v[224:227], a[128:131], a[8:11], v[224:227]\n"
    "v_mfma_f32_16x16x32_bf16 v[228:231], a[144:147], a[8:11], v[228:231]\n"
    "ds_read_b128 a[8:11], %[rd0] offset:53248\n"
    "v_mfma_f32_16x16x32_bf16 v[228:231], a[148:151], a[12:15], v[228:231]\n"
    "buffer_load_dwordx4 a[92:95], %[l16], %[srdW], s54 offen offset:3072\n"
    "v_mfma_f32_16x16x32_bf16 v[228:231], a[152:155], a[40:43], v[228:231]\n"
    "ds_read_b128 a[40:43], %[rd1] offset:53248\n"
    "v_mfma_f32_16x16x32_bf16 v[228:231], a[156:159], a[44:47], v[228:231]\n"
    "v_mfma_f32_16x16x32_bf16 v[232:235], a[140:143], a[48:51], v[232:235]\n"
    "s_add_u32 s54, s54, 4096\n"
    "v_mfma_f32_16x16x32_bf16 v[232:235], a[136:139], a[44:47], v[232:235]\n"
    "ds_read_b128 a[72:75], %[re1] offset:32768\n"
    "v_mfma_f32_16x16x32_bf16 v[232:235], a[132:135], a[16:19], v[232:235]\n"
    "buffer_load_dwordx4 a[96:99], %[l16], %[srdW], s54 offen offset:0\n"
    "v_mfma_f32_16x16x32_bf16 v[232:235], a[128:131], a[12:15], v[232:235]\n"
    "v_mfma_f32_16x16x32_bf16 v[236:239], a[144:147], a[12:15], v[236:239]\n"
    "ds_read_b128 a[12:15], %[rd0] offset:55296\n"
    "v_mfma_f32_16x16x32_bf16 v[236:239], a[148:151], a[16:19], v[236:239]\n"
    "buffer_load_dwordx4 a[100:103], %[l16], %[srdW], s54 offen offset:1024\n"
    "v_mfma_f32_16x16x32_bf16 v[236:239], a[152:155], a[44:47], v[236:239]\n"
    "ds_read_b128 a[44:47], %[rd1] offset:55296\n"
    "v_mfma_f32_16x16x32_bf16 v[236:239], a[156:159], a[48:51], v[236:239]\n"
    "s_waitcnt lgkmcnt(12)\n"
    "v_mfma_f32_16x16x32_bf16 v[240:243], a[140:143], a[52:55], v[240:243]\n"
    "v_mfma_f32_16x16x32_bf16 v[240:243], a[136:139], a[48:51], v[240:243]\n"
    "v_mfma_f32_16x16x32_bf16 v[240:243], a[132:135], a[20:23], v[240:243]\n"
    "buffer_load_dwordx4 a[104:107], %[l16], %[srdW], s54 offen offset:2048\n"
    "v_mfma_f32_16x16x32_bf16 v[240:243], a[128:131], a[16:19], v[240:243]\n"
    "v_mfma_f32_16x16x32_bf16 v[244:247], a[144:147], a[16:19], v[244:247]\n"
    "ds_read_b128 a[16:19], %[rd0] offset:57344\n"
    "v_mfma_f32_16x16x32_bf16 v[244:247], a[148:151], a[20:23], v[244:247]\n"
    "buffer_load_dwordx4 a[108:111], %[l16], %[srdW], s54 offen offset:3072\n"
    "v_mfma_f32_16x16x32_bf16 v[244:247], a[152:155], a[48:51], v[244:247]\n"
    "ds_read_b128 a[48:51], %[rd1] offset:57344\n"
    "v_mfma_f32_16x16x32_bf16 v[244:247], a[156:159], a[52:55], v[244:247]\n"
    "v_mfma_f32_16x16x32_bf16 v[248:251], a[140:143], a[56:59], v[248:251]\n"
    "s_add_u32 s54, s54, 4096\n"
    "v_mfma_f32_16x16x32_bf16 v[248:251], a[136:139], a[52:55], v[248:251]\n"
    "v_mfma_f32_16x16x32_bf16 v[248:251], a[132:135], a[24:27], v[248:251]\n"
    "v_mfma_f32_16x16x32_bf16 v[248:251], a[128:131], a[20:23], v[248:251]\n"
    "v_mfma_f32_16x16x32_bf16 v[252:255], a[144:147], a[20:23], v[252:255]\n"
    "ds_read_b128 a[20:23], %[rd0] offset:59392\n"
    "v_mfma_f32_16x16x32_bf16 v[252:255], a[148:151], a[24:27], v[252:255]\n"
    "v_mfma_f32_16x16x32_bf16 v[252:255], a[152:155], a[52:55], v[252:255]\n"
    "ds_read_b128 a[52:55], %[rd1] offset:59392\n"
    "v_mfma_f32_16x16x32_bf16 v[252:255], a[156:159], a[56:59], v[252:255]\n"
    "s_waitcnt lgkmcnt(12)\n"
    "v_mfma_f32_16x16x32_bf16 a[176:179], a[140:143], a[60:63], a[176:179]\n"
    "s_waitcnt vmcnt(12)\n"
    "v_cmp_gt_u32 vcc, 32, v79\n"
    "v_mfma_f32_16x16x32_bf16 a[176:179], a[136:139], a[56:59], a[176:179]\n"
    "s_cbranch_vccnz Lqs%=\n"
    "Lqb%=:\n"
    "v_mfma_f32_16x16x32_bf16 a[176:179], a[132:135], a[28:31], a[176:179]\n"
    "v_mfma_f32_16x16x32_bf16 a[176:179], a[128:131], a[24:27], a[176:179]\n"
    "v_mfma_f32_16x16x32_bf16 a[180:183], a[144:147], a[24:27], a[180:183]\n"
    "ds_read_b128 a[24:27], %[rd0] offset:61440\n"
    "v_mfma_f32_16x16x32_bf16 a[180:183], a[148:151], a[28:31], a[180:183]\n"
    "v_mfma_f32_16x16x32_bf16 a[180:183], a[152:155], a[56:59], a[180:183]\n"
    "ds_read_b128 a[56:59], %[rd1] offset:61440\n"
    "v_mfma_f32_16x16x32_bf16 a[180:183], a[156:159], a[60:63], a[180:183]\n"
    "v_mfma_f32_16x16x32_bf16 a[184:187], a[140:143], a[32:35], a[184:187]\n"
    "v_mfma_f32_16x16x32_bf16 a[184:187], a[136:139], a[60:63], a[184:187]\n"
    "s_add_u32 m0, s69, 0\n"
    "s_add_u32 s57, s70, 0\n"
    "v_mfma_f32_16x16x32_bf16 a[184:187], a[132:135], a[0:3], a[184:187]\n"
    "buffer_load_dwordx4 %[l16], %[srdD], s57 offen sc1 lds\n"
    "v_mfma_f32_16x16x32_bf16 a[184:187], a[128:131], a[28:31], a[184:187]\n"
    "v_mfma_f32_16x16x32_bf16 a[188:191], a[144:147], a[28:31], a[188:191]\n"
    "ds_read_b128 a[28:31], %[rd0] offset:63488\n"
    "v_mfma_f32_16x16x32_bf16 a[188:191], a[148:151], a[0:3], a[188:191]\n"
    "v_mfma_f32_16x16x32_bf16 a[188:191], a[152:155], a[60:63], a[188:191]\n"
    "ds_read_b128 a[60:63], %[rd1] offset:63488\n"
    "v_mfma_f32_16x16x32_bf16 a[188:191], a[156:159], a[32:35], a[188:191]\n"
    "s_waitcnt lgkmcnt(11)\n"
    "v_mfma_f32_16x16x32_bf16 a[192:195], a[140:143], a[36:39], a[192:195]\n"
    "v_xor_b32 %[rd0], 0x10000, %[rd0]\n"
    "v_mfma_f32_16x16x32_bf16 a[192:195], a[136:139], a[32:35], a[192:195]\n"
    "v_xor_b32 %[rd1], 0x10000, %[rd1]\n"
    "v_mfma_f32_16x16x32_bf16 a[192:195], a[132:135], a[4:7], a[192:195]\n"
    "s_add_u32 s63, s63, 1\n"
    "v_mfma_f32_16x16x32_bf16 a[192:195], a[128:131], a[0:3], a[192:195]\n"
    "v_mfma_f32_16x16x32_bf16 a[196:199], a[144:147], a[0:3], a[196:199]\n"
    "s_waitcnt lgkmcnt(0)\n"
    "v_mov_b32 v13, s63\n"
    "v_mfma_f32_16x16x32_bf16 a[196:199], a[148:151], a[4:7], a[196:199]\n"
    "ds_write_b32 v14, v13\n"
    "v_mfma_f32_16x16x32_bf16 a[196:199], a[152:155], a[32:35], a[196:199]\n"
    "v_mfma_f32_16x16x32_bf16 a[196:199], a[156:159], a[36:39], a[196:199]\n"
    "v_mfma_f32_16x16x32_bf16 a[200:203], a[140:143], a[40:43], a[200:203]\n"
    "v_xor_b32 %[re0], 0x10000, %[re0]\n"
    "v_mfma_f32_16x16x32_bf16 a[200:203], a[136:139], a[36:39], a[200:203]\n"
    "v_xor_b32 %[re1], 0x10000, %[re1]\n"
    "v_mfma_f32_16x16x32_bf16 a[200:203], a[132:135], a[8:11], a[200:203]\n"
    "ds_read_b32 v13, v12\n"
    "v_mfma_f32_16x16x32_bf16 a[200:203], a[128:131], a[4:7], a[200:203]\n"
    "v_mfma_f32_16x16x32_bf16 a[204:207], a[144:147], a[4:7], a[204:207]\n"
    "v_mfma_f32_16x16x32_bf16 a[204:207], a[148:151], a[8:11], a[204:207]\n"
    "v_mfma_f32_16x16x32_bf16 a[204:207], a[152:155], a[36:39], a[204:207]\n"
    "s_waitcnt lgkmcnt(0)\n"
    "v_cmp_gt_u32 vcc, s63, v13\n"
    "v_mfma_f32_16x16x32_bf16 a[204:207], a[156:159], a[40:43], a[204:207]\n"
    "s_cbranch_vccnz Ls10%=\n"
    "Lb10%=:\n"
    "v_mfma_f32_16x16x32_bf16 a[208:211], a[140:143], a[44:47], a[208:211]\n"
    "ds_read_b128 a[0:3], %[rd0] offset:0\n"
    "v_mfma_f32_16x16x32_bf16 a[208:211], a[136:139], a[40:43], a[208:211]\n"
    "ds_read_b128 a[32:35], %[rd1] offset:0\n"
    "v_mfma_f32_16x16x32_bf16 a[208:211], a[132:135], a[12:15], a[208:211]\n"
    "ds_read_b128 a[4:7], %[rd0] offset:2048\n"
    "v_mfma_f32_16x16x32_bf16 a[208:211], a[128:131], a[8:11], a[208:211]\n"
    "ds_read_b128 a[36:39], %[rd1] offset:2048\n"
    "v_mfma_f32_16x16x32_bf16 a[212:215], a[144:147], a[8:11], a[212:215]\n"
    "ds_read_b128 a[8:11], %[rd0] offset:4096\n"
    "v_mfma_f32_16x16x32_bf16 a[212:215], a[148:151], a[12:15], a[212:215]\n"
    "v_mfma_f32_16x16x32_bf16 a[212:215], a[152:155], a[40:43], a[212:215]\n"
    "ds_read_b128 a[40:43], %[rd1] offset:4096\n"
    "v_mfma_f32_16x16x32_bf16 a[212:215], a[156:159], a[44:47], a[212:215]\n"
    "v_mfma_f32_16x16x32_bf16 a[216:219], a[140:143], a[48:51], a[216:219]\n"
    "s_add_u32 m0, s69, 4096\n"
    "s_add_u32 s57, s70, 4096\n"
    "v_mfma_f32_16x16x32_bf16 a[216:219], a[136:139], a[44:47], a[216:219]\n"
    "buffer_load_dwordx4 %[l16], %[srdD], s57 offen sc1 lds\n"
    "v_mfma_f32_16x16x32_bf16 a[216:219], a[132:135], a[16:19], a[216:219]\n"
    "s_add_u32 m0, s69, 8192\n"
    "s_add_u32 s57, s70, 8192\n"
    "v_mfma_f32_16x16x32_bf16 a[216:219], a[128:131], a[12:15], a[216:219]\n"
    "buffer_load_dwordx4 %[l16], %[srdD], s57 offen sc1 lds\n"
    "v_mfma_f32_16x16x32_bf16 a[220:223], a[144:147], a[12:15], a[220:223]\n"
    "ds_read_b128 a[12:15], %[rd0] offset:6144\n"
    "v_mfma_f32_16x16x32_bf16 a[220:223], a[148:151], a[16:19], a[220:223]\n"
    "v_mfma_f32_16x16x32_bf16 a[220:223], a[152:155], a[44:47], a[220:223]\n"
    "ds_read_b128 a[44:47], %[rd1] offset:6144\n"
    "v_mfma_f32_16x16x32_bf16 a[220:223], a[156:159], a[48:51], a[220:223]\n"
    "v_mfma_f32_16x16x32_bf16 a[224:227], a[140:143], a[52:55], a[224:227]\n"
    "v_mfma_f32_16x16x32_bf16 a[224:227], a[136:139], a[48:51], a[224:227]\n"
    "s_add_u32 m0, s69, 12288\n"
    "s_add_u32 s57, s70, 12288\n"
    "v_mfma_f32_16x16x32_bf16 a[224:227], a[132:135], a[20:23], a[224:227]\n"
    "buffer_load_dwordx4 %[l16], %[srdD], s57 offen sc1 lds\n"
    "v_mfma_f32_16x16x32_bf16 a[224:227], a[128:131], a[16:19], a[224:227]\n"
    "v_mfma_f32_16x16x32_bf16 a[228:231], a[144:147], a[16:19], a[228:231]\n"
    "ds_read_b128 a[16:19], %[rd0] offset:8192\n"
    "v_mfma_f32_16x16x32_bf16 a[228:231], a[148:151], a[20:23], a[228:231]\n"
    "v_mfma_f32_16x16x32_bf16 a[228:231], a[152:155], a[48:51], a[228:231]\n"
    "ds_read_b128 a[48:51], %[rd1] offset:8192\n"
    "v_mfma_f32_16x16x32_bf16 a[228:231], a[156:159], a[52:55], a[228:231]\n"
    "v_mfma_f32_16x16x32_bf16 a[232:235], a[140:143], a[56:59], a[232:235]\n"
    "s_add_u32 m0, s69, 16384\n"
    "s_add_u32 s57, s70, 16384\n"
    "v_mfma_f32_16x16x32_bf16 a[232:235], a[136:139], a[52:55], a[232:235]\n"
    "buffer_load_dwordx4 %[l16], %[srdD], s57 offen sc1 lds\n"
    "v_mfma_f32_16x16x32_bf16 a[232:235], a[132:135], a[24:27], a[232:235]\n"
    "s_add_u32 m0, s69, 20480\n"
    "s_add_u32 s57, s70, 20480\n"
    "v_mfma_f32_16x16x32_bf16 a[232:235], a[128:131], a[20:23], a[232:235]\n"
    "buffer_load_dwordx4 %[l16], %[srdD], s57 offen sc1 lds\n"
    "v_mfma_f32_16x16x32_bf16 a[236:239], a[144:147], a[20:23], a[236:239]\n"
    "ds_read_b128 a[20:23], %[rd0] offset:10240\n"
    "v_mfma_f32_16x16x32_bf16 a[236:239], a[148:151], a[24:27], a[236:239]\n"
    "v_mfma_f32_16x16x32_bf16 a[236:239], a[152:155], a[52:55], a[236:239]\n"
    "ds_read_b128 a[52:55], %[rd1] offset:10240\n"
    "v_mfma_f32_16x16x32_bf16 a[236:239], a[156:159], a[56:59], a[236:239]\n"
    "v_mfma_f32_16x16x32_bf16 a[240:243], a[140:143], a[60:63], a[240:243]\n"
    "v_mfma_f32_16x16x32_bf16 a[240:243], a[136:139], a[56:59], a[240:243]\n"
    "s_add_u32 m0, s69, 24576\n"
    "s_add_u32 s57, s70, 24576\n"
    "v_mfma_f32_16x16x32_bf16 a[240:243], a[132:135], a[28:31], a[240:243]\n"
    "buffer_load_dwordx4 %[l16], %[srdD], s57 offen sc1 lds\n"
    "v_mfma_f32_16x16x32_bf16 a[240:243], a[128:131], a[24:27], a[240:243]\n"
    "v_mfma_f32_16x16x32_bf16 a[244:247], a[144:147], a[24:27], a[244:247]\n"
    "ds_read_b128 a[24:27], %[rd0] offset:12288\n"
    "v_mfma_f32_16x16x32_bf16 a[244:247], a[148:151], a[28:31], a[244:247]\n"
    "v_mfma_f32_16x16x32_bf16 a[244:247], a[152:155], a[56:59], a[244:247]\n"
    "ds_read_b128 a[56:59], %[rd1] offset:12288\n"
    "v_mfma_f32_16x16x32_bf16 a[244:247], a[156:159], a[60:63], a[244:247]\n"
    "v_mfma_f32_16x16x32_bf16 a[248:251], a[140:143], a[72:75], a[248:251]\n"
    "v_mfma_f32_16x16x32_bf16 a[248:251], a[136:139], a[60:63], a[248:251]\n"
    "s_add_u32 m0, s69, 28672\n"
    "s_add_u32 s57, s70, 28672\n"
    "v_mfma_f32_16x16x32_bf16 a[248:251], a[132:135], a[64:67], a[248:251]\n"
    "buffer_load_dwordx4 %[l16], %[srdD], s57 offen sc1 lds\n"
    "v_mfma_f32_16x16x32_bf16 a[248:251], a[128:131], a[28:31], a[248:251]\n"
    "s_add_u32 s70, s70, 0x8000\n"
    "v_mfma_f32_16x16x32_bf16 a[252:255], a[144:147], a[28:31], a[252:255]\n"
    "ds_read_b128 a[28:31], %[rd0] offset:14336\n"
    "v_mfma_f32_16x16x32_bf16 a[252:255], a[148:151], a[64:67], a[252:255]\n"
    "s_add_u32 s69, s69, 0x8000\n"
    "s_and_b32 s69, s69, 0x1ffff\n"
    "v_mfma_f32_16x16x32_bf16 a[252:255], a[152:155], a[60:63], a[252:255]\n"
    "ds_read_b128 a[60:63], %[rd1] offset:14336\n"
    "v_mfma_f32_16x16x32_bf16 a[252:255], a[156:159], a[72:75], a[252:255]\n"
    "s_sub_u32 s59, s59, 1\n"
    "s_cmp_lg_u32 s59, 0\n"
    "s_cbranch_scc1 Lloop%=\n"
    "s_waitcnt vmcnt(8)\n"
    "s_waitcnt lgkmcnt(10)\n"
    "s_cmp_lg_u32 s67, 0\n"
    "s_cbranch_scc1 Lc11%=\n"
    "v_mfma_f32_16x16x32_bf16 v[208:211], a[92:95], a[36:39], v[208:211]\n"
    "v_mfma_f32_16x16x32_bf16 v[208:211], a[88:91], a[32:35], v[208:211]\n"
    "v_mfma_f32_16x16x32_bf16 v[208:211], a[84:87], a[4:7], v[208:211]\n"
    "v_mfma_f32_16x16x32_bf16 v[208:211], a[80:83], a[0:3], v[208:211]\n"
    "v_mfma_f32_16x16x32_bf16 v[212:215], a[96:99], a[0:3], v[212:215]\n"
    "v_mfma_f32_16x16x32_bf16 v[212:215], a[100:103], a[4:7], v[212:215]\n"
    "v_mfma_f32_16x16x32_bf16 v[212:215], a[104:107], a[32:35], v[212:215]\n"
    "v_mfma_f32_16x16x32_bf16 v[212:215], a[108:111], a[36:39], v[212:215]\n"
    "Lc11%=:\n"
    "v_mfma_f32_16x16x32_bf16 v[216:219], a[92:95], a[40:43], v[216:219]\n"
    "ds_read_b128 a[0:3], %[rd0] offset:16384\n"
    "v_mfma_f32_16x16x32_bf16 v[216:219], a[88:91], a[36:39], v[216:219]\n"
    "v_mfma_f32_16x16x32_bf16 v[216:219], a[84:87], a[8:11], v[216:219]\n"
    "buffer_load_dwordx4 a[128:131], %[l16], %[srdW], s54 offen offset:0\n"
    "v_mfma_f32_16x16x32_bf16 v[216:219], a[80:83], a[4:7], v[216:219]\n"
    "ds_read_b128 a[32:35], %[rd1] offset:16384\n"
    "v_mfma_f32_16x16x32_bf16 v[220:223], a[96:99], a[4:7], v[220:223]\n"
    "ds_read_b128 a[4:7], %[rd0] offset:18432\n"
    "v_mfma_f32_16x16x32_bf16 v[220:223], a[100:103], a[8:11], v[220:223]\n"
    "buffer_load_dwordx4 a[132:135], %[l16], %[srdW], s54 offen offset:1024\n"
    "v_mfma_f32_16x16x32_bf16 v[220:223], a[104:107], a[36:39], v[220:223]\n"
    "ds_read_b128 a[36:39], %[rd1] offset:18432\n"
    "v_mfma_f32_16x16x32_bf16 v[220:223], a[108:111], a[40:43], v[220:223]\n"
    "s_waitcnt lgkmcnt(10)\n"
    "v_mfma_f32_16x16x32_bf16 v[224:227], a[92:95], a[44:47], v[224:227]\n"
    "v_mfma_f32_16x16x32_bf16 v[224:227], a[88:91], a[40:43], v[224:227]\n"
    "ds_read_b128 a[64:67], %[re0] offset:0\n"
    "v_mfma_f32_16x16x32_bf16 v[224:227], a[84:87], a[12:15], v[224:227]\n"
    "buffer_load_dwordx4 a[136:139], %[l16], %[srdW], s54 offen offset:2048\n"
    "v_mfma_f32_16x16x32_bf16 v[224:227], a[80:83], a[8:11], v[224:227]\n"
    "v_mfma_f32_16x16x32_bf16 v[228:231], a[96:99], a[8:11], v[228:231]\n"
    "ds_read_b128 a[8:11], %[rd0] offset:20480\n"
    "v_mfma_f32_16x16x32_bf16 v[228:231], a[100:103], a[12:15], v[228:231]\n"
    "buffer_load_dwordx4 a[140:143], %[l16], %[srdW], s54 offen offset:3072\n"
    "v_mfma_f32_16x16x32_bf16 v[228:231], a[104:107], a[40:43], v[228:231]\n"
    "ds_read_b128 a[40:43], %[rd1] offset:20480\n"
    "v_mfma_f32_16x16x32_bf16 v[228:231], a[108:111], a[44:47], v[228:231]\n"
    "v_mfma_f32_16x16x32_bf16 v[232:235], a[92:95], a[48:51], v[232:235]\n"
    "s_add_u32 s54, s54, 4096\n"
    "v_mfma_f32_16x16x32_bf16 v[232:235], a[88:91], a[44:47], v[232:235]\n"
    "ds_read_b128 a[72:75], %[re1] offset:0\n"
    "v_mfma_f32_16x16x32_bf16 v[232:235], a[84:87], a[16:19], v[232:235]\n"
    "buffer_load_dwordx4 a[144:147], %[l16], %[srdW], s54 offen offset:0\n"
    "v_mfma_f32_16x16x32_bf16 v[232:235], a[80:83], a[12:15], v[232:235]\n"
    "v_mfma_f32_16x16x32_bf16 v[236:239], a[96:99], a[12:15], v[236:239]\n"
    "ds_read_b128 a[12:15], %[rd0] offset:22528\n"
    "v_mfma_f32_16x16x32_bf16 v[236:239], a[100:103], a[16:19], v[236:239]\n"
    "buffer_load_dwordx4 a[148:151], %[l16], %[srdW], s54 offen offset:1024\n"
    "v_mfma_f32_16x16x32_bf16 v[236:239], a[104:107], a[44:47], v[236:239]\n"
    "ds_read_b128 a[44:47], %[rd1] offset:22528\n"
    "v_mfma_f32_16x16x32_bf16 v[236:239], a[108:111], a[48:51], v[236:239]\n"
    "s_waitcnt lgkmcnt(12)\n"
    "v_mfma_f32_16x16x32_bf16 v[240:243], a[92:95], a[52:55], v[240:243]\n"
    "v_mfma_f32_16x16x32_bf16 v[240:243], a[88:91], a[48:51], v[240:243]\n"
    "v_mfma_f32_16x16x32_bf16 v[240:243], a[84:87], a[20:23], v[240:243]\n"
    "buffer_load_dwordx4 a[152:155], %[l16], %[srdW], s54 offen offset:2048\n"
    "v_mfma_f32_16x16x32_bf16 v[240:243], a[80:83], a[16:19], v[240:243]\n"
    "v_mfma_f32_16x16x32_bf16 v[244:247], a[96:99], a[16:19], v[244:247]\n"
    "ds_read_b128 a[16:19], %[rd0] offset:24576\n"
    "v_mfma_f32_16x16x32_bf16 v[244:247], a[100:103], a[20:23], v[244:247]\n"
    "buffer_load_dwordx4 a[156:159], %[l16], %[srdW], s54 offen offset:3072\n"
    "v_mfma_f32_16x16x32_bf16 v[244:247], a[104:107], a[48:51], v[244:247]\n"
    "ds_read_b128 a[48:51], %[rd1] offset:24576\n"
    "v_mfma_f32_16x16x32_bf16 v[244:247], a[108:111], a[52:55], v[244:247]\n"
    "v_mfma_f32_16x16x32_bf16 v[248:251], a[92:95], a[56:59], v[248:251]\n"
    "s_lshl_b32 s56, s62, 30\n"
    "s_add_u32 s54, s54, 4096\n"
    "v_mfma_f32_16x16x32_bf16 v[248:251], a[88:91], a[52:55], v[248:251]\n"
    "v_mfma_f32_16x16x32_bf16 v[248:251], a[84:87], a[24:27], v[248:251]\n"
    "v_mfma_f32_16x16x32_bf16 v[248:251], a[80:83], a[20:23], v[248:251]\n"
    "s_mul_i32 s58, s66, %[prs]\n"
    "s_sub_u32 s58, s68, s58\n"
    "v_mfma_f32_16x16x32_bf16 v[252:255], a[96:99], a[20:23], v[252:255]\n"
    "ds_read_b128 a[20:23], %[rd0] offset:26624\n"
    "v_mfma_f32_16x16x32_bf16 v[252:255], a[100:103], a[24:27], v[252:255]\n"
    "v_mfma_f32_16x16x32_bf16 v[252:255], a[104:107], a[52:55], v[252:255]\n"
    "ds_read_b128 a[52:55], %[rd1] offset:26624\n"
    "v_mfma_f32_16x16x32_bf16 v[252:255], a[108:111], a[56:59], v[252:255]\n"
    "s_waitcnt lgkmcnt(12)\n"
    "s_add_u32 s71, s71, 0x60000\n"
    "v_add_u32 %[xr1], s58, %[xr1]\n"
    "v_mfma_f32_16x16x32_bf16 a[176:179], a[92:95], a[60:63], a[176:179]\n"
    "v_subrev_u32 v76, 0x2000, %[xr1]\n"
    "v_mfma_f32_16x16x32_bf16 a[176:179], a[88:91], a[56:59], a[176:179]\n"
    "s_add_u32 s64, s56, 0\n"
    "buffer_load_dwordx4 v[32:35], v76, %[srdX], s64 offen\n"
    "v_mfma_f32_16x16x32_bf16 a[176:179], a[84:87], a[28:31], a[176:179]\n"
    "v_mfma_f32_16x16x32_bf16 a[176:179], a[80:83], a[24:27], a[176:179]\n"
    "v_mfma_f32_16x16x32_bf16 a[180:183], a[96:99], a[24:27], a[180:183]\n"
    "ds_read_b128 a[24:27], %[rd0] offset:28672\n"
    "v_mfma_f32_16x16x32_bf16 a[180:183], a[100:103], a[28:31], a[180:183]\n"
    "v_mfma_f32_16x16x32_bf16 a[180:183], a[104:107], a[56:59], a[180:183]\n"
    "ds_read_b128 a[56:59], %[rd1] offset:28672\n"
    "v_mfma_f32_16x16x32_bf16 a[180:183], a[108:111], a[60:63], a[180:183]\n"
    "v_mfma_f32_16x16x32_bf16 a[184:187], a[92:95], a[32:35], a[184:187]\n"
    "v_mfma_f32_16x16x32_bf16 a[184:187], a[88:91], a[60:63], a[184:187]\n"
    "s_add_u32 m0, s69, 0\n"
    "s_add_u32 s57, s70, 0\n"
    "v_mfma_f32_16x16x32_bf16 a[184:187], a[84:87], a[0:3], a[184:187]\n"
    "buffer_load_dwordx4 %[l16], %[srdD], s57 offen sc1 lds\n"
    "v_mfma_f32_16x16x32_bf16 a[184:187], a[80:83], a[28:31], a[184:187]\n"
    "v_mfma_f32_16x16x32_bf16 a[188:191], a[96:99], a[28:31], a[188:191]\n"
    "ds_read_b128 a[28:31], %[rd0] offset:30720\n"
    "v_mfma_f32_16x16x32_bf16 a[188:191], a[100:103], a[0:3], a[188:191]\n"
    "v_mfma_f32_16x16x32_bf16 a[188:191], a[104:107], a[60:63], a[188:191]\n"
    "ds_read_b128 a[60:63], %[rd1] offset:30720\n"
    "v_mfma_f32_16x16x32_bf16 a[188:191], a[108:111], a[32:35], a[188:191]\n"
    "s_waitcnt lgkmcnt(11)\n"
    "v_mfma_f32_16x16x32_bf16 a[192:195], a[92:95], a[36:39], a[192:195]\n"
    "v_mfma_f32_16x16x32_bf16 a[192:195], a[88:91], a[32:35], a[192:195]\n"
    "v_mfma_f32_16x16x32_bf16 a[192:195], a[84:87], a[4:7], a[192:195]\n"
    "s_add_u32 s63, s63, 1\n"
    "v_mfma_f32_16x16x32_bf16 a[192:195], a[80:83], a[0:3], a[192:195]\n"
    "v_mfma_f32_16x16x32_bf16 a[196:199], a[96:99], a[0:3], a[196:199]\n"
    "s_waitcnt lgkmcnt(0)\n"
    "v_mov_b32 v13, s63\n"
    "v_mfma_f32_16x16x32_bf16 a[196:199], a[100:103], a[4:7], a[196:199]\n"
    "ds_write_b32 v14, v13\n"
    "v_mfma_f32_16x16x32_bf16 a[196:199], a[104:107], a[32:35], a[196:199]\n"
    "v_mfma_f32_16x16x32_bf16 a[196:199], a[108:111], a[36:39], a[196:199]\n"
    "v_mfma_f32_16x16x32_bf16 a[200:203], a[92:95], a[40:43], a[200:203]\n"
    "v_mfma_f32_16x16x32_bf16 a[200:203], a[88:91], a[36:39], a[200:203]\n"
    "v_mfma_f32_16x16x32_bf16 a[200:203], a[84:87], a[8:11], a[200:203]\n"
    "ds_read_b32 v13, v12\n"
    "v_mfma_f32_16x16x32_bf16 a[200:203], a[80:83], a[4:7], a[200:203]\n"
    "v_mfma_f32_16x16x32_bf16 a[204:207], a[96:99], a[4:7], a[204:207]\n"
    "v_mfma_f32_16x16x32_bf16 a[204:207], a[100:103], a[8:11], a[204:207]\n"
    "v_mfma_f32_16x16x32_bf16 a[204:207], a[104:107], a[36:39], a[204:207]\n"
    "s_waitcnt lgkmcnt(0)\n"
    "v_cmp_gt_u32 vcc, s63, v13\n"
    "v_mfma_f32_16x16x32_bf16 a[204:207], a[108:111], a[40:43], a[204:207]\n"
    "s_cbranch_vccnz Ls11%=\n"
    "Lb11%=:\n"
    "v_mfma_f32_16x16x32_bf16 a[208:211], a[92:95], a[44:47], a[208:211]\n"
    "ds_read_b128 a[0:3], %[rd0] offset:32768\n"
    "v_mfma_f32_16x16x32_bf16 a[208:211], a[88:91], a[40:43], a[208:211]\n"
    "ds_read_b128 a[32:35], %[rd1] offset:32768\n"
    "v_mfma_f32_16x16x32_bf16 a[208:211], a[84:87], a[12:15], a[208:211]\n"
    "ds_read_b128 a[4:7], %[rd0] offset:34816\n"
    "v_mfma_f32_16x16x32_bf16 a[208:211], a[80:83], a[8:11], a[208:211]\n"
    "ds_read_b128 a[36:39], %[rd1] offset:34816\n"
    "v_mfma_f32_16x16x32_bf16 a[212:215], a[96:99], a[8:11], a[212:215]\n"
    "ds_read_b128 a[8:11], %[rd0] offset:36864\n"
    "v_mfma_f32_16x16x32_bf16 a[212:215], a[100:103], a[12:15], a[212:215]\n"
    "v_mfma_f32_16x16x32_bf16 a[212:215], a[104:107], a[40:43], a[212:215]\n"
    "ds_read_b128 a[40:43], %[rd1] offset:36864\n"
    "v_mfma_f32_16x16x32_bf16 a[212:215], a[108:111], a[44:47], a[212:215]\n"
    "v_mfma_f32_16x16x32_bf16 a[216:219], a[92:95], a[48:51], a[216:219]\n"
    "s_add_u32 m0, s69, 4096\n"
    "s_add_u32 s57, s70, 4096\n"
    "v_mfma_f32_16x16x32_bf16 a[216:219], a[88:91], a[44:47], a[216:219]\n"
    "buffer_load_dwordx4 %[l16], %[srdD], s57 offen sc1 lds\n"
    "v_mfma_f32_16x16x32_bf16 a[216:219], a[84:87], a[16:19], a[216:219]\n"
    "s_add_u32 m0, s69, 8192\n"
    "s_add_u32 s57, s70, 8192\n"
    "v_mfma_f32_16x16x32_bf16 a[216:219], a[80:83], a[12:15], a[216:219]\n"
    "buffer_load_dwordx4 %[l16], %[srdD], s57 offen sc1 lds\n"
    "v_mfma_f32_16x16x32_bf16 a[220:223], a[96:99], a[12:15], a[220:223]\n"
    "ds_read_b128 a[12:15], %[rd0] offset:38912\n"
    "v_mfma_f32_16x16x32_bf16 a[220:223], a[100:103], a[16:19], a[220:223]\n"
    "v_mfma_f32_16x16x32_bf16 a[220:223], a[104:107], a[44:47], a[220:223]\n"
    "ds_read_b128 a[44:47], %[rd1] offset:38912\n"
    "v_mfma_f32_16x16x32_bf16 a[220:223], a[108:111], a[48:51], a[220:223]\n"
    "v_mfma_f32_16x16x32_bf16 a[224:227], a[92:95], a[52:55], a[224:227]\n"
    "v_mfma_f32_16x16x32_bf16 a[224:227], a[88:91], a[48:51], a[224:227]\n"
    "s_add_u32 m0, s69, 12288\n"
    "s_add_u32 s57, s70, 12288\n"
    "v_mfma_f32_16x16x32_bf16 a[224:227], a[84:87], a[20:23], a[224:227]\n"
    "buffer_load_dwordx4 %[l16], %[srdD], s57 offen sc1 lds\n"
    "v_mfma_f32_16x16x32_bf16 a[224:227], a[80:83], a[16:19], a[224:227]\n"
    "v_mfma_f32_16x16x32_bf16 a[228:231], a[96:99], a[16:19], a[228:231]\n"
    "ds_read_b128 a[16:19], %[rd0] offset:40960\n"
    "v_mfma_f32_16x16x32_bf16 a[228:231], a[100:103], a[20:23], a[228:231]\n"
    "v_mfma_f32_16x16x32_bf16 a[228:231], a[104:107], a[48:51], a[228:231]\n"
    "ds_read_b128 a[48:51], %[rd1] offset:40960\n"
    "v_mfma_f32_16x16x32_bf16 a[228:231], a[108:111], a[52:55], a[228:231]\n"
    "v_mfma_f32_16x16x32_bf16 a[232:235], a[92:95], a[56:59], a[232:235]\n"
    "s_add_u32 m0, s69, 16384\n"
    "s_add_u32 s57, s70, 16384\n"
    "v_mfma_f32_16x16x32_bf16 a[232:235], a[88:91], a[52:55], a[232:235]\n"
    "buffer_load_dwordx4 %[l16], %[srdD], s57 offen sc1 lds\n"
    "v_mfma_f32_16x16x32_bf16 a[232:235], a[84:87], a[24:27], a[232:235]\n"
    "s_add_u32 m0, s69, 20480\n"
    "s_add_u32 s57, s70, 20480\n"
    "v_mfma_f32_16x16x32_bf16 a[232:235], a[80:83], a[20:23], a[232:235]\n"
    "buffer_load_dwordx4 %[l16], %[srdD], s57 offen sc1 lds\n"
    "v_mfma_f32_16x16x32_bf16 a[236:239], a[96:99], a[20:23], a[236:239]\n"
    "ds_read_b128 a[20:23], %[rd0] offset:43008\n"
    "v_mfma_f32_16x16x32_bf16 a[236:239], a[100:103], a[24:27], a[236:239]\n"
    "v_mfma_f32_16x16x32_bf16 a[236:239], a[104:107], a[52:55], a[236:239]\n"
    "ds_read_b128 a[52:55], %[rd1] offset:43008\n"
    "v_mfma_f32_16x16x32_bf16 a[236:239], a[108:111], a[56:59], a[236:239]\n"
    "v_mfma_f32_16x16x32_bf16 a[240:243], a[92:95], a[60:63], a[240:243]\n"
    "v_mfma_f32_16x16x32_bf16 a[240:243], a[88:91], a[56:59], a[240:243]\n"
    "s_add_u32 m0, s69, 24576\n"
    "s_add_u32 s57, s70, 24576\n"
    "v_mfma_f32_16x16x32_bf16 a[240:243], a[84:87], a[28:31], a[240:243]\n"
    "buffer_load_dwordx4 %[l16], %[srdD], s57 offen sc1 lds\n"
    "v_mfma_f32_16x16x32_bf16 a[240:243], a[80:83], a[24:27], a[240:243]\n"
    "v_mfma_f32_16x16x32_bf16 a[244:247], a[96:99], a[24:27], a[244:247]\n"
    "ds_read_b128 a[24:27], %[rd0] offset:45056\n"
    "v_mfma_f32_16x16x32_bf16 a[244:247], a[100:103], a[28:31], a[244:247]\n"
    "v_mfma_f32_16x16x32_bf16 a[244:247], a[104:107], a[56:59], a[244:247]\n"
    "ds_read_b128 a[56:59], %[rd1] offset:45056\n"
    "v_mfma_f32_16x16x32_bf16 a[244:247], a[108:111], a[60:63], a[244:247]\n"
    "v_mfma_f32_16x16x32_bf16 a[248:251], a[92:95], a[72:75], a[248:251]\n"
    "v_mfma_f32_16x16x32_bf16 a[248:251], a[88:91], a[60:63], a[248:251]\n"
    "s_add_u32 m0, s69, 28672\n"
    "s_add_u32 s57, s70, 28672\n"
    "v_mfma_f32_16x16x32_bf16 a[248:251], a[84:87], a[64:67], a[248:251]\n"
    "buffer_load_dwordx4 %[l16], %[srdD], s57 offen sc1 lds\n"
    "v_mfma_f32_16x16x32_bf16 a[248:251], a[80:83], a[28:31], a[248:251]\n"
    "s_add_u32 s70, s70, 0x8000\n"
    "v_mfma_f32_16x16x32_bf16 a[252:255], a[96:99], a[28:31], a[252:255]\n"
    "ds_read_b128 a[28:31], %[rd0] offset:47104\n"
    "v_mfma_f32_16x16x32_bf16 a[252:255], a[100:103], a[64:67], a[252:255]\n"
    "s_add_u32 s69, s69, 0x8000\n"
    "s_and_b32 s69, s69, 0x1ffff\n"
    "v_mfma_f32_16x16x32_bf16 a[252:255], a[104:107], a[60:63], a[252:255]\n"
    "ds_read_b128 a[60:63], %[rd1] offset:47104\n"
    "v_mfma_f32_16x16x32_bf16 a[252:255], a[108:111], a[72:75], a[252:255]\n"
    "s_waitcnt vmcnt(9)\n"
    "s_waitcnt lgkmcnt(10)\n"
    "s_cmp_lg_u32 s67, 0\n"
    "s_cbranch_scc1 Lc12%=\n"
    "v_mfma_f32_16x16x32_bf16 v[208:211], a[140:143], a[36:39], v[208:211]\n"
    "v_mfma_f32_16x16x32_bf16 v[208:211], a[136:139], a[32:35], v[208:211]\n"
    "v_mfma_f32_16x16x32_bf16 v[208:211], a[132:135], a[4:7], v[208:211]\n"
    "v_mfma_f32_16x16x32_bf16 v[208:211], a[128:131], a[0:3], v[208:211]\n"
    "v_mfma_f32_16x16x32_bf16 v[212:215], a[144:147], a[0:3], v[212:215]\n"
    "v_mfma_f32_16x16x32_bf16 v[212:215], a[148:151], a[4:7], v[212:215]\n"
    "v_mfma_f32_16x16x32_bf16 v[212:215], a[152:155], a[32:35], v[212:215]\n"
    "v_mfma_f32_16x16x32_bf16 v[212:215], a[156:159], a[36:39], v[212:215]\n"
    "Lc12%=:\n"
    "v_mfma_f32_16x16x32_bf16 v[216:219], a[140:143], a[40:43], v[216:219]\n"
    "ds_read_b128 a[0:3], %[rd0] offset:49152\n"
    "v_mfma_f32_16x16x32_bf16 v[216:219], a[136:139], a[36:39], v[216:219]\n"
    "v_mfma_f32_16x16x32_bf16 v[216:219], a[132:135], a[8:11], v[216:219]\n"
    "buffer_load_dwordx4 a[80:83], %[l16], %[srdW], s54 offen offset:0\n"
    "v_mfma_f32_16x16x32_bf16 v[216:219], a[128:131], a[4:7], v[216:219]\n"
    "ds_read_b128 a[32:35], %[rd1] offset:49152\n"
    "v_mfma_f32_16x16x32_bf16 v[220:223], a[144:147], a[4:7], v[220:223]\n"
    "ds_read_b128 a[4:7], %[rd0] offset:51200\n"
    "v_mfma_f32_16x16x32_bf16 v[220:223], a[148:151], a[8:11], v[220:223]\n"
    "buffer_load_dwordx4 a[84:87], %[l16], %[srdW], s54 offen offset:1024\n"
    "v_mfma_f32_16x16x32_bf16 v[220:223], a[152:155], a[36:39], v[220:223]\n"
    "ds_read_b128 a[36:39], %[rd1] offset:51200\n"
    "v_mfma_f32_16x16x32_bf16 v[220:223], a[156:159], a[40:43], v[220:223]\n"
    "s_waitcnt lgkmcnt(10)\n"
    "v_mfma_f32_16x16x32_bf16 v[224:227], a[140:143], a[44:47], v[224:227]\n"
    "v_mfma_f32_16x16x32_bf16 v[224:227], a[136:139], a[40:43], v[224:227]\n"
    "ds_read_b128 a[64:67], %[re0] offset:32768\n"
    "v_mfma_f32_16x16x32_bf16 v[224:227], a[132:135], a[12:15], v[224:227]\n"
    "buffer_load_dwordx4 a[88:91], %[l16], %[srdW], s54 offen offset:2048\n"
    "v_mfma_f32_16x16x32_bf16 v[224:227], a[128:131], a[8:11], v[224:227]\n"
    "v_mfma_f32_16x16x32_bf16 v[228:231], a[144:147], a[8:11], v[228:231]\n"
    "ds_read_b128 a[8:11], %[rd0] offset:53248\n"
    "v_mfma_f32_16x16x32_bf16 v[228:231], a[148:151], a[12:15], v[228:231]\n"
    "buffer_load_dwordx4 a[92:95], %[l16], %[srdW], s54 offen offset:3072\n"
    "v_mfma_f32_16x16x32_bf16 v[228:231], a[152:155], a[40:43], v[228:231]\n"
    "ds_read_b128 a[40:43], %[rd1] offset:53248\n"
    "v_mfma_f32_16x16x32_bf16 v[228:231], a[156:159], a[44:47], v[228:231]\n"
    "v_mfma_f32_16x16x32_bf16 v[232:235], a[140:143], a[48:51], v[232:235]\n"
    "s_add_u32 s54, s54, 4096\n"
    "v_mul_f32 v212, s73, v212\n"
    "v_mul_f32 v213, s73, v213\n"
    "v_mfma_f32_16x16x32_bf16 v[232:235], a[136:139], a[44:47], v[232:235]\n"
    "ds_read_b128 a[72:75], %[re1] offset:32768\n"
    "v_mul_f32 v214, s73, v214\n"
    "v_mfma_f32_16x16x32_bf16 v[232:235], a[132:135], a[16:19], v[232:235]\n"
    "buffer_load_dwordx4 a[96:99], %[l16], %[srdW], s54 offen offset:0\n"
    "v_mul_f32 v215, s73, v215\n"
    "v_mfma_f32_16x16x32_bf16 v[232:235], a[128:131], a[12:15], v[232:235]\n"
    "v_mul_f32 v220, s73, v220\n"
    "v_mul_f32 v221, s73, v221\n"
    "v_mfma_f32_16x16x32_bf16 v[236:239], a[144:147], a[12:15], v[236:239]\n"
    "ds_read_b128 a[12:15], %[rd0] offset:55296\n"
    "v_mul_f32 v222, s73, v222\n"
    "v_mfma_f32_16x16x32_bf16 v[236:239], a[148:151], a[16:19], v[236:239]\n"
    "buffer_load_dwordx4 a[100:103], %[l16], %[srdW], s54 offen offset:1024\n"
    "v_mul_f32 v223, s73, v223\n"
    "v_mfma_f32_16x16x32_bf16 v[236:239], a[152:155], a[44:47], v[236:239]\n"
    "ds_read_b128 a[44:47], %[rd1] offset:55296\n"
    "v_mul_f32 v208, s72, v208\n"
    "v_mfma_f32_16x16x32_bf16 v[236:239], a[156:159], a[48:51], v[236:239]\n"
    "s_waitcnt lgkmcnt(12)\n"
    "v_mul_f32 v209, s72, v209\n"
    "v_mul_f32 v210, s72, v210\n"
    "v_mfma_f32_16x16x32_bf16 v[240:243], a[140:143], a[52:55], v[240:243]\n"
    "v_mul_f32 v211, s72, v211\n"
    "v_mul_f32 v216, s72, v216\n"
    "v_mfma_f32_16x16x32_bf16 v[240:243], a[136:139], a[48:51], v[240:243]\n"
    "v_mul_f32 v217, s72, v217\n"
    "v_mul_f32 v218, s72, v218\n"
    "v_mfma_f32_16x16x32_bf16 v[240:243], a[132:135], a[20:23], v[240:243]\n"
    "buffer_load_dwordx4 a[104:107], %[l16], %[srdW], s54 offen offset:2048\n"
    "v_mul_f32 v219, s72, v219\n"
    "v_mfma_f32_16x16x32_bf16 v[240:243], a[128:131], a[16:19], v[240:243]\n"
    "v_exp_f32 v212, v212\n"
    "v_mfma_f32_16x16x32_bf16 v[244:247], a[144:147], a[16:19], v[244:247]\n"
    "ds_read_b128 a[16:19], %[rd0] offset:57344\n"
    "v_mfma_f32_16x16x32_bf16 v[244:247], a[148:151], a[20:23], v[244:247]\n"
    "buffer_load_dwordx4 a[108:111], %[l16], %[srdW], s54 offen offset:3072\n"
    "v_mfma_f32_16x16x32_bf16 v[244:247], a[152:155], a[48:51], v[244:247]\n"
    "ds_read_b128 a[48:51], %[rd1] offset:57344\n"
    "v_mfma_f32_16x16x32_bf16 v[244:247], a[156:159], a[52:55], v[244:247]\n"
    "v_exp_f32 v213, v213\n"
    "v_mfma_f32_16x16x32_bf16 v[248:251], a[140:143], a[56:59], v[248:251]\n"
    "s_add_u32 s54, s54, 4096\n"
    "v_exp_f32 v214, v214\n"
    "v_mfma_f32_16x16x32_bf16 v[248:251], a[136:139], a[52:55], v[248:251]\n"
    "v_exp_f32 v215, v215\n"
    "v_mfma_f32_16x16x32_bf16 v[248:251], a[132:135], a[24:27], v[248:251]\n"
    "buffer_load_dwordx4 a[112:115], %[l16], %[srdW], s54 offen offset:0\n"
    "v_mul_f32 v228, s73, v228\n"
    "v_mfma_f32_16x16x32_bf16 v[248:251], a[128:131], a[20:23], v[248:251]\n"
    "v_exp_f32 v220, v220\n"
    "v_mfma_f32_16x16x32_bf16 v[252:255], a[144:147], a[20:23], v[252:255]\n"
    "ds_read_b128 a[20:23], %[rd0] offset:59392\n"
    "v_mul_f32 v229, s73, v229\n"
    "v_mfma_f32_16x16x32_bf16 v[252:255], a[148:151], a[24:27], v[252:255]\n"
    "buffer_load_dwordx4 a[116:119], %[l16], %[srdW], s54 offen offset:1024\n"
    "v_mul_f32 v230, s73, v230\n"
    "v_mfma_f32_16x16x32_bf16 v[252:255], a[152:155], a[52:55], v[252:255]\n"
    "ds_read_b128 a[52:55], %[rd1] offset:59392\n"
    "v_mul_f32 v231, s73, v231\n"
    "v_mfma_f32_16x16x32_bf16 v[252:255], a[156:159], a[56:59], v[252:255]\n"
    "s_waitcnt lgkmcnt(12)\n"
    "v_exp_f32 v221, v221\n"
    "v_mfma_f32_16x16x32_bf16 a[176:179], a[140:143], a[60:63], a[176:179]\n"
    "v_exp_f32 v222, v222\n"
    "v_mfma_f32_16x16x32_bf16 a[176:179], a[136:139], a[56:59], a[176:179]\n"
    "v_exp_f32 v223, v223\n"
    "v_mfma_f32_16x16x32_bf16 a[176:179], a[132:135], a[28:31], a[176:179]\n"
    "buffer_load_dwordx4 a[120:123], %[l16], %[srdW], s54 offen offset:2048\n"
    "v_mul_f32 v236, s73, v236\n"
    "v_mfma_f32_16x16x32_bf16 a[176:179], a[128:131], a[24:27], a[176:179]\n"
    "v_exp_f32_e64 v16, -abs(v208)\n"
    "v_mfma_f32_16x16x32_bf16 a[180:183], a[144:147], a[24:27], a[180:183]\n"
    "ds_read_b128 a[24:27], %[rd0] offset:61440\n"
    "v_mul_f32 v237, s73, v237\n"
    "v_mfma_f32_16x16x32_bf16 a[180:183], a[148:151], a[28:31], a[180:183]\n"
    "buffer_load_dwordx4 a[124:127], %[l16], %[srdW], s54 offen offset:3072\n"
    "v_mul_f32 v238, s73, v238\n"
    "v_mfma_f32_16x16x32_bf16 a[180:183], a[152:155], a[56:59], a[180:183]\n"
    "ds_read_b128 a[56:59], %[rd1] offset:61440\n"
    "v_mul_f32 v239, s73, v239\n"
    "v_mfma_f32_16x16x32_bf16 a[180:183], a[156:159], a[60:63], a[180:183]\n"
    "v_exp_f32_e64 v17, -abs(v209)\n"
    "v_mfma_f32_16x16x32_bf16 a[184:187], a[140:143], a[32:35], a[184:187]\n"
    "s_add_u32 s54, s54, 4096\n"
    "v_exp_f32_e64 v18, -abs(v210)\n"
    "v_mfma_f32_16x16x32_bf16 a[184:187], a[136:139], a[60:63], a[184:187]\n"
    "s_add_u32 m0, s69, 0\n"
    "s_add_u32 s57, s70, 0\n"
    "v_mul_f32 v224, s72, v224\n"
    "v_mfma_f32_16x16x32_bf16 a[184:187], a[132:135], a[0:3], a[184:187]\n"
    "buffer_load_dwordx4 %[l16], %[srdD], s57 offen sc1 lds\n"
    "v_mul_f32 v225, s72, v225\n"
    "v_mfma_f32_16x16x32_bf16 a[184:187], a[128:131], a[28:31], a[184:187]\n"
    "s_add_u32 s64, s56, 2048\n"
    "buffer_load_dwordx4 v[36:39], v76, %[srdX], s64 offen\n"
    "v_mfma_f32_16x16x32_bf16 a[188:191], a[144:147], a[28:31], a[188:191]\n"
    "ds_read_b128 a[28:31], %[rd0] offset:63488\n"
    "v_mul_f32 v226, s72, v226\n"
    "v_mfma_f32_16x16x32_bf16 a[188:191], a[148:151], a[0:3], a[188:191]\n"
    "v_exp_f32_e64 v19, -abs(v211)\n"
    "v_mfma_f32_16x16x32_bf16 a[188:191], a[152:155], a[60:63], a[188:191]\n"
    "ds_read_b128 a[60:63], %[rd1] offset:63488\n"
    "v_bfi_b32 v208, s61, 0.5, v208\n"
    "v_mfma_f32_16x16x32_bf16 a[188:191], a[156:159], a[32:35], a[188:191]\n"
    "s_waitcnt lgkmcnt(11)\n"
    "v_bfi_b32 v209, s61, 0.5, v209\n"
    "v_bfi_b32 v210, s61, 0.5, v210\n"
    "v_mfma_f32_16x16x32_bf16 a[192:195], a[140:143], a[36:39], a[192:195]\n"
    "v_xor_b32 %[rd0], 0x10000, %[rd0]\n"
    "v_bfi_b32 v211, s61, 0.5, v211\n"
    "v_mfma_f32_16x16x32_bf16 a[192:195], a[136:139], a[32:35], a[192:195]\n"
    "v_xor_b32 %[rd1], 0x10000, %[rd1]\n"
    "v_fma_f32 v208, v16, v208, v208\n"
    "v_mfma_f32_16x16x32_bf16 a[192:195], a[132:135], a[4:7], a[192:195]\n"
    "s_add_u32 s63, s63, 1\n"
    "v_fma_f32 v209, v17, v209, v209\n"
    "v_fma_f32 v210, v18, v210, v210\n"
    "v_mfma_f32_16x16x32_bf16 a[192:195], a[128:131], a[0:3], a[192:195]\n"
    "v_fma_f32 v211, v19, v211, v211\n"
    "v_mul_f32 v227, s72, v227\n"
    "v_mfma_f32_16x16x32_bf16 a[196:199], a[144:147], a[0:3], a[196:199]\n"
    "s_waitcnt lgkmcnt(0)\n"
    "v_mov_b32 v13, s63\n"
    "v_mfma_f32_16x16x32_bf16 a[196:199], a[148:151], a[4:7], a[196:199]\n"
    "ds_write_b32 v14, v13\n"
    "v_mfma_f32_16x16x32_bf16 a[196:199], a[152:155], a[32:35], a[196:199]\n"
    "v_exp_f32_e64 v16, -abs(v216)\n"
    "v_mfma_f32_16x16x32_bf16 a[196:199], a[156:159], a[36:39], a[196:199]\n"
    "v_exp_f32_e64 v17, -abs(v217)\n"
    "v_mfma_f32_16x16x32_bf16 a[200:203], a[140:143], a[40:43], a[200:203]\n"
    "v_xor_b32 %[re0], 0x10000, %[re0]\n"
    "v_mul_f32 v232, s72, v232\n"
    "v_mfma_f32_16x16x32_bf16 a[200:203], a[136:139], a[36:39], a[200:203]\n"
    "v_xor_b32 %[re1], 0x10000, %[re1]\n"
    "v_mul_f32 v233, s72, v233\n"
    "v_mfma_f32_16x16x32_bf16 a[200:203], a[132:135], a[8:11], a[200:203]\n"
    "ds_read_b32 v13, v12\n"
    "v_mul_f32 v234, s72, v234\n"
    "v_mfma_f32_16x16x32_bf16 a[200:203], a[128:131], a[4:7], a[200:203]\n"
    "v_exp_f32_e64 v18, -abs(v218)\n"
    "v_mfma_f32_16x16x32_bf16 a[204:207], a[144:147], a[4:7], a[204:207]\n"
    "v_exp_f32_e64 v19, -abs(v219)\n"
    "v_mfma_f32_16x16x32_bf16 a[204:207], a[148:151], a[8:11], a[204:207]\n"
    "v_bfi_b32 v216, s61, 0.5, v216\n"
    "v_bfi_b32 v217, s61, 0.5, v217\n"
    "v_mfma_f32_16x16x32_bf16 a[204:207], a[152:155], a[36:39], a[204:207]\n"
    "s_waitcnt lgkmcnt(0)\n"
    "v_cmp_gt_u32 vcc, s63, v13\n"
    "v_bfi_b32 v218, s61, 0.5, v218\n"
    "v_mfma_f32_16x16x32_bf16 a[204:207], a[156:159], a[40:43], a[204:207]\n"
    "s_cbranch_vccnz Ls12%=\n"
    "Lb12%=:\n"
    "v_bfi_b32 v219, s61, 0.5, v219\n"
    "v_mfma_f32_16x16x32_bf16 a[208:211], a[140:143], a[44:47], a[208:211]\n"
    "ds_read_b128 a[0:3], %[rd0] offset:0\n"
    "v_fma_f32 v216, v16, v216, v216\n"
    "v_mfma_f32_16x16x32_bf16 a[208:211], a[136:139], a[40:43], a[208:211]\n"
    "ds_read_b128 a[32:35], %[rd1] offset:0\n"
    "v_fma_f32 v217, v17, v217, v217\n"
    "v_mfma_f32_16x16x32_bf16 a[208:211], a[132:135], a[12:15], a[208:211]\n"
    "ds_read_b128 a[4:7], %[rd0] offset:2048\n"
    "v_fma_f32 v218, v18, v218, v218\n"
    "v_mfma_f32_16x16x32_bf16 a[208:211], a[128:131], a[8:11], a[208:211]\n"
    "ds_read_b128 a[36:39], %[rd1] offset:2048\n"
    "v_fma_f32 v219, v19, v219, v219\n"
    "v_mfma_f32_16x16x32_bf16 a[212:215], a[144:147], a[8:11], a[212:215]\n"
    "ds_read_b128 a[8:11], %[rd0] offset:4096\n"
    "v_fma_f32 v212, v212, v208, v208\n"
    "v_mfma_f32_16x16x32_bf16 a[212:215], a[148:151], a[12:15], a[212:215]\n"
    "v_fma_f32 v213, v213, v209, v209\n"
    "v_fma_f32 v214, v214, v210, v210\n"
    "v_mfma_f32_16x16x32_bf16 a[212:215], a[152:155], a[40:43], a[212:215]\n"
    "ds_read_b128 a[40:43], %[rd1] offset:4096\n"
    "v_fma_f32 v215, v215, v211, v211\n"
    "v_mfma_f32_16x16x32_bf16 a[212:215], a[156:159], a[44:47], a[212:215]\n"
    "v_fma_f32 v220, v220, v216, v216\n"
    "v_fma_f32 v221, v221, v217, v217\n"
    "v_mfma_f32_16x16x32_bf16 a[216:219], a[140:143], a[48:51], a[216:219]\n"
    "s_add_u32 m0, s69, 4096\n"
    "s_add_u32 s57, s70, 4096\n"
    "v_fma_f32 v222, v222, v218, v218\n"
    "v_mfma_f32_16x16x32_bf16 a[216:219], a[136:139], a[44:47], a[216:219]\n"
    "buffer_load_dwordx4 %[l16], %[srdD], s57 offen sc1 lds\n"
    "v_fma_f32 v223, v223, v219, v219\n"
    "v_mfma_f32_16x16x32_bf16 a[216:219], a[132:135], a[16:19], a[216:219]\n"
    "s_add_u32 m0, s69, 8192\n"
    "s_add_u32 s57, s70, 8192\n"
    "v_mul_f32 v235, s72, v235\n"
    "v_mfma_f32_16x16x32_bf16 a[216:219], a[128:131], a[12:15], a[216:219]\n"
    "buffer_load_dwordx4 %[l16], %[srdD], s57 offen sc1 lds\n"
    "v_mfma_f32_16x16x32_bf16 a[220:223], a[144:147], a[12:15], a[220:223]\n"
    "ds_read_b128 a[12:15], %[rd0] offset:6144\n"
    "v_mfma_f32_16x16x32_bf16 a[220:223], a[148:151], a[16:19], a[220:223]\n"
    "v_rcp_f32 v212, v212\n"
    "v_mfma_f32_16x16x32_bf16 a[220:223], a[152:155], a[44:47], a[220:223]\n"
    "ds_read_b128 a[44:47], %[rd1] offset:6144\n"
    "v_mfma_f32_16x16x32_bf16 a[220:223], a[156:159], a[48:51], a[220:223]\n"
    "v_rcp_f32 v213, v213\n"
    "v_mfma_f32_16x16x32_bf16 a[224:227], a[140:143], a[52:55], a[224:227]\n"
    "v_rcp_f32 v214, v214\n"
    "v_mfma_f32_16x16x32_bf16 a[224:227], a[136:139], a[48:51], a[224:227]\n"
    "s_add_u32 m0, s69, 12288\n"
    "s_add_u32 s57, s70, 12288\n"
    "v_mfma_f32_16x16x32_bf16 a[224:227], a[132:135], a[20:23], a[224:227]\n"
    "buffer_load_dwordx4 %[l16], %[srdD], s57 offen sc1 lds\n"
    "v_mfma_f32_16x16x32_bf16 a[224:227], a[128:131], a[16:19], a[224:227]\n"
    "v_rcp_f32 v215, v215\n"
    "v_mfma_f32_16x16x32_bf16 a[228:231], a[144:147], a[16:19], a[228:231]\n"
    "ds_read_b128 a[16:19], %[rd0] offset:8192\n"
    "v_mfma_f32_16x16x32_bf16 a[228:231], a[148:151], a[20:23], a[228:231]\n"
    "v_rcp_f32 v220, v220\n"
    "v_mfma_f32_16x16x32_bf16 a[228:231], a[152:155], a[48:51], a[228:231]\n"
    "ds_read_b128 a[48:51], %[rd1] offset:8192\n"
    "v_mfma_f32_16x16x32_bf16 a[228:231], a[156:159], a[52:55], a[228:231]\n"
    "v_rcp_f32 v221, v221\n"
    "v_mfma_f32_16x16x32_bf16 a[232:235], a[140:143], a[56:59], a[232:235]\n"
    "s_add_u32 m0, s69, 16384\n"
    "s_add_u32 s57, s70, 16384\n"
    "v_mfma_f32_16x16x32_bf16 a[232:235], a[136:139], a[52:55], a[232:235]\n"
    "buffer_load_dwordx4 %[l16], %[srdD], s57 offen sc1 lds\n"
    "v_mfma_f32_16x16x32_bf16 a[232:235], a[132:135], a[24:27], a[232:235]\n"
    "s_add_u32 m0, s69, 20480\n"
    "s_add_u32 s57, s70, 20480\n"
    "v_mfma_f32_16x16x32_bf16 a[232:235], a[128:131], a[20:23], a[232:235]\n"
    "buffer_load_dwordx4 %[l16], %[srdD], s57 offen sc1 lds\n"
    "v_mfma_f32_16x16x32_bf16 a[236:239], a[144:147], a[20:23], a[236:239]\n"
    "ds_read_b128 a[20:23], %[rd0] offset:10240\n"
    "v_mfma_f32_16x16x32_bf16 a[236:239], a[148:151], a[24:27], a[236:239]\n"
    "v_rcp_f32 v222, v222\n"
    "v_mfma_f32_16x16x32_bf16 a[236:239], a[152:155], a[52:55], a[236:239]\n"
    "ds_read_b128 a[52:55], %[rd1] offset:10240\n"
    "v_mfma_f32_16x16x32_bf16 a[236:239], a[156:159], a[56:59], a[236:239]\n"
    "v_rcp_f32 v223, v223\n"
    "v_mfma_f32_16x16x32_bf16 a[240:243], a[140:143], a[60:63], a[240:243]\n"
    "v_exp_f32 v228, v228\n"
    "v_mfma_f32_16x16x32_bf16 a[240:243], a[136:139], a[56:59], a[240:243]\n"
    "s_add_u32 m0, s69, 24576\n"
    "s_add_u32 s57, s70, 24576\n"
    "v_fma_f32 v212, -abs(v208), v212, v212\n"
    "v_mfma_f32_16x16x32_bf16 a[240:243], a[132:135], a[28:31], a[240:243]\n"
    "buffer_load_dwordx4 %[l16], %[srdD], s57 offen sc1 lds\n"
    "v_fma_f32 v213, -abs(v209), v213, v213\n"
    "v_mfma_f32_16x16x32_bf16 a[240:243], a[128:131], a[24:27], a[240:243]\n"
    "v_exp_f32 v229, v229\n"
    "v_mfma_f32_16x16x32_bf16 a[244:247], a[144:147], a[24:27], a[244:247]\n"
    "ds_read_b128 a[24:27], %[rd0] offset:12288\n"
    "v_fma_f32 v214, -abs(v210), v214, v214\n"
    "v_mfma_f32_16x16x32_bf16 a[244:247], a[148:151], a[28:31], a[244:247]\n"
    "v_exp_f32 v230, v230\n"
    "v_mfma_f32_16x16x32_bf16 a[244:247], a[152:155], a[56:59], a[244:247]\n"
    "ds_read_b128 a[56:59], %[rd1] offset:12288\n"
    "v_fma_f32 v215, -abs(v211), v215, v215\n"
    "v_mfma_f32_16x16x32_bf16 a[244:247], a[156:159], a[60:63], a[244:247]\n"
    "v_exp_f32 v231, v231\n"
    "v_mfma_f32_16x16x32_bf16 a[248:251], a[140:143], a[72:75], a[248:251]\n"
    "v_exp_f32 v236, v236\n"
    "v_mfma_f32_16x16x32_bf16 a[248:251], a[136:139], a[60:63], a[248:251]\n"
    "s_add_u32 m0, s69, 28672\n"
    "s_add_u32 s57, s70, 28672\n"
    "v_fma_f32 v220, -abs(v216), v220, v220\n"
    "v_mfma_f32_16x16x32_bf16 a[248:251], a[132:135], a[64:67], a[248:251]\n"
    "buffer_load_dwordx4 %[l16], %[srdD], s57 offen sc1 lds\n"
    "v_fma_f32 v221, -abs(v217), v221, v221\n"
    "v_mfma_f32_16x16x32_bf16 a[248:251], a[128:131], a[28:31], a[248:251]\n"
    "s_add_u32 s70, s70, 0x8000\n"
    "v_exp_f32 v237, v237\n"
    "v_mfma_f32_16x16x32_bf16 a[252:255], a[144:147], a[28:31], a[252:255]\n"
    "ds_read_b128 a[28:31], %[rd0] offset:14336\n"
    "v_fma_f32 v222, -abs(v218), v222, v222\n"
    "v_mfma_f32_16x16x32_bf16 a[252:255], a[148:151], a[64:67], a[252:255]\n"
    "s_add_u32 s69, s69, 0x8000\n"
    "s_and_b32 s69, s69, 0x1ffff\n"
    "v_fma_f32 v223, -abs(v219), v223, v223\n"
    "v_mfma_f32_16x16x32_bf16 a[252:255], a[152:155], a[60:63], a[252:255]\n"
    "ds_read_b128 a[60:63], %[rd1] offset:14336\n"
    "v_cvt_pk_bf16_f32 v16, v212, v213\n"
    "v_mfma_f32_16x16x32_bf16 a[252:255], a[156:159], a[72:75], a[252:255]\n"
    "s_waitcnt vmcnt(9)\n"
    "s_waitcnt lgkmcnt(8)\n"
    "s_cmp_lg_u32 s67, 0\n"
    "s_cbranch_scc1 Lc13%=\n"
    "v_mfma_f32_16x16x32_bf16 v[80:83], a[100:103], a[40:43], v[80:83]\n"
    "v_mfma_f32_16x16x32_bf16 v[80:83], a[96:99], a[36:39], v[80:83]\n"
    "v_mfma_f32_16x16x32_bf16 v[80:83], a[92:95], a[32:35], v[80:83]\n"
    "v_mfma_f32_16x16x32_bf16 v[80:83], a[88:91], a[8:11], v[80:83]\n"
    "v_mfma_f32_16x16x32_bf16 v[80:83], a[84:87], a[4:7], v[80:83]\n"
    "v_mfma_f32_16x16x32_bf16 v[80:83], a[80:83], a[0:3], v[80:83]\n"
    "v_mfma_f32_16x16x32_bf16 v[84:87], a[104:107], a[0:3], v[84:87]\n"
    "v_mfma_f32_16x16x32_bf16 v[84:87], a[108:111], a[4:7], v[84:87]\n"
    "v_mfma_f32_16x16x32_bf16 v[84:87], a[112:115], a[8:11], v[84:87]\n"
    "v_mfma_f32_16x16x32_bf16 v[84:87], a[116:119], a[32:35], v[84:87]\n"
    "v_mfma_f32_16x16x32_bf16 v[84:87], a[120:123], a[36:39], v[84:87]\n"
    "v_mfma_f32_16x16x32_bf16 v[84:87], a[124:127], a[40:43], v[84:87]\n"
    "Lc13%=:\n"
    "v_mfma_f32_16x16x32_bf16 v[88:91], a[100:103], a[44:47], v[88:91]\n"
    "ds_read_b128 a[0:3], %[rd0] offset:16384\n"
    "v_cvt_pk_bf16_f32 v17, v214, v215\n"
    "v_mfma_f32_16x16x32_bf16 v[88:91], a[96:99], a[40:43], v[88:91]\n"
    "v_exp_f32 v238, v238\n"
    "v_mfma_f32_16x16x32_bf16 v[88:91], a[92:95], a[36:39], v[88:91]\n"
    "buffer_load_dwordx4 a[128:131], %[l16], %[srdW], s54 offen offset:0\n"
    "v_cvt_pk_bf16_f32 v18, v220, v221\n"
    "v_mfma_f32_16x16x32_bf16 v[88:91], a[88:91], a[12:15], v[88:91]\n"
    "v_exp_f32 v239, v239\n"
    "v_mfma_f32_16x16x32_bf16 v[88:91], a[84:87], a[8:11], v[88:91]\n"
    "ds_read_b128 a[32:35], %[rd1] offset:16384\n"
    "v_cvt_pk_bf16_f32 v19, v222, v223\n"
    "v_mfma_f32_16x16x32_bf16 v[88:91], a[80:83], a[4:7], v[88:91]\n"
    "ds_write_b64 %[vsw], v[16:17] offset:0\n"
    "v_mfma_f32_16x16x32_bf16 v[92:95], a[104:107], a[4:7], v[92:95]\n"
    "ds_read_b128 a[4:7], %[rd0] offset:18432\n"
    "v_mul_f32 v244, s73, v244\n"
    "v_mfma_f32_16x16x32_bf16 v[92:95], a[108:111], a[8:11], v[92:95]\n"
    "ds_write_b64 %[vsw], v[18:19] offset:128\n"
    "v_mfma_f32_16x16x32_bf16 v[92:95], a[112:115], a[12:15], v[92:95]\n"
    "v_exp_f32_e64 v20, -abs(v224)\n"
    "v_mfma_f32_16x16x32_bf16 v[92:95], a[116:119], a[36:39], v[92:95]\n"
    "ds_read_b128 a[36:39], %[rd1] offset:18432\n"
    "v_mul_f32 v245, s73, v245\n"
    "v_mfma_f32_16x16x32_bf16 v[92:95], a[120:123], a[40:43], v[92:95]\n"
    "buffer_load_dwordx4 a[132:135], %[l16], %[srdW], s54 offen offset:1024\n"
    "v_mul_f32 v246, s73, v246\n"
    "v_mfma_f32_16x16x32_bf16 v[92:95], a[124:127], a[44:47], v[92:95]\n"
    "s_waitcnt lgkmcnt(10)\n"
    "v_exp_f32_e64 v21, -abs(v225)\n"
    "v_mfma_f32_16x16x32_bf16 v[96:99], a[100:103], a[48:51], v[96:99]\n"
    "v_exp_f32_e64 v22, -abs(v226)\n"
    "v_mfma_f32_16x16x32_bf16 v[96:99], a[96:99], a[44:47], v[96:99]\n"
    "ds_read_b128 a[64:67], %[re0] offset:0\n"
    "v_mul_f32 v247, s73, v247\n"
    "v_mfma_f32_16x16x32_bf16 v[96:99], a[92:95], a[40:43], v[96:99]\n"
    "buffer_load_dwordx4 a[136:139], %[l16], %[srdW], s54 offen offset:2048\n"
    "v_mul_f32 v252, s73, v252\n"
    "v_mfma_f32_16x16x32_bf16 v[96:99], a[88:91], a[16:19], v[96:99]\n"
    "v_exp_f32_e64 v23, -abs(v227)\n"
    "v_mfma_f32_16x16x32_bf16 v[96:99], a[84:87], a[12:15], v[96:99]\n"
    "v_bfi_b32 v224, s61, 0.5, v224\n"
    "v_bfi_b32 v225, s61, 0.5, v225\n"
    "v_mfma_f32_16x16x32_bf16 v[96:99], a[80:83], a[8:11], v[96:99]\n"
    "v_bfi_b32 v226, s61, 0.5, v226\n"
    "v_bfi_b32 v227, s61, 0.5, v227\n"
    "v_mfma_f32_16x16x32_bf16 v[100:103], a[104:107], a[8:11], v[100:103]\n"
    "ds_read_b128 a[8:11], %[rd0] offset:20480\n"
    "v_fma_f32 v224, v20, v224, v224\n"
    "v_mfma_f32_16x16x32_bf16 v[100:103], a[108:111], a[12:15], v[100:103]\n"
    "v_fma_f32 v225, v21, v225, v225\n"
    "v_fma_f32 v226, v22, v226, v226\n"
    "v_mfma_f32_16x16x32_bf16 v[100:103], a[112:115], a[16:19], v[100:103]\n"
    "v_fma_f32 v227, v23, v227, v227\n"
    "v_mul_f32 v253, s73, v253\n"
    "v_mfma_f32_16x16x32_bf16 v[100:103], a[116:119], a[40:43], v[100:103]\n"
    "ds_read_b128 a[40:43], %[rd1] offset:20480\n"
    "v_mul_f32 v254, s73, v254\n"
    "v_mfma_f32_16x16x32_bf16 v[100:103], a[120:123], a[44:47], v[100:103]\n"
    "buffer_load_dwordx4 a[140:143], %[l16], %[srdW], s54 offen offset:3072\n"
    "v_mul_f32 v255, s73, v255\n"
    "v_mfma_f32_16x16x32_bf16 v[100:103], a[124:127], a[48:51], v[100:103]\n"
    "v_exp_f32_e64 v20, -abs(v232)\n"
    "v_mfma_f32_16x16x32_bf16 v[104:107], a[100:103], a[52:55], v[104:107]\n"
    "s_add_u32 s54, s54, 4096\n"
    "v_exp_f32_e64 v21, -abs(v233)\n"
    "v_mfma_f32_16x16x32_bf16 v[104:107], a[96:99], a[48:51], v[104:107]\n"
    "ds_read_b128 a[72:75], %[re1] offset:0\n"
    "v_mul_f32 v240, s72, v240\n"
    "v_mfma_f32_16x16x32_bf16 v[104:107], a[92:95], a[44:47], v[104:107]\n"
    "buffer_load_dwordx4 a[144:147], %[l16], %[srdW], s54 offen offset:0\n"
    "v_mul_f32 v241, s72, v241\n"
    "v_mfma_f32_16x16x32_bf16 v[104:107], a[88:91], a[20:23], v[104:107]\n"
    "v_exp_f32_e64 v22, -abs(v234)\n"
    "v_mfma_f32_16x16x32_bf16 v[104:107], a[84:87], a[16:19], v[104:107]\n"
    "v_exp_f32_e64 v23, -abs(v235)\n"
    "v_mfma_f32_16x16x32_bf16 v[104:107], a[80:83], a[12:15], v[104:107]\n"
    "v_bfi_b32 v232, s61, 0.5, v232\n"
    "v_bfi_b32 v233, s61, 0.5, v233\n"
    "v_mfma_f32_16x16x32_bf16 v[108:111], a[104:107], a[12:15], v[108:111]\n"
    "ds_read_b128 a[12:15], %[rd0] offset:22528\n"
    "v_bfi_b32 v234, s61, 0.5, v234\n"
    "v_mfma_f32_16x16x32_bf16 v[108:111], a[108:111], a[16:19], v[108:111]\n"
    "v_bfi_b32 v235, s61, 0.5, v235\n"
    "v_fma_f32 v232, v20, v232, v232\n"
    "v_mfma_f32_16x16x32_bf16 v[108:111], a[112:115], a[20:23], v[108:111]\n"
    "v_fma_f32 v233, v21, v233, v233\n"
    "v_fma_f32 v234, v22, v234, v234\n"
    "v_mfma_f32_16x16x32_bf16 v[108:111], a[116:119], a[44:47], v[108:111]\n"
    "ds_read_b128 a[44:47], %[rd1] offset:22528\n"
    "v_fma_f32 v235, v23, v235, v235\n"
    "v_mfma_f32_16x16x32_bf16 v[108:111], a[120:123], a[48:51], v[108:111]\n"
    "buffer_load_dwordx4 a[148:151], %[l16], %[srdW], s54 offen offset:1024\n"
    "v_fma_f32 v228, v228, v224, v224\n"
    "v_mfma_f32_16x16x32_bf16 v[108:111], a[124:127], a[52:55], v[108:111]\n"
    "s_waitcnt lgkmcnt(12)\n"
    "v_fma_f32 v229, v229, v225, v225\n"
    "v_fma_f32 v230, v230, v226, v226\n"
    "v_mfma_f32_16x16x32_bf16 v[112:115], a[100:103], a[56:59], v[112:115]\n"
    "v_fma_f32 v231, v231, v227, v227\n"
    "v_fma_f32 v236, v236, v232, v232\n"
    "v_mfma_f32_16x16x32_bf16 v[112:115], a[96:99], a[52:55], v[112:115]\n"
    "ds_read_b128 a[68:71], %[re0] offset:2048\n"
    "v_fma_f32 v237, v237, v233, v233\n"
    "v_mfma_f32_16x16x32_bf16 v[112:115], a[92:95], a[48:51], v[112:115]\n"
    "buffer_load_dwordx4 a[152:155], %[l16], %[srdW], s54 offen offset:2048\n"
    "v_fma_f32 v238, v238, v234, v234\n"
    "v_mfma_f32_16x16x32_bf16 v[112:115], a[88:91], a[24:27], v[112:115]\n"
    "v_fma_f32 v239, v239, v235, v235\n"
    "v_mul_f32 v242, s72, v242\n"
    "v_mfma_f32_16x16x32_bf16 v[112:115], a[84:87], a[20:23], v[112:115]\n"
    "v_rcp_f32 v228, v228\n"
    "v_mfma_f32_16x16x32_bf16 v[112:115], a[80:83], a[16:19], v[112:115]\n"
    "v_rcp_f32 v229, v229\n"
    "v_mfma_f32_16x16x32_bf16 v[116:119], a[104:107], a[16:19], v[116:119]\n"
    "ds_read_b128 a[16:19], %[rd0] offset:24576\n"
    "v_mul_f32 v243, s72, v243\n"
    "v_mfma_f32_16x16x32_bf16 v[116:119], a[108:111], a[20:23], v[116:119]\n"
    "v_rcp_f32 v230, v230\n"
    "v_mfma_f32_16x16x32_bf16 v[116:119], a[112:115], a[24:27], v[116:119]\n"
    "v_rcp_f32 v231, v231\n"
    "v_mfma_f32_16x16x32_bf16 v[116:119], a[116:119], a[48:51], v[116:119]\n"
    "ds_read_b128 a[48:51], %[rd1] offset:24576\n"
    "v_mul_f32 v248, s72, v248\n"
    "v_mfma_f32_16x16x32_bf16 v[116:119], a[120:123], a[52:55], v[116:119]\n"
    "buffer_load_dwordx4 a[156:159], %[l16], %[srdW], s54 offen offset:3072\n"
    "v_mul_f32 v249, s72, v249\n"
    "v_mfma_f32_16x16x32_bf16 v[116:119], a[124:127], a[56:59], v[116:119]\n"
    "v_rcp_f32 v236, v236\n"
    "v_mfma_f32_16x16x32_bf16 v[120:123], a[100:103], a[60:63], v[120:123]\n"
    "s_add_u32 s54, s54, 4096\n"
    "v_rcp_f32 v237, v237\n"
    "v_mfma_f32_16x16x32_bf16 v[120:123], a[96:99], a[56:59], v[120:123]\n"
    "ds_read_b128 a[76:79], %[re1] offset:2048\n"
    "v_mul_f32 v250, s72, v250\n"
    "v_mfma_f32_16x16x32_bf16 v[120:123], a[92:95], a[52:55], v[120:123]\n"
    "buffer_load_dwordx4 a[160:163], %[l16], %[srdW], s54 offen offset:0\n"
    "v_mul_f32 v251, s72, v251\n"
    "v_mfma_f32_16x16x32_bf16 v[120:123], a[88:91], a[28:31], v[120:123]\n"
    "v_rcp_f32 v238, v238\n"
    "v_mfma_f32_16x16x32_bf16 v[120:123], a[84:87], a[24:27], v[120:123]\n"
    "v_rcp_f32 v239, v239\n"
    "v_mfma_f32_16x16x32_bf16 v[120:123], a[80:83], a[20:23], v[120:123]\n"
    "v_exp_f32 v244, v244\n"
    "v_mfma_f32_16x16x32_bf16 v[124:127], a[104:107], a[20:23], v[124:127]\n"
    "ds_read_b128 a[20:23], %[rd0] offset:26624\n"
    "v_fma_f32 v228, -abs(v224), v228, v228\n"
    "v_mfma_f32_16x16x32_bf16 v[124:127], a[108:111], a[24:27], v[124:127]\n"
    "v_exp_f32 v245, v245\n"
    "v_mfma_f32_16x16x32_bf16 v[124:127], a[112:115], a[28:31], v[124:127]\n"
    "v_exp_f32 v246, v246\n"
    "v_mfma_f32_16x16x32_bf16 v[124:127], a[116:119], a[52:55], v[124:127]\n"
    "ds_read_b128 a[52:55], %[rd1] offset:26624\n"
    "v_fma_f32 v229, -abs(v225), v229, v229\n"
    "v_mfma_f32_16x16x32_bf16 v[124:127], a[120:123], a[56:59], v[124:127]\n"
    "buffer_load_dwordx4 a[164:167], %[l16], %[srdW], s54 offen offset:1024\n"
    "v_fma_f32 v230, -abs(v226), v230, v230\n"
    "v_mfma_f32_16x16x32_bf16 v[124:127], a[124:127], a[60:63], v[124:127]\n"
    "s_waitcnt lgkmcnt(12)\n"
    "v_exp_f32 v247, v247\n"
    "v_mfma_f32_16x16x32_bf16 v[128:131], a[100:103], a[32:35], v[128:131]\n"
    "v_exp_f32 v252, v252\n"
    "v_mfma_f32_16x16x32_bf16 v[128:131], a[96:99], a[60:63], v[128:131]\n"
    "v_exp_f32 v253, v253\n"
    "v_mfma_f32_16x16x32_bf16 v[128:131], a[92:95], a[56:59], v[128:131]\n"
    "buffer_load_dwordx4 a[168:171], %[l16], %[srdW], s54 offen offset:2048\n"
    "v_fma_f32 v231, -abs(v227), v231, v231\n"
    "v_mfma_f32_16x16x32_bf16 v[128:131], a[88:91], a[0:3], v[128:131]\n"
    "v_exp_f32 v254, v254\n"
    "v_mfma_f32_16x16x32_bf16 v[128:131], a[84:87], a[28:31], v[128:131]\n"
    "v_exp_f32 v255, v255\n"
    "v_mfma_f32_16x16x32_bf16 v[128:131], a[80:83], a[24:27], v[128:131]\n"
    "v_exp_f32_e64 v16, -abs(v240)\n"
    "v_mfma_f32_16x16x32_bf16 v[132:135], a[104:107], a[24:27], v[132:135]\n"
    "ds_read_b128 a[24:27], %[rd0] offset:28672\n"
    "v_fma_f32 v236, -abs(v232), v236, v236\n"
    "v_mfma_f32_16x16x32_bf16 v[132:135], a[108:111], a[28:31], v[132:135]\n"
    "v_exp_f32_e64 v17, -abs(v241)\n"
    "v_mfma_f32_16x16x32_bf16 v[132:135], a[112:115], a[0:3], v[132:135]\n"
    "v_exp_f32_e64 v18, -abs(v242)\n"
    "v_mfma_f32_16x16x32_bf16 v[132:135], a[116:119], a[56:59], v[132:135]\n"
    "ds_read_b128 a[56:59], %[rd1] offset:28672\n"
    "v_fma_f32 v237, -abs(v233), v237, v237\n"
    "v_mfma_f32_16x16x32_bf16 v[132:135], a[120:123], a[60:63], v[132:135]\n"
    "buffer_load_dwordx4 a[172:175], %[l16], %[srdW], s54 offen offset:3072\n"
    "v_fma_f32 v238, -abs(v234), v238, v238\n"
    "v_mfma_f32_16x16x32_bf16 v[132:135], a[124:127], a[32:35], v[132:135]\n"
    "v_exp_f32_e64 v19, -abs(v243)\n"
    "v_mfma_f32_16x16x32_bf16 v[136:139], a[100:103], a[36:39], v[136:139]\n"
    "s_add_u32 s54, s54, 4096\n"
    "v_fma_f32 v239, -abs(v235), v239, v239\n"
    "v_cvt_pk_bf16_f32 v20, v228, v229\n"
    "v_mfma_f32_16x16x32_bf16 v[136:139], a[96:99], a[32:35], v[136:139]\n"
    "s_add_u32 m0, s69, 0\n"
    "s_add_u32 s57, s70, 0\n"
    "v_cvt_pk_bf16_f32 v21, v230, v231\n"
    "v_mfma_f32_16x16x32_bf16 v[136:139], a[92:95], a[60:63], v[136:139]\n"
    "buffer_load_dwordx4 %[l16], %[srdD], s57 offen sc1 lds\n"
    "v_cvt_pk_bf16_f32 v22, v236, v237\n"
    "v_mfma_f32_16x16x32_bf16 v[136:139], a[88:91], a[4:7], v[136:139]\n"
    "s_add_u32 m0, s69, 4096\n"
    "s_add_u32 s57, s70, 4096\n"
    "v_cvt_pk_bf16_f32 v23, v238, v239\n"
    "v_mfma_f32_16x16x32_bf16 v[136:139], a[84:87], a[0:3], v[136:139]\n"
    "buffer_load_dwordx4 %[l16], %[srdD], s57 offen sc1 lds\n"
    "v_bfi_b32 v240, s61, 0.5, v240\n"
    "v_mfma_f32_16x16x32_bf16 v[136:139], a[80:83], a[28:31], v[136:139]\n"
    "s_add_u32 s64, s56, 4096\n"
    "buffer_load_dwordx4 v[40:43], v76, %[srdX], s64 offen\n"
    "v_mfma_f32_16x16x32_bf16 v[140:143], a[104:107], a[28:31], v[140:143]\n"
    "ds_read_b128 a[28:31], %[rd0] offset:30720\n"
    "v_bfi_b32 v241, s61, 0.5, v241\n"
    "v_mfma_f32_16x16x32_bf16 v[140:143], a[108:111], a[0:3], v[140:143]\n"
    "ds_write_b64 %[vsw], v[20:21] offset:4096\n"
    "v_mfma_f32_16x16x32_bf16 v[140:143], a[112:115], a[4:7], v[140:143]\n"
    "ds_write_b64 %[vsw], v[22:23] offset:4224\n"
    "v_mfma_f32_16x16x32_bf16 v[140:143], a[116:119], a[60:63], v[140:143]\n"
    "ds_read_b128 a[60:63], %[rd1] offset:30720\n"
    "v_bfi_b32 v242, s61, 0.5, v242\n"
    "v_mfma_f32_16x16x32_bf16 v[140:143], a[120:123], a[32:35], v[140:143]\n"
    "v_bfi_b32 v243, s61, 0.5, v243\n"
    "v_fma_f32 v240, v16, v240, v240\n"
    "v_mfma_f32_16x16x32_bf16 v[140:143], a[124:127], a[36:39], v[140:143]\n"
    "s_waitcnt lgkmcnt(12)\n"
    "v_fma_f32 v241, v17, v241, v241\n"
    "v_fma_f32 v242, v18, v242, v242\n"
    "v_mfma_f32_16x16x32_bf16 v[144:147], a[100:103], a[40:43], v[144:147]\n"
    "v_fma_f32 v243, v19, v243, v243\n"
    "v_accvgpr_read_b32 v224, a176\n"
    "v_mfma_f32_16x16x32_bf16 v[144:147], a[96:99], a[36:39], v[144:147]\n"
    "v_exp_f32_e64 v16, -abs(v248)\n"
    "v_mfma_f32_16x16x32_bf16 v[144:147], a[92:95], a[32:35], v[144:147]\n"
    "s_add_u32 s63, s63, 1\n"
    "v_exp_f32_e64 v17, -abs(v249)\n"
    "v_mfma_f32_16x16x32_bf16 v[144:147], a[88:91], a[8:11], v[144:147]\n"
    "v_exp_f32_e64 v18, -abs(v250)\n"
    "v_mfma_f32_16x16x32_bf16 v[144:147], a[84:87], a[4:7], v[144:147]\n"
    "v_exp_f32_e64 v19, -abs(v251)\n"
    "v_mfma_f32_16x16x32_bf16 v[144:147], a[80:83], a[0:3], v[144:147]\n"
    "v_bfi_b32 v248, s61, 0.5, v248\n"
    "v_bfi_b32 v249, s61, 0.5, v249\n"
    "v_mfma_f32_16x16x32_bf16 v[148:151], a[104:107], a[0:3], v[148:151]\n"
    "s_waitcnt lgkmcnt(0)\n"
    "v_mov_b32 v13, s63\n"
    "v_mfma_f32_16x16x32_bf16 v[148:151], a[108:111], a[4:7], v[148:151]\n"
    "ds_write_b32 v14, v13\n"
    "v_mfma_f32_16x16x32_bf16 v[148:151], a[112:115], a[8:11], v[148:151]\n"
    "v_bfi_b32 v250, s61, 0.5, v250\n"
    "v_bfi_b32 v251, s61, 0.5, v251\n"
    "v_mfma_f32_16x16x32_bf16 v[148:151], a[116:119], a[32:35], v[148:151]\n"
    "v_fma_f32 v248, v16, v248, v248\n"
    "v_fma_f32 v249, v17, v249, v249\n"
    "v_mfma_f32_16x16x32_bf16 v[148:151], a[120:123], a[36:39], v[148:151]\n"
    "v_fma_f32 v250, v18, v250, v250\n"
    "v_fma_f32 v251, v19, v251, v251\n"
    "v_mfma_f32_16x16x32_bf16 v[148:151], a[124:127], a[40:43], v[148:151]\n"
    "v_fma_f32 v244, v244, v240, v240\n"
    "v_fma_f32 v245, v245, v241, v241\n"
    "v_mfma_f32_16x16x32_bf16 v[152:155], a[100:103], a[44:47], v[152:155]\n"
    "v_fma_f32 v246, v246, v242, v242\n"
    "v_fma_f32 v247, v247, v243, v243\n"
    "v_mfma_f32_16x16x32_bf16 v[152:155], a[96:99], a[40:43], v[152:155]\n"
    "v_fma_f32 v252, v252, v248, v248\n"
    "v_fma_f32 v253, v253, v249, v249\n"
    "v_mfma_f32_16x16x32_bf16 v[152:155], a[92:95], a[36:39], v[152:155]\n"
    "v_fma_f32 v254, v254, v250, v250\n"
    "v_fma_f32 v255, v255, v251, v251\n"
    "v_mfma_f32_16x16x32_bf16 v[152:155], a[88:91], a[12:15], v[152:155]\n"
    "v_rcp_f32 v244, v244\n"
    "v_mfma_f32_16x16x32_bf16 v[152:155], a[84:87], a[8:11], v[152:155]\n"
    "ds_read_b32 v13, v12\n"
    "v_accvgpr_read_b32 v225, a177\n"
    "v_mfma_f32_16x16x32_bf16 v[152:155], a[80:83], a[4:7], v[152:155]\n"
    "v_rcp_f32 v245, v245\n"
    "v_mfma_f32_16x16x32_bf16 v[156:159], a[104:107], a[4:7], v[156:159]\n"
    "v_rcp_f32 v246, v246\n"
    "v_mfma_f32_16x16x32_bf16 v[156:159], a[108:111], a[8:11], v[156:159]\n"
    "v_rcp_f32 v247, v247\n"
    "v_mfma_f32_16x16x32_bf16 v[156:159], a[112:115], a[12:15], v[156:159]\n"
    "v_rcp_f32 v252, v252\n"
    "v_mfma_f32_16x16x32_bf16 v[156:159], a[116:119], a[36:39], v[156:159]\n"
    "v_rcp_f32 v253, v253\n"
    "v_mfma_f32_16x16x32_bf16 v[156:159], a[120:123], a[40:43], v[156:159]\n"
    "s_waitcnt lgkmcnt(0)\n"
    "v_cmp_gt_u32 vcc, s63, v13\n"
    "v_accvgpr_read_b32 v226, a178\n"
    "v_mfma_f32_16x16x32_bf16 v[156:159], a[124:127], a[44:47], v[156:159]\n"
    "s_cbranch_vccnz Ls13%=\n"
    "Lb13%=:\n"
    "v_accvgpr_read_b32 v227, a179\n"
    "v_mfma_f32_16x16x32_bf16 v[160:163], a[100:103], a[48:51], v[160:163]\n"
    "ds_read_b128 a[0:3], %[rd0] offset:32768\n"
    "v_accvgpr_read_b32 v228, a180\n"
    "v_mfma_f32_16x16x32_bf16 v[160:163], a[96:99], a[44:47], v[160:163]\n"
    "ds_read_b128 a[32:35], %[rd1] offset:32768\n"
    "v_accvgpr_read_b32 v229, a181\n"
    "v_mfma_f32_16x16x32_bf16 v[160:163], a[92:95], a[40:43], v[160:163]\n"
    "ds_read_b128 a[4:7], %[rd0] offset:34816\n"
    "v_accvgpr_read_b32 v230, a182\n"
    "v_mfma_f32_16x16x32_bf16 v[160:163], a[88:91], a[16:19], v[160:163]\n"
    "ds_read_b128 a[36:39], %[rd1] offset:34816\n"
    "v_accvgpr_read_b32 v231, a183\n"
    "v_mfma_f32_16x16x32_bf16 v[160:163], a[84:87], a[12:15], v[160:163]\n"
    "ds_read_b128 v[24:27], v14 offset:4096\n"
    "v_accvgpr_read_b32 v232, a184\n"
    "v_mfma_f32_16x16x32_bf16 v[160:163], a[80:83], a[8:11], v[160:163]\n"
    "ds_read_b128 v[28:31], v14 offset:8192\n"
    "v_accvgpr_read_b32 v233, a185\n"
    "v_mfma_f32_16x16x32_bf16 v[164:167], a[104:107], a[8:11], v[164:167]\n"
    "ds_read_b128 a[8:11], %[rd0] offset:36864\n"
    "v_accvgpr_read_b32 v234, a186\n"
    "v_mfma_f32_16x16x32_bf16 v[164:167], a[108:111], a[12:15], v[164:167]\n"
    "s_add_u32 m0, s69, 8192\n"
    "s_add_u32 s57, s70, 8192\n"
    "v_accvgpr_read_b32 v235, a187\n"
    "v_mfma_f32_16x16x32_bf16 v[164:167], a[112:115], a[16:19], v[164:167]\n"
    "buffer_load_dwordx4 %[l16], %[srdD], s57 offen sc1 lds\n"
    "v_accvgpr_read_b32 v236, a188\n"
    "v_mfma_f32_16x16x32_bf16 v[164:167], a[116:119], a[40:43], v[164:167]\n"
    "ds_read_b128 a[40:43], %[rd1] offset:36864\n"
    "v_accvgpr_read_b32 v237, a189\n"
    "v_mfma_f32_16x16x32_bf16 v[164:167], a[120:123], a[44:47], v[164:167]\n"
    "v_rcp_f32 v254, v254\n"
    "v_mfma_f32_16x16x32_bf16 v[164:167], a[124:127], a[48:51], v[164:167]\n"
    "v_rcp_f32 v255, v255\n"
    "v_mfma_f32_16x16x32_bf16 v[168:171], a[100:103], a[52:55], v[168:171]\n"
    "v_fma_f32 v244, -abs(v240), v244, v244\n"
    "v_fma_f32 v245, -abs(v241), v245, v245\n"
    "v_mfma_f32_16x16x32_bf16 v[168:171], a[96:99], a[48:51], v[168:171]\n"
    "v_fma_f32 v246, -abs(v242), v246, v246\n"
    "v_fma_f32 v247, -abs(v243), v247, v247\n"
    "v_mfma_f32_16x16x32_bf16 v[168:171], a[92:95], a[44:47], v[168:171]\n"
    "s_add_u32 m0, s69, 12288\n"
    "s_add_u32 s57, s70, 12288\n"
    "s_waitcnt lgkmcnt(3)\n"
    "v_mfma_f32_16x16x32_bf16 v[168:171], a[88:91], a[20:23], v[168:171]\n"
    "buffer_load_dwordx4 %[l16], %[srdD], s57 offen sc1 lds\n"
    "v_fma_f32 v252, -abs(v248), v252, v252\n"
    "v_mfma_f32_16x16x32_bf16 v[168:171], a[84:87], a[16:19], v[168:171]\n"
    "s_mov_b32 s60, 2048\n"
    "buffer_store_dwordx4 v[24:27], v15, s[76:79], s60 offen sc0 sc1\n"
    "v_mfma_f32_16x16x32_bf16 v[168:171], a[80:83], a[12:15], v[168:171]\n"
    "v_fma_f32 v253, -abs(v249), v253, v253\n"
    "v_fma_f32 v254, -abs(v250), v254, v254\n"
    "v_mfma_f32_16x16x32_bf16 v[172:175], a[104:107], a[12:15], v[172:175]\n"
    "ds_read_b128 a[12:15], %[rd0] offset:38912\n"
    "v_fma_f32 v255, -abs(v251), v255, v255\n"
    "v_mfma_f32_16x16x32_bf16 v[172:175], a[108:111], a[16:19], v[172:175]\n"
    "v_cvt_pk_bf16_f32 v16, v244, v245\n"
    "v_cvt_pk_bf16_f32 v17, v246, v247\n"
    "v_mfma_f32_16x16x32_bf16 v[172:175], a[112:115], a[20:23], v[172:175]\n"
    "v_cvt_pk_bf16_f32 v18, v252, v253\n"
    "v_cvt_pk_bf16_f32 v19, v254, v255\n"
    "v_mfma_f32_16x16x32_bf16 v[172:175], a[116:119], a[44:47], v[172:175]\n"
    "ds_read_b128 a[44:47], %[rd1] offset:38912\n"
    "v_accvgpr_read_b32 v238, a190\n"
    "v_mfma_f32_16x16x32_bf16 v[172:175], a[120:123], a[48:51], v[172:175]\n"
    "ds_write_b64 %[vsw], v[16:17] offset:8192\n"
    "v_mfma_f32_16x16x32_bf16 v[172:175], a[124:127], a[52:55], v[172:175]\n"
    "v_accvgpr_read_b32 v239, a191\n"
    "v_mul_f32 v228, s73, v228\n"
    "v_mfma_f32_16x16x32_bf16 v[176:179], a[100:103], a[56:59], v[176:179]\n"
    "ds_write_b64 %[vsw], v[18:19] offset:8320\n"
    "v_mfma_f32_16x16x32_bf16 v[176:179], a[96:99], a[52:55], v[176:179]\n"
    "v_mul_f32 v229, s73, v229\n"
    "v_mul_f32 v230, s73, v230\n"
    "v_mfma_f32_16x16x32_bf16 v[176:179], a[92:95], a[48:51], v[176:179]\n"
    "s_add_u32 m0, s69, 16384\n"
    "s_add_u32 s57, s70, 16384\n"
    "v_mul_f32 v231, s73, v231\n"
    "v_mfma_f32_16x16x32_bf16 v[176:179], a[88:91], a[24:27], v[176:179]\n"
    "buffer_load_dwordx4 %[l16], %[srdD], s57 offen sc1 lds\n"
    "v_mul_f32 v236, s73, v236\n"
    "v_mfma_f32_16x16x32_bf16 v[176:179], a[84:87], a[20:23], v[176:179]\n"
    "v_mul_f32 v237, s73, v237\n"
    "v_mul_f32 v238, s73, v238\n"
    "v_mfma_f32_16x16x32_bf16 v[176:179], a[80:83], a[16:19], v[176:179]\n"
    "v_mul_f32 v239, s73, v239\n"
    "v_mul_f32 v224, s72, v224\n"
    "v_mfma_f32_16x16x32_bf16 v[180:183], a[104:107], a[16:19], v[180:183]\n"
    "ds_read_b128 a[16:19], %[rd0] offset:40960\n"
    "v_mul_f32 v225, s72, v225\n"
    "v_mfma_f32_16x16x32_bf16 v[180:183], a[108:111], a[20:23], v[180:183]\n"
    "v_mul_f32 v226, s72, v226\n"
    "v_mul_f32 v227, s72, v227\n"
    "v_mfma_f32_16x16x32_bf16 v[180:183], a[112:115], a[24:27], v[180:183]\n"
    "v_mul_f32 v232, s72, v232\n"
    "v_mul_f32 v233, s72, v233\n"
    "v_mfma_f32_16x16x32_bf16 v[180:183], a[116:119], a[48:51], v[180:183]\n"
    "ds_read_b128 a[48:51], %[rd1] offset:40960\n"
    "v_mul_f32 v234, s72, v234\n"
    "v_mfma_f32_16x16x32_bf16 v[180:183], a[120:123], a[52:55], v[180:183]\n"
    "v_mul_f32 v235, s72, v235\n"
    "v_accvgpr_read_b32 v208, a192\n"
    "v_mfma_f32_16x16x32_bf16 v[180:183], a[124:127], a[56:59], v[180:183]\n"
    "v_exp_f32 v228, v228\n"
    "v_mfma_f32_16x16x32_bf16 v[184:187], a[100:103], a[60:63], v[184:187]\n"
    "v_exp_f32 v229, v229\n"
    "v_mfma_f32_16x16x32_bf16 v[184:187], a[96:99], a[56:59], v[184:187]\n"
    "v_exp_f32 v230, v230\n"
    "v_mfma_f32_16x16x32_bf16 v[184:187], a[92:95], a[52:55], v[184:187]\n"
    "s_add_u32 m0, s69, 20480\n"
    "s_add_u32 s57, s70, 20480\n"
    "v_accvgpr_read_b32 v209, a193\n"
    "v_mfma_f32_16x16x32_bf16 v[184:187], a[88:91], a[28:31], v[184:187]\n"
    "buffer_load_dwordx4 %[l16], %[srdD], s57 offen sc1 lds\n"
    "v_accvgpr_read_b32 v210, a194\n"
    "v_mfma_f32_16x16x32_bf16 v[184:187], a[84:87], a[24:27], v[184:187]\n"
    "v_exp_f32 v231, v231\n"
    "v_mfma_f32_16x16x32_bf16 v[184:187], a[80:83], a[20:23], v[184:187]\n"
    "v_exp_f32 v236, v236\n"
    "v_mfma_f32_16x16x32_bf16 v[188:191], a[104:107], a[20:23], v[188:191]\n"
    "ds_read_b128 a[20:23], %[rd0] offset:43008\n"
    "v_accvgpr_read_b32 v211, a195\n"
    "v_mfma_f32_16x16x32_bf16 v[188:191], a[108:111], a[24:27], v[188:191]\n"
    "v_exp_f32 v237, v237\n"
    "v_mfma_f32_16x16x32_bf16 v[188:191], a[112:115], a[28:31], v[188:191]\n"
    "v_exp_f32 v238, v238\n"
    "v_mfma_f32_16x16x32_bf16 v[188:191], a[116:119], a[52:55], v[188:191]\n"
    "ds_read_b128 a[52:55], %[rd1] offset:43008\n"
    "v_accvgpr_read_b32 v212, a196\n"
    "v_mfma_f32_16x16x32_bf16 v[188:191], a[120:123], a[56:59], v[188:191]\n"
    "v_exp_f32 v239, v239\n"
    "v_mfma_f32_16x16x32_bf16 v[188:191], a[124:127], a[60:63], v[188:191]\n"
    "v_exp_f32_e64 v20, -abs(v224)\n"
    "v_mfma_f32_16x16x32_bf16 v[192:195], a[100:103], a[72:75], v[192:195]\n"
    "v_exp_f32_e64 v21, -abs(v225)\n"
    "v_mfma_f32_16x16x32_bf16 v[192:195], a[96:99], a[60:63], v[192:195]\n"
    "v_exp_f32_e64 v22, -abs(v226)\n"
    "v_mfma_f32_16x16x32_bf16 v[192:195], a[92:95], a[56:59], v[192:195]\n"
    "s_add_u32 m0, s69, 24576\n"
    "s_add_u32 s57, s70, 24576\n"
    "v_accvgpr_read_b32 v213, a197\n"
    "v_mfma_f32_16x16x32_bf16 v[192:195], a[88:91], a[64:67], v[192:195]\n"
    "buffer_load_dwordx4 %[l16], %[srdD], s57 offen sc1 lds\n"
    "v_accvgpr_read_b32 v214, a198\n"
    "v_mfma_f32_16x16x32_bf16 v[192:195], a[84:87], a[28:31], v[192:195]\n"
    "v_exp_f32_e64 v23, -abs(v227)\n"
    "v_mfma_f32_16x16x32_bf16 v[192:195], a[80:83], a[24:27], v[192:195]\n"
    "v_bfi_b32 v224, s61, 0.5, v224\n"
    "v_bfi_b32 v225, s61, 0.5, v225\n"
    "v_mfma_f32_16x16x32_bf16 v[196:199], a[104:107], a[24:27], v[196:199]\n"
    "ds_read_b128 a[24:27], %[rd0] offset:45056\n"
    "v_bfi_b32 v226, s61, 0.5, v226\n"
    "v_mfma_f32_16x16x32_bf16 v[196:199], a[108:111], a[28:31], v[196:199]\n"
    "v_bfi_b32 v227, s61, 0.5, v227\n"
    "v_fma_f32 v224, v20, v224, v224\n"
    "v_mfma_f32_16x16x32_bf16 v[196:199], a[112:115], a[64:67], v[196:199]\n"
    "s_waitcnt lgkmcnt(11)\n"
    "v_fma_f32 v225, v21, v225, v225\n"
    "v_fma_f32 v226, v22, v226, v226\n"
    "v_mfma_f32_16x16x32_bf16 v[196:199], a[116:119], a[56:59], v[196:199]\n"
    "ds_read_b128 a[56:59], %[rd1] offset:45056\n"
    "v_fma_f32 v227, v23, v227, v227\n"
    "v_mfma_f32_16x16x32_bf16 v[196:199], a[120:123], a[60:63], v[196:199]\n"
    "s_mov_b32 s60, 10240\n"
    "buffer_store_dwordx4 v[28:31], %[oo], s[76:79], s60 offen sc0 sc1\n"
    "v_mfma_f32_16x16x32_bf16 v[196:199], a[124:127], a[72:75], v[196:199]\n"
    "s_add_u32 s64, s56, 6144\n"
    "buffer_load_dwordx4 v[44:47], v76, %[srdX], s64 offen\n"
    "v_mfma_f32_16x16x32_bf16 v[200:203], a[100:103], a[76:79], v[200:203]\n"
    "v_exp_f32_e64 v20, -abs(v232)\n"
    "v_mfma_f32_16x16x32_bf16 v[200:203], a[96:99], a[72:75], v[200:203]\n"
    "v_exp_f32_e64 v21, -abs(v233)\n"
    "v_mfma_f32_16x16x32_bf16 v[200:203], a[92:95], a[60:63], v[200:203]\n"
    "s_add_u32 m0, s69, 28672\n"
    "s_add_u32 s57, s70, 28672\n"
    "v_accvgpr_read_b32 v215, a199\n"
    "v_mfma_f32_16x16x32_bf16 v[200:203], a[88:91], a[68:71], v[200:203]\n"
    "buffer_load_dwordx4 %[l16], %[srdD], s57 offen sc1 lds\n"
    "v_accvgpr_read_b32 v216, a200\n"
    "v_mfma_f32_16x16x32_bf16 v[200:203], a[84:87], a[64:67], v[200:203]\n"
    "v_exp_f32_e64 v22, -abs(v234)\n"
    "v_mfma_f32_16x16x32_bf16 v[200:203], a[80:83], a[28:31], v[200:203]\n"
    "s_add_u32 s69, s69, 0x8000\n"
    "s_and_b32 s69, s69, 0x1ffff\n"
    "v_accvgpr_read_b32 v217, a201\n"
    "v_mfma_f32_16x16x32_bf16 v[204:207], a[104:107], a[28:31], v[204:207]\n"
    "ds_read_b128 a[28:31], %[rd0] offset:47104\n"
    "v_accvgpr_read_b32 v218, a202\n"
    "v_mfma_f32_16x16x32_bf16 v[204:207], a[108:111], a[64:67], v[204:207]\n"
    "s_add_u32 s70, s70, 0x8000\n"
    "v_exp_f32_e64 v23, -abs(v235)\n"
    "v_mfma_f32_16x16x32_bf16 v[204:207], a[112:115], a[68:71], v[204:207]\n"
    "v_bfi_b32 v232, s61, 0.5, v232\n"
    "v_bfi_b32 v233, s61, 0.5, v233\n"
    "v_mfma_f32_16x16x32_bf16 v[204:207], a[116:119], a[60:63], v[204:207]\n"
    "ds_read_b128 a[60:63], %[rd1] offset:47104\n"
    "v_bfi_b32 v234, s61, 0.5, v234\n"
    "v_mfma_f32_16x16x32_bf16 v[204:207], a[120:123], a[72:75], v[204:207]\n"
    "v_bfi_b32 v235, s61, 0.5, v235\n"
    "v_fma_f32 v232, v20, v232, v232\n"
    "v_mfma_f32_16x16x32_bf16 v[204:207], a[124:127], a[76:79], v[204:207]\n"
    "s_waitcnt vmcnt(12)\n"
    "s_waitcnt lgkmcnt(10)\n"
    "s_cmp_lg_u32 s67, 0\n"
    "s_cbranch_scc1 Lc14%=\n"
    "v_mfma_f32_16x16x32_bf16 v[80:83], a[148:151], a[40:43], v[80:83]\n"
    "v_mfma_f32_16x16x32_bf16 v[80:83], a[144:147], a[36:39], v[80:83]\n"
    "v_mfma_f32_16x16x32_bf16 v[80:83], a[140:143], a[32:35], v[80:83]\n"
    "v_mfma_f32_16x16x32_bf16 v[80:83], a[136:139], a[8:11], v[80:83]\n"
    "v_mfma_f32_16x16x32_bf16 v[80:83], a[132:135], a[4:7], v[80:83]\n"
    "v_mfma_f32_16x16x32_bf16 v[80:83], a[128:131], a[0:3], v[80:83]\n"
    "v_mfma_f32_16x16x32_bf16 v[84:87], a[152:155], a[0:3], v[84:87]\n"
    "v_mfma_f32_16x16x32_bf16 v[84:87], a[156:159], a[4:7], v[84:87]\n"
    "v_mfma_f32_16x16x32_bf16 v[84:87], a[160:163], a[8:11], v[84:87]\n"
    "v_mfma_f32_16x16x32_bf16 v[84:87], a[164:167], a[32:35], v[84:87]\n"
    "v_mfma_f32_16x16x32_bf16 v[84:87], a[168:171], a[36:39], v[84:87]\n"
    "v_mfma_f32_16x16x32_bf16 v[84:87], a[172:175], a[40:43], v[84:87]\n"
    "Lc14%=:\n"
    "v_mfma_f32_16x16x32_bf16 v[88:91], a[148:151], a[44:47], v[88:91]\n"
    "ds_read_b128 a[0:3], %[rd0] offset:49152\n"
    "v_mfma_f32_16x16x32_bf16 v[88:91], a[144:147], a[40:43], v[88:91]\n"
    "v_lshlrev_b32 v60, 16, v32\n"
    "v_and_b32 v61, s65, v32\n"
    "v_mfma_f32_16x16x32_bf16 v[88:91], a[140:143], a[36:39], v[88:91]\n"
    "buffer_load_dwordx4 a[80:83], %[l16], %[srdW], s54 offen offset:0\n"
    "v_lshlrev_b32 v62, 16, v33\n"
    "v_mfma_f32_16x16x32_bf16 v[88:91], a[136:139], a[12:15], v[88:91]\n"
    "v_and_b32 v63, s65, v33\n"
    "v_lshlrev_b32 v64, 16, v34\n"
    "v_mfma_f32_16x16x32_bf16 v[88:91], a[132:135], a[8:11], v[88:91]\n"
    "ds_read_b128 a[32:35], %[rd1] offset:49152\n"
    "v_and_b32 v65, s65, v34\n"
    "v_mfma_f32_16x16x32_bf16 v[88:91], a[128:131], a[4:7], v[88:91]\n"
    "v_lshlrev_b32 v66, 16, v35\n"
    "v_and_b32 v67, s65, v35\n"
    "v_mfma_f32_16x16x32_bf16 v[92:95], a[152:155], a[4:7], v[92:95]\n"
    "ds_read_b128 a[4:7], %[rd0] offset:51200\n"
    "v_fma_f32 v233, v21, v233, v233\n"
    "v_mfma_f32_16x16x32_bf16 v[92:95], a[156:159], a[8:11], v[92:95]\n"
    "v_fma_f32 v234, v22, v234, v234\n"
    "v_fma_f32 v235, v23, v235, v235\n"
    "v_mfma_f32_16x16x32_bf16 v[92:95], a[160:163], a[12:15], v[92:95]\n"
    "v_fma_f32 v228, v228, v224, v224\n"
    "v_fma_f32 v229, v229, v225, v225\n"
    "v_mfma_f32_16x16x32_bf16 v[92:95], a[164:167], a[36:39], v[92:95]\n"
    "ds_read_b128 a[36:39], %[rd1] offset:51200\n"
    "v_fma_f32 v230, v230, v226, v226\n"
    "v_mfma_f32_16x16x32_bf16 v[92:95], a[168:171], a[40:43], v[92:95]\n"
    "buffer_load_dwordx4 a[84:87], %[l16], %[srdW], s54 offen offset:1024\n"
    "v_fma_f32 v231, v231, v227, v227\n"
    "v_mfma_f32_16x16x32_bf16 v[92:95], a[172:175], a[44:47], v[92:95]\n"
    "s_waitcnt lgkmcnt(8)\n"
    "v_fma_f32 v236, v236, v232, v232\n"
    "v_fma_f32 v237, v237, v233, v233\n"
    "v_mfma_f32_16x16x32_bf16 v[96:99], a[148:151], a[48:51], v[96:99]\n"
    "v_fma_f32 v238, v238, v234, v234\n"
    "v_fma_f32 v239, v239, v235, v235\n"
    "v_mfma_f32_16x16x32_bf16 v[96:99], a[144:147], a[44:47], v[96:99]\n"
    "ds_read_b128 a[64:67], %[re0] offset:32768\n"
    "v_accvgpr_read_b32 v219, a203\n"
    "v_mfma_f32_16x16x32_bf16 v[96:99], a[140:143], a[40:43], v[96:99]\n"
    "buffer_load_dwordx4 a[88:91], %[l16], %[srdW], s54 offen offset:2048\n"
    "v_accvgpr_read_b32 v220, a204\n"
    "v_mfma_f32_16x16x32_bf16 v[96:99], a[136:139], a[16:19], v[96:99]\n"
    "v_rcp_f32 v228, v228\n"
    "v_mfma_f32_16x16x32_bf16 v[96:99], a[132:135], a[12:15], v[96:99]\n"
    "v_rcp_f32 v229, v229\n"
    "v_mfma_f32_16x16x32_bf16 v[96:99], a[128:131], a[8:11], v[96:99]\n"
    "v_rcp_f32 v230, v230\n"
    "v_mfma_f32_16x16x32_bf16 v[100:103], a[152:155], a[8:11], v[100:103]\n"
    "ds_read_b128 a[8:11], %[rd0] offset:53248\n"
    "v_accvgpr_read_b32 v221, a205\n"
    "v_mfma_f32_16x16x32_bf16 v[100:103], a[156:159], a[12:15], v[100:103]\n"
    "v_rcp_f32 v231, v231\n"
    "v_mfma_f32_16x16x32_bf16 v[100:103], a[160:163], a[16:19], v[100:103]\n"
    "v_rcp_f32 v236, v236\n"
    "v_mfma_f32_16x16x32_bf16 v[100:103], a[164:167], a[40:43], v[100:103]\n"
    "ds_read_b128 a[40:43], %[rd1] offset:53248\n"
    "v_accvgpr_read_b32 v222, a206\n"
    "v_mfma_f32_16x16x32_bf16 v[100:103], a[168:171], a[44:47], v[100:103]\n"
    "buffer_load_dwordx4 a[92:95], %[l16], %[srdW], s54 offen offset:3072\n"
    "v_accvgpr_read_b32 v223, a207\n"
    "v_mfma_f32_16x16x32_bf16 v[100:103], a[172:175], a[48:51], v[100:103]\n"
    "v_rcp_f32 v237, v237\n"
    "v_mfma_f32_16x16x32_bf16 v[104:107], a[148:151], a[52:55], v[104:107]\n"
    "s_add_u32 s54, s54, 4096\n"
    "v_rcp_f32 v238, v238\n"
    "v_mfma_f32_16x16x32_bf16 v[104:107], a[144:147], a[48:51], v[104:107]\n"
    "ds_read_b128 a[72:75], %[re1] offset:32768\n"
    "v_mul_f32 v212, s73, v212\n"
    "v_mfma_f32_16x16x32_bf16 v[104:107], a[140:143], a[44:47], v[104:107]\n"
    "buffer_load_dwordx4 a[96:99], %[l16], %[srdW], s54 offen offset:0\n"
    "v_mul_f32 v213, s73, v213\n"
    "v_mfma_f32_16x16x32_bf16 v[104:107], a[136:139], a[20:23], v[104:107]\n"
    "v_rcp_f32 v239, v239\n"
    "v_mfma_f32_16x16x32_bf16 v[104:107], a[132:135], a[16:19], v[104:107]\n"
    "v_fma_f32 v228, -abs(v224), v228, v228\n"
    "v_fma_f32 v229, -abs(v225), v229, v229\n"
    "v_mfma_f32_16x16x32_bf16 v[104:107], a[128:131], a[12:15], v[104:107]\n"
    "v_fma_f32 v230, -abs(v226), v230, v230\n"
    "v_fma_f32 v231, -abs(v227), v231, v231\n"
    "v_mfma_f32_16x16x32_bf16 v[108:111], a[152:155], a[12:15], v[108:111]\n"
    "ds_read_b128 a[12:15], %[rd0] offset:55296\n"
    "v_fma_f32 v236, -abs(v232), v236, v236\n"
    "v_mfma_f32_16x16x32_bf16 v[108:111], a[156:159], a[16:19], v[108:111]\n"
    "v_fma_f32 v237, -abs(v233), v237, v237\n"
    "v_fma_f32 v238, -abs(v234), v238, v238\n"
    "v_mfma_f32_16x16x32_bf16 v[108:111], a[160:163], a[20:23], v[108:111]\n"
    "v_fma_f32 v239, -abs(v235), v239, v239\n"
    "v_cvt_pk_bf16_f32 v20, v228, v229\n"
    "v_mfma_f32_16x16x32_bf16 v[108:111], a[164:167], a[44:47], v[108:111]\n"
    "ds_read_b128 a[44:47], %[rd1] offset:55296\n"
    "v_cvt_pk_bf16_f32 v21, v230, v231\n"
    "v_mfma_f32_16x16x32_bf16 v[108:111], a[168:171], a[48:51], v[108:111]\n"
    "buffer_load_dwordx4 a[100:103], %[l16], %[srdW], s54 offen offset:1024\n"
    "v_cvt_pk_bf16_f32 v22, v236, v237\n"
    "v_mfma_f32_16x16x32_bf16 v[108:111], a[172:175], a[52:55], v[108:111]\n"
    "s_waitcnt lgkmcnt(10)\n"
    "v_cvt_pk_bf16_f32 v23, v238, v239\n"
    "v_mul_f32 v214, s73, v214\n"
    "v_mfma_f32_16x16x32_bf16 v[112:115], a[148:151], a[56:59], v[112:115]\n"
    "ds_write_b64 %[vsw], v[20:21] offset:12288\n"
    "v_mfma_f32_16x16x32_bf16 v[112:115], a[144:147], a[52:55], v[112:115]\n"
    "ds_read_b128 a[68:71], %[re0] offset:34816\n"
    "v_mul_f32 v215, s73, v215\n"
    "v_mfma_f32_16x16x32_bf16 v[112:115], a[140:143], a[48:51], v[112:115]\n"
    "buffer_load_dwordx4 a[104:107], %[l16], %[srdW], s54 offen offset:2048\n"
    "v_mul_f32 v220, s73, v220\n"
    "v_mfma_f32_16x16x32_bf16 v[112:115], a[136:139], a[24:27], v[112:115]\n"
    "ds_write_b64 %[vsw], v[22:23] offset:12416\n"
    "v_mfma_f32_16x16x32_bf16 v[112:115], a[132:135], a[20:23], v[112:115]\n"
    "v_mul_f32 v221, s73, v221\n"
    "v_mul_f32 v222, s73, v222\n"
    "v_mfma_f32_16x16x32_bf16 v[112:115], a[128:131], a[16:19], v[112:115]\n"
    "v_mul_f32 v223, s73, v223\n"
    "v_mul_f32 v208, s72, v208\n"
    "v_mfma_f32_16x16x32_bf16 v[116:119], a[152:155], a[16:19], v[116:119]\n"
    "ds_read_b128 a[16:19], %[rd0] offset:57344\n"
    "v_mul_f32 v209, s72, v209\n"
    "v_mfma_f32_16x16x32_bf16 v[116:119], a[156:159], a[20:23], v[116:119]\n"
    "v_mul_f32 v210, s72, v210\n"
    "v_mul_f32 v211, s72, v211\n"
    "v_mfma_f32_16x16x32_bf16 v[116:119], a[160:163], a[24:27], v[116:119]\n"
    "v_mul_f32 v216, s72, v216\n"
    "v_mul_f32 v217, s72, v217\n"
    "v_mfma_f32_16x16x32_bf16 v[116:119], a[164:167], a[48:51], v[116:119]\n"
    "ds_read_b128 a[48:51], %[rd1] offset:57344\n"
    "v_mul_f32 v218, s72, v218\n"
    "v_mfma_f32_16x16x32_bf16 v[116:119], a[168:171], a[52:55], v[116:119]\n"
    "buffer_load_dwordx4 a[108:111], %[l16], %[srdW], s54 offen offset:3072\n"
    "v_mul_f32 v219, s72, v219\n"
    "v_mfma_f32_16x16x32_bf16 v[116:119], a[172:175], a[56:59], v[116:119]\n"
    "v_exp_f32 v212, v212\n"
    "v_mfma_f32_16x16x32_bf16 v[120:123], a[148:151], a[60:63], v[120:123]\n"
    "s_add_u32 s54, s54, 4096\n"
    "v_exp_f32 v213, v213\n"
    "v_mfma_f32_16x16x32_bf16 v[120:123], a[144:147], a[56:59], v[120:123]\n"
    "ds_read_b128 a[76:79], %[re1] offset:34816\n"
    "v_accvgpr_read_b32 v224, a208\n"
    "v_mfma_f32_16x16x32_bf16 v[120:123], a[140:143], a[52:55], v[120:123]\n"
    "buffer_load_dwordx4 a[112:115], %[l16], %[srdW], s54 offen offset:0\n"
    "v_accvgpr_read_b32 v225, a209\n"
    "v_mfma_f32_16x16x32_bf16 v[120:123], a[136:139], a[28:31], v[120:123]\n"
    "v_exp_f32 v214, v214\n"
    "v_mfma_f32_16x16x32_bf16 v[120:123], a[132:135], a[24:27], v[120:123]\n"
    "v_exp_f32 v215, v215\n"
    "v_mfma_f32_16x16x32_bf16 v[120:123], a[128:131], a[20:23], v[120:123]\n"
    "v_exp_f32 v220, v220\n"
    "v_mfma_f32_16x16x32_bf16 v[124:127], a[152:155], a[20:23], v[124:127]\n"
    "ds_read_b128 a[20:23], %[rd0] offset:59392\n"
    "v_accvgpr_read_b32 v226, a210\n"
    "v_mfma_f32_16x16x32_bf16 v[124:127], a[156:159], a[24:27], v[124:127]\n"
    "v_exp_f32 v221, v221\n"
    "v_mfma_f32_16x16x32_bf16 v[124:127], a[160:163], a[28:31], v[124:127]\n"
    "v_exp_f32 v222, v222\n"
    "v_mfma_f32_16x16x32_bf16 v[124:127], a[164:167], a[52:55], v[124:127]\n"
    "ds_read_b128 a[52:55], %[rd1] offset:59392\n"
    "v_accvgpr_read_b32 v227, a211\n"
    "v_mfma_f32_16x16x32_bf16 v[124:127], a[168:171], a[56:59], v[124:127]\n"
    "buffer_load_dwordx4 a[116:119], %[l16], %[srdW], s54 offen offset:1024\n"
    "v_accvgpr_read_b32 v228, a212\n"
    "v_mfma_f32_16x16x32_bf16 v[124:127], a[172:175], a[60:63], v[124:127]\n"
    "s_waitcnt lgkmcnt(14)\n"
    "v_exp_f32 v223, v223\n"
    "v_mfma_f32_16x16x32_bf16 v[128:131], a[148:151], a[32:35], v[128:131]\n"
    "v_exp_f32_e64 v16, -abs(v208)\n"
    "v_mfma_f32_16x16x32_bf16 v[128:131], a[144:147], a[60:63], v[128:131]\n"
    "v_exp_f32_e64 v17, -abs(v209)\n"
    "v_mfma_f32_16x16x32_bf16 v[128:131], a[140:143], a[56:59], v[128:131]\n"
    "buffer_load_dwordx4 a[120:123], %[l16], %[srdW], s54 offen offset:2048\n"
    "v_accvgpr_read_b32 v229, a213\n"
    "v_mfma_f32_16x16x32_bf16 v[128:131], a[136:139], a[0:3], v[128:131]\n"
    "v_exp_f32_e64 v18, -abs(v210)\n"
    "v_mfma_f32_16x16x32_bf16 v[128:131], a[132:135], a[28:31], v[128:131]\n"
    "v_exp_f32_e64 v19, -abs(v211)\n"
    "v_mfma_f32_16x16x32_bf16 v[128:131], a[128:131], a[24:27], v[128:131]\n"
    "v_bfi_b32 v208, s61, 0.5, v208\n"
    "v_bfi_b32 v209, s61, 0.5, v209\n"
    "v_mfma_f32_16x16x32_bf16 v[132:135], a[152:155], a[24:27], v[132:135]\n"
    "ds_read_b128 a[24:27], %[rd0] offset:61440\n"
    "v_bfi_b32 v210, s61, 0.5, v210\n"
    "v_mfma_f32_16x16x32_bf16 v[132:135], a[156:159], a[28:31], v[132:135]\n"
    "v_bfi_b32 v211, s61, 0.5, v211\n"
    "v_fma_f32 v208, v16, v208, v208\n"
    "v_mfma_f32_16x16x32_bf16 v[132:135], a[160:163], a[0:3], v[132:135]\n"
    "v_fma_f32 v209, v17, v209, v209\n"
    "v_fma_f32 v210, v18, v210, v210\n"
    "v_mfma_f32_16x16x32_bf16 v[132:135], a[164:167], a[56:59], v[132:135]\n"
    "ds_read_b128 a[56:59], %[rd1] offset:61440\n"
    "v_fma_f32 v211, v19, v211, v211\n"
    "v_mfma_f32_16x16x32_bf16 v[132:135], a[168:171], a[60:63], v[132:135]\n"
    "buffer_load_dwordx4 a[124:127], %[l16], %[srdW], s54 offen offset:3072\n"
    "v_accvgpr_read_b32 v230, a214\n"
    "v_mfma_f32_16x16x32_bf16 v[132:135], a[172:175], a[32:35], v[132:135]\n"
    "v_exp_f32_e64 v16, -abs(v216)\n"
    "v_mfma_f32_16x16x32_bf16 v[136:139], a[148:151], a[36:39], v[136:139]\n"
    "s_add_u32 s54, s54, 4096\n"
    "v_exp_f32_e64 v17, -abs(v217)\n"
    "v_mfma_f32_16x16x32_bf16 v[136:139], a[144:147], a[32:35], v[136:139]\n"
    "s_add_u32 m0, s69, 0\n"
    "s_add_u32 s57, s70, 0\n"
    "v_accvgpr_read_b32 v231, a215\n"
    "v_mfma_f32_16x16x32_bf16 v[136:139], a[140:143], a[60:63], v[136:139]\n"
    "buffer_load_dwordx4 %[l16], %[srdD], s57 offen sc1 lds\n"
    "v_accvgpr_read_b32 v232, a216\n"
    "v_mfma_f32_16x16x32_bf16 v[136:139], a[136:139], a[4:7], v[136:139]\n"
    "s_add_u32 m0, s69, 4096\n"
    "s_add_u32 s57, s70, 4096\n"
    "v_accvgpr_read_b32 v233, a217\n"
    "v_mfma_f32_16x16x32_bf16 v[136:139], a[132:135], a[0:3], v[136:139]\n"
    "buffer_load_dwordx4 %[l16], %[srdD], s57 offen sc1 lds\n"
    "v_accvgpr_read_b32 v234, a218\n"
    "v_mfma_f32_16x16x32_bf16 v[136:139], a[128:131], a[28:31], v[136:139]\n"
    "s_add_u32 s64, s56, 0\n"
    "buffer_load_dwordx4 v[48:51], %[xr1], %[srdX], s64 offen\n"
    "v_mfma_f32_16x16x32_bf16 v[140:143], a[152:155], a[28:31], v[140:143]\n"
    "ds_read_b128 a[28:31], %[rd0] offset:63488\n"
    "v_accvgpr_read_b32 v235, a219\n"
    "v_mfma_f32_16x16x32_bf16 v[140:143], a[156:159], a[0:3], v[140:143]\n"
    "v_exp_f32_e64 v18, -abs(v218)\n"
    "v_mfma_f32_16x16x32_bf16 v[140:143], a[160:163], a[4:7], v[140:143]\n"
    "v_exp_f32_e64 v19, -abs(v219)\n"
    "v_mfma_f32_16x16x32_bf16 v[140:143], a[164:167], a[60:63], v[140:143]\n"
    "ds_read_b128 a[60:63], %[rd1] offset:63488\n"
    "v_bfi_b32 v216, s61, 0.5, v216\n"
    "v_mfma_f32_16x16x32_bf16 v[140:143], a[168:171], a[32:35], v[140:143]\n"
    "v_bfi_b32 v217, s61, 0.5, v217\n"
    "v_bfi_b32 v218, s61, 0.5, v218\n"
    "v_mfma_f32_16x16x32_bf16 v[140:143], a[172:175], a[36:39], v[140:143]\n"
    "s_waitcnt lgkmcnt(12)\n"
    "v_bfi_b32 v219, s61, 0.5, v219\n"
    "v_fma_f32 v216, v16, v216, v216\n"
    "v_mfma_f32_16x16x32_bf16 v[144:147], a[148:151], a[40:43], v[144:147]\n"
    "v_xor_b32 %[rd0], 0x10000, %[rd0]\n"
    "v_fma_f32 v217, v17, v217, v217\n"
    "v_mfma_f32_16x16x32_bf16 v[144:147], a[144:147], a[36:39], v[144:147]\n"
    "v_xor_b32 %[rd1], 0x10000, %[rd1]\n"
    "v_fma_f32 v218, v18, v218, v218\n"
    "v_mfma_f32_16x16x32_bf16 v[144:147], a[140:143], a[32:35], v[144:147]\n"
    "s_add_u32 s63, s63, 1\n"
    "v_fma_f32 v219, v19, v219, v219\n"
    "v_fma_f32 v212, v212, v208, v208\n"
    "v_mfma_f32_16x16x32_bf16 v[144:147], a[136:139], a[8:11], v[144:147]\n"
    "v_fma_f32 v213, v213, v209, v209\n"
    "v_fma_f32 v214, v214, v210, v210\n"
    "v_mfma_f32_16x16x32_bf16 v[144:147], a[132:135], a[4:7], v[144:147]\n"
    "v_fma_f32 v215, v215, v211, v211\n"
    "v_fma_f32 v220, v220, v216, v216\n"
    "v_mfma_f32_16x16x32_bf16 v[144:147], a[128:131], a[0:3], v[144:147]\n"
    "v_fma_f32 v221, v221, v217, v217\n"
    "v_fma_f32 v222, v222, v218, v218\n"
    "v_mfma_f32_16x16x32_bf16 v[148:151], a[152:155], a[0:3], v[148:151]\n"
    "s_waitcnt lgkmcnt(0)\n"
    "v_mov_b32 v13, s63\n"
    "v_mfma_f32_16x16x32_bf16 v[148:151], a[156:159], a[4:7], v[148:151]\n"
    "ds_write_b32 v14, v13\n"
    "v_mfma_f32_16x16x32_bf16 v[148:151], a[160:163], a[8:11], v[148:151]\n"
    "v_fma_f32 v223, v223, v219, v219\n"
    "v_accvgpr_read_b32 v236, a220\n"
    "v_mfma_f32_16x16x32_bf16 v[148:151], a[164:167], a[32:35], v[148:151]\n"
    "v_rcp_f32 v212, v212\n"
    "v_mfma_f32_16x16x32_bf16 v[148:151], a[168:171], a[36:39], v[148:151]\n"
    "v_rcp_f32 v213, v213\n"
    "v_mfma_f32_16x16x32_bf16 v[148:151], a[172:175], a[40:43], v[148:151]\n"
    "v_rcp_f32 v214, v214\n"
    "v_mfma_f32_16x16x32_bf16 v[152:155], a[148:151], a[44:47], v[152:155]\n"
    "v_xor_b32 %[re0], 0x10000, %[re0]\n"
    "v_accvgpr_read_b32 v237, a221\n"
    "v_mfma_f32_16x16x32_bf16 v[152:155], a[144:147], a[40:43], v[152:155]\n"
    "v_xor_b32 %[re1], 0x10000, %[re1]\n"
    "v_accvgpr_read_b32 v238, a222\n"
    "v_mfma_f32_16x16x32_bf16 v[152:155], a[140:143], a[36:39], v[152:155]\n"
    "v_rcp_f32 v215, v215\n"
    "v_mfma_f32_16x16x32_bf16 v[152:155], a[136:139], a[12:15], v[152:155]\n"
    "v_rcp_f32 v220, v220\n"
    "v_mfma_f32_16x16x32_bf16 v[152:155], a[132:135], a[8:11], v[152:155]\n"
    "ds_read_b32 v13, v12\n"
    "v_accvgpr_read_b32 v239, a223\n"
    "v_mfma_f32_16x16x32_bf16 v[152:155], a[128:131], a[4:7], v[152:155]\n"
    "v_rcp_f32 v221, v221\n"
    "v_mfma_f32_16x16x32_bf16 v[156:159], a[152:155], a[4:7], v[156:159]\n"
    "v_rcp_f32 v222, v222\n"
    "v_mfma_f32_16x16x32_bf16 v[156:159], a[156:159], a[8:11], v[156:159]\n"
    "v_rcp_f32 v223, v223\n"
    "v_mfma_f32_16x16x32_bf16 v[156:159], a[160:163], a[12:15], v[156:159]\n"
    "v_fma_f32 v212, -abs(v208), v212, v212\n"
    "v_fma_f32 v213, -abs(v209), v213, v213\n"
    "v_mfma_f32_16x16x32_bf16 v[156:159], a[164:167], a[36:39], v[156:159]\n"
    "v_fma_f32 v214, -abs(v210), v214, v214\n"
    "v_fma_f32 v215, -abs(v211), v215, v215\n"
    "v_mfma_f32_16x16x32_bf16 v[156:159], a[168:171], a[40:43], v[156:159]\n"
    "s_waitcnt lgkmcnt(0)\n"
    "v_cmp_gt_u32 vcc, s63, v13\n"
    "v_fma_f32 v220, -abs(v216), v220, v220\n"
    "v_mfma_f32_16x16x32_bf16 v[156:159], a[172:175], a[44:47], v[156:159]\n"
    "s_cbranch_vccnz Ls14%=\n"
    "Lb14%=:\n"
    "v_fma_f32 v221, -abs(v217), v221, v221\n"
    "v_mfma_f32_16x16x32_bf16 v[160:163], a[148:151], a[48:51], v[160:163]\n"
    "ds_read_b128 a[0:3], %[rd0] offset:0\n"
    "v_fma_f32 v222, -abs(v218), v222, v222\n"
    "v_mfma_f32_16x16x32_bf16 v[160:163], a[144:147], a[44:47], v[160:163]\n"
    "ds_read_b128 a[32:35], %[rd1] offset:0\n"
    "v_fma_f32 v223, -abs(v219), v223, v223\n"
    "v_mfma_f32_16x16x32_bf16 v[160:163], a[140:143], a[40:43], v[160:163]\n"
    "ds_read_b128 a[4:7], %[rd0] offset:2048\n"
    "v_cvt_pk_bf16_f32 v16, v212, v213\n"
    "v_mfma_f32_16x16x32_bf16 v[160:163], a[136:139], a[16:19], v[160:163]\n"
    "ds_read_b128 a[36:39], %[rd1] offset:2048\n"
    "v_cvt_pk_bf16_f32 v17, v214, v215\n"
    "v_mfma_f32_16x16x32_bf16 v[160:163], a[132:135], a[12:15], v[160:163]\n"
    "ds_read_b128 v[24:27], v14 offset:12288\n"
    "v_cvt_pk_bf16_f32 v18, v220, v221\n"
    "v_mfma_f32_16x16x32_bf16 v[160:163], a[128:131], a[8:11], v[160:163]\n"
    "ds_read_b128 v[28:31], v14 offset:16384\n"
    "v_cvt_pk_bf16_f32 v19, v222, v223\n"
    "v_mfma_f32_16x16x32_bf16 v[164:167], a[152:155], a[8:11], v[164:167]\n"
    "ds_read_b128 a[8:11], %[rd0] offset:4096\n"
    "v_mul_f32 v228, s73, v228\n"
    "v_mfma_f32_16x16x32_bf16 v[164:167], a[156:159], a[12:15], v[164:167]\n"
    "s_add_u32 m0, s69, 8192\n"
    "s_add_u32 s57, s70, 8192\n"
    "v_mul_f32 v229, s73, v229\n"
    "v_mfma_f32_16x16x32_bf16 v[164:167], a[160:163], a[16:19], v[164:167]\n"
    "buffer_load_dwordx4 %[l16], %[srdD], s57 offen sc1 lds\n"
    "v_mul_f32 v230, s73, v230\n"
    "v_mfma_f32_16x16x32_bf16 v[164:167], a[164:167], a[40:43], v[164:167]\n"
    "ds_read_b128 a[40:43], %[rd1] offset:4096\n"
    "v_mul_f32 v231, s73, v231\n"
    "v_mfma_f32_16x16x32_bf16 v[164:167], a[168:171], a[44:47], v[164:167]\n"
    "ds_write_b64 %[vsw], v[16:17] offset:16384\n"
    "v_mfma_f32_16x16x32_bf16 v[164:167], a[172:175], a[48:51], v[164:167]\n"
    "ds_write_b64 %[vsw], v[18:19] offset:16512\n"
    "v_mfma_f32_16x16x32_bf16 v[168:171], a[148:151], a[52:55], v[168:171]\n"
    "v_mul_f32 v236, s73, v236\n"
    "v_mul_f32 v237, s73, v237\n"
    "v_mfma_f32_16x16x32_bf16 v[168:171], a[144:147], a[48:51], v[168:171]\n"
    "v_mul_f32 v238, s73, v238\n"
    "v_mul_f32 v239, s73, v239\n"
    "v_mfma_f32_16x16x32_bf16 v[168:171], a[140:143], a[44:47], v[168:171]\n"
    "s_add_u32 m0, s69, 12288\n"
    "s_add_u32 s57, s70, 12288\n"
    "s_waitcnt lgkmcnt(5)\n"
    "v_mfma_f32_16x16x32_bf16 v[168:171], a[136:139], a[20:23], v[168:171]\n"
    "buffer_load_dwordx4 %[l16], %[srdD], s57 offen sc1 lds\n"
    "v_mul_f32 v224, s72, v224\n"
    "v_mfma_f32_16x16x32_bf16 v[168:171], a[132:135], a[16:19], v[168:171]\n"
    "s_mov_b32 s60, 18432\n"
    "buffer_store_dwordx4 v[24:27], %[oo], s[76:79], s60 offen sc0 sc1\n"
    "v_mfma_f32_16x16x32_bf16 v[168:171], a[128:131], a[12:15], v[168:171]\n"
    "v_mul_f32 v225, s72, v225\n"
    "v_mul_f32 v226, s72, v226\n"
    "v_mfma_f32_16x16x32_bf16 v[172:175], a[152:155], a[12:15], v[172:175]\n"
    "ds_read_b128 a[12:15], %[rd0] offset:6144\n"
    "v_mul_f32 v227, s72, v227\n"
    "v_mfma_f32_16x16x32_bf16 v[172:175], a[156:159], a[16:19], v[172:175]\n"
    "v_lshlrev_b32 v68, 16, v36\n"
    "v_and_b32 v69, s65, v36\n"
    "v_mfma_f32_16x16x32_bf16 v[172:175], a[160:163], a[20:23], v[172:175]\n"
    "v_lshlrev_b32 v70, 16, v37\n"
    "v_and_b32 v71, s65, v37\n"
    "v_mfma_f32_16x16x32_bf16 v[172:175], a[164:167], a[44:47], v[172:175]\n"
    "ds_read_b128 a[44:47], %[rd1] offset:6144\n"
    "v_lshlrev_b32 v72, 16, v38\n"
    "v_mfma_f32_16x16x32_bf16 v[172:175], a[168:171], a[48:51], v[172:175]\n"
    "v_and_b32 v73, s65, v38\n"
    "v_lshlrev_b32 v74, 16, v39\n"
    "v_mfma_f32_16x16x32_bf16 v[172:175], a[172:175], a[52:55], v[172:175]\n"
    "v_and_b32 v75, s65, v39\n"
    "v_sub_f32 v60, v60, v68\n"
    "v_mfma_f32_16x16x32_bf16 v[176:179], a[148:151], a[56:59], v[176:179]\n"
    "v_sub_f32 v61, v61, v69\n"
    "v_sub_f32 v62, v62, v70\n"
    "v_mfma_f32_16x16x32_bf16 v[176:179], a[144:147], a[52:55], v[176:179]\n"
    "v_sub_f32 v63, v63, v71\n"
    "v_sub_f32 v64, v64, v72\n"
    "v_mfma_f32_16x16x32_bf16 v[176:179], a[140:143], a[48:51], v[176:179]\n"
    "s_add_u32 m0, s69, 16384\n"
    "s_add_u32 s57, s70, 16384\n"
    "v_sub_f32 v65, v65, v73\n"
    "v_mfma_f32_16x16x32_bf16 v[176:179], a[136:139], a[24:27], v[176:179]\n"
    "buffer_load_dwordx4 %[l16], %[srdD], s57 offen sc1 lds\n"
    "v_sub_f32 v66, v66, v74\n"
    "v_mfma_f32_16x16x32_bf16 v[176:179], a[132:135], a[20:23], v[176:179]\n"
    "v_sub_f32 v67, v67, v75\n"
    "v_cvt_pk_bf16_f32 v36, v60, v61\n"
    "v_mfma_f32_16x16x32_bf16 v[176:179], a[128:131], a[16:19], v[176:179]\n"
    "v_cvt_pk_bf16_f32 v37, v62, v63\n"
    "v_cvt_pk_bf16_f32 v38, v64, v65\n"
    "v_mfma_f32_16x16x32_bf16 v[180:183], a[152:155], a[16:19], v[180:183]\n"
    "ds_read_b128 a[16:19], %[rd0] offset:8192\n"
    "v_cvt_pk_bf16_f32 v39, v66, v67\n"
    "v_mfma_f32_16x16x32_bf16 v[180:183], a[156:159], a[20:23], v[180:183]\n"
    "s_add_u32 s64, s71, 262144\n"
    "buffer_store_dwordx4 v[36:39], %[vst], %[srdD], s64 offen sc0 sc1\n"
    "v_mfma_f32_16x16x32_bf16 v[180:183], a[160:163], a[24:27], v[180:183]\n"
    "v_mul_f32 v232, s72, v232\n"
    "v_mul_f32 v233, s72, v233\n"
    "v_mfma_f32_16x16x32_bf16 v[180:183], a[164:167], a[48:51], v[180:183]\n"
    "ds_read_b128 a[48:51], %[rd1] offset:8192\n"
    "v_mul_f32 v234, s72, v234\n"
    "v_mfma_f32_16x16x32_bf16 v[180:183], a[168:171], a[52:55], v[180:183]\n"
    "v_mul_f32 v235, s72, v235\n"
    "v_accvgpr_read_b32 v208, a224\n"
    "v_mfma_f32_16x16x32_bf16 v[180:183], a[172:175], a[56:59], v[180:183]\n"
    "v_exp_f32 v228, v228\n"
    "v_mfma_f32_16x16x32_bf16 v[184:187], a[148:151], a[60:63], v[184:187]\n"
    "v_exp_f32 v229, v229\n"
    "v_mfma_f32_16x16x32_bf16 v[184:187], a[144:147], a[56:59], v[184:187]\n"
    "v_exp_f32 v230, v230\n"
    "v_mfma_f32_16x16x32_bf16 v[184:187], a[140:143], a[52:55], v[184:187]\n"
    "s_add_u32 m0, s69, 20480\n"
    "s_add_u32 s57, s70, 20480\n"
    "v_accvgpr_read_b32 v209, a225\n"
    "v_mfma_f32_16x16x32_bf16 v[184:187], a[136:139], a[28:31], v[184:187]\n"
    "buffer_load_dwordx4 %[l16], %[srdD], s57 offen sc1 lds\n"
    "v_accvgpr_read_b32 v210, a226\n"
    "v_mfma_f32_16x16x32_bf16 v[184:187], a[132:135], a[24:27], v[184:187]\n"
    "v_exp_f32 v231, v231\n"
    "v_mfma_f32_16x16x32_bf16 v[184:187], a[128:131], a[20:23], v[184:187]\n"
    "v_exp_f32 v236, v236\n"
    "v_mfma_f32_16x16x32_bf16 v[188:191], a[152:155], a[20:23], v[188:191]\n"
    "ds_read_b128 a[20:23], %[rd0] offset:10240\n"
    "v_accvgpr_read_b32 v211, a227\n"
    "v_mfma_f32_16x16x32_bf16 v[188:191], a[156:159], a[24:27], v[188:191]\n"
    "v_exp_f32 v237, v237\n"
    "v_mfma_f32_16x16x32_bf16 v[188:191], a[160:163], a[28:31], v[188:191]\n"
    "v_exp_f32 v238, v238\n"
    "v_mfma_f32_16x16x32_bf16 v[188:191], a[164:167], a[52:55], v[188:191]\n"
    "ds_read_b128 a[52:55], %[rd1] offset:10240\n"
    "v_accvgpr_read_b32 v212, a228\n"
    "v_mfma_f32_16x16x32_bf16 v[188:191], a[168:171], a[56:59], v[188:191]\n"
    "v_exp_f32 v239, v239\n"
    "v_mfma_f32_16x16x32_bf16 v[188:191], a[172:175], a[60:63], v[188:191]\n"
    "v_exp_f32_e64 v20, -abs(v224)\n"
    "v_mfma_f32_16x16x32_bf16 v[192:195], a[148:151], a[72:75], v[192:195]\n"
    "v_exp_f32_e64 v21, -abs(v225)\n"
    "v_mfma_f32_16x16x32_bf16 v[192:195], a[144:147], a[60:63], v[192:195]\n"
    "v_exp_f32_e64 v22, -abs(v226)\n"
    "v_mfma_f32_16x16x32_bf16 v[192:195], a[140:143], a[56:59], v[192:195]\n"
    "s_add_u32 m0, s69, 24576\n"
    "s_add_u32 s57, s70, 24576\n"
    "v_accvgpr_read_b32 v213, a229\n"
    "v_mfma_f32_16x16x32_bf16 v[192:195], a[136:139], a[64:67], v[192:195]\n"
    "buffer_load_dwordx4 %[l16], %[srdD], s57 offen sc1 lds\n"
    "v_accvgpr_read_b32 v214, a230\n"
    "v_mfma_f32_16x16x32_bf16 v[192:195], a[132:135], a[28:31], v[192:195]\n"
    "v_exp_f32_e64 v23, -abs(v227)\n"
    "v_mfma_f32_16x16x32_bf16 v[192:195], a[128:131], a[24:27], v[192:195]\n"
    "v_bfi_b32 v224, s61, 0.5, v224\n"
    "v_bfi_b32 v225, s61, 0.5, v225\n"
    "v_mfma_f32_16x16x32_bf16 v[196:199], a[152:155], a[24:27], v[196:199]\n"
    "ds_read_b128 a[24:27], %[rd0] offset:12288\n"
    "v_bfi_b32 v226, s61, 0.5, v226\n"
    "v_mfma_f32_16x16x32_bf16 v[196:199], a[156:159], a[28:31], v[196:199]\n"
    "v_bfi_b32 v227, s61, 0.5, v227\n"
    "v_fma_f32 v224, v20, v224, v224\n"
    "v_mfma_f32_16x16x32_bf16 v[196:199], a[160:163], a[64:67], v[196:199]\n"
    "s_waitcnt lgkmcnt(11)\n"
    "v_fma_f32 v225, v21, v225, v225\n"
    "v_fma_f32 v226, v22, v226, v226\n"
    "v_mfma_f32_16x16x32_bf16 v[196:199], a[164:167], a[56:59], v[196:199]\n"
    "ds_read_b128 a[56:59], %[rd1] offset:12288\n"
    "v_fma_f32 v227, v23, v227, v227\n"
    "v_mfma_f32_16x16x32_bf16 v[196:199], a[168:171], a[60:63], v[196:199]\n"
    "s_mov_b32 s60, 26624\n"
    "buffer_store_dwordx4 v[28:31], %[oo], s[76:79], s60 offen sc0 sc1\n"
    "v_mfma_f32_16x16x32_bf16 v[196:199], a[172:175], a[72:75], v[196:199]\n"
    "s_add_u32 s64, s56, 2048\n"
    "buffer_load_dwordx4 v[52:55], %[xr1], %[srdX], s64 offen\n"
    "v_mfma_f32_16x16x32_bf16 v[200:203], a[148:151], a[76:79], v[200:203]\n"
    "v_exp_f32_e64 v20, -abs(v232)\n"
    "v_mfma_f32_16x16x32_bf16 v[200:203], a[144:147], a[72:75], v[200:203]\n"
    "v_exp_f32_e64 v21, -abs(v233)\n"
    "v_mfma_f32_16x16x32_bf16 v[200:203], a[140:143], a[60:63], v[200:203]\n"
    "s_add_u32 m0, s69, 28672\n"
    "s_add_u32 s57, s70, 28672\n"
    "v_accvgpr_read_b32 v215, a231\n"
    "v_mfma_f32_16x16x32_bf16 v[200:203], a[136:139], a[68:71], v[200:203]\n"
    "buffer_load_dwordx4 %[l16], %[srdD], s57 offen sc1 lds\n"
    "v_accvgpr_read_b32 v216, a232\n"
    "v_mfma_f32_16x16x32_bf16 v[200:203], a[132:135], a[64:67], v[200:203]\n"
    "v_exp_f32_e64 v22, -abs(v234)\n"
    "v_mfma_f32_16x16x32_bf16 v[200:203], a[128:131], a[28:31], v[200:203]\n"
    "s_add_u32 s69, s69, 0x8000\n"
    "s_and_b32 s69, s69, 0x1ffff\n"
    "v_accvgpr_read_b32 v217, a233\n"
    "v_mfma_f32_16x16x32_bf16 v[204:207], a[152:155], a[28:31], v[204:207]\n"
    "ds_read_b128 a[28:31], %[rd0] offset:14336\n"
    "v_accvgpr_read_b32 v218, a234\n"
    "v_mfma_f32_16x16x32_bf16 v[204:207], a[156:159], a[64:67], v[204:207]\n"
    "s_add_u32 s70, s70, 0x8000\n"
    "v_exp_f32_e64 v23, -abs(v235)\n"
    "v_mfma_f32_16x16x32_bf16 v[204:207], a[160:163], a[68:71], v[204:207]\n"
    "v_bfi_b32 v232, s61, 0.5, v232\n"
    "v_bfi_b32 v233, s61, 0.5, v233\n"
    "v_mfma_f32_16x16x32_bf16 v[204:207], a[164:167], a[60:63], v[204:207]\n"
    "ds_read_b128 a[60:63], %[rd1] offset:14336\n"
    "v_bfi_b32 v234, s61, 0.5, v234\n"
    "v_mfma_f32_16x16x32_bf16 v[204:207], a[168:171], a[72:75], v[204:207]\n"
    "v_bfi_b32 v235, s61, 0.5, v235\n"
    "v_fma_f32 v232, v20, v232, v232\n"
    "v_mfma_f32_16x16x32_bf16 v[204:207], a[172:175], a[76:79], v[204:207]\n"
    "s_waitcnt vmcnt(13)\n"
    "s_waitcnt lgkmcnt(8)\n"
    "s_cmp_lg_u32 s67, 0\n"
    "s_cbranch_scc1 Lc15%=\n"
    "v_mfma_f32_16x16x32_bf16 v[80:83], a[100:103], a[40:43], v[80:83]\n"
    "v_mfma_f32_16x16x32_bf16 v[80:83], a[96:99], a[36:39], v[80:83]\n"
    "v_mfma_f32_16x16x32_bf16 v[80:83], a[92:95], a[32:35], v[80:83]\n"
    "v_mfma_f32_16x16x32_bf16 v[80:83], a[88:91], a[8:11], v[80:83]\n"
    "v_mfma_f32_16x16x32_bf16 v[80:83], a[84:87], a[4:7], v[80:83]\n"
    "v_mfma_f32_16x16x32_bf16 v[80:83], a[80:83], a[0:3], v[80:83]\n"
    "v_mfma_f32_16x16x32_bf16 v[84:87], a[104:107], a[0:3], v[84:87]\n"
    "v_mfma_f32_16x16x32_bf16 v[84:87], a[108:111], a[4:7], v[84:87]\n"
    "v_mfma_f32_16x16x32_bf16 v[84:87], a[112:115], a[8:11], v[84:87]\n"
    "v_mfma_f32_16x16x32_bf16 v[84:87], a[116:119], a[32:35], v[84:87]\n"
    "v_mfma_f32_16x16x32_bf16 v[84:87], a[120:123], a[36:39], v[84:87]\n"
    "v_mfma_f32_16x16x32_bf16 v[84:87], a[124:127], a[40:43], v[84:87]\n"
    "Lc15%=:\n"
    "v_mfma_f32_16x16x32_bf16 v[88:91], a[100:103], a[44:47], v[88:91]\n"
    "ds_read_b128 a[0:3], %[rd0] offset:16384\n"
    "v_fma_f32 v233, v21, v233, v233\n"
    "v_mfma_f32_16x16x32_bf16 v[88:91], a[96:99], a[40:43], v[88:91]\n"
    "v_fma_f32 v234, v22, v234, v234\n"
    "v_fma_f32 v235, v23, v235, v235\n"
    "v_mfma_f32_16x16x32_bf16 v[88:91], a[92:95], a[36:39], v[88:91]\n"
    "buffer_load_dwordx4 a[128:131], %[l16], %[srdW], s54 offen offset:0\n"
    "v_fma_f32 v228, v228, v224, v224\n"
    "v_mfma_f32_16x16x32_bf16 v[88:91], a[88:91], a[12:15], v[88:91]\n"
    "v_fma_f32 v229, v229, v225, v225\n"
    "v_fma_f32 v230, v230, v226, v226\n"
    "v_mfma_f32_16x16x32_bf16 v[88:91], a[84:87], a[8:11], v[88:91]\n"
    "ds_read_b128 a[32:35], %[rd1] offset:16384\n"
    "v_fma_f32 v231, v231, v227, v227\n"
    "v_mfma_f32_16x16x32_bf16 v[88:91], a[80:83], a[4:7], v[88:91]\n"
    "v_fma_f32 v236, v236, v232, v232\n"
    "v_fma_f32 v237, v237, v233, v233\n"
    "v_mfma_f32_16x16x32_bf16 v[92:95], a[104:107], a[4:7], v[92:95]\n"
    "ds_read_b128 a[4:7], %[rd0] offset:18432\n"
    "v_fma_f32 v238, v238, v234, v234\n"
    "v_mfma_f32_16x16x32_bf16 v[92:95], a[108:111], a[8:11], v[92:95]\n"
    "v_fma_f32 v239, v239, v235, v235\n"
    "v_accvgpr_read_b32 v219, a235\n"
    "v_mfma_f32_16x16x32_bf16 v[92:95], a[112:115], a[12:15], v[92:95]\n"
    "v_rcp_f32 v228, v228\n"
    "v_mfma_f32_16x16x32_bf16 v[92:95], a[116:119], a[36:39], v[92:95]\n"
    "ds_read_b128 a[36:39], %[rd1] offset:18432\n"
    "v_accvgpr_read_b32 v220, a236\n"
    "v_mfma_f32_16x16x32_bf16 v[92:95], a[120:123], a[40:43], v[92:95]\n"
    "buffer_load_dwordx4 a[132:135], %[l16], %[srdW], s54 offen offset:1024\n"
    "v_accvgpr_read_b32 v221, a237\n"
    "v_mfma_f32_16x16x32_bf16 v[92:95], a[124:127], a[44:47], v[92:95]\n"
    "s_waitcnt lgkmcnt(8)\n"
    "v_rcp_f32 v229, v229\n"
    "v_mfma_f32_16x16x32_bf16 v[96:99], a[100:103], a[48:51], v[96:99]\n"
    "v_rcp_f32 v230, v230\n"
    "v_mfma_f32_16x16x32_bf16 v[96:99], a[96:99], a[44:47], v[96:99]\n"
    "ds_read_b128 a[64:67], %[re0] offset:0\n"
    "v_accvgpr_read_b32 v222, a238\n"
    "v_mfma_f32_16x16x32_bf16 v[96:99], a[92:95], a[40:43], v[96:99]\n"
    "buffer_load_dwordx4 a[136:139], %[l16], %[srdW], s54 offen offset:2048\n"
    "v_accvgpr_read_b32 v223, a239\n"
    "v_mfma_f32_16x16x32_bf16 v[96:99], a[88:91], a[16:19], v[96:99]\n"
    "v_rcp_f32 v231, v231\n"
    "v_mfma_f32_16x16x32_bf16 v[96:99], a[84:87], a[12:15], v[96:99]\n"
    "v_rcp_f32 v236, v236\n"
    "v_mfma_f32_16x16x32_bf16 v[96:99], a[80:83], a[8:11], v[96:99]\n"
    "v_rcp_f32 v237, v237\n"
    "v_mfma_f32_16x16x32_bf16 v[100:103], a[104:107], a[8:11], v[100:103]\n"
    "ds_read_b128 a[8:11], %[rd0] offset:20480\n"
    "v_mul_f32 v212, s73, v212\n"
    "v_mfma_f32_16x16x32_bf16 v[100:103], a[108:111], a[12:15], v[100:103]\n"
    "v_rcp_f32 v238, v238\n"
    "v_mfma_f32_16x16x32_bf16 v[100:103], a[112:115], a[16:19], v[100:103]\n"
    "v_rcp_f32 v239, v239\n"
    "v_mfma_f32_16x16x32_bf16 v[100:103], a[116:119], a[40:43], v[100:103]\n"
    "ds_read_b128 a[40:43], %[rd1] offset:20480\n"
    "v_fma_f32 v228, -abs(v224), v228, v228\n"
    "v_mfma_f32_16x16x32_bf16 v[100:103], a[120:123], a[44:47], v[100:103]\n"
    "buffer_load_dwordx4 a[140:143], %[l16], %[srdW], s54 offen offset:3072\n"
    "v_fma_f32 v229, -abs(v225), v229, v229\n"
    "v_mfma_f32_16x16x32_bf16 v[100:103], a[124:127], a[48:51], v[100:103]\n"
    "v_fma_f32 v230, -abs(v226), v230, v230\n"
    "v_fma_f32 v231, -abs(v227), v231, v231\n"
    "v_mfma_f32_16x16x32_bf16 v[104:107], a[100:103], a[52:55], v[104:107]\n"
    "s_add_u32 s54, s54, 4096\n"
    "v_fma_f32 v236, -abs(v232), v236, v236\n"
    "v_fma_f32 v237, -abs(v233), v237, v237\n"
    "v_mfma_f32_16x16x32_bf16 v[104:107], a[96:99], a[48:51], v[104:107]\n"
    "ds_read_b128 a[72:75], %[re1] offset:0\n"
    "v_fma_f32 v238, -abs(v234), v238, v238\n"
    "v_mfma_f32_16x16x32_bf16 v[104:107], a[92:95], a[44:47], v[104:107]\n"
    "buffer_load_dwordx4 a[144:147], %[l16], %[srdW], s54 offen offset:0\n"
    "v_fma_f32 v239, -abs(v235), v239, v239\n"
    "v_mfma_f32_16x16x32_bf16 v[104:107], a[88:91], a[20:23], v[104:107]\n"
    "v_cvt_pk_bf16_f32 v20, v228, v229\n"
    "v_cvt_pk_bf16_f32 v21, v230, v231\n"
    "v_mfma_f32_16x16x32_bf16 v[104:107], a[84:87], a[16:19], v[104:107]\n"
    "v_cvt_pk_bf16_f32 v22, v236, v237\n"
    "v_cvt_pk_bf16_f32 v23, v238, v239\n"
    "v_mfma_f32_16x16x32_bf16 v[104:107], a[80:83], a[12:15], v[104:107]\n"
    "ds_write_b64 %[vsw], v[20:21] offset:20480\n"
    "v_mfma_f32_16x16x32_bf16 v[108:111], a[104:107], a[12:15], v[108:111]\n"
    "ds_read_b128 a[12:15], %[rd0] offset:22528\n"
    "v_mul_f32 v213, s73, v213\n"
    "v_mfma_f32_16x16x32_bf16 v[108:111], a[108:111], a[16:19], v[108:111]\n"
    "ds_write_b64 %[vsw], v[22:23] offset:20608\n"
    "v_mfma_f32_16x16x32_bf16 v[108:111], a[112:115], a[20:23], v[108:111]\n"
    "v_mul_f32 v214, s73, v214\n"
    "v_mul_f32 v215, s73, v215\n"
    "v_mfma_f32_16x16x32_bf16 v[108:111], a[116:119], a[44:47], v[108:111]\n"
    "ds_read_b128 a[44:47], %[rd1] offset:22528\n"
    "v_mul_f32 v220, s73, v220\n"
    "v_mfma_f32_16x16x32_bf16 v[108:111], a[120:123], a[48:51], v[108:111]\n"
    "buffer_load_dwordx4 a[148:151], %[l16], %[srdW], s54 offen offset:1024\n"
    "v_mul_f32 v221, s73, v221\n"
    "v_mfma_f32_16x16x32_bf16 v[108:111], a[124:127], a[52:55], v[108:111]\n"
    "s_waitcnt lgkmcnt(12)\n"
    "v_mul_f32 v222, s73, v222\n"
    "v_mul_f32 v223, s73, v223\n"
    "v_mfma_f32_16x16x32_bf16 v[112:115], a[100:103], a[56:59], v[112:115]\n"
    "v_mul_f32 v208, s72, v208\n"
    "v_mul_f32 v209, s72, v209\n"
    "v_mfma_f32_16x16x32_bf16 v[112:115], a[96:99], a[52:55], v[112:115]\n"
    "ds_read_b128 a[68:71], %[re0] offset:2048\n"
    "v_mul_f32 v210, s72, v210\n"
    "v_mfma_f32_16x16x32_bf16 v[112:115], a[92:95], a[48:51], v[112:115]\n"
    "buffer_load_dwordx4 a[152:155], %[l16], %[srdW], s54 offen offset:2048\n"
    "v_mul_f32 v211, s72, v211\n"
    "v_mfma_f32_16x16x32_bf16 v[112:115], a[88:91], a[24:27], v[112:115]\n"
    "v_mul_f32 v216, s72, v216\n"
    "v_mul_f32 v217, s72, v217\n"
    "v_mfma_f32_16x16x32_bf16 v[112:115], a[84:87], a[20:23], v[112:115]\n"
    "v_mul_f32 v218, s72, v218\n"
    "v_mul_f32 v219, s72, v219\n"
    "v_mfma_f32_16x16x32_bf16 v[112:115], a[80:83], a[16:19], v[112:115]\n"
    "v_exp_f32 v212, v212\n"
    "v_mfma_f32_16x16x32_bf16 v[116:119], a[104:107], a[16:19], v[116:119]\n"
    "ds_read_b128 a[16:19], %[rd0] offset:24576\n"
    "v_accvgpr_read_b32 v224, a240\n"
    "v_mfma_f32_16x16x32_bf16 v[116:119], a[108:111], a[20:23], v[116:119]\n"
    "v_exp_f32 v213, v213\n"
    "v_mfma_f32_16x16x32_bf16 v[116:119], a[112:115], a[24:27], v[116:119]\n"
    "v_exp_f32 v214, v214\n"
    "v_mfma_f32_16x16x32_bf16 v[116:119], a[116:119], a[48:51], v[116:119]\n"
    "ds_read_b128 a[48:51], %[rd1] offset:24576\n"
    "v_accvgpr_read_b32 v225, a241\n"
    "v_mfma_f32_16x16x32_bf16 v[116:119], a[120:123], a[52:55], v[116:119]\n"
    "buffer_load_dwordx4 a[156:159], %[l16], %[srdW], s54 offen offset:3072\n"
    "v_accvgpr_read_b32 v226, a242\n"
    "v_mfma_f32_16x16x32_bf16 v[116:119], a[124:127], a[56:59], v[116:119]\n"
    "v_exp_f32 v215, v215\n"
    "v_mfma_f32_16x16x32_bf16 v[120:123], a[100:103], a[60:63], v[120:123]\n"
    "s_add_u32 s54, s54, 4096\n"
    "v_exp_f32 v220, v220\n"
    "v_mfma_f32_16x16x32_bf16 v[120:123], a[96:99], a[56:59], v[120:123]\n"
    "ds_read_b128 a[76:79], %[re1] offset:2048\n"
    "v_accvgpr_read_b32 v227, a243\n"
    "v_mfma_f32_16x16x32_bf16 v[120:123], a[92:95], a[52:55], v[120:123]\n"
    "buffer_load_dwordx4 a[160:163], %[l16], %[srdW], s54 offen offset:0\n"
    "v_accvgpr_read_b32 v228, a244\n"
    "v_mfma_f32_16x16x32_bf16 v[120:123], a[88:91], a[28:31], v[120:123]\n"
    "v_exp_f32 v221, v221\n"
    "v_mfma_f32_16x16x32_bf16 v[120:123], a[84:87], a[24:27], v[120:123]\n"
    "v_exp_f32 v222, v222\n"
    "v_mfma_f32_16x16x32_bf16 v[120:123], a[80:83], a[20:23], v[120:123]\n"
    "v_exp_f32 v223, v223\n"
    "v_mfma_f32_16x16x32_bf16 v[124:127], a[104:107], a[20:23], v[124:127]\n"
    "ds_read_b128 a[20:23], %[rd0] offset:26624\n"
    "v_accvgpr_read_b32 v229, a245\n"
    "v_mfma_f32_16x16x32_bf16 v[124:127], a[108:111], a[24:27], v[124:127]\n"
    "v_exp_f32_e64 v16, -abs(v208)\n"
    "v_mfma_f32_16x16x32_bf16 v[124:127], a[112:115], a[28:31], v[124:127]\n"
    "v_exp_f32_e64 v17, -abs(v209)\n"
    "v_mfma_f32_16x16x32_bf16 v[124:127], a[116:119], a[52:55], v[124:127]\n"
    "ds_read_b128 a[52:55], %[rd1] offset:26624\n"
    "v_accvgpr_read_b32 v230, a246\n"
    "v_mfma_f32_16x16x32_bf16 v[124:127], a[120:123], a[56:59], v[124:127]\n"
    "buffer_load_dwordx4 a[164:167], %[l16], %[srdW], s54 offen offset:1024\n"
    "v_accvgpr_read_b32 v231, a247\n"
    "v_mfma_f32_16x16x32_bf16 v[124:127], a[124:127], a[60:63], v[124:127]\n"
    "s_waitcnt lgkmcnt(14)\n"
    "v_exp_f32_e64 v18, -abs(v210)\n"
    "v_mfma_f32_16x16x32_bf16 v[128:131], a[100:103], a[32:35], v[128:131]\n"
    "v_exp_f32_e64 v19, -abs(v211)\n"
    "v_mfma_f32_16x16x32_bf16 v[128:131], a[96:99], a[60:63], v[128:131]\n"
    "v_bfi_b32 v208, s61, 0.5, v208\n"
    "v_bfi_b32 v209, s61, 0.5, v209\n"
    "v_mfma_f32_16x16x32_bf16 v[128:131], a[92:95], a[56:59], v[128:131]\n"
    "buffer_load_dwordx4 a[168:171], %[l16], %[srdW], s54 offen offset:2048\n"
    "v_bfi_b32 v210, s61, 0.5, v210\n"
    "v_mfma_f32_16x16x32_bf16 v[128:131], a[88:91], a[0:3], v[128:131]\n"
    "v_bfi_b32 v211, s61, 0.5, v211\n"
    "v_fma_f32 v208, v16, v208, v208\n"
    "v_mfma_f32_16x16x32_bf16 v[128:131], a[84:87], a[28:31], v[128:131]\n"
    "v_fma_f32 v209, v17, v209, v209\n"
    "v_fma_f32 v210, v18, v210, v210\n"
    "v_mfma_f32_16x16x32_bf16 v[128:131], a[80:83], a[24:27], v[128:131]\n"
    "v_fma_f32 v211, v19, v211, v211\n"
    "v_accvgpr_read_b32 v232, a248\n"
    "v_mfma_f32_16x16x32_bf16 v[132:135], a[104:107], a[24:27], v[132:135]\n"
    "ds_read_b128 a[24:27], %[rd0] offset:28672\n"
    "v_accvgpr_read_b32 v233, a249\n"
    "v_mfma_f32_16x16x32_bf16 v[132:135], a[108:111], a[28:31], v[132:135]\n"
    "v_exp_f32_e64 v16, -abs(v216)\n"
    "v_mfma_f32_16x16x32_bf16 v[132:135], a[112:115], a[0:3], v[132:135]\n"
    "v_exp_f32_e64 v17, -abs(v217)\n"
    "v_mfma_f32_16x16x32_bf16 v[132:135], a[116:119], a[56:59], v[132:135]\n"
    "ds_read_b128 a[56:59], %[rd1] offset:28672\n"
    "v_accvgpr_read_b32 v234, a250\n"
    "v_mfma_f32_16x16x32_bf16 v[132:135], a[120:123], a[60:63], v[132:135]\n"
    "buffer_load_dwordx4 a[172:175], %[l16], %[srdW], s54 offen offset:3072\n"
    "v_accvgpr_read_b32 v235, a251\n"
    "v_mfma_f32_16x16x32_bf16 v[132:135], a[124:127], a[32:35], v[132:135]\n"
    "v_exp_f32_e64 v18, -abs(v218)\n"
    "v_mfma_f32_16x16x32_bf16 v[136:139], a[100:103], a[36:39], v[136:139]\n"
    "s_add_u32 s54, s54, 4096\n"
    "v_exp_f32_e64 v19, -abs(v219)\n"
    "v_mfma_f32_16x16x32_bf16 v[136:139], a[96:99], a[32:35], v[136:139]\n"
    "s_add_u32 m0, s69, 0\n"
    "s_add_u32 s57, s70, 0\n"
    "v_bfi_b32 v216, s61, 0.5, v216\n"
    "v_mfma_f32_16x16x32_bf16 v[136:139], a[92:95], a[60:63], v[136:139]\n"
    "buffer_load_dwordx4 %[l16], %[srdD], s57 offen sc1 lds\n"
    "v_bfi_b32 v217, s61, 0.5, v217\n"
    "v_mfma_f32_16x16x32_bf16 v[136:139], a[88:91], a[4:7], v[136:139]\n"
    "s_add_u32 m0, s69, 4096\n"
    "s_add_u32 s57, s70, 4096\n"
    "v_bfi_b32 v218, s61, 0.5, v218\n"
    "v_mfma_f32_16x16x32_bf16 v[136:139], a[84:87], a[0:3], v[136:139]\n"
    "buffer_load_dwordx4 %[l16], %[srdD], s57 offen sc1 lds\n"
    "v_bfi_b32 v219, s61, 0.5, v219\n"
    "v_mfma_f32_16x16x32_bf16 v[136:139], a[80:83], a[28:31], v[136:139]\n"
    "s_add_u32 s64, s56, 4096\n"
    "buffer_load_dwordx4 v[56:59], %[xr1], %[srdX], s64 offen\n"
    "v_mfma_f32_16x16x32_bf16 v[140:143], a[104:107], a[28:31], v[140:143]\n"
    "ds_read_b128 a[28:31], %[rd0] offset:30720\n"
    "v_fma_f32 v216, v16, v216, v216\n"
    "v_mfma_f32_16x16x32_bf16 v[140:143], a[108:111], a[0:3], v[140:143]\n"
    "v_fma_f32 v217, v17, v217, v217\n"
    "v_fma_f32 v218, v18, v218, v218\n"
    "v_mfma_f32_16x16x32_bf16 v[140:143], a[112:115], a[4:7], v[140:143]\n"
    "v_fma_f32 v219, v19, v219, v219\n"
    "v_fma_f32 v212, v212, v208, v208\n"
    "v_mfma_f32_16x16x32_bf16 v[140:143], a[116:119], a[60:63], v[140:143]\n"
    "ds_read_b128 a[60:63], %[rd1] offset:30720\n"
    "v_fma_f32 v213, v213, v209, v209\n"
    "v_mfma_f32_16x16x32_bf16 v[140:143], a[120:123], a[32:35], v[140:143]\n"
    "v_fma_f32 v214, v214, v210, v210\n"
    "v_fma_f32 v215, v215, v211, v211\n"
    "v_mfma_f32_16x16x32_bf16 v[140:143], a[124:127], a[36:39], v[140:143]\n"
    "s_waitcnt lgkmcnt(10)\n"
    "v_fma_f32 v220, v220, v216, v216\n"
    "v_fma_f32 v221, v221, v217, v217\n"
    "v_mfma_f32_16x16x32_bf16 v[144:147], a[100:103], a[40:43], v[144:147]\n"
    "v_fma_f32 v222, v222, v218, v218\n"
    "v_fma_f32 v223, v223, v219, v219\n"
    "v_mfma_f32_16x16x32_bf16 v[144:147], a[96:99], a[36:39], v[144:147]\n"
    "v_rcp_f32 v212, v212\n"
    "v_mfma_f32_16x16x32_bf16 v[144:147], a[92:95], a[32:35], v[144:147]\n"
    "s_add_u32 s63, s63, 1\n"
    "v_rcp_f32 v213, v213\n"
    "v_mfma_f32_16x16x32_bf16 v[144:147], a[88:91], a[8:11], v[144:147]\n"
    "v_rcp_f32 v214, v214\n"
    "v_mfma_f32_16x16x32_bf16 v[144:147], a[84:87], a[4:7], v[144:147]\n"
    "v_rcp_f32 v215, v215\n"
    "v_mfma_f32_16x16x32_bf16 v[144:147], a[80:83], a[0:3], v[144:147]\n"
    "v_rcp_f32 v220, v220\n"
    "v_mfma_f32_16x16x32_bf16 v[148:151], a[104:107], a[0:3], v[148:151]\n"
    "s_waitcnt lgkmcnt(0)\n"
    "v_mov_b32 v13, s63\n"
    "v_mfma_f32_16x16x32_bf16 v[148:151], a[108:111], a[4:7], v[148:151]\n"
    "ds_write_b32 v14, v13\n"
    "v_mfma_f32_16x16x32_bf16 v[148:151], a[112:115], a[8:11], v[148:151]\n"
    "v_rcp_f32 v221, v221\n"
    "v_mfma_f32_16x16x32_bf16 v[148:151], a[116:119], a[32:35], v[148:151]\n"
    "v_lshlrev_b32 v60, 16, v40\n"
    "v_and_b32 v61, s65, v40\n"
    "v_mfma_f32_16x16x32_bf16 v[148:151], a[120:123], a[36:39], v[148:151]\n"
    "v_lshlrev_b32 v62, 16, v41\n"
    "v_and_b32 v63, s65, v41\n"
    "v_mfma_f32_16x16x32_bf16 v[148:151], a[124:127], a[40:43], v[148:151]\n"
    "v_lshlrev_b32 v64, 16, v42\n"
    "v_and_b32 v65, s65, v42\n"
    "v_mfma_f32_16x16x32_bf16 v[152:155], a[100:103], a[44:47], v[152:155]\n"
    "v_lshlrev_b32 v66, 16, v43\n"
    "v_and_b32 v67, s65, v43\n"
    "v_mfma_f32_16x16x32_bf16 v[152:155], a[96:99], a[40:43], v[152:155]\n"
    "v_sub_f32 v68, v60, v68\n"
    "v_sub_f32 v69, v61, v69\n"
    "v_mfma_f32_16x16x32_bf16 v[152:155], a[92:95], a[36:39], v[152:155]\n"
    "v_sub_f32 v70, v62, v70\n"
    "v_sub_f32 v71, v63, v71\n"
    "v_mfma_f32_16x16x32_bf16 v[152:155], a[88:91], a[12:15], v[152:155]\n"
    "v_sub_f32 v72, v64, v72\n"
    "v_sub_f32 v73, v65, v73\n"
    "v_mfma_f32_16x16x32_bf16 v[152:155], a[84:87], a[8:11], v[152:155]\n"
    "ds_read_b32 v13, v12\n"
    "v_sub_f32 v74, v66, v74\n"
    "v_mfma_f32_16x16x32_bf16 v[152:155], a[80:83], a[4:7], v[152:155]\n"
    "v_sub_f32 v75, v67, v75\n"
    "v_cvt_pk_bf16_f32 v40, v68, v69\n"
    "v_mfma_f32_16x16x32_bf16 v[156:159], a[104:107], a[4:7], v[156:159]\n"
    "v_cvt_pk_bf16_f32 v41, v70, v71\n"
    "v_cvt_pk_bf16_f32 v42, v72, v73\n"
    "v_mfma_f32_16x16x32_bf16 v[156:159], a[108:111], a[8:11], v[156:159]\n"
    "v_cvt_pk_bf16_f32 v43, v74, v75\n"
    "v_accvgpr_read_b32 v236, a252\n"
    "v_mfma_f32_16x16x32_bf16 v[156:159], a[112:115], a[12:15], v[156:159]\n"
    "s_add_u32 s64, s71, 0\n"
    "buffer_store_dwordx4 v[40:43], %[vst], %[srdD], s64 offen sc0 sc1\n"
    "v_mfma_f32_16x16x32_bf16 v[156:159], a[116:119], a[36:39], v[156:159]\n"
    "v_rcp_f32 v222, v222\n"
    "v_mfma_f32_16x16x32_bf16 v[156:159], a[120:123], a[40:43], v[156:159]\n"
    "s_waitcnt lgkmcnt(0)\n"
    "v_cmp_gt_u32 vcc, s63, v13\n"
    "v_accvgpr_read_b32 v237, a253\n"
    "v_mfma_f32_16x16x32_bf16 v[156:159], a[124:127], a[44:47], v[156:159]\n"
    "s_cbranch_vccnz Ls15%=\n"
    "Lb15%=:\n"
    "v_accvgpr_read_b32 v238, a254\n"
    "v_mfma_f32_16x16x32_bf16 v[160:163], a[100:103], a[48:51], v[160:163]\n"
    "ds_read_b128 a[0:3], %[rd0] offset:32768\n"
    "v_accvgpr_read_b32 v239, a255\n"
    "v_mfma_f32_16x16x32_bf16 v[160:163], a[96:99], a[44:47], v[160:163]\n"
    "ds_read_b128 a[32:35], %[rd1] offset:32768\n"
    "v_mul_f32 v228, s73, v228\n"
    "v_mfma_f32_16x16x32_bf16 v[160:163], a[92:95], a[40:43], v[160:163]\n"
    "ds_read_b128 a[4:7], %[rd0] offset:34816\n"
    "v_mul_f32 v229, s73, v229\n"
    "v_mfma_f32_16x16x32_bf16 v[160:163], a[88:91], a[16:19], v[160:163]\n"
    "ds_read_b128 a[36:39], %[rd1] offset:34816\n"
    "v_mul_f32 v230, s73, v230\n"
    "v_mfma_f32_16x16x32_bf16 v[160:163], a[84:87], a[12:15], v[160:163]\n"
    "ds_read_b128 v[24:27], v14 offset:20480\n"
    "v_mul_f32 v231, s73, v231\n"
    "v_mfma_f32_16x16x32_bf16 v[160:163], a[80:83], a[8:11], v[160:163]\n"
    "ds_read_b128 v[28:31], v14 offset:24576\n"
    "v_mul_f32 v236, s73, v236\n"
    "v_mfma_f32_16x16x32_bf16 v[164:167], a[104:107], a[8:11], v[164:167]\n"
    "ds_read_b128 a[8:11], %[rd0] offset:36864\n"
    "v_mul_f32 v237, s73, v237\n"
    "v_mfma_f32_16x16x32_bf16 v[164:167], a[108:111], a[12:15], v[164:167]\n"
    "s_add_u32 m0, s69, 8192\n"
    "s_add_u32 s57, s70, 8192\n"
    "v_mul_f32 v238, s73, v238\n"
    "v_mfma_f32_16x16x32_bf16 v[164:167], a[112:115], a[16:19], v[164:167]\n"
    "buffer_load_dwordx4 %[l16], %[srdD], s57 offen sc1 lds\n"
    "v_mul_f32 v239, s73, v239\n"
    "v_mfma_f32_16x16x32_bf16 v[164:167], a[116:119], a[40:43], v[164:167]\n"
    "ds_read_b128 a[40:43], %[rd1] offset:36864\n"
    "v_mul_f32 v224, s72, v224\n"
    "v_mfma_f32_16x16x32_bf16 v[164:167], a[120:123], a[44:47], v[164:167]\n"
    "v_rcp_f32 v223, v223\n"
    "v_mfma_f32_16x16x32_bf16 v[164:167], a[124:127], a[48:51], v[164:167]\n"
    "v_fma_f32 v212, -abs(v208), v212, v212\n"
    "v_fma_f32 v213, -abs(v209), v213, v213\n"
    "v_mfma_f32_16x16x32_bf16 v[168:171], a[100:103], a[52:55], v[168:171]\n"
    "v_fma_f32 v214, -abs(v210), v214, v214\n"
    "v_fma_f32 v215, -abs(v211), v215, v215\n"
    "v_mfma_f32_16x16x32_bf16 v[168:171], a[96:99], a[48:51], v[168:171]\n"
    "v_fma_f32 v220, -abs(v216), v220, v220\n"
    "v_fma_f32 v221, -abs(v217), v221, v221\n"
    "v_mfma_f32_16x16x32_bf16 v[168:171], a[92:95], a[44:47], v[168:171]\n"
    "s_add_u32 m0, s69, 12288\n"
    "s_add_u32 s57, s70, 12288\n"
    "s_waitcnt lgkmcnt(3)\n"
    "v_mfma_f32_16x16x32_bf16 v[168:171], a[88:91], a[20:23], v[168:171]\n"
    "buffer_load_dwordx4 %[l16], %[srdD], s57 offen sc1 lds\n"
    "v_fma_f32 v222, -abs(v218), v222, v222\n"
    "v_mfma_f32_16x16x32_bf16 v[168:171], a[84:87], a[16:19], v[168:171]\n"
    "s_mov_b32 s60, 34816\n"
    "buffer_store_dwordx4 v[24:27], %[oo], s[76:79], s60 offen sc0 sc1\n"
    "v_mfma_f32_16x16x32_bf16 v[168:171], a[80:83], a[12:15], v[168:171]\n"
    "v_fma_f32 v223, -abs(v219), v223, v223\n"
    "v_cvt_pk_bf16_f32 v16, v212, v213\n"
    "v_mfma_f32_16x16x32_bf16 v[172:175], a[104:107], a[12:15], v[172:175]\n"
    "ds_read_b128 a[12:15], %[rd0] offset:38912\n"
    "v_cvt_pk_bf16_f32 v17, v214, v215\n"
    "v_mfma_f32_16x16x32_bf16 v[172:175], a[108:111], a[16:19], v[172:175]\n"
    "v_cvt_pk_bf16_f32 v18, v220, v221\n"
    "v_cvt_pk_bf16_f32 v19, v222, v223\n"
    "v_mfma_f32_16x16x32_bf16 v[172:175], a[112:115], a[20:23], v[172:175]\n"
    "ds_write_b64 %[vsw], v[16:17] offset:24576\n"
    "v_mfma_f32_16x16x32_bf16 v[172:175], a[116:119], a[44:47], v[172:175]\n"
    "ds_read_b128 a[44:47], %[rd1] offset:38912\n"
    "v_mul_f32 v225, s72, v225\n"
    "v_mfma_f32_16x16x32_bf16 v[172:175], a[120:123], a[48:51], v[172:175]\n"
    "ds_write_b64 %[vsw], v[18:19] offset:24704\n"
    "v_mfma_f32_16x16x32_bf16 v[172:175], a[124:127], a[52:55], v[172:175]\n"
    "v_mul_f32 v226, s72, v226\n"
    "v_mul_f32 v227, s72, v227\n"
    "v_mfma_f32_16x16x32_bf16 v[176:179], a[100:103], a[56:59], v[176:179]\n"
    "v_mul_f32 v232, s72, v232\n"
    "v_mul_f32 v233, s72, v233\n"
    "v_mfma_f32_16x16x32_bf16 v[176:179], a[96:99], a[52:55], v[176:179]\n"
    "v_mul_f32 v234, s72, v234\n"
    "v_mul_f32 v235, s72, v235\n"
    "v_mfma_f32_16x16x32_bf16 v[176:179], a[92:95], a[48:51], v[176:179]\n"
    "s_add_u32 m0, s69, 16384\n"
    "s_add_u32 s57, s70, 16384\n"
    "v_mfma_f32_16x16x32_bf16 v[176:179], a[88:91], a[24:27], v[176:179]\n"
    "buffer_load_dwordx4 %[l16], %[srdD], s57 offen sc1 lds\n"
    "v_mfma_f32_16x16x32_bf16 v[176:179], a[84:87], a[20:23], v[176:179]\n"
    "v_exp_f32 v228, v228\n"
    "v_mfma_f32_16x16x32_bf16 v[176:179], a[80:83], a[16:19], v[176:179]\n"
    "v_exp_f32 v229, v229\n"
    "v_mfma_f32_16x16x32_bf16 v[180:183], a[104:107], a[16:19], v[180:183]\n"
    "ds_read_b128 a[16:19], %[rd0] offset:40960\n"
    "v_mfma_f32_16x16x32_bf16 v[180:183], a[108:111], a[20:23], v[180:183]\n"
    "v_exp_f32 v230, v230\n"
    "v_mfma_f32_16x16x32_bf16 v[180:183], a[112:115], a[24:27], v[180:183]\n"
    "v_exp_f32 v231, v231\n"
    "v_mfma_f32_16x16x32_bf16 v[180:183], a[116:119], a[48:51], v[180:183]\n"
    "ds_read_b128 a[48:51], %[rd1] offset:40960\n"
    "v_mfma_f32_16x16x32_bf16 v[180:183], a[120:123], a[52:55], v[180:183]\n"
    "v_exp_f32 v236, v236\n"
    "v_mfma_f32_16x16x32_bf16 v[180:183], a[124:127], a[56:59], v[180:183]\n"
    "v_exp_f32 v237, v237\n"
    "v_mfma_f32_16x16x32_bf16 v[184:187], a[100:103], a[60:63], v[184:187]\n"
    "v_exp_f32 v238, v238\n"
    "v_mfma_f32_16x16x32_bf16 v[184:187], a[96:99], a[56:59], v[184:187]\n"
    "v_exp_f32 v239, v239\n"
    "v_mfma_f32_16x16x32_bf16 v[184:187], a[92:95], a[52:55], v[184:187]\n"
    "s_add_u32 m0, s69, 20480\n"
    "s_add_u32 s57, s70, 20480\n"
    "v_mfma_f32_16x16x32_bf16 v[184:187], a[88:91], a[28:31], v[184:187]\n"
    "buffer_load_dwordx4 %[l16], %[srdD], s57 offen sc1 lds\n"
    "v_mfma_f32_16x16x32_bf16 v[184:187], a[84:87], a[24:27], v[184:187]\n"
    "v_exp_f32_e64 v20, -abs(v224)\n"
    "v_mfma_f32_16x16x32_bf16 v[184:187], a[80:83], a[20:23], v[184:187]\n"
    "v_exp_f32_e64 v21, -abs(v225)\n"
    "v_mfma_f32_16x16x32_bf16 v[188:191], a[104:107], a[20:23], v[188:191]\n"
    "ds_read_b128 a[20:23], %[rd0] offset:43008\n"
    "v_mfma_f32_16x16x32_bf16 v[188:191], a[108:111], a[24:27], v[188:191]\n"
    "v_exp_f32_e64 v22, -abs(v226)\n"
    "v_mfma_f32_16x16x32_bf16 v[188:191], a[112:115], a[28:31], v[188:191]\n"
    "v_exp_f32_e64 v23, -abs(v227)\n"
    "v_mfma_f32_16x16x32_bf16 v[188:191], a[116:119], a[52:55], v[188:191]\n"
    "ds_read_b128 a[52:55], %[rd1] offset:43008\n"
    "v_bfi_b32 v224, s61, 0.5, v224\n"
    "v_mfma_f32_16x16x32_bf16 v[188:191], a[120:123], a[56:59], v[188:191]\n"
    "v_bfi_b32 v225, s61, 0.5, v225\n"
    "v_bfi_b32 v226, s61, 0.5, v226\n"
    "v_mfma_f32_16x16x32_bf16 v[188:191], a[124:127], a[60:63], v[188:191]\n"
    "v_bfi_b32 v227, s61, 0.5, v227\n"
    "v_fma_f32 v224, v20, v224, v224\n"
    "v_mfma_f32_16x16x32_bf16 v[192:195], a[100:103], a[72:75], v[192:195]\n"
    "v_fma_f32 v225, v21, v225, v225\n"
    "v_fma_f32 v226, v22, v226, v226\n"
    "v_mfma_f32_16x16x32_bf16 v[192:195], a[96:99], a[60:63], v[192:195]\n"
    "v_fma_f32 v227, v23, v227, v227\n"
    "v_mfma_f32_16x16x32_bf16 v[192:195], a[92:95], a[56:59], v[192:195]\n"
    "s_add_u32 m0, s69, 24576\n"
    "s_add_u32 s57, s70, 24576\n"
    "v_mfma_f32_16x16x32_bf16 v[192:195], a[88:91], a[64:67], v[192:195]\n"
    "buffer_load_dwordx4 %[l16], %[srdD], s57 offen sc1 lds\n"
    "v_mfma_f32_16x16x32_bf16 v[192:195], a[84:87], a[28:31], v[192:195]\n"
    "v_exp_f32_e64 v20, -abs(v232)\n"
    "v_mfma_f32_16x16x32_bf16 v[192:195], a[80:83], a[24:27], v[192:195]\n"
    "v_exp_f32_e64 v21, -abs(v233)\n"
    "v_mfma_f32_16x16x32_bf16 v[196:199], a[104:107], a[24:27], v[196:199]\n"
    "ds_read_b128 a[24:27], %[rd0] offset:45056\n"
    "v_mfma_f32_16x16x32_bf16 v[196:199], a[108:111], a[28:31], v[196:199]\n"
    "v_exp_f32_e64 v22, -abs(v234)\n"
    "v_mfma_f32_16x16x32_bf16 v[196:199], a[112:115], a[64:67], v[196:199]\n"
    "s_waitcnt lgkmcnt(11)\n"
    "v_exp_f32_e64 v23, -abs(v235)\n"
    "v_mfma_f32_16x16x32_bf16 v[196:199], a[116:119], a[56:59], v[196:199]\n"
    "ds_read_b128 a[56:59], %[rd1] offset:45056\n"
    "v_bfi_b32 v232, s61, 0.5, v232\n"
    "v_mfma_f32_16x16x32_bf16 v[196:199], a[120:123], a[60:63], v[196:199]\n"
    "s_mov_b32 s60, 43008\n"
    "buffer_store_dwordx4 v[28:31], %[oo], s[76:79], s60 offen sc0 sc1\n"
    "v_mfma_f32_16x16x32_bf16 v[196:199], a[124:127], a[72:75], v[196:199]\n"
    "s_add_u32 s64, s56, 6144\n"
    "buffer_load_dwordx4 v[32:35], %[xr1], %[srdX], s64 offen\n"
    "v_mfma_f32_16x16x32_bf16 v[200:203], a[100:103], a[76:79], v[200:203]\n"
    "v_bfi_b32 v233, s61, 0.5, v233\n"
    "v_bfi_b32 v234, s61, 0.5, v234\n"
    "v_mfma_f32_16x16x32_bf16 v[200:203], a[96:99], a[72:75], v[200:203]\n"
    "v_bfi_b32 v235, s61, 0.5, v235\n"
    "v_fma_f32 v232, v20, v232, v232\n"
    "v_mfma_f32_16x16x32_bf16 v[200:203], a[92:95], a[60:63], v[200:203]\n"
    "s_add_u32 m0, s69, 28672\n"
    "s_add_u32 s57, s70, 28672\n"
    "v_fma_f32 v233, v21, v233, v233\n"
    "v_mfma_f32_16x16x32_bf16 v[200:203], a[88:91], a[68:71], v[200:203]\n"
    "buffer_load_dwordx4 %[l16], %[srdD], s57 offen sc1 lds\n"
    "v_fma_f32 v234, v22, v234, v234\n"
    "v_mfma_f32_16x16x32_bf16 v[200:203], a[84:87], a[64:67], v[200:203]\n"
    "v_fma_f32 v235, v23, v235, v235\n"
    "v_fma_f32 v228, v228, v224, v224\n"
    "v_mfma_f32_16x16x32_bf16 v[200:203], a[80:83], a[28:31], v[200:203]\n"
    "s_add_u32 s69, s69, 0x8000\n"
    "s_and_b32 s69, s69, 0x1ffff\n"
    "v_fma_f32 v229, v229, v225, v225\n"
    "v_mfma_f32_16x16x32_bf16 v[204:207], a[104:107], a[28:31], v[204:207]\n"
    "ds_read_b128 a[28:31], %[rd0] offset:47104\n"
    "v_fma_f32 v230, v230, v226, v226\n"
    "v_mfma_f32_16x16x32_bf16 v[204:207], a[108:111], a[64:67], v[204:207]\n"
    "s_add_u32 s70, s70, 0x8000\n"
    "v_fma_f32 v231, v231, v227, v227\n"
    "v_fma_f32 v236, v236, v232, v232\n"
    "v_mfma_f32_16x16x32_bf16 v[204:207], a[112:115], a[68:71], v[204:207]\n"
    "v_fma_f32 v237, v237, v233, v233\n"
    "v_fma_f32 v238, v238, v234, v234\n"
    "v_mfma_f32_16x16x32_bf16 v[204:207], a[116:119], a[60:63], v[204:207]\n"
    "ds_read_b128 a[60:63], %[rd1] offset:47104\n"
    "v_fma_f32 v239, v239, v235, v235\n"
    "v_mfma_f32_16x16x32_bf16 v[204:207], a[120:123], a[72:75], v[204:207]\n"
    "v_rcp_f32 v228, v228\n"
    "v_mfma_f32_16x16x32_bf16 v[204:207], a[124:127], a[76:79], v[204:207]\n"
    "s_waitcnt vmcnt(13)\n"
    "s_waitcnt lgkmcnt(9)\n"
    "s_cmp_lg_u32 s67, 0\n"
    "s_cbranch_scc1 Lc16%=\n"
    "v_mfma_f32_16x16x32_bf16 v[80:83], a[148:151], a[40:43], v[80:83]\n"
    "v_mfma_f32_16x16x32_bf16 v[80:83], a[144:147], a[36:39], v[80:83]\n"
    "v_mfma_f32_16x16x32_bf16 v[80:83], a[140:143], a[32:35], v[80:83]\n"
    "v_mfma_f32_16x16x32_bf16 v[80:83], a[136:139], a[8:11], v[80:83]\n"
    "v_mfma_f32_16x16x32_bf16 v[80:83], a[132:135], a[4:7], v[80:83]\n"
    "v_mfma_f32_16x16x32_bf16 v[80:83], a[128:131], a[0:3], v[80:83]\n"
    "v_mfma_f32_16x16x32_bf16 v[84:87], a[152:155], a[0:3], v[84:87]\n"
    "v_mfma_f32_16x16x32_bf16 v[84:87], a[156:159], a[4:7], v[84:87]\n"
    "v_mfma_f32_16x16x32_bf16 v[84:87], a[160:163], a[8:11], v[84:87]\n"
    "v_mfma_f32_16x16x32_bf16 v[84:87], a[164:167], a[32:35], v[84:87]\n"
    "v_mfma_f32_16x16x32_bf16 v[84:87], a[168:171], a[36:39], v[84:87]\n"
    "v_mfma_f32_16x16x32_bf16 v[84:87], a[172:175], a[40:43], v[84:87]\n"
    "Lc16%=:\n"
    "v_mfma_f32_16x16x32_bf16 v[88:91], a[148:151], a[44:47], v[88:91]\n"
    "ds_read_b128 a[0:3], %[rd0] offset:49152\n"
    "v_mfma_f32_16x16x32_bf16 v[88:91], a[144:147], a[40:43], v[88:91]\n"
    "v_lshlrev_b32 v68, 16, v44\n"
    "v_and_b32 v69, s65, v44\n"
    "v_mfma_f32_16x16x32_bf16 v[88:91], a[140:143], a[36:39], v[88:91]\n"
    "buffer_load_dwordx4 a[80:83], %[l16], %[srdW], s54 offen offset:0\n"
    "v_lshlrev_b32 v70, 16, v45\n"
    "v_mfma_f32_16x16x32_bf16 v[88:91], a[136:139], a[12:15], v[88:91]\n"
    "v_and_b32 v71, s65, v45\n"
    "v_lshlrev_b32 v72, 16, v46\n"
    "v_mfma_f32_16x16x32_bf16 v[88:91], a[132:135], a[8:11], v[88:91]\n"
    "ds_read_b128 a[32:35], %[rd1] offset:49152\n"
    "v_and_b32 v73, s65, v46\n"
    "v_mfma_f32_16x16x32_bf16 v[88:91], a[128:131], a[4:7], v[88:91]\n"
    "v_lshlrev_b32 v74, 16, v47\n"
    "v_and_b32 v75, s65, v47\n"
    "v_mfma_f32_16x16x32_bf16 v[92:95], a[152:155], a[4:7], v[92:95]\n"
    "ds_read_b128 a[4:7], %[rd0] offset:51200\n"
    "v_sub_f32 v60, v60, v68\n"
    "v_mfma_f32_16x16x32_bf16 v[92:95], a[156:159], a[8:11], v[92:95]\n"
    "v_sub_f32 v61, v61, v69\n"
    "v_sub_f32 v62, v62, v70\n"
    "v_mfma_f32_16x16x32_bf16 v[92:95], a[160:163], a[12:15], v[92:95]\n"
    "v_sub_f32 v63, v63, v71\n"
    "v_sub_f32 v64, v64, v72\n"
    "v_mfma_f32_16x16x32_bf16 v[92:95], a[164:167], a[36:39], v[92:95]\n"
    "ds_read_b128 a[36:39], %[rd1] offset:51200\n"
    "v_sub_f32 v65, v65, v73\n"
    "v_mfma_f32_16x16x32_bf16 v[92:95], a[168:171], a[40:43], v[92:95]\n"
    "buffer_load_dwordx4 a[84:87], %[l16], %[srdW], s54 offen offset:1024\n"
    "v_sub_f32 v66, v66, v74\n"
    "v_mfma_f32_16x16x32_bf16 v[92:95], a[172:175], a[44:47], v[92:95]\n"
    "s_waitcnt lgkmcnt(8)\n"
    "v_sub_f32 v67, v67, v75\n"
    "v_cvt_pk_bf16_f32 v44, v60, v61\n"
    "v_mfma_f32_16x16x32_bf16 v[96:99], a[148:151], a[48:51], v[96:99]\n"
    "v_cvt_pk_bf16_f32 v45, v62, v63\n"
    "v_cvt_pk_bf16_f32 v46, v64, v65\n"
    "v_mfma_f32_16x16x32_bf16 v[96:99], a[144:147], a[44:47], v[96:99]\n"
    "ds_read_b128 a[64:67], %[re0] offset:32768\n"
    "v_cvt_pk_bf16_f32 v47, v66, v67\n"
    "v_mfma_f32_16x16x32_bf16 v[96:99], a[140:143], a[40:43], v[96:99]\n"
    "buffer_load_dwordx4 a[88:91], %[l16], %[srdW], s54 offen offset:2048\n"
    "v_mfma_f32_16x16x32_bf16 v[96:99], a[136:139], a[16:19], v[96:99]\n"
    "v_rcp_f32 v229, v229\n"
    "v_mfma_f32_16x16x32_bf16 v[96:99], a[132:135], a[12:15], v[96:99]\n"
    "v_rcp_f32 v230, v230\n"
    "v_mfma_f32_16x16x32_bf16 v[96:99], a[128:131], a[8:11], v[96:99]\n"
    "v_rcp_f32 v231, v231\n"
    "v_mfma_f32_16x16x32_bf16 v[100:103], a[152:155], a[8:11], v[100:103]\n"
    "ds_read_b128 a[8:11], %[rd0] offset:53248\n"
    "v_mfma_f32_16x16x32_bf16 v[100:103], a[156:159], a[12:15], v[100:103]\n"
    "v_rcp_f32 v236, v236\n"
    "v_mfma_f32_16x16x32_bf16 v[100:103], a[160:163], a[16:19], v[100:103]\n"
    "v_rcp_f32 v237, v237\n"
    "v_mfma_f32_16x16x32_bf16 v[100:103], a[164:167], a[40:43], v[100:103]\n"
    "ds_read_b128 a[40:43], %[rd1] offset:53248\n"
    "v_mfma_f32_16x16x32_bf16 v[100:103], a[168:171], a[44:47], v[100:103]\n"
    "buffer_load_dwordx4 a[92:95], %[l16], %[srdW], s54 offen offset:3072\n"
    "v_mfma_f32_16x16x32_bf16 v[100:103], a[172:175], a[48:51], v[100:103]\n"
    "v_rcp_f32 v238, v238\n"
    "v_mfma_f32_16x16x32_bf16 v[104:107], a[148:151], a[52:55], v[104:107]\n"
    "s_add_u32 s54, s54, 4096\n"
    "v_rcp_f32 v239, v239\n"
    "v_mfma_f32_16x16x32_bf16 v[104:107], a[144:147], a[48:51], v[104:107]\n"
    "ds_read_b128 a[72:75], %[re1] offset:32768\n"
    "v_fma_f32 v228, -abs(v224), v228, v228\n"
    "v_mfma_f32_16x16x32_bf16 v[104:107], a[140:143], a[44:47], v[104:107]\n"
    "buffer_load_dwordx4 a[96:99], %[l16], %[srdW], s54 offen offset:0\n"
    "v_fma_f32 v229, -abs(v225), v229, v229\n"
    "v_mfma_f32_16x16x32_bf16 v[104:107], a[136:139], a[20:23], v[104:107]\n"
    "v_fma_f32 v230, -abs(v226), v230, v230\n"
    "v_fma_f32 v231, -abs(v227), v231, v231\n"
    "v_mfma_f32_16x16x32_bf16 v[104:107], a[132:135], a[16:19], v[104:107]\n"
    "v_fma_f32 v236, -abs(v232), v236, v236\n"
    "v_fma_f32 v237, -abs(v233), v237, v237\n"
    "v_mfma_f32_16x16x32_bf16 v[104:107], a[128:131], a[12:15], v[104:107]\n"
    "v_fma_f32 v238, -abs(v234), v238, v238\n"
    "v_fma_f32 v239, -abs(v235), v239, v239\n"
    "v_mfma_f32_16x16x32_bf16 v[108:111], a[152:155], a[12:15], v[108:111]\n"
    "ds_read_b128 a[12:15], %[rd0] offset:55296\n"
    "v_cvt_pk_bf16_f32 v20, v228, v229\n"
    "v_mfma_f32_16x16x32_bf16 v[108:111], a[156:159], a[16:19], v[108:111]\n"
    "v_cvt_pk_bf16_f32 v21, v230, v231\n"
    "v_cvt_pk_bf16_f32 v22, v236, v237\n"
    "v_mfma_f32_16x16x32_bf16 v[108:111], a[160:163], a[20:23], v[108:111]\n"
    "v_cvt_pk_bf16_f32 v23, v238, v239\n"
    "v_mfma_f32_16x16x32_bf16 v[108:111], a[164:167], a[44:47], v[108:111]\n"
    "ds_read_b128 a[44:47], %[rd1] offset:55296\n"
    "v_mfma_f32_16x16x32_bf16 v[108:111], a[168:171], a[48:51], v[108:111]\n"
    "buffer_load_dwordx4 a[100:103], %[l16], %[srdW], s54 offen offset:1024\n"
    "v_mfma_f32_16x16x32_bf16 v[108:111], a[172:175], a[52:55], v[108:111]\n"
    "s_waitcnt lgkmcnt(10)\n"
    "v_mfma_f32_16x16x32_bf16 v[112:115], a[148:151], a[56:59], v[112:115]\n"
    "ds_write_b64 %[vsw], v[20:21] offset:0\n"
    "v_mfma_f32_16x16x32_bf16 v[112:115], a[144:147], a[52:55], v[112:115]\n"
    "ds_read_b128 a[68:71], %[re0] offset:34816\n"
    "v_mfma_f32_16x16x32_bf16 v[112:115], a[140:143], a[48:51], v[112:115]\n"
    "buffer_load_dwordx4 a[104:107], %[l16], %[srdW], s54 offen offset:2048\n"
    "v_mfma_f32_16x16x32_bf16 v[112:115], a[136:139], a[24:27], v[112:115]\n"
    "ds_write_b64 %[vsw], v[22:23] offset:128\n"
    "v_mfma_f32_16x16x32_bf16 v[112:115], a[132:135], a[20:23], v[112:115]\n"
    "v_mfma_f32_16x16x32_bf16 v[112:115], a[128:131], a[16:19], v[112:115]\n"
    "v_mfma_f32_16x16x32_bf16 v[116:119], a[152:155], a[16:19], v[116:119]\n"
    "ds_read_b128 a[16:19], %[rd0] offset:57344\n"
    "v_mfma_f32_16x16x32_bf16 v[116:119], a[156:159], a[20:23], v[116:119]\n"
    "v_mfma_f32_16x16x32_bf16 v[116:119], a[160:163], a[24:27], v[116:119]\n"
    "v_mfma_f32_16x16x32_bf16 v[116:119], a[164:167], a[48:51], v[116:119]\n"
    "ds_read_b128 a[48:51], %[rd1] offset:57344\n"
    "v_mfma_f32_16x16x32_bf16 v[116:119], a[168:171], a[52:55], v[116:119]\n"
    "buffer_load_dwordx4 a[108:111], %[l16], %[srdW], s54 offen offset:3072\n"
    "v_mfma_f32_16x16x32_bf16 v[116:119], a[172:175], a[56:59], v[116:119]\n"
    "v_mfma_f32_16x16x32_bf16 v[120:123], a[148:151], a[60:63], v[120:123]\n"
    "s_add_u32 s54, s54, 4096\n"
    "v_mfma_f32_16x16x32_bf16 v[120:123], a[144:147], a[56:59], v[120:123]\n"
    "ds_read_b128 a[76:79], %[re1] offset:34816\n"
    "v_mfma_f32_16x16x32_bf16 v[120:123], a[140:143], a[52:55], v[120:123]\n"
    "buffer_load_dwordx4 a[112:115], %[l16], %[srdW], s54 offen offset:0\n"
    "v_mfma_f32_16x16x32_bf16 v[120:123], a[136:139], a[28:31], v[120:123]\n"
    "v_mfma_f32_16x16x32_bf16 v[120:123], a[132:135], a[24:27], v[120:123]\n"
    "v_mfma_f32_16x16x32_bf16 v[120:123], a[128:131], a[20:23], v[120:123]\n"
    "v_mfma_f32_16x16x32_bf16 v[124:127], a[152:155], a[20:23], v[124:127]\n"
    "ds_read_b128 a[20:23], %[rd0] offset:59392\n"
    "v_mfma_f32_16x16x32_bf16 v[124:127], a[156:159], a[24:27], v[124:127]\n"
    "v_mfma_f32_16x16x32_bf16 v[124:127], a[160:163], a[28:31], v[124:127]\n"
    "v_mfma_f32_16x16x32_bf16 v[124:127], a[164:167], a[52:55], v[124:127]\n"
    "ds_read_b128 a[52:55], %[rd1] offset:59392\n"
    "v_mfma_f32_16x16x32_bf16 v[124:127], a[168:171], a[56:59], v[124:127]\n"
    "buffer_load_dwordx4 a[116:119], %[l16], %[srdW], s54 offen offset:1024\n"
    "v_mfma_f32_16x16x32_bf16 v[124:127], a[172:175], a[60:63], v[124:127]\n"
    "s_waitcnt lgkmcnt(14)\n"
    "v_mfma_f32_16x16x32_bf16 v[128:131], a[148:151], a[32:35], v[128:131]\n"
    "v_mfma_f32_16x16x32_bf16 v[128:131], a[144:147], a[60:63], v[128:131]\n"
    "v_mfma_f32_16x16x32_bf16 v[128:131], a[140:143], a[56:59], v[128:131]\n"
    "buffer_load_dwordx4 a[120:123], %[l16], %[srdW], s54 offen offset:2048\n"
    "v_mfma_f32_16x16x32_bf16 v[128:131], a[136:139], a[0:3], v[128:131]\n"
    "v_mfma_f32_16x16x32_bf16 v[128:131], a[132:135], a[28:31], v[128:131]\n"
    "v_mfma_f32_16x16x32_bf16 v[128:131], a[128:131], a[24:27], v[128:131]\n"
    "v_mfma_f32_16x16x32_bf16 v[132:135], a[152:155], a[24:27], v[132:135]\n"
    "ds_read_b128 a[24:27], %[rd0] offset:61440\n"
    "v_mfma_f32_16x16x32_bf16 v[132:135], a[156:159], a[28:31], v[132:135]\n"
    "v_mfma_f32_16x16x32_bf16 v[132:135], a[160:163], a[0:3], v[132:135]\n"
    "v_mfma_f32_16x16x32_bf16 v[132:135], a[164:167], a[56:59], v[132:135]\n"
    "ds_read_b128 a[56:59], %[rd1] offset:61440\n"
    "v_mfma_f32_16x16x32_bf16 v[132:135], a[168:171], a[60:63], v[132:135]\n"
    "buffer_load_dwordx4 a[124:127], %[l16], %[srdW], s54 offen offset:3072\n"
    "v_mfma_f32_16x16x32_bf16 v[132:135], a[172:175], a[32:35], v[132:135]\n"
    "v_mfma_f32_16x16x32_bf16 v[136:139], a[148:151], a[36:39], v[136:139]\n"
    "s_add_u32 s54, s54, 4096\n"
    "v_mfma_f32_16x16x32_bf16 v[136:139], a[144:147], a[32:35], v[136:139]\n"
    "s_add_u32 m0, s69, 0\n"
    "s_add_u32 s57, s70, 0\n"
    "v_mfma_f32_16x16x32_bf16 v[136:139], a[140:143], a[60:63], v[136:139]\n"
    "buffer_load_dwordx4 %[l16], %[srdD], s57 offen sc1 lds\n"
    "v_mfma_f32_16x16x32_bf16 v[136:139], a[136:139], a[4:7], v[136:139]\n"
    "s_add_u32 m0, s69, 4096\n"
    "s_add_u32 s57, s70, 4096\n"
    "v_mfma_f32_16x16x32_bf16 v[136:139], a[132:135], a[0:3], v[136:139]\n"
    "buffer_load_dwordx4 %[l16], %[srdD], s57 offen sc1 lds\n"
    "v_mfma_f32_16x16x32_bf16 v[136:139], a[128:131], a[28:31], v[136:139]\n"
    "s_add_u32 s64, s56, 8192\n"
    "buffer_load_dwordx4 v[36:39], %[xr1], %[srdX], s64 offen\n"
    "v_mfma_f32_16x16x32_bf16 v[140:143], a[152:155], a[28:31], v[140:143]\n"
    "ds_read_b128 a[28:31], %[rd0] offset:63488\n"
    "v_mfma_f32_16x16x32_bf16 v[140:143], a[156:159], a[0:3], v[140:143]\n"
    "s_add_u32 s64, s71, 264192\n"
    "buffer_store_dwordx4 v[44:47], %[vst], %[srdD], s64 offen sc0 sc1\n"
    "v_mfma_f32_16x16x32_bf16 v[140:143], a[160:163], a[4:7], v[140:143]\n"
    "v_mfma_f32_16x16x32_bf16 v[140:143], a[164:167], a[60:63], v[140:143]\n"
    "ds_read_b128 a[60:63], %[rd1] offset:63488\n"
    "v_mfma_f32_16x16x32_bf16 v[140:143], a[168:171], a[32:35], v[140:143]\n"
    "v_mfma_f32_16x16x32_bf16 v[140:143], a[172:175], a[36:39], v[140:143]\n"
    "s_waitcnt lgkmcnt(12)\n"
    "v_mfma_f32_16x16x32_bf16 v[144:147], a[148:151], a[40:43], v[144:147]\n"
    "v_xor_b32 %[rd0], 0x10000, %[rd0]\n"
    "v_mfma_f32_16x16x32_bf16 v[144:147], a[144:147], a[36:39], v[144:147]\n"
    "v_xor_b32 %[rd1], 0x10000, %[rd1]\n"
    "v_mfma_f32_16x16x32_bf16 v[144:147], a[140:143], a[32:35], v[144:147]\n"
    "s_add_u32 s63, s63, 1\n"
    "v_mfma_f32_16x16x32_bf16 v[144:147], a[136:139], a[8:11], v[144:147]\n"
    "v_mfma_f32_16x16x32_bf16 v[144:147], a[132:135], a[4:7], v[144:147]\n"
    "v_mfma_f32_16x16x32_bf16 v[144:147], a[128:131], a[0:3], v[144:147]\n"
    "v_mfma_f32_16x16x32_bf16 v[148:151], a[152:155], a[0:3], v[148:151]\n"
    "s_waitcnt lgkmcnt(0)\n"
    "v_mov_b32 v13, s63\n"
    "v_mfma_f32_16x16x32_bf16 v[148:151], a[156:159], a[4:7], v[148:151]\n"
    "ds_write_b32 v14, v13\n"
    "v_mfma_f32_16x16x32_bf16 v[148:151], a[160:163], a[8:11], v[148:151]\n"
    "v_mfma_f32_16x16x32_bf16 v[148:151], a[164:167], a[32:35], v[148:151]\n"
    "v_lshlrev_b32 v60, 16, v48\n"
    "v_and_b32 v61, s65, v48\n"
    "v_mfma_f32_16x16x32_bf16 v[148:151], a[168:171], a[36:39], v[148:151]\n"
    "v_lshlrev_b32 v62, 16, v49\n"
    "v_and_b32 v63, s65, v49\n"
    "v_mfma_f32_16x16x32_bf16 v[148:151], a[172:175], a[40:43], v[148:151]\n"
    "v_lshlrev_b32 v64, 16, v50\n"
    "v_and_b32 v65, s65, v50\n"
    "v_mfma_f32_16x16x32_bf16 v[152:155], a[148:151], a[44:47], v[152:155]\n"
    "v_xor_b32 %[re0], 0x10000, %[re0]\n"
    "v_lshlrev_b32 v66, 16, v51\n"
    "v_mfma_f32_16x16x32_bf16 v[152:155], a[144:147], a[40:43], v[152:155]\n"
    "v_xor_b32 %[re1], 0x10000, %[re1]\n"
    "v_and_b32 v67, s65, v51\n"
    "v_mfma_f32_16x16x32_bf16 v[152:155], a[140:143], a[36:39], v[152:155]\n"
    "v_sub_f32 v68, v60, v68\n"
    "v_sub_f32 v69, v61, v69\n"
    "v_mfma_f32_16x16x32_bf16 v[152:155], a[136:139], a[12:15], v[152:155]\n"
    "v_sub_f32 v70, v62, v70\n"
    "v_sub_f32 v71, v63, v71\n"
    "v_mfma_f32_16x16x32_bf16 v[152:155], a[132:135], a[8:11], v[152:155]\n"
    "ds_read_b32 v13, v12\n"
    "v_sub_f32 v72, v64, v72\n"
    "v_mfma_f32_16x16x32_bf16 v[152:155], a[128:131], a[4:7], v[152:155]\n"
    "v_sub_f32 v73, v65, v73\n"
    "v_sub_f32 v74, v66, v74\n"
    "v_mfma_f32_16x16x32_bf16 v[156:159], a[152:155], a[4:7], v[156:159]\n"
    "v_sub_f32 v75, v67, v75\n"
    "v_cvt_pk_bf16_f32 v48, v68, v69\n"
    "v_mfma_f32_16x16x32_bf16 v[156:159], a[156:159], a[8:11], v[156:159]\n"
    "v_cvt_pk_bf16_f32 v49, v70, v71\n"
    "v_cvt_pk_bf16_f32 v50, v72, v73\n"
    "v_mfma_f32_16x16x32_bf16 v[156:159], a[160:163], a[12:15], v[156:159]\n"
    "v_cvt_pk_bf16_f32 v51, v74, v75\n"
    "v_mfma_f32_16x16x32_bf16 v[156:159], a[164:167], a[36:39], v[156:159]\n"
    "s_add_u32 s64, s71, 2048\n"
    "buffer_store_dwordx4 v[48:51], %[vst], %[srdD], s64 offen sc0 sc1\n"
    "v_mfma_f32_16x16x32_bf16 v[156:159], a[168:171], a[40:43], v[156:159]\n"
    "s_waitcnt lgkmcnt(0)\n"
    "v_cmp_gt_u32 vcc, s63, v13\n"
    "v_mfma_f32_16x16x32_bf16 v[156:159], a[172:175], a[44:47], v[156:159]\n"
    "s_cbranch_vccnz Ls16%=\n"
    "Lb16%=:\n"
    "v_mfma_f32_16x16x32_bf16 v[160:163], a[148:151], a[48:51], v[160:163]\n"
    "ds_read_b128 a[0:3], %[rd0] offset:0\n"
    "v_mfma_f32_16x16x32_bf16 v[160:163], a[144:147], a[44:47], v[160:163]\n"
    "ds_read_b128 a[32:35], %[rd1] offset:0\n"
    "v_mfma_f32_16x16x32_bf16 v[160:163], a[140:143], a[40:43], v[160:163]\n"
    "ds_read_b128 a[4:7], %[rd0] offset:2048\n"
    "v_mfma_f32_16x16x32_bf16 v[160:163], a[136:139], a[16:19], v[160:163]\n"
    "ds_read_b128 a[36:39], %[rd1] offset:2048\n"
    "v_mfma_f32_16x16x32_bf16 v[160:163], a[132:135], a[12:15], v[160:163]\n"
    "ds_read_b128 v[24:27], v14 offset:28672\n"
    "v_mfma_f32_16x16x32_bf16 v[160:163], a[128:131], a[8:11], v[160:163]\n"
    "ds_read_b128 v[28:31], v14 offset:4096\n"
    "v_mfma_f32_16x16x32_bf16 v[164:167], a[152:155], a[8:11], v[164:167]\n"
    "ds_read_b128 a[8:11], %[rd0] offset:4096\n"
    "v_mfma_f32_16x16x32_bf16 v[164:167], a[156:159], a[12:15], v[164:167]\n"
    "s_add_u32 m0, s69, 8192\n"
    "s_add_u32 s57, s70, 8192\n"
    "v_mfma_f32_16x16x32_bf16 v[164:167], a[160:163], a[16:19], v[164:167]\n"
    "buffer_load_dwordx4 %[l16], %[srdD], s57 offen sc1 lds\n"
    "v_mfma_f32_16x16x32_bf16 v[164:167], a[164:167], a[40:43], v[164:167]\n"
    "ds_read_b128 a[40:43], %[rd1] offset:4096\n"
    "v_mfma_f32_16x16x32_bf16 v[164:167], a[168:171], a[44:47], v[164:167]\n"
    "v_mfma_f32_16x16x32_bf16 v[164:167], a[172:175], a[48:51], v[164:167]\n"
    "v_mfma_f32_16x16x32_bf16 v[168:171], a[148:151], a[52:55], v[168:171]\n"
    "v_mfma_f32_16x16x32_bf16 v[168:171], a[144:147], a[48:51], v[168:171]\n"
    "v_mfma_f32_16x16x32_bf16 v[168:171], a[140:143], a[44:47], v[168:171]\n"
    "s_add_u32 m0, s69, 12288\n"
    "s_add_u32 s57, s70, 12288\n"
    "s_waitcnt lgkmcnt(3)\n"
    "v_mfma_f32_16x16x32_bf16 v[168:171], a[136:139], a[20:23], v[168:171]\n"
    "buffer_load_dwordx4 %[l16], %[srdD], s57 offen sc1 lds\n"
    "v_mfma_f32_16x16x32_bf16 v[168:171], a[132:135], a[16:19], v[168:171]\n"
    "s_mov_b32 s60, 51200\n"
    "buffer_store_dwordx4 v[24:27], %[oo], s[76:79], s60 offen sc0 sc1\n"
    "v_mfma_f32_16x16x32_bf16 v[168:171], a[128:131], a[12:15], v[168:171]\n"
    "v_mfma_f32_16x16x32_bf16 v[172:175], a[152:155], a[12:15], v[172:175]\n"
    "ds_read_b128 a[12:15], %[rd0] offset:6144\n"
    "v_mfma_f32_16x16x32_bf16 v[172:175], a[156:159], a[16:19], v[172:175]\n"
    "v_mfma_f32_16x16x32_bf16 v[172:175], a[160:163], a[20:23], v[172:175]\n"
    "v_mfma_f32_16x16x32_bf16 v[172:175], a[164:167], a[44:47], v[172:175]\n"
    "ds_read_b128 a[44:47], %[rd1] offset:6144\n"
    "v_mfma_f32_16x16x32_bf16 v[172:175], a[168:171], a[48:51], v[172:175]\n"
    "v_mfma_f32_16x16x32_bf16 v[172:175], a[172:175], a[52:55], v[172:175]\n"
    "v_mfma_f32_16x16x32_bf16 v[176:179], a[148:151], a[56:59], v[176:179]\n"
    "v_mfma_f32_16x16x32_bf16 v[176:179], a[144:147], a[52:55], v[176:179]\n"
    "v_mfma_f32_16x16x32_bf16 v[176:179], a[140:143], a[48:51], v[176:179]\n"
    "s_add_u32 m0, s69, 16384\n"
    "s_add_u32 s57, s70, 16384\n"
    "v_mfma_f32_16x16x32_bf16 v[176:179], a[136:139], a[24:27], v[176:179]\n"
    "buffer_load_dwordx4 %[l16], %[srdD], s57 offen sc1 lds\n"
    "v_mfma_f32_16x16x32_bf16 v[176:179], a[132:135], a[20:23], v[176:179]\n"
    "v_mfma_f32_16x16x32_bf16 v[176:179], a[128:131], a[16:19], v[176:179]\n"
    "v_mfma_f32_16x16x32_bf16 v[180:183], a[152:155], a[16:19], v[180:183]\n"
    "ds_read_b128 a[16:19], %[rd0] offset:8192\n"
    "v_mfma_f32_16x16x32_bf16 v[180:183], a[156:159], a[20:23], v[180:183]\n"
    "v_mfma_f32_16x16x32_bf16 v[180:183], a[160:163], a[24:27], v[180:183]\n"
    "v_mfma_f32_16x16x32_bf16 v[180:183], a[164:167], a[48:51], v[180:183]\n"
    "ds_read_b128 a[48:51], %[rd1] offset:8192\n"
    "v_mfma_f32_16x16x32_bf16 v[180:183], a[168:171], a[52:55], v[180:183]\n"
    "v_mfma_f32_16x16x32_bf16 v[180:183], a[172:175], a[56:59], v[180:183]\n"
    "v_mfma_f32_16x16x32_bf16 v[184:187], a[148:151], a[60:63], v[184:187]\n"
    "v_mfma_f32_16x16x32_bf16 v[184:187], a[144:147], a[56:59], v[184:187]\n"
    "v_mfma_f32_16x16x32_bf16 v[184:187], a[140:143], a[52:55], v[184:187]\n"
    "s_add_u32 m0, s69, 20480\n"
    "s_add_u32 s57, s70, 20480\n"
    "v_mfma_f32_16x16x32_bf16 v[184:187], a[136:139], a[28:31], v[184:187]\n"
    "buffer_load_dwordx4 %[l16], %[srdD], s57 offen sc1 lds\n"
    "v_mfma_f32_16x16x32_bf16 v[184:187], a[132:135], a[24:27], v[184:187]\n"
    "v_mfma_f32_16x16x32_bf16 v[184:187], a[128:131], a[20:23], v[184:187]\n"
    "v_mfma_f32_16x16x32_bf16 v[188:191], a[152:155], a[20:23], v[188:191]\n"
    "ds_read_b128 a[20:23], %[rd0] offset:10240\n"
    "v_mfma_f32_16x16x32_bf16 v[188:191], a[156:159], a[24:27], v[188:191]\n"
    "v_mfma_f32_16x16x32_bf16 v[188:191], a[160:163], a[28:31], v[188:191]\n"
    "v_mfma_f32_16x16x32_bf16 v[188:191], a[164:167], a[52:55], v[188:191]\n"
    "ds_read_b128 a[52:55], %[rd1] offset:10240\n"
    "v_mfma_f32_16x16x32_bf16 v[188:191], a[168:171], a[56:59], v[188:191]\n"
    "v_mfma_f32_16x16x32_bf16 v[188:191], a[172:175], a[60:63], v[188:191]\n"
    "v_mfma_f32_16x16x32_bf16 v[192:195], a[148:151], a[72:75], v[192:195]\n"
    "v_mfma_f32_16x16x32_bf16 v[192:195], a[144:147], a[60:63], v[192:195]\n"
    "v_mfma_f32_16x16x32_bf16 v[192:195], a[140:143], a[56:59], v[192:195]\n"
    "s_add_u32 m0, s69, 24576\n"
    "s_add_u32 s57, s70, 24576\n"
    "v_mfma_f32_16x16x32_bf16 v[192:195], a[136:139], a[64:67], v[192:195]\n"
    "buffer_load_dwordx4 %[l16], %[srdD], s57 offen sc1 lds\n"
    "v_mfma_f32_16x16x32_bf16 v[192:195], a[132:135], a[28:31], v[192:195]\n"
    "v_mfma_f32_16x16x32_bf16 v[192:195], a[128:131], a[24:27], v[192:195]\n"
    "v_mfma_f32_16x16x32_bf16 v[196:199], a[152:155], a[24:27], v[196:199]\n"
    "ds_read_b128 a[24:27], %[rd0] offset:12288\n"
    "v_mfma_f32_16x16x32_bf16 v[196:199], a[156:159], a[28:31], v[196:199]\n"
    "v_mfma_f32_16x16x32_bf16 v[196:199], a[160:163], a[64:67], v[196:199]\n"
    "s_waitcnt lgkmcnt(9)\n"
    "v_mfma_f32_16x16x32_bf16 v[196:199], a[164:167], a[56:59], v[196:199]\n"
    "ds_read_b128 a[56:59], %[rd1] offset:12288\n"
    "v_mfma_f32_16x16x32_bf16 v[196:199], a[168:171], a[60:63], v[196:199]\n"
    "s_mov_b32 s60, 59392\n"
    "buffer_store_dwordx4 v[28:31], %[oo], s[76:79], s60 offen sc0 sc1\n"
    "v_mfma_f32_16x16x32_bf16 v[196:199], a[172:175], a[72:75], v[196:199]\n"
    "v_mfma_f32_16x16x32_bf16 v[200:203], a[148:151], a[76:79], v[200:203]\n"
    "v_mfma_f32_16x16x32_bf16 v[200:203], a[144:147], a[72:75], v[200:203]\n"
    "v_mfma_f32_16x16x32_bf16 v[200:203], a[140:143], a[60:63], v[200:203]\n"
    "s_add_u32 m0, s69, 28672\n"
    "s_add_u32 s57, s70, 28672\n"
    "v_mfma_f32_16x16x32_bf16 v[200:203], a[136:139], a[68:71], v[200:203]\n"
    "buffer_load_dwordx4 %[l16], %[srdD], s57 offen sc1 lds\n"
    "v_mfma_f32_16x16x32_bf16 v[200:203], a[132:135], a[64:67], v[200:203]\n"
    "v_mfma_f32_16x16x32_bf16 v[200:203], a[128:131], a[28:31], v[200:203]\n"
    "s_add_u32 s69, s69, 0x8000\n"
    "s_and_b32 s69, s69, 0x1ffff\n"
    "v_mfma_f32_16x16x32_bf16 v[204:207], a[152:155], a[28:31], v[204:207]\n"
    "ds_read_b128 a[28:31], %[rd0] offset:14336\n"
    "v_mfma_f32_16x16x32_bf16 v[204:207], a[156:159], a[64:67], v[204:207]\n"
    "s_add_u32 s70, s70, 0x8000\n"
    "v_mfma_f32_16x16x32_bf16 v[204:207], a[160:163], a[68:71], v[204:207]\n"
    "v_mfma_f32_16x16x32_bf16 v[204:207], a[164:167], a[60:63], v[204:207]\n"
    "ds_read_b128 a[60:63], %[rd1] offset:14336\n"
    "v_mfma_f32_16x16x32_bf16 v[204:207], a[168:171], a[72:75], v[204:207]\n"
    "v_mfma_f32_16x16x32_bf16 v[204:207], a[172:175], a[76:79], v[204:207]\n"
    "s_waitcnt vmcnt(13)\n"
    "s_waitcnt lgkmcnt(8)\n"
    "s_cmp_lg_u32 s67, 0\n"
    "s_cbranch_scc1 Lc17%=\n"
    "v_mfma_f32_16x16x32_bf16 v[80:83], a[100:103], a[40:43], v[80:83]\n"
    "v_mfma_f32_16x16x32_bf16 v[80:83], a[96:99], a[36:39], v[80:83]\n"
    "v_mfma_f32_16x16x32_bf16 v[80:83], a[92:95], a[32:35], v[80:83]\n"
    "v_mfma_f32_16x16x32_bf16 v[80:83], a[88:91], a[8:11], v[80:83]\n"
    "v_mfma_f32_16x16x32_bf16 v[80:83], a[84:87], a[4:7], v[80:83]\n"
    "v_mfma_f32_16x16x32_bf16 v[80:83], a[80:83], a[0:3], v[80:83]\n"
    "v_mfma_f32_16x16x32_bf16 v[84:87], a[104:107], a[0:3], v[84:87]\n"
    "v_mfma_f32_16x16x32_bf16 v[84:87], a[108:111], a[4:7], v[84:87]\n"
    "v_mfma_f32_16x16x32_bf16 v[84:87], a[112:115], a[8:11], v[84:87]\n"
    "v_mfma_f32_16x16x32_bf16 v[84:87], a[116:119], a[32:35], v[84:87]\n"
    "v_mfma_f32_16x16x32_bf16 v[84:87], a[120:123], a[36:39], v[84:87]\n"
    "v_mfma_f32_16x16x32_bf16 v[84:87], a[124:127], a[40:43], v[84:87]\n"
    "Lc17%=:\n"
    "v_mfma_f32_16x16x32_bf16 v[88:91], a[100:103], a[44:47], v[88:91]\n"
    "ds_read_b128 a[0:3], %[rd0] offset:16384\n"
    "v_mfma_f32_16x16x32_bf16 v[88:91], a[96:99], a[40:43], v[88:91]\n"
    "v_lshlrev_b32 v68, 16, v52\n"
    "v_and_b32 v69, s65, v52\n"
    "v_mfma_f32_16x16x32_bf16 v[88:91], a[92:95], a[36:39], v[88:91]\n"
    "buffer_load_dwordx4 a[128:131], %[l16], %[srdW], s54 offen offset:0\n"
    "v_lshlrev_b32 v70, 16, v53\n"
    "v_mfma_f32_16x16x32_bf16 v[88:91], a[88:91], a[12:15], v[88:91]\n"
    "v_and_b32 v71, s65, v53\n"
    "v_lshlrev_b32 v72, 16, v54\n"
    "v_mfma_f32_16x16x32_bf16 v[88:91], a[84:87], a[8:11], v[88:91]\n"
    "ds_read_b128 a[32:35], %[rd1] offset:16384\n"
    "v_and_b32 v73, s65, v54\n"
    "v_mfma_f32_16x16x32_bf16 v[88:91], a[80:83], a[4:7], v[88:91]\n"
    "v_lshlrev_b32 v74, 16, v55\n"
    "v_and_b32 v75, s65, v55\n"
    "v_mfma_f32_16x16x32_bf16 v[92:95], a[104:107], a[4:7], v[92:95]\n"
    "ds_read_b128 a[4:7], %[rd0] offset:18432\n"
    "v_sub_f32 v60, v60, v68\n"
    "v_mfma_f32_16x16x32_bf16 v[92:95], a[108:111], a[8:11], v[92:95]\n"
    "v_sub_f32 v61, v61, v69\n"
    "v_sub_f32 v62, v62, v70\n"
    "v_mfma_f32_16x16x32_bf16 v[92:95], a[112:115], a[12:15], v[92:95]\n"
    "v_sub_f32 v63, v63, v71\n"
    "v_sub_f32 v64, v64, v72\n"
    "v_mfma_f32_16x16x32_bf16 v[92:95], a[116:119], a[36:39], v[92:95]\n"
    "ds_read_b128 a[36:39], %[rd1] offset:18432\n"
    "v_sub_f32 v65, v65, v73\n"
    "v_mfma_f32_16x16x32_bf16 v[92:95], a[120:123], a[40:43], v[92:95]\n"
    "buffer_load_dwordx4 a[132:135], %[l16], %[srdW], s54 offen offset:1024\n"
    "v_sub_f32 v66, v66, v74\n"
    "v_mfma_f32_16x16x32_bf16 v[92:95], a[124:127], a[44:47], v[92:95]\n"
    "s_waitcnt lgkmcnt(8)\n"
    "v_sub_f32 v67, v67, v75\n"
    "v_cvt_pk_bf16_f32 v52, v60, v61\n"
    "v_mfma_f32_16x16x32_bf16 v[96:99], a[100:103], a[48:51], v[96:99]\n"
    "v_cvt_pk_bf16_f32 v53, v62, v63\n"
    "v_cvt_pk_bf16_f32 v54, v64, v65\n"
    "v_mfma_f32_16x16x32_bf16 v[96:99], a[96:99], a[44:47], v[96:99]\n"
    "ds_read_b128 a[64:67], %[re0] offset:0\n"
    "v_cvt_pk_bf16_f32 v55, v66, v67\n"
    "v_mfma_f32_16x16x32_bf16 v[96:99], a[92:95], a[40:43], v[96:99]\n"
    "buffer_load_dwordx4 a[136:139], %[l16], %[srdW], s54 offen offset:2048\n"
    "v_mfma_f32_16x16x32_bf16 v[96:99], a[88:91], a[16:19], v[96:99]\n"
    "v_mfma_f32_16x16x32_bf16 v[96:99], a[84:87], a[12:15], v[96:99]\n"
    "v_mfma_f32_16x16x32_bf16 v[96:99], a[80:83], a[8:11], v[96:99]\n"
    "v_mfma_f32_16x16x32_bf16 v[100:103], a[104:107], a[8:11], v[100:103]\n"
    "ds_read_b128 a[8:11], %[rd0] offset:20480\n"
    "v_mfma_f32_16x16x32_bf16 v[100:103], a[108:111], a[12:15], v[100:103]\n"
    "v_mfma_f32_16x16x32_bf16 v[100:103], a[112:115], a[16:19], v[100:103]\n"
    "v_mfma_f32_16x16x32_bf16 v[100:103], a[116:119], a[40:43], v[100:103]\n"
    "ds_read_b128 a[40:43], %[rd1] offset:20480\n"
    "v_mfma_f32_16x16x32_bf16 v[100:103], a[120:123], a[44:47], v[100:103]\n"
    "buffer_load_dwordx4 a[140:143], %[l16], %[srdW], s54 offen offset:3072\n"
    "v_mfma_f32_16x16x32_bf16 v[100:103], a[124:127], a[48:51], v[100:103]\n"
    "v_mfma_f32_16x16x32_bf16 v[104:107], a[100:103], a[52:55], v[104:107]\n"
    "s_add_u32 s54, s54, 4096\n"
    "v_mfma_f32_16x16x32_bf16 v[104:107], a[96:99], a[48:51], v[104:107]\n"
    "ds_read_b128 a[72:75], %[re1] offset:0\n"
    "v_mfma_f32_16x16x32_bf16 v[104:107], a[92:95], a[44:47], v[104:107]\n"
    "buffer_load_dwordx4 a[144:147], %[l16], %[srdW], s54 offen offset:0\n"
    "v_mfma_f32_16x16x32_bf16 v[104:107], a[88:91], a[20:23], v[104:107]\n"
    "v_mfma_f32_16x16x32_bf16 v[104:107], a[84:87], a[16:19], v[104:107]\n"
    "v_mfma_f32_16x16x32_bf16 v[104:107], a[80:83], a[12:15], v[104:107]\n"
    "v_mfma_f32_16x16x32_bf16 v[108:111], a[104:107], a[12:15], v[108:111]\n"
    "ds_read_b128 a[12:15], %[rd0] offset:22528\n"
    "v_mfma_f32_16x16x32_bf16 v[108:111], a[108:111], a[16:19], v[108:111]\n"
    "v_mfma_f32_16x16x32_bf16 v[108:111], a[112:115], a[20:23], v[108:111]\n"
    "v_mfma_f32_16x16x32_bf16 v[108:111], a[116:119], a[44:47], v[108:111]\n"
    "ds_read_b128 a[44:47], %[rd1] offset:22528\n"
    "v_mfma_f32_16x16x32_bf16 v[108:111], a[120:123], a[48:51], v[108:111]\n"
    "buffer_load_dwordx4 a[148:151], %[l16], %[srdW], s54 offen offset:1024\n"
    "v_mfma_f32_16x16x32_bf16 v[108:111], a[124:127], a[52:55], v[108:111]\n"
    "s_waitcnt lgkmcnt(10)\n"
    "v_mfma_f32_16x16x32_bf16 v[112:115], a[100:103], a[56:59], v[112:115]\n"
    "v_mfma_f32_16x16x32_bf16 v[112:115], a[96:99], a[52:55], v[112:115]\n"
    "ds_read_b128 a[68:71], %[re0] offset:2048\n"
    "v_mfma_f32_16x16x32_bf16 v[112:115], a[92:95], a[48:51], v[112:115]\n"
    "buffer_load_dwordx4 a[152:155], %[l16], %[srdW], s54 offen offset:2048\n"
    "v_mfma_f32_16x16x32_bf16 v[112:115], a[88:91], a[24:27], v[112:115]\n"
    "v_mfma_f32_16x16x32_bf16 v[112:115], a[84:87], a[20:23], v[112:115]\n"
    "v_mfma_f32_16x16x32_bf16 v[112:115], a[80:83], a[16:19], v[112:115]\n"
    "v_mfma_f32_16x16x32_bf16 v[116:119], a[104:107], a[16:19], v[116:119]\n"
    "ds_read_b128 a[16:19], %[rd0] offset:24576\n"
    "v_mfma_f32_16x16x32_bf16 v[116:119], a[108:111], a[20:23], v[116:119]\n"
    "v_mfma_f32_16x16x32_bf16 v[116:119], a[112:115], a[24:27], v[116:119]\n"
    "v_mfma_f32_16x16x32_bf16 v[116:119], a[116:119], a[48:51], v[116:119]\n"
    "ds_read_b128 a[48:51], %[rd1] offset:24576\n"
    "v_mfma_f32_16x16x32_bf16 v[116:119], a[120:123], a[52:55], v[116:119]\n"
    "buffer_load_dwordx4 a[156:159], %[l16], %[srdW], s54 offen offset:3072\n"
    "v_mfma_f32_16x16x32_bf16 v[116:119], a[124:127], a[56:59], v[116:119]\n"
    "v_mfma_f32_16x16x32_bf16 v[120:123], a[100:103], a[60:63], v[120:123]\n"
    "s_add_u32 s54, s54, 4096\n"
    "v_mfma_f32_16x16x32_bf16 v[120:123], a[96:99], a[56:59], v[120:123]\n"
    "ds_read_b128 a[76:79], %[re1] offset:2048\n"
    "v_mfma_f32_16x16x32_bf16 v[120:123], a[92:95], a[52:55], v[120:123]\n"
    "buffer_load_dwordx4 a[160:163], %[l16], %[srdW], s54 offen offset:0\n"
    "v_mfma_f32_16x16x32_bf16 v[120:123], a[88:91], a[28:31], v[120:123]\n"
    "v_mfma_f32_16x16x32_bf16 v[120:123], a[84:87], a[24:27], v[120:123]\n"
    "v_mfma_f32_16x16x32_bf16 v[120:123], a[80:83], a[20:23], v[120:123]\n"
    "v_mfma_f32_16x16x32_bf16 v[124:127], a[104:107], a[20:23], v[124:127]\n"
    "ds_read_b128 a[20:23], %[rd0] offset:26624\n"
    "v_mfma_f32_16x16x32_bf16 v[124:127], a[108:111], a[24:27], v[124:127]\n"
    "v_mfma_f32_16x16x32_bf16 v[124:127], a[112:115], a[28:31], v[124:127]\n"
    "v_mfma_f32_16x16x32_bf16 v[124:127], a[116:119], a[52:55], v[124:127]\n"
    "ds_read_b128 a[52:55], %[rd1] offset:26624\n"
    "v_mfma_f32_16x16x32_bf16 v[124:127], a[120:123], a[56:59], v[124:127]\n"
    "buffer_load_dwordx4 a[164:167], %[l16], %[srdW], s54 offen offset:1024\n"
    "v_mfma_f32_16x16x32_bf16 v[124:127], a[124:127], a[60:63], v[124:127]\n"
    "s_waitcnt lgkmcnt(12)\n"
    "v_mfma_f32_16x16x32_bf16 v[128:131], a[100:103], a[32:35], v[128:131]\n"
    "v_mfma_f32_16x16x32_bf16 v[128:131], a[96:99], a[60:63], v[128:131]\n"
    "v_mfma_f32_16x16x32_bf16 v[128:131], a[92:95], a[56:59], v[128:131]\n"
    "buffer_load_dwordx4 a[168:171], %[l16], %[srdW], s54 offen offset:2048\n"
    "v_mfma_f32_16x16x32_bf16 v[128:131], a[88:91], a[0:3], v[128:131]\n"
    "v_mfma_f32_16x16x32_bf16 v[128:131], a[84:87], a[28:31], v[128:131]\n"
    "v_mfma_f32_16x16x32_bf16 v[128:131], a[80:83], a[24:27], v[128:131]\n"
    "v_mfma_f32_16x16x32_bf16 v[132:135], a[104:107], a[24:27], v[132:135]\n"
    "ds_read_b128 a[24:27], %[rd0] offset:28672\n"
    "v_mfma_f32_16x16x32_bf16 v[132:135], a[108:111], a[28:31], v[132:135]\n"
    "v_mfma_f32_16x16x32_bf16 v[132:135], a[112:115], a[0:3], v[132:135]\n"
    "v_mfma_f32_16x16x32_bf16 v[132:135], a[116:119], a[56:59], v[132:135]\n"
    "ds_read_b128 a[56:59], %[rd1] offset:28672\n"
    "v_mfma_f32_16x16x32_bf16 v[132:135], a[120:123], a[60:63], v[132:135]\n"
    "buffer_load_dwordx4 a[172:175], %[l16], %[srdW], s54 offen offset:3072\n"
    "v_mfma_f32_16x16x32_bf16 v[132:135], a[124:127], a[32:35], v[132:135]\n"
    "v_mfma_f32_16x16x32_bf16 v[136:139], a[100:103], a[36:39], v[136:139]\n"
    "s_add_u32 s54, s54, 4096\n"
    "v_mfma_f32_16x16x32_bf16 v[136:139], a[96:99], a[32:35], v[136:139]\n"
    "s_add_u32 m0, s69, 0\n"
    "s_add_u32 s57, s70, 0\n"
    "v_mfma_f32_16x16x32_bf16 v[136:139], a[92:95], a[60:63], v[136:139]\n"
    "buffer_load_dwordx4 %[l16], %[srdD], s57 offen sc1 lds\n"
    "v_mfma_f32_16x16x32_bf16 v[136:139], a[88:91], a[4:7], v[136:139]\n"
    "s_add_u32 m0, s69, 4096\n"
    "s_add_u32 s57, s70, 4096\n"
    "v_mfma_f32_16x16x32_bf16 v[136:139], a[84:87], a[0:3], v[136:139]\n"
    "buffer_load_dwordx4 %[l16], %[srdD], s57 offen sc1 lds\n"
    "v_mfma_f32_16x16x32_bf16 v[136:139], a[80:83], a[28:31], v[136:139]\n"
    "s_add_u32 s64, s71, 266240\n"
    "buffer_store_dwordx4 v[52:55], %[vst], %[srdD], s64 offen sc0 sc1\n"
    "v_mfma_f32_16x16x32_bf16 v[140:143], a[104:107], a[28:31], v[140:143]\n"
    "ds_read_b128 a[28:31], %[rd0] offset:30720\n"
    "v_mfma_f32_16x16x32_bf16 v[140:143], a[108:111], a[0:3], v[140:143]\n"
    "v_mfma_f32_16x16x32_bf16 v[140:143], a[112:115], a[4:7], v[140:143]\n"
    "v_mfma_f32_16x16x32_bf16 v[140:143], a[116:119], a[60:63], v[140:143]\n"
    "ds_read_b128 a[60:63], %[rd1] offset:30720\n"
    "v_mfma_f32_16x16x32_bf16 v[140:143], a[120:123], a[32:35], v[140:143]\n"
    "v_mfma_f32_16x16x32_bf16 v[140:143], a[124:127], a[36:39], v[140:143]\n"
    "s_waitcnt lgkmcnt(10)\n"
    "v_mfma_f32_16x16x32_bf16 v[144:147], a[100:103], a[40:43], v[144:147]\n"
    "v_mfma_f32_16x16x32_bf16 v[144:147], a[96:99], a[36:39], v[144:147]\n"
    "v_mfma_f32_16x16x32_bf16 v[144:147], a[92:95], a[32:35], v[144:147]\n"
    "s_add_u32 s63, s63, 1\n"
    "v_mfma_f32_16x16x32_bf16 v[144:147], a[88:91], a[8:11], v[144:147]\n"
    "v_mfma_f32_16x16x32_bf16 v[144:147], a[84:87], a[4:7], v[144:147]\n"
    "v_mfma_f32_16x16x32_bf16 v[144:147], a[80:83], a[0:3], v[144:147]\n"
    "v_mfma_f32_16x16x32_bf16 v[148:151], a[104:107], a[0:3], v[148:151]\n"
    "s_waitcnt lgkmcnt(0)\n"
    "v_mov_b32 v13, s63\n"
    "v_mfma_f32_16x16x32_bf16 v[148:151], a[108:111], a[4:7], v[148:151]\n"
    "ds_write_b32 v14, v13\n"
    "v_mfma_f32_16x16x32_bf16 v[148:151], a[112:115], a[8:11], v[148:151]\n"
    "v_mfma_f32_16x16x32_bf16 v[148:151], a[116:119], a[32:35], v[148:151]\n"
    "v_lshlrev_b32 v60, 16, v56\n"
    "v_and_b32 v61, s65, v56\n"
    "v_mfma_f32_16x16x32_bf16 v[148:151], a[120:123], a[36:39], v[148:151]\n"
    "v_lshlrev_b32 v62, 16, v57\n"
    "v_and_b32 v63, s65, v57\n"
    "v_mfma_f32_16x16x32_bf16 v[148:151], a[124:127], a[40:43], v[148:151]\n"
    "v_lshlrev_b32 v64, 16, v58\n"
    "v_and_b32 v65, s65, v58\n"
    "v_mfma_f32_16x16x32_bf16 v[152:155], a[100:103], a[44:47], v[152:155]\n"
    "v_lshlrev_b32 v66, 16, v59\n"
    "v_and_b32 v67, s65, v59\n"
    "v_mfma_f32_16x16x32_bf16 v[152:155], a[96:99], a[40:43], v[152:155]\n"
    "v_sub_f32 v68, v60, v68\n"
    "v_sub_f32 v69, v61, v69\n"
    "v_mfma_f32_16x16x32_bf16 v[152:155], a[92:95], a[36:39], v[152:155]\n"
    "v_sub_f32 v70, v62, v70\n"
    "v_sub_f32 v71, v63, v71\n"
    "v_mfma_f32_16x16x32_bf16 v[152:155], a[88:91], a[12:15], v[152:155]\n"
    "v_sub_f32 v72, v64, v72\n"
    "v_sub_f32 v73, v65, v73\n"
    "v_mfma_f32_16x16x32_bf16 v[152:155], a[84:87], a[8:11], v[152:155]\n"
    "ds_read_b32 v13, v12\n"
    "v_sub_f32 v74, v66, v74\n"
    "v_mfma_f32_16x16x32_bf16 v[152:155], a[80:83], a[4:7], v[152:155]\n"
    "v_sub_f32 v75, v67, v75\n"
    "v_cvt_pk_bf16_f32 v56, v68, v69\n"
    "v_mfma_f32_16x16x32_bf16 v[156:159], a[104:107], a[4:7], v[156:159]\n"
    "v_cvt_pk_bf16_f32 v57, v70, v71\n"
    "v_cvt_pk_bf16_f32 v58, v72, v73\n"
    "v_mfma_f32_16x16x32_bf16 v[156:159], a[108:111], a[8:11], v[156:159]\n"
    "v_cvt_pk_bf16_f32 v59, v74, v75\n"
    "v_mfma_f32_16x16x32_bf16 v[156:159], a[112:115], a[12:15], v[156:159]\n"
    "s_add_u32 s64, s71, 4096\n"
    "buffer_store_dwordx4 v[56:59], %[vst], %[srdD], s64 offen sc0 sc1\n"
    "v_mfma_f32_16x16x32_bf16 v[156:159], a[116:119], a[36:39], v[156:159]\n"
    "v_mfma_f32_16x16x32_bf16 v[156:159], a[120:123], a[40:43], v[156:159]\n"
    "s_waitcnt lgkmcnt(0)\n"
    "v_cmp_gt_u32 vcc, s63, v13\n"
    "v_mfma_f32_16x16x32_bf16 v[156:159], a[124:127], a[44:47], v[156:159]\n"
    "s_cbranch_vccnz Ls17%=\n"
    "Lb17%=:\n"
    "v_mfma_f32_16x16x32_bf16 v[160:163], a[100:103], a[48:51], v[160:163]\n"
    "ds_read_b128 a[0:3], %[rd0] offset:32768\n"
    "v_mfma_f32_16x16x32_bf16 v[160:163], a[96:99], a[44:47], v[160:163]\n"
    "ds_read_b128 a[32:35], %[rd1] offset:32768\n"
    "v_mfma_f32_16x16x32_bf16 v[160:163], a[92:95], a[40:43], v[160:163]\n"
    "ds_read_b128 a[4:7], %[rd0] offset:34816\n"
    "v_mfma_f32_16x16x32_bf16 v[160:163], a[88:91], a[16:19], v[160:163]\n"
    "ds_read_b128 a[36:39], %[rd1] offset:34816\n"
    "v_mfma_f32_16x16x32_bf16 v[160:163], a[84:87], a[12:15], v[160:163]\n"
    "v_mfma_f32_16x16x32_bf16 v[160:163], a[80:83], a[8:11], v[160:163]\n"
    "v_mfma_f32_16x16x32_bf16 v[164:167], a[104:107], a[8:11], v[164:167]\n"
    "ds_read_b128 a[8:11], %[rd0] offset:36864\n"
    "v_mfma_f32_16x16x32_bf16 v[164:167], a[108:111], a[12:15], v[164:167]\n"
    "s_add_u32 m0, s69, 8192\n"
    "s_add_u32 s57, s70, 8192\n"
    "v_mfma_f32_16x16x32_bf16 v[164:167], a[112:115], a[16:19], v[164:167]\n"
    "buffer_load_dwordx4 %[l16], %[srdD], s57 offen sc1 lds\n"
    "v_mfma_f32_16x16x32_bf16 v[164:167], a[116:119], a[40:43], v[164:167]\n"
    "ds_read_b128 a[40:43], %[rd1] offset:36864\n"
    "v_mfma_f32_16x16x32_bf16 v[164:167], a[120:123], a[44:47], v[164:167]\n"
    "v_mfma_f32_16x16x32_bf16 v[164:167], a[124:127], a[48:51], v[164:167]\n"
    "v_mfma_f32_16x16x32_bf16 v[168:171], a[100:103], a[52:55], v[168:171]\n"
    "v_mfma_f32_16x16x32_bf16 v[168:171], a[96:99], a[48:51], v[168:171]\n"
    "v_mfma_f32_16x16x32_bf16 v[168:171], a[92:95], a[44:47], v[168:171]\n"
    "s_add_u32 m0, s69, 12288\n"
    "s_add_u32 s57, s70, 12288\n"
    "v_mfma_f32_16x16x32_bf16 v[168:171], a[88:91], a[20:23], v[168:171]\n"
    "buffer_load_dwordx4 %[l16], %[srdD], s57 offen sc1 lds\n"
    "v_mfma_f32_16x16x32_bf16 v[168:171], a[84:87], a[16:19], v[168:171]\n"
    "v_mfma_f32_16x16x32_bf16 v[168:171], a[80:83], a[12:15], v[168:171]\n"
    "v_mfma_f32_16x16x32_bf16 v[172:175], a[104:107], a[12:15], v[172:175]\n"
    "ds_read_b128 a[12:15], %[rd0] offset:38912\n"
    "v_mfma_f32_16x16x32_bf16 v[172:175], a[108:111], a[16:19], v[172:175]\n"
    "v_mfma_f32_16x16x32_bf16 v[172:175], a[112:115], a[20:23], v[172:175]\n"
    "v_mfma_f32_16x16x32_bf16 v[172:175], a[116:119], a[44:47], v[172:175]\n"
    "ds_read_b128 a[44:47], %[rd1] offset:38912\n"
    "v_mfma_f32_16x16x32_bf16 v[172:175], a[120:123], a[48:51], v[172:175]\n"
    "v_mfma_f32_16x16x32_bf16 v[172:175], a[124:127], a[52:55], v[172:175]\n"
    "v_mfma_f32_16x16x32_bf16 v[176:179], a[100:103], a[56:59], v[176:179]\n"
    "v_mfma_f32_16x16x32_bf16 v[176:179], a[96:99], a[52:55], v[176:179]\n"
    "v_mfma_f32_16x16x32_bf16 v[176:179], a[92:95], a[48:51], v[176:179]\n"
    "s_add_u32 m0, s69, 16384\n"
    "s_add_u32 s57, s70, 16384\n"
    "v_mfma_f32_16x16x32_bf16 v[176:179], a[88:91], a[24:27], v[176:179]\n"
    "buffer_load_dwordx4 %[l16], %[srdD], s57 offen sc1 lds\n"
    "v_mfma_f32_16x16x32_bf16 v[176:179], a[84:87], a[20:23], v[176:179]\n"
    "v_mfma_f32_16x16x32_bf16 v[176:179], a[80:83], a[16:19], v[176:179]\n"
    "v_mfma_f32_16x16x32_bf16 v[180:183], a[104:107], a[16:19], v[180:183]\n"
    "ds_read_b128 a[16:19], %[rd0] offset:40960\n"
    "v_mfma_f32_16x16x32_bf16 v[180:183], a[108:111], a[20:23], v[180:183]\n"
    "v_mfma_f32_16x16x32_bf16 v[180:183], a[112:115], a[24:27], v[180:183]\n"
    "v_mfma_f32_16x16x32_bf16 v[180:183], a[116:119], a[48:51], v[180:183]\n"
    "ds_read_b128 a[48:51], %[rd1] offset:40960\n"
    "v_mfma_f32_16x16x32_bf16 v[180:183], a[120:123], a[52:55], v[180:183]\n"
    "v_mfma_f32_16x16x32_bf16 v[180:183], a[124:127], a[56:59], v[180:183]\n"
    "v_mfma_f32_16x16x32_bf16 v[184:187], a[100:103], a[60:63], v[184:187]\n"
    "v_mfma_f32_16x16x32_bf16 v[184:187], a[96:99], a[56:59], v[184:187]\n"
    "v_mfma_f32_16x16x32_bf16 v[184:187], a[92:95], a[52:55], v[184:187]\n"
    "s_add_u32 m0, s69, 20480\n"
    "s_add_u32 s57, s70, 20480\n"
    "v_mfma_f32_16x16x32_bf16 v[184:187], a[88:91], a[28:31], v[184:187]\n"
    "buffer_load_dwordx4 %[l16], %[srdD], s57 offen sc1 lds\n"
    "v_mfma_f32_16x16x32_bf16 v[184:187], a[84:87], a[24:27], v[184:187]\n"
    "v_mfma_f32_16x16x32_bf16 v[184:187], a[80:83], a[20:23], v[184:187]\n"
    "v_mfma_f32_16x16x32_bf16 v[188:191], a[104:107], a[20:23], v[188:191]\n"
    "ds_read_b128 a[20:23], %[rd0] offset:43008\n"
    "v_mfma_f32_16x16x32_bf16 v[188:191], a[108:111], a[24:27], v[188:191]\n"
    "v_mfma_f32_16x16x32_bf16 v[188:191], a[112:115], a[28:31], v[188:191]\n"
    "v_mfma_f32_16x16x32_bf16 v[188:191], a[116:119], a[52:55], v[188:191]\n"
    "ds_read_b128 a[52:55], %[rd1] offset:43008\n"
    "v_mfma_f32_16x16x32_bf16 v[188:191], a[120:123], a[56:59], v[188:191]\n"
    "v_mfma_f32_16x16x32_bf16 v[188:191], a[124:127], a[60:63], v[188:191]\n"
    "v_mfma_f32_16x16x32_bf16 v[192:195], a[100:103], a[72:75], v[192:195]\n"
    "v_mfma_f32_16x16x32_bf16 v[192:195], a[96:99], a[60:63], v[192:195]\n"
    "v_mfma_f32_16x16x32_bf16 v[192:195], a[92:95], a[56:59], v[192:195]\n"
    "s_add_u32 m0, s69, 24576\n"
    "s_add_u32 s57, s70, 24576\n"
    "v_mfma_f32_16x16x32_bf16 v[192:195], a[88:91], a[64:67], v[192:195]\n"
    "buffer_load_dwordx4 %[l16], %[srdD], s57 offen sc1 lds\n"
    "v_mfma_f32_16x16x32_bf16 v[192:195], a[84:87], a[28:31], v[192:195]\n"
    "v_mfma_f32_16x16x32_bf16 v[192:195], a[80:83], a[24:27], v[192:195]\n"
    "v_mfma_f32_16x16x32_bf16 v[196:199], a[104:107], a[24:27], v[196:199]\n"
    "ds_read_b128 a[24:27], %[rd0] offset:45056\n"
    "v_mfma_f32_16x16x32_bf16 v[196:199], a[108:111], a[28:31], v[196:199]\n"
    "v_mfma_f32_16x16x32_bf16 v[196:199], a[112:115], a[64:67], v[196:199]\n"
    "v_mfma_f32_16x16x32_bf16 v[196:199], a[116:119], a[56:59], v[196:199]\n"
    "ds_read_b128 a[56:59], %[rd1] offset:45056\n"
    "v_mfma_f32_16x16x32_bf16 v[196:199], a[120:123], a[60:63], v[196:199]\n"
    "v_mfma_f32_16x16x32_bf16 v[196:199], a[124:127], a[72:75], v[196:199]\n"
    "v_mfma_f32_16x16x32_bf16 v[200:203], a[100:103], a[76:79], v[200:203]\n"
    "v_mfma_f32_16x16x32_bf16 v[200:203], a[96:99], a[72:75], v[200:203]\n"
    "v_mfma_f32_16x16x32_bf16 v[200:203], a[92:95], a[60:63], v[200:203]\n"
    "s_add_u32 m0, s69, 28672\n"
    "s_add_u32 s57, s70, 28672\n"
    "v_mfma_f32_16x16x32_bf16 v[200:203], a[88:91], a[68:71], v[200:203]\n"
    "buffer_load_dwordx4 %[l16], %[srdD], s57 offen sc1 lds\n"
    "v_mfma_f32_16x16x32_bf16 v[200:203], a[84:87], a[64:67], v[200:203]\n"
    "v_mfma_f32_16x16x32_bf16 v[200:203], a[80:83], a[28:31], v[200:203]\n"
    "s_add_u32 s69, s69, 0x8000\n"
    "s_and_b32 s69, s69, 0x1ffff\n"
    "v_mfma_f32_16x16x32_bf16 v[204:207], a[104:107], a[28:31], v[204:207]\n"
    "ds_read_b128 a[28:31], %[rd0] offset:47104\n"
    "v_mfma_f32_16x16x32_bf16 v[204:207], a[108:111], a[64:67], v[204:207]\n"
    "s_add_u32 s70, s70, 0x8000\n"
    "v_mfma_f32_16x16x32_bf16 v[204:207], a[112:115], a[68:71], v[204:207]\n"
    "v_mfma_f32_16x16x32_bf16 v[204:207], a[116:119], a[60:63], v[204:207]\n"
    "ds_read_b128 a[60:63], %[rd1] offset:47104\n"
    "v_mfma_f32_16x16x32_bf16 v[204:207], a[120:123], a[72:75], v[204:207]\n"
    "v_mfma_f32_16x16x32_bf16 v[204:207], a[124:127], a[76:79], v[204:207]\n"
    "s_waitcnt vmcnt(10)\n"
    "s_waitcnt lgkmcnt(8)\n"
    "s_cmp_lg_u32 s67, 0\n"
    "s_cbranch_scc1 Lc18%=\n"
    "v_mfma_f32_16x16x32_bf16 v[80:83], a[148:151], a[40:43], v[80:83]\n"
    "v_mfma_f32_16x16x32_bf16 v[80:83], a[144:147], a[36:39], v[80:83]\n"
    "v_mfma_f32_16x16x32_bf16 v[80:83], a[140:143], a[32:35], v[80:83]\n"
    "v_mfma_f32_16x16x32_bf16 v[80:83], a[136:139], a[8:11], v[80:83]\n"
    "v_mfma_f32_16x16x32_bf16 v[80:83], a[132:135], a[4:7], v[80:83]\n"
    "v_mfma_f32_16x16x32_bf16 v[80:83], a[128:131], a[0:3], v[80:83]\n"
    "v_mfma_f32_16x16x32_bf16 v[84:87], a[152:155], a[0:3], v[84:87]\n"
    "v_mfma_f32_16x16x32_bf16 v[84:87], a[156:159], a[4:7], v[84:87]\n"
    "v_mfma_f32_16x16x32_bf16 v[84:87], a[160:163], a[8:11], v[84:87]\n"
    "v_mfma_f32_16x16x32_bf16 v[84:87], a[164:167], a[32:35], v[84:87]\n"
    "v_mfma_f32_16x16x32_bf16 v[84:87], a[168:171], a[36:39], v[84:87]\n"
    "v_mfma_f32_16x16x32_bf16 v[84:87], a[172:175], a[40:43], v[84:87]\n"
    "Lc18%=:\n"
    "v_mfma_f32_16x16x32_bf16 v[88:91], a[148:151], a[44:47], v[88:91]\n"
    "ds_read_b128 a[0:3], %[rd0] offset:49152\n"
    "v_mfma_f32_16x16x32_bf16 v[88:91], a[144:147], a[40:43], v[88:91]\n"
    "v_lshlrev_b32 v68, 16, v32\n"
    "v_and_b32 v69, s65, v32\n"
    "v_mfma_f32_16x16x32_bf16 v[88:91], a[140:143], a[36:39], v[88:91]\n"
    "buffer_load_dwordx4 a[80:83], %[l16], %[srdW], s54 offen offset:0\n"
    "v_lshlrev_b32 v70, 16, v33\n"
    "v_mfma_f32_16x16x32_bf16 v[88:91], a[136:139], a[12:15], v[88:91]\n"
    "v_and_b32 v71, s65, v33\n"
    "v_lshlrev_b32 v72, 16, v34\n"
    "v_mfma_f32_16x16x32_bf16 v[88:91], a[132:135], a[8:11], v[88:91]\n"
    "ds_read_b128 a[32:35], %[rd1] offset:49152\n"
    "v_and_b32 v73, s65, v34\n"
    "v_mfma_f32_16x16x32_bf16 v[88:91], a[128:131], a[4:7], v[88:91]\n"
    "v_lshlrev_b32 v74, 16, v35\n"
    "v_and_b32 v75, s65, v35\n"
    "v_mfma_f32_16x16x32_bf16 v[92:95], a[152:155], a[4:7], v[92:95]\n"
    "ds_read_b128 a[4:7], %[rd0] offset:51200\n"
    "v_sub_f32 v60, v60, v68\n"
    "v_mfma_f32_16x16x32_bf16 v[92:95], a[156:159], a[8:11], v[92:95]\n"
    "v_sub_f32 v61, v61, v69\n"
    "v_sub_f32 v62, v62, v70\n"
    "v_mfma_f32_16x16x32_bf16 v[92:95], a[160:163], a[12:15], v[92:95]\n"
    "v_sub_f32 v63, v63, v71\n"
    "v_sub_f32 v64, v64, v72\n"
    "v_mfma_f32_16x16x32_bf16 v[92:95], a[164:167], a[36:39], v[92:95]\n"
    "ds_read_b128 a[36:39], %[rd1] offset:51200\n"
    "v_sub_f32 v65, v65, v73\n"
    "v_mfma_f32_16x16x32_bf16 v[92:95], a[168:171], a[40:43], v[92:95]\n"
    "buffer_load_dwordx4 a[84:87], %[l16], %[srdW], s54 offen offset:1024\n"
    "v_sub_f32 v66, v66, v74\n"
    "v_mfma_f32_16x16x32_bf16 v[92:95], a[172:175], a[44:47], v[92:95]\n"
    "s_waitcnt lgkmcnt(8)\n"
    "v_sub_f32 v67, v67, v75\n"
    "v_cvt_pk_bf16_f32 v32, v60, v61\n"
    "v_mfma_f32_16x16x32_bf16 v[96:99], a[148:151], a[48:51], v[96:99]\n"
    "v_cvt_pk_bf16_f32 v33, v62, v63\n"
    "v_cvt_pk_bf16_f32 v34, v64, v65\n"
    "v_mfma_f32_16x16x32_bf16 v[96:99], a[144:147], a[44:47], v[96:99]\n"
    "ds_read_b128 a[64:67], %[re0] offset:32768\n"
    "v_cvt_pk_bf16_f32 v35, v66, v67\n"
    "v_mfma_f32_16x16x32_bf16 v[96:99], a[140:143], a[40:43], v[96:99]\n"
    "buffer_load_dwordx4 a[88:91], %[l16], %[srdW], s54 offen offset:2048\n"
    "v_mfma_f32_16x16x32_bf16 v[96:99], a[136:139], a[16:19], v[96:99]\n"
    "v_mfma_f32_16x16x32_bf16 v[96:99], a[132:135], a[12:15], v[96:99]\n"
    "v_mfma_f32_16x16x32_bf16 v[96:99], a[128:131], a[8:11], v[96:99]\n"
    "v_mfma_f32_16x16x32_bf16 v[100:103], a[152:155], a[8:11], v[100:103]\n"
    "ds_read_b128 a[8:11], %[rd0] offset:53248\n"
    "v_mfma_f32_16x16x32_bf16 v[100:103], a[156:159], a[12:15], v[100:103]\n"
    "v_mfma_f32_16x16x32_bf16 v[100:103], a[160:163], a[16:19], v[100:103]\n"
    "v_mfma_f32_16x16x32_bf16 v[100:103], a[164:167], a[40:43], v[100:103]\n"
    "ds_read_b128 a[40:43], %[rd1] offset:53248\n"
    "v_mfma_f32_16x16x32_bf16 v[100:103], a[168:171], a[44:47], v[100:103]\n"
    "buffer_load_dwordx4 a[92:95], %[l16], %[srdW], s54 offen offset:3072\n"
    "v_mfma_f32_16x16x32_bf16 v[100:103], a[172:175], a[48:51], v[100:103]\n"
    "v_mfma_f32_16x16x32_bf16 v[104:107], a[148:151], a[52:55], v[104:107]\n"
    "s_add_u32 s54, s54, 4096\n"
    "v_mfma_f32_16x16x32_bf16 v[104:107], a[144:147], a[48:51], v[104:107]\n"
    "ds_read_b128 a[72:75], %[re1] offset:32768\n"
    "v_mfma_f32_16x16x32_bf16 v[104:107], a[140:143], a[44:47], v[104:107]\n"
    "buffer_load_dwordx4 a[96:99], %[l16], %[srdW], s54 offen offset:0\n"
    "v_mfma_f32_16x16x32_bf16 v[104:107], a[136:139], a[20:23], v[104:107]\n"
    "v_mfma_f32_16x16x32_bf16 v[104:107], a[132:135], a[16:19], v[104:107]\n"
    "v_mfma_f32_16x16x32_bf16 v[104:107], a[128:131], a[12:15], v[104:107]\n"
    "v_mfma_f32_16x16x32_bf16 v[108:111], a[152:155], a[12:15], v[108:111]\n"
    "ds_read_b128 a[12:15], %[rd0] offset:55296\n"
    "v_mfma_f32_16x16x32_bf16 v[108:111], a[156:159], a[16:19], v[108:111]\n"
    "v_mfma_f32_16x16x32_bf16 v[108:111], a[160:163], a[20:23], v[108:111]\n"
    "v_mfma_f32_16x16x32_bf16 v[108:111], a[164:167], a[44:47], v[108:111]\n"
    "ds_read_b128 a[44:47], %[rd1] offset:55296\n"
    "v_mfma_f32_16x16x32_bf16 v[108:111], a[168:171], a[48:51], v[108:111]\n"
    "buffer_load_dwordx4 a[100:103], %[l16], %[srdW], s54 offen offset:1024\n"
    "v_mfma_f32_16x16x32_bf16 v[108:111], a[172:175], a[52:55], v[108:111]\n"
    "s_waitcnt lgkmcnt(10)\n"
    "v_mfma_f32_16x16x32_bf16 v[112:115], a[148:151], a[56:59], v[112:115]\n"
    "v_mfma_f32_16x16x32_bf16 v[112:115], a[144:147], a[52:55], v[112:115]\n"
    "ds_read_b128 a[68:71], %[re0] offset:34816\n"
    "v_mfma_f32_16x16x32_bf16 v[112:115], a[140:143], a[48:51], v[112:115]\n"
    "buffer_load_dwordx4 a[104:107], %[l16], %[srdW], s54 offen offset:2048\n"
    "v_mfma_f32_16x16x32_bf16 v[112:115], a[136:139], a[24:27], v[112:115]\n"
    "v_mfma_f32_16x16x32_bf16 v[112:115], a[132:135], a[20:23], v[112:115]\n"
    "v_mfma_f32_16x16x32_bf16 v[112:115], a[128:131], a[16:19], v[112:115]\n"
    "v_mfma_f32_16x16x32_bf16 v[116:119], a[152:155], a[16:19], v[116:119]\n"
    "ds_read_b128 a[16:19], %[rd0] offset:57344\n"
    "v_mfma_f32_16x16x32_bf16 v[116:119], a[156:159], a[20:23], v[116:119]\n"
    "v_mfma_f32_16x16x32_bf16 v[116:119], a[160:163], a[24:27], v[116:119]\n"
    "v_mfma_f32_16x16x32_bf16 v[116:119], a[164:167], a[48:51], v[116:119]\n"
    "ds_read_b128 a[48:51], %[rd1] offset:57344\n"
    "v_mfma_f32_16x16x32_bf16 v[116:119], a[168:171], a[52:55], v[116:119]\n"
    "buffer_load_dwordx4 a[108:111], %[l16], %[srdW], s54 offen offset:3072\n"
    "v_mfma_f32_16x16x32_bf16 v[116:119], a[172:175], a[56:59], v[116:119]\n"
    "v_mfma_f32_16x16x32_bf16 v[120:123], a[148:151], a[60:63], v[120:123]\n"
    "s_add_u32 s54, s54, 4096\n"
    "v_mfma_f32_16x16x32_bf16 v[120:123], a[144:147], a[56:59], v[120:123]\n"
    "ds_read_b128 a[76:79], %[re1] offset:34816\n"
    "v_mfma_f32_16x16x32_bf16 v[120:123], a[140:143], a[52:55], v[120:123]\n"
    "buffer_load_dwordx4 a[112:115], %[l16], %[srdW], s54 offen offset:0\n"
    "v_mfma_f32_16x16x32_bf16 v[120:123], a[136:139], a[28:31], v[120:123]\n"
    "v_mfma_f32_16x16x32_bf16 v[120:123], a[132:135], a[24:27], v[120:123]\n"
    "v_mfma_f32_16x16x32_bf16 v[120:123], a[128:131], a[20:23], v[120:123]\n"
    "v_mfma_f32_16x16x32_bf16 v[124:127], a[152:155], a[20:23], v[124:127]\n"
    "ds_read_b128 a[20:23], %[rd0] offset:59392\n"
    "v_mfma_f32_16x16x32_bf16 v[124:127], a[156:159], a[24:27], v[124:127]\n"
    "v_mfma_f32_16x16x32_bf16 v[124:127], a[160:163], a[28:31], v[124:127]\n"
    "v_mfma_f32_16x16x32_bf16 v[124:127], a[164:167], a[52:55], v[124:127]\n"
    "ds_read_b128 a[52:55], %[rd1] offset:59392\n"
    "v_mfma_f32_16x16x32_bf16 v[124:127], a[168:171], a[56:59], v[124:127]\n"
    "buffer_load_dwordx4 a[116:119], %[l16], %[srdW], s54 offen offset:1024\n"
    "v_mfma_f32_16x16x32_bf16 v[124:127], a[172:175], a[60:63], v[124:127]\n"
    "s_waitcnt lgkmcnt(12)\n"
    "v_mfma_f32_16x16x32_bf16 v[128:131], a[148:151], a[32:35], v[128:131]\n"
    "v_mfma_f32_16x16x32_bf16 v[128:131], a[144:147], a[60:63], v[128:131]\n"
    "v_mfma_f32_16x16x32_bf16 v[128:131], a[140:143], a[56:59], v[128:131]\n"
    "buffer_load_dwordx4 a[120:123], %[l16], %[srdW], s54 offen offset:2048\n"
    "v_mfma_f32_16x16x32_bf16 v[128:131], a[136:139], a[0:3], v[128:131]\n"
    "v_mfma_f32_16x16x32_bf16 v[128:131], a[132:135], a[28:31], v[128:131]\n"
    "v_mfma_f32_16x16x32_bf16 v[128:131], a[128:131], a[24:27], v[128:131]\n"
    "v_mfma_f32_16x16x32_bf16 v[132:135], a[152:155], a[24:27], v[132:135]\n"
    "ds_read_b128 a[24:27], %[rd0] offset:61440\n"
    "v_mfma_f32_16x16x32_bf16 v[132:135], a[156:159], a[28:31], v[132:135]\n"
    "v_mfma_f32_16x16x32_bf16 v[132:135], a[160:163], a[0:3], v[132:135]\n"
    "v_mfma_f32_16x16x32_bf16 v[132:135], a[164:167], a[56:59], v[132:135]\n"
    "ds_read_b128 a[56:59], %[rd1] offset:61440\n"
    "v_mfma_f32_16x16x32_bf16 v[132:135], a[168:171], a[60:63], v[132:135]\n"
    "buffer_load_dwordx4 a[124:127], %[l16], %[srdW], s54 offen offset:3072\n"
    "v_mfma_f32_16x16x32_bf16 v[132:135], a[172:175], a[32:35], v[132:135]\n"
    "v_mfma_f32_16x16x32_bf16 v[136:139], a[148:151], a[36:39], v[136:139]\n"
    "s_add_u32 s54, s54, 4096\n"
    "v_mfma_f32_16x16x32_bf16 v[136:139], a[144:147], a[32:35], v[136:139]\n"
    "s_add_u32 m0, s69, 0\n"
    "s_add_u32 s57, s55, 0\n"
    "v_subrev_u32 v13, 0x2000, %[od1]\n"
    "v_mfma_f32_16x16x32_bf16 v[136:139], a[140:143], a[60:63], v[136:139]\n"
    "buffer_load_dwordx4 v13, %[srdX], s57 offen  lds\n"
    "v_mfma_f32_16x16x32_bf16 v[136:139], a[136:139], a[4:7], v[136:139]\n"
    "s_add_u32 m0, s69, 4096\n"
    "s_add_u32 s57, s55, 0\n"
    "v_mfma_f32_16x16x32_bf16 v[136:139], a[132:135], a[0:3], v[136:139]\n"
    "buffer_load_dwordx4 %[od1], %[srdX], s57 offen  lds\n"
    "v_mfma_f32_16x16x32_bf16 v[136:139], a[128:131], a[28:31], v[136:139]\n"
    "s_add_u32 s64, s71, 268288\n"
    "buffer_store_dwordx4 v[32:35], %[vst], %[srdD], s64 offen sc0 sc1\n"
    "v_mfma_f32_16x16x32_bf16 v[140:143], a[152:155], a[28:31], v[140:143]\n"
    "ds_read_b128 a[28:31], %[rd0] offset:63488\n"
    "v_mfma_f32_16x16x32_bf16 v[140:143], a[156:159], a[0:3], v[140:143]\n"
    "v_mfma_f32_16x16x32_bf16 v[140:143], a[160:163], a[4:7], v[140:143]\n"
    "v_mfma_f32_16x16x32_bf16 v[140:143], a[164:167], a[60:63], v[140:143]\n"
    "ds_read_b128 a[60:63], %[rd1] offset:63488\n"
    "v_mfma_f32_16x16x32_bf16 v[140:143], a[168:171], a[32:35], v[140:143]\n"
    "v_mfma_f32_16x16x32_bf16 v[140:143], a[172:175], a[36:39], v[140:143]\n"
    "s_waitcnt lgkmcnt(10)\n"
    "v_mfma_f32_16x16x32_bf16 v[144:147], a[148:151], a[40:43], v[144:147]\n"
    "v_xor_b32 %[rd0], 0x10000, %[rd0]\n"
    "v_mfma_f32_16x16x32_bf16 v[144:147], a[144:147], a[36:39], v[144:147]\n"
    "v_xor_b32 %[rd1], 0x10000, %[rd1]\n"
    "v_mfma_f32_16x16x32_bf16 v[144:147], a[140:143], a[32:35], v[144:147]\n"
    "s_add_u32 s63, s63, 1\n"
    "v_mfma_f32_16x16x32_bf16 v[144:147], a[136:139], a[8:11], v[144:147]\n"
    "v_mfma_f32_16x16x32_bf16 v[144:147], a[132:135], a[4:7], v[144:147]\n"
    "v_mfma_f32_16x16x32_bf16 v[144:147], a[128:131], a[0:3], v[144:147]\n"
    "v_mfma_f32_16x16x32_bf16 v[148:151], a[152:155], a[0:3], v[148:151]\n"
    "s_waitcnt lgkmcnt(0)\n"
    "v_mov_b32 v13, s63\n"
    "v_mfma_f32_16x16x32_bf16 v[148:151], a[156:159], a[4:7], v[148:151]\n"
    "ds_write_b32 v14, v13\n"
    "v_mfma_f32_16x16x32_bf16 v[148:151], a[160:163], a[8:11], v[148:151]\n"
    "v_mfma_f32_16x16x32_bf16 v[148:151], a[164:167], a[32:35], v[148:151]\n"
    "v_lshlrev_b32 v60, 16, v36\n"
    "v_and_b32 v61, s65, v36\n"
    "v_mfma_f32_16x16x32_bf16 v[148:151], a[168:171], a[36:39], v[148:151]\n"
    "v_lshlrev_b32 v62, 16, v37\n"
    "v_and_b32 v63, s65, v37\n"
    "v_mfma_f32_16x16x32_bf16 v[148:151], a[172:175], a[40:43], v[148:151]\n"
    "v_lshlrev_b32 v64, 16, v38\n"
    "v_and_b32 v65, s65, v38\n"
    "v_mfma_f32_16x16x32_bf16 v[152:155], a[148:151], a[44:47], v[152:155]\n"
    "v_xor_b32 %[re0], 0x10000, %[re0]\n"
    "v_lshlrev_b32 v66, 16, v39\n"
    "v_mfma_f32_16x16x32_bf16 v[152:155], a[144:147], a[40:43], v[152:155]\n"
    "v_xor_b32 %[re1], 0x10000, %[re1]\n"
    "v_and_b32 v67, s65, v39\n"
    "v_mfma_f32_16x16x32_bf16 v[152:155], a[140:143], a[36:39], v[152:155]\n"
    "v_sub_f32 v68, v60, v68\n"
    "v_sub_f32 v69, v61, v69\n"
    "v_mfma_f32_16x16x32_bf16 v[152:155], a[136:139], a[12:15], v[152:155]\n"
    "v_sub_f32 v70, v62, v70\n"
    "v_sub_f32 v71, v63, v71\n"
    "v_mfma_f32_16x16x32_bf16 v[152:155], a[132:135], a[8:11], v[152:155]\n"
    "ds_read_b32 v13, v12\n"
    "v_sub_f32 v72, v64, v72\n"
    "v_mfma_f32_16x16x32_bf16 v[152:155], a[128:131], a[4:7], v[152:155]\n"
    "v_sub_f32 v73, v65, v73\n"
    "v_sub_f32 v74, v66, v74\n"
    "v_mfma_f32_16x16x32_bf16 v[156:159], a[152:155], a[4:7], v[156:159]\n"
    "v_sub_f32 v75, v67, v75\n"
    "v_cvt_pk_bf16_f32 v36, v68, v69\n"
    "v_mfma_f32_16x16x32_bf16 v[156:159], a[156:159], a[8:11], v[156:159]\n"
    "v_cvt_pk_bf16_f32 v37, v70, v71\n"
    "v_cvt_pk_bf16_f32 v38, v72, v73\n"
    "v_mfma_f32_16x16x32_bf16 v[156:159], a[160:163], a[12:15], v[156:159]\n"
    "v_cvt_pk_bf16_f32 v39, v74, v75\n"
    "v_mfma_f32_16x16x32_bf16 v[156:159], a[164:167], a[36:39], v[156:159]\n"
    "s_add_u32 s64, s71, 6144\n"
    "buffer_store_dwordx4 v[36:39], %[vst], %[srdD], s64 offen sc0 sc1\n"
    "v_mfma_f32_16x16x32_bf16 v[156:159], a[168:171], a[40:43], v[156:159]\n"
    "s_waitcnt lgkmcnt(0)\n"
    "v_cmp_gt_u32 vcc, s63, v13\n"
    "v_mfma_f32_16x16x32_bf16 v[156:159], a[172:175], a[44:47], v[156:159]\n"
    "s_cbranch_vccnz Ls18%=\n"
    "Lb18%=:\n"
    "v_mfma_f32_16x16x32_bf16 v[160:163], a[148:151], a[48:51], v[160:163]\n"
    "ds_read_b128 a[0:3], %[rd0] offset:0\n"
    "v_mfma_f32_16x16x32_bf16 v[160:163], a[144:147], a[44:47], v[160:163]\n"
    "ds_read_b128 a[32:35], %[rd1] offset:0\n"
    "v_mfma_f32_16x16x32_bf16 v[160:163], a[140:143], a[40:43], v[160:163]\n"
    "ds_read_b128 a[4:7], %[rd0] offset:2048\n"
    "v_mfma_f32_16x16x32_bf16 v[160:163], a[136:139], a[16:19], v[160:163]\n"
    "ds_read_b128 a[36:39], %[rd1] offset:2048\n"
    "v_mfma_f32_16x16x32_bf16 v[160:163], a[132:135], a[12:15], v[160:163]\n"
    "v_mfma_f32_16x16x32_bf16 v[160:163], a[128:131], a[8:11], v[160:163]\n"
    "v_mfma_f32_16x16x32_bf16 v[164:167], a[152:155], a[8:11], v[164:167]\n"
    "ds_read_b128 a[8:11], %[rd0] offset:4096\n"
    "v_mfma_f32_16x16x32_bf16 v[164:167], a[156:159], a[12:15], v[164:167]\n"
    "s_add_u32 m0, s69, 8192\n"
    "s_add_u32 s57, s55, 8192\n"
    "v_mfma_f32_16x16x32_bf16 v[164:167], a[160:163], a[16:19], v[164:167]\n"
    "buffer_load_dwordx4 %[od1], %[srdX], s57 offen  lds\n"
    "v_mfma_f32_16x16x32_bf16 v[164:167], a[164:167], a[40:43], v[164:167]\n"
    "ds_read_b128 a[40:43], %[rd1] offset:4096\n"
    "v_mfma_f32_16x16x32_bf16 v[164:167], a[168:171], a[44:47], v[164:167]\n"
    "v_mfma_f32_16x16x32_bf16 v[164:167], a[172:175], a[48:51], v[164:167]\n"
    "v_mfma_f32_16x16x32_bf16 v[168:171], a[148:151], a[52:55], v[168:171]\n"
    "v_mfma_f32_16x16x32_bf16 v[168:171], a[144:147], a[48:51], v[168:171]\n"
    "v_mfma_f32_16x16x32_bf16 v[168:171], a[140:143], a[44:47], v[168:171]\n"
    "s_add_u32 m0, s69, 12288\n"
    "s_add_u32 s57, s55, 16384\n"
    "v_mfma_f32_16x16x32_bf16 v[168:171], a[136:139], a[20:23], v[168:171]\n"
    "buffer_load_dwordx4 %[od1], %[srdX], s57 offen  lds\n"
    "v_mfma_f32_16x16x32_bf16 v[168:171], a[132:135], a[16:19], v[168:171]\n"
    "v_mfma_f32_16x16x32_bf16 v[168:171], a[128:131], a[12:15], v[168:171]\n"
    "v_mfma_f32_16x16x32_bf16 v[172:175], a[152:155], a[12:15], v[172:175]\n"
    "ds_read_b128 a[12:15], %[rd0] offset:6144\n"
    "v_mfma_f32_16x16x32_bf16 v[172:175], a[156:159], a[16:19], v[172:175]\n"
    "v_mfma_f32_16x16x32_bf16 v[172:175], a[160:163], a[20:23], v[172:175]\n"
    "v_mfma_f32_16x16x32_bf16 v[172:175], a[164:167], a[44:47], v[172:175]\n"
    "ds_read_b128 a[44:47], %[rd1] offset:6144\n"
    "v_mfma_f32_16x16x32_bf16 v[172:175], a[168:171], a[48:51], v[172:175]\n"
    "v_mfma_f32_16x16x32_bf16 v[172:175], a[172:175], a[52:55], v[172:175]\n"
    "v_mfma_f32_16x16x32_bf16 v[176:179], a[148:151], a[56:59], v[176:179]\n"
    "v_mfma_f32_16x16x32_bf16 v[176:179], a[144:147], a[52:55], v[176:179]\n"
    "v_mfma_f32_16x16x32_bf16 v[176:179], a[140:143], a[48:51], v[176:179]\n"
    "s_add_u32 m0, s69, 16384\n"
    "s_add_u32 s57, s55, 24576\n"
    "v_mfma_f32_16x16x32_bf16 v[176:179], a[136:139], a[24:27], v[176:179]\n"
    "buffer_load_dwordx4 %[od1], %[srdX], s57 offen  lds\n"
    "v_mfma_f32_16x16x32_bf16 v[176:179], a[132:135], a[20:23], v[176:179]\n"
    "v_mfma_f32_16x16x32_bf16 v[176:179], a[128:131], a[16:19], v[176:179]\n"
    "v_mfma_f32_16x16x32_bf16 v[180:183], a[152:155], a[16:19], v[180:183]\n"
    "ds_read_b128 a[16:19], %[rd0] offset:8192\n"
    "v_mfma_f32_16x16x32_bf16 v[180:183], a[156:159], a[20:23], v[180:183]\n"
    "v_mfma_f32_16x16x32_bf16 v[180:183], a[160:163], a[24:27], v[180:183]\n"
    "v_mfma_f32_16x16x32_bf16 v[180:183], a[164:167], a[48:51], v[180:183]\n"
    "ds_read_b128 a[48:51], %[rd1] offset:8192\n"
    "v_mfma_f32_16x16x32_bf16 v[180:183], a[168:171], a[52:55], v[180:183]\n"
    "v_mfma_f32_16x16x32_bf16 v[180:183], a[172:175], a[56:59], v[180:183]\n"
    "v_mfma_f32_16x16x32_bf16 v[184:187], a[148:151], a[60:63], v[184:187]\n"
    "v_mfma_f32_16x16x32_bf16 v[184:187], a[144:147], a[56:59], v[184:187]\n"
    "v_mfma_f32_16x16x32_bf16 v[184:187], a[140:143], a[52:55], v[184:187]\n"
    "s_add_u32 m0, s69, 20480\n"
    "s_add_u32 s57, s55, 32768\n"
    "v_mfma_f32_16x16x32_bf16 v[184:187], a[136:139], a[28:31], v[184:187]\n"
    "buffer_load_dwordx4 %[od1], %[srdX], s57 offen  lds\n"
    "v_mfma_f32_16x16x32_bf16 v[184:187], a[132:135], a[24:27], v[184:187]\n"
    "v_mfma_f32_16x16x32_bf16 v[184:187], a[128:131], a[20:23], v[184:187]\n"
    "v_mfma_f32_16x16x32_bf16 v[188:191], a[152:155], a[20:23], v[188:191]\n"
    "ds_read_b128 a[20:23], %[rd0] offset:10240\n"
    "v_mfma_f32_16x16x32_bf16 v[188:191], a[156:159], a[24:27], v[188:191]\n"
    "v_mfma_f32_16x16x32_bf16 v[188:191], a[160:163], a[28:31], v[188:191]\n"
    "v_mfma_f32_16x16x32_bf16 v[188:191], a[164:167], a[52:55], v[188:191]\n"
    "ds_read_b128 a[52:55], %[rd1] offset:10240\n"
    "v_mfma_f32_16x16x32_bf16 v[188:191], a[168:171], a[56:59], v[188:191]\n"
    "v_mfma_f32_16x16x32_bf16 v[188:191], a[172:175], a[60:63], v[188:191]\n"
    "v_mfma_f32_16x16x32_bf16 v[192:195], a[148:151], a[72:75], v[192:195]\n"
    "v_mfma_f32_16x16x32_bf16 v[192:195], a[144:147], a[60:63], v[192:195]\n"
    "v_mfma_f32_16x16x32_bf16 v[192:195], a[140:143], a[56:59], v[192:195]\n"
    "s_add_u32 m0, s69, 24576\n"
    "s_add_u32 s57, s55, 40960\n"
    "v_mfma_f32_16x16x32_bf16 v[192:195], a[136:139], a[64:67], v[192:195]\n"
    "buffer_load_dwordx4 %[od1], %[srdX], s57 offen  lds\n"
    "v_mfma_f32_16x16x32_bf16 v[192:195], a[132:135], a[28:31], v[192:195]\n"
    "v_mfma_f32_16x16x32_bf16 v[192:195], a[128:131], a[24:27], v[192:195]\n"
    "v_mfma_f32_16x16x32_bf16 v[196:199], a[152:155], a[24:27], v[196:199]\n"
    "ds_read_b128 a[24:27], %[rd0] offset:12288\n"
    "v_mfma_f32_16x16x32_bf16 v[196:199], a[156:159], a[28:31], v[196:199]\n"
    "v_mfma_f32_16x16x32_bf16 v[196:199], a[160:163], a[64:67], v[196:199]\n"
    "v_mfma_f32_16x16x32_bf16 v[196:199], a[164:167], a[56:59], v[196:199]\n"
    "ds_read_b128 a[56:59], %[rd1] offset:12288\n"
    "v_mfma_f32_16x16x32_bf16 v[196:199], a[168:171], a[60:63], v[196:199]\n"
    "v_mfma_f32_16x16x32_bf16 v[196:199], a[172:175], a[72:75], v[196:199]\n"
    "v_mfma_f32_16x16x32_bf16 v[200:203], a[148:151], a[76:79], v[200:203]\n"
    "v_mfma_f32_16x16x32_bf16 v[200:203], a[144:147], a[72:75], v[200:203]\n"
    "v_mfma_f32_16x16x32_bf16 v[200:203], a[140:143], a[60:63], v[200:203]\n"
    "s_add_u32 m0, s69, 28672\n"
    "s_add_u32 s57, s55, 49152\n"
    "v_mfma_f32_16x16x32_bf16 v[200:203], a[136:139], a[68:71], v[200:203]\n"
    "buffer_load_dwordx4 %[od1], %[srdX], s57 offen  lds\n"
    "v_mfma_f32_16x16x32_bf16 v[200:203], a[132:135], a[64:67], v[200:203]\n"
    "v_mfma_f32_16x16x32_bf16 v[200:203], a[128:131], a[28:31], v[200:203]\n"
    "s_add_u32 s69, s69, 0x8000\n"
    "s_and_b32 s69, s69, 0x1ffff\n"
    "v_mfma_f32_16x16x32_bf16 v[204:207], a[152:155], a[28:31], v[204:207]\n"
    "ds_read_b128 a[28:31], %[rd0] offset:14336\n"
    "v_mfma_f32_16x16x32_bf16 v[204:207], a[156:159], a[64:67], v[204:207]\n"
    "s_add_u32 s55, s55, 128\n"
    "v_mfma_f32_16x16x32_bf16 v[204:207], a[160:163], a[68:71], v[204:207]\n"
    "v_mfma_f32_16x16x32_bf16 v[204:207], a[164:167], a[60:63], v[204:207]\n"
    "ds_read_b128 a[60:63], %[rd1] offset:14336\n"
    "v_mfma_f32_16x16x32_bf16 v[204:207], a[168:171], a[72:75], v[204:207]\n"
    "v_mfma_f32_16x16x32_bf16 v[204:207], a[172:175], a[76:79], v[204:207]\n"
    "s_waitcnt vmcnt(10)\n"
    "s_waitcnt lgkmcnt(8)\n"
    "s_cmp_lg_u32 s67, 0\n"
    "s_cbranch_scc1 Lc19%=\n"
    "v_mfma_f32_16x16x32_bf16 v[80:83], a[100:103], a[40:43], v[80:83]\n"
    "v_mfma_f32_16x16x32_bf16 v[80:83], a[96:99], a[36:39], v[80:83]\n"
    "v_mfma_f32_16x16x32_bf16 v[80:83], a[92:95], a[32:35], v[80:83]\n"
    "v_mfma_f32_16x16x32_bf16 v[80:83], a[88:91], a[8:11], v[80:83]\n"
    "v_mfma_f32_16x16x32_bf16 v[80:83], a[84:87], a[4:7], v[80:83]\n"
    "v_mfma_f32_16x16x32_bf16 v[80:83], a[80:83], a[0:3], v[80:83]\n"
    "v_mfma_f32_16x16x32_bf16 v[84:87], a[104:107], a[0:3], v[84:87]\n"
    "v_mfma_f32_16x16x32_bf16 v[84:87], a[108:111], a[4:7], v[84:87]\n"
    "v_mfma_f32_16x16x32_bf16 v[84:87], a[112:115], a[8:11], v[84:87]\n"
    "v_mfma_f32_16x16x32_bf16 v[84:87], a[116:119], a[32:35], v[84:87]\n"
    "v_mfma_f32_16x16x32_bf16 v[84:87], a[120:123], a[36:39], v[84:87]\n"
    "v_mfma_f32_16x16x32_bf16 v[84:87], a[124:127], a[40:43], v[84:87]\n"
    "Lc19%=:\n"
    "v_mfma_f32_16x16x32_bf16 v[88:91], a[100:103], a[44:47], v[88:91]\n"
    "ds_read_b128 a[0:3], %[rd0] offset:16384\n"
    "v_mfma_f32_16x16x32_bf16 v[88:91], a[96:99], a[40:43], v[88:91]\n"
    "v_mfma_f32_16x16x32_bf16 v[88:91], a[92:95], a[36:39], v[88:91]\n"
    "buffer_load_dwordx4 a[128:131], %[l16], %[srdW], s54 offen offset:0\n"
    "v_mfma_f32_16x16x32_bf16 v[88:91], a[88:91], a[12:15], v[88:91]\n"
    "v_mfma_f32_16x16x32_bf16 v[88:91], a[84:87], a[8:11], v[88:91]\n"
    "ds_read_b128 a[32:35], %[rd1] offset:16384\n"
    "v_mfma_f32_16x16x32_bf16 v[88:91], a[80:83], a[4:7], v[88:91]\n"
    "v_mfma_f32_16x16x32_bf16 v[92:95], a[104:107], a[4:7], v[92:95]\n"
    "ds_read_b128 a[4:7], %[rd0] offset:18432\n"
    "v_mfma_f32_16x16x32_bf16 v[92:95], a[108:111], a[8:11], v[92:95]\n"
    "v_mfma_f32_16x16x32_bf16 v[92:95], a[112:115], a[12:15], v[92:95]\n"
    "v_mfma_f32_16x16x32_bf16 v[92:95], a[116:119], a[36:39], v[92:95]\n"
    "ds_read_b128 a[36:39], %[rd1] offset:18432\n"
    "v_mfma_f32_16x16x32_bf16 v[92:95], a[120:123], a[40:43], v[92:95]\n"
    "buffer_load_dwordx4 a[132:135], %[l16], %[srdW], s54 offen offset:1024\n"
    "v_mfma_f32_16x16x32_bf16 v[92:95], a[124:127], a[44:47], v[92:95]\n"
    "s_waitcnt lgkmcnt(8)\n"
    "v_mfma_f32_16x16x32_bf16 v[96:99], a[100:103], a[48:51], v[96:99]\n"
    "v_mfma_f32_16x16x32_bf16 v[96:99], a[96:99], a[44:47], v[96:99]\n"
    "ds_read_b128 a[64:67], %[re0] offset:0\n"
    "v_mfma_f32_16x16x32_bf16 v[96:99], a[92:95], a[40:43], v[96:99]\n"
    "buffer_load_dwordx4 a[136:139], %[l16], %[srdW], s54 offen offset:2048\n"
    "v_mfma_f32_16x16x32_bf16 v[96:99], a[88:91], a[16:19], v[96:99]\n"
    "v_mfma_f32_16x16x32_bf16 v[96:99], a[84:87], a[12:15], v[96:99]\n"
    "v_mfma_f32_16x16x32_bf16 v[96:99], a[80:83], a[8:11], v[96:99]\n"
    "v_mfma_f32_16x16x32_bf16 v[100:103], a[104:107], a[8:11], v[100:103]\n"
    "ds_read_b128 a[8:11], %[rd0] offset:20480\n"
    "v_mfma_f32_16x16x32_bf16 v[100:103], a[108:111], a[12:15], v[100:103]\n"
    "v_mfma_f32_16x16x32_bf16 v[100:103], a[112:115], a[16:19], v[100:103]\n"
    "v_mfma_f32_16x16x32_bf16 v[100:103], a[116:119], a[40:43], v[100:103]\n"
    "ds_read_b128 a[40:43], %[rd1] offset:20480\n"
    "v_mfma_f32_16x16x32_bf16 v[100:103], a[120:123], a[44:47], v[100:103]\n"
    "buffer_load_dwordx4 a[140:143], %[l16], %[srdW], s54 offen offset:3072\n"
    "v_mfma_f32_16x16x32_bf16 v[100:103], a[124:127], a[48:51], v[100:103]\n"
    "v_mfma_f32_16x16x32_bf16 v[104:107], a[100:103], a[52:55], v[104:107]\n"
    "s_add_u32 s54, s54, 4096\n"
    "v_mfma_f32_16x16x32_bf16 v[104:107], a[96:99], a[48:51], v[104:107]\n"
    "ds_read_b128 a[72:75], %[re1] offset:0\n"
    "v_mfma_f32_16x16x32_bf16 v[104:107], a[92:95], a[44:47], v[104:107]\n"
    "buffer_load_dwordx4 a[144:147], %[l16], %[srdW], s54 offen offset:0\n"
    "v_mfma_f32_16x16x32_bf16 v[104:107], a[88:91], a[20:23], v[104:107]\n"
    "v_mfma_f32_16x16x32_bf16 v[104:107], a[84:87], a[16:19], v[104:107]\n"
    "v_mfma_f32_16x16x32_bf16 v[104:107], a[80:83], a[12:15], v[104:107]\n"
    "v_mfma_f32_16x16x32_bf16 v[108:111], a[104:107], a[12:15], v[108:111]\n"
    "ds_read_b128 a[12:15], %[rd0] offset:22528\n"
    "v_mfma_f32_16x16x32_bf16 v[108:111], a[108:111], a[16:19], v[108:111]\n"
    "v_mfma_f32_16x16x32_bf16 v[108:111], a[112:115], a[20:23], v[108:111]\n"
    "v_mfma_f32_16x16x32_bf16 v[108:111], a[116:119], a[44:47], v[108:111]\n"
    "ds_read_b128 a[44:47], %[rd1] offset:22528\n"
    "v_mfma_f32_16x16x32_bf16 v[108:111], a[120:123], a[48:51], v[108:111]\n"
    "buffer_load_dwordx4 a[148:151], %[l16], %[srdW], s54 offen offset:1024\n"
    "v_mfma_f32_16x16x32_bf16 v[108:111], a[124:127], a[52:55], v[108:111]\n"
    "s_waitcnt lgkmcnt(10)\n"
    "v_mfma_f32_16x16x32_bf16 v[112:115], a[100:103], a[56:59], v[112:115]\n"
    "v_mfma_f32_16x16x32_bf16 v[112:115], a[96:99], a[52:55], v[112:115]\n"
    "ds_read_b128 a[68:71], %[re0] offset:2048\n"
    "v_mfma_f32_16x16x32_bf16 v[112:115], a[92:95], a[48:51], v[112:115]\n"
    "buffer_load_dwordx4 a[152:155], %[l16], %[srdW], s54 offen offset:2048\n"
    "v_mfma_f32_16x16x32_bf16 v[112:115], a[88:91], a[24:27], v[112:115]\n"
    "v_mfma_f32_16x16x32_bf16 v[112:115], a[84:87], a[20:23], v[112:115]\n"
    "v_mfma_f32_16x16x32_bf16 v[112:115], a[80:83], a[16:19], v[112:115]\n"
    "v_mfma_f32_16x16x32_bf16 v[116:119], a[104:107], a[16:19], v[116:119]\n"
    "ds_read_b128 a[16:19], %[rd0] offset:24576\n"
    "v_mfma_f32_16x16x32_bf16 v[116:119], a[108:111], a[20:23], v[116:119]\n"
    "v_mfma_f32_16x16x32_bf16 v[116:119], a[112:115], a[24:27], v[116:119]\n"
    "v_mfma_f32_16x16x32_bf16 v[116:119], a[116:119], a[48:51], v[116:119]\n"
    "ds_read_b128 a[48:51], %[rd1] offset:24576\n"
    "v_mfma_f32_16x16x32_bf16 v[116:119], a[120:123], a[52:55], v[116:119]\n"
    "buffer_load_dwordx4 a[156:159], %[l16], %[srdW], s54 offen offset:3072\n"
    "v_mfma_f32_16x16x32_bf16 v[116:119], a[124:127], a[56:59], v[116:119]\n"
    "v_mfma_f32_16x16x32_bf16 v[120:123], a[100:103], a[60:63], v[120:123]\n"
    "s_add_u32 s54, s54, 4096\n"
    "v_mfma_f32_16x16x32_bf16 v[120:123], a[96:99], a[56:59], v[120:123]\n"
    "ds_read_b128 a[76:79], %[re1] offset:2048\n"
    "v_mfma_f32_16x16x32_bf16 v[120:123], a[92:95], a[52:55], v[120:123]\n"
    "buffer_load_dwordx4 a[160:163], %[l16], %[srdW], s54 offen offset:0\n"
    "v_mfma_f32_16x16x32_bf16 v[120:123], a[88:91], a[28:31], v[120:123]\n"
    "v_mfma_f32_16x16x32_bf16 v[120:123], a[84:87], a[24:27], v[120:123]\n"
    "v_mfma_f32_16x16x32_bf16 v[120:123], a[80:83], a[20:23], v[120:123]\n"
    "v_mfma_f32_16x16x32_bf16 v[124:127], a[104:107], a[20:23], v[124:127]\n"
    "ds_read_b128 a[20:23], %[rd0] offset:26624\n"
    "v_mfma_f32_16x16x32_bf16 v[124:127], a[108:111], a[24:27], v[124:127]\n"
    "v_mfma_f32_16x16x32_bf16 v[124:127], a[112:115], a[28:31], v[124:127]\n"
    "v_mfma_f32_16x16x32_bf16 v[124:127], a[116:119], a[52:55], v[124:127]\n"
    "ds_read_b128 a[52:55], %[rd1] offset:26624\n"
    "v_mfma_f32_16x16x32_bf16 v[124:127], a[120:123], a[56:59], v[124:127]\n"
    "buffer_load_dwordx4 a[164:167], %[l16], %[srdW], s54 offen offset:1024\n"
    "v_mfma_f32_16x16x32_bf16 v[124:127], a[124:127], a[60:63], v[124:127]\n"
    "s_waitcnt lgkmcnt(12)\n"
    "v_mfma_f32_16x16x32_bf16 v[128:131], a[100:103], a[32:35], v[128:131]\n"
    "v_mfma_f32_16x16x32_bf16 v[128:131], a[96:99], a[60:63], v[128:131]\n"
    "v_mfma_f32_16x16x32_bf16 v[128:131], a[92:95], a[56:59], v[128:131]\n"
    "buffer_load_dwordx4 a[168:171], %[l16], %[srdW], s54 offen offset:2048\n"
    "v_mfma_f32_16x16x32_bf16 v[128:131], a[88:91], a[0:3], v[128:131]\n"
    "v_mfma_f32_16x16x32_bf16 v[128:131], a[84:87], a[28:31], v[128:131]\n"
    "v_mfma_f32_16x16x32_bf16 v[128:131], a[80:83], a[24:27], v[128:131]\n"
    "v_mfma_f32_16x16x32_bf16 v[132:135], a[104:107], a[24:27], v[132:135]\n"
    "ds_read_b128 a[24:27], %[rd0] offset:28672\n"
    "v_mfma_f32_16x16x32_bf16 v[132:135], a[108:111], a[28:31], v[132:135]\n"
    "v_mfma_f32_16x16x32_bf16 v[132:135], a[112:115], a[0:3], v[132:135]\n"
    "v_mfma_f32_16x16x32_bf16 v[132:135], a[116:119], a[56:59], v[132:135]\n"
    "ds_read_b128 a[56:59], %[rd1] offset:28672\n"
    "v_mfma_f32_16x16x32_bf16 v[132:135], a[120:123], a[60:63], v[132:135]\n"
    "buffer_load_dwordx4 a[172:175], %[l16], %[srdW], s54 offen offset:3072\n"
    "v_mfma_f32_16x16x32_bf16 v[132:135], a[124:127], a[32:35], v[132:135]\n"
    "v_mfma_f32_16x16x32_bf16 v[136:139], a[100:103], a[36:39], v[136:139]\n"
    "s_add_u32 s54, s54, 4096\n"
    "v_mfma_f32_16x16x32_bf16 v[136:139], a[96:99], a[32:35], v[136:139]\n"
    "s_add_u32 m0, s69, 0\n"
    "s_add_u32 s57, s55, 0\n"
    "v_subrev_u32 v13, 0x2000, %[od1]\n"
    "v_mfma_f32_16x16x32_bf16 v[136:139], a[92:95], a[60:63], v[136:139]\n"
    "buffer_load_dwordx4 v13, %[srdX], s57 offen  lds\n"
    "v_mfma_f32_16x16x32_bf16 v[136:139], a[88:91], a[4:7], v[136:139]\n"
    "s_add_u32 m0, s69, 4096\n"
    "s_add_u32 s57, s55, 0\n"
    "v_mfma_f32_16x16x32_bf16 v[136:139], a[84:87], a[0:3], v[136:139]\n"
    "buffer_load_dwordx4 %[od1], %[srdX], s57 offen  lds\n"
    "v_mfma_f32_16x16x32_bf16 v[136:139], a[80:83], a[28:31], v[136:139]\n"
    "v_mfma_f32_16x16x32_bf16 v[140:143], a[104:107], a[28:31], v[140:143]\n"
    "ds_read_b128 a[28:31], %[rd0] offset:30720\n"
    "v_mfma_f32_16x16x32_bf16 v[140:143], a[108:111], a[0:3], v[140:143]\n"
    "v_mfma_f32_16x16x32_bf16 v[140:143], a[112:115], a[4:7], v[140:143]\n"
    "v_mfma_f32_16x16x32_bf16 v[140:143], a[116:119], a[60:63], v[140:143]\n"
    "ds_read_b128 a[60:63], %[rd1] offset:30720\n"
    "v_mfma_f32_16x16x32_bf16 v[140:143], a[120:123], a[32:35], v[140:143]\n"
    "v_mfma_f32_16x16x32_bf16 v[140:143], a[124:127], a[36:39], v[140:143]\n"
    "s_waitcnt lgkmcnt(10)\n"
    "v_mfma_f32_16x16x32_bf16 v[144:147], a[100:103], a[40:43], v[144:147]\n"
    "v_mfma_f32_16x16x32_bf16 v[144:147], a[96:99], a[36:39], v[144:147]\n"
    "v_mfma_f32_16x16x32_bf16 v[144:147], a[92:95], a[32:35], v[144:147]\n"
    "s_add_u32 s63, s63, 1\n"
    "v_mfma_f32_16x16x32_bf16 v[144:147], a[88:91], a[8:11], v[144:147]\n"
    "v_mfma_f32_16x16x32_bf16 v[144:147], a[84:87], a[4:7], v[144:147]\n"
    "v_mfma_f32_16x16x32_bf16 v[144:147], a[80:83], a[0:3], v[144:147]\n"
    "v_mfma_f32_16x16x32_bf16 v[148:151], a[104:107], a[0:3], v[148:151]\n"
    "s_waitcnt lgkmcnt(0)\n"
    "v_mov_b32 v13, s63\n"
    "v_mfma_f32_16x16x32_bf16 v[148:151], a[108:111], a[4:7], v[148:151]\n"
    "ds_write_b32 v14, v13\n"
    "v_mfma_f32_16x16x32_bf16 v[148:151], a[112:115], a[8:11], v[148:151]\n"
    "v_mfma_f32_16x16x32_bf16 v[148:151], a[116:119], a[32:35], v[148:151]\n"
    "v_mfma_f32_16x16x32_bf16 v[148:151], a[120:123], a[36:39], v[148:151]\n"
    "v_mfma_f32_16x16x32_bf16 v[148:151], a[124:127], a[40:43], v[148:151]\n"
    "v_mfma_f32_16x16x32_bf16 v[152:155], a[100:103], a[44:47], v[152:155]\n"
    "v_mfma_f32_16x16x32_bf16 v[152:155], a[96:99], a[40:43], v[152:155]\n"
    "v_mfma_f32_16x16x32_bf16 v[152:155], a[92:95], a[36:39], v[152:155]\n"
    "v_mfma_f32_16x16x32_bf16 v[152:155], a[88:91], a[12:15], v[152:155]\n"
    "v_mfma_f32_16x16x32_bf16 v[152:155], a[84:87], a[8:11], v[152:155]\n"
    "ds_read_b32 v13, v12\n"
    "v_mfma_f32_16x16x32_bf16 v[152:155], a[80:83], a[4:7], v[152:155]\n"
    "v_mfma_f32_16x16x32_bf16 v[156:159], a[104:107], a[4:7], v[156:159]\n"
    "v_mfma_f32_16x16x32_bf16 v[156:159], a[108:111], a[8:11], v[156:159]\n"
    "v_mfma_f32_16x16x32_bf16 v[156:159], a[112:115], a[12:15], v[156:159]\n"
    "v_mfma_f32_16x16x32_bf16 v[156:159], a[116:119], a[36:39], v[156:159]\n"
    "v_mfma_f32_16x16x32_bf16 v[156:159], a[120:123], a[40:43], v[156:159]\n"
    "s_waitcnt lgkmcnt(0)\n"
    "v_cmp_gt_u32 vcc, s63, v13\n"
    "v_mfma_f32_16x16x32_bf16 v[156:159], a[124:127], a[44:47], v[156:159]\n"
    "s_cbranch_vccnz Ls19%=\n"
    "Lb19%=:\n"
    "v_mfma_f32_16x16x32_bf16 v[160:163], a[100:103], a[48:51], v[160:163]\n"
    "ds_read_b128 a[0:3], %[rd0] offset:32768\n"
    "v_mfma_f32_16x16x32_bf16 v[160:163], a[96:99], a[44:47], v[160:163]\n"
    "ds_read_b128 a[32:35], %[rd1] offset:32768\n"
    "v_mfma_f32_16x16x32_bf16 v[160:163], a[92:95], a[40:43], v[160:163]\n"
    "ds_read_b128 a[4:7], %[rd0] offset:34816\n"
    "v_mfma_f32_16x16x32_bf16 v[160:163], a[88:91], a[16:19], v[160:163]\n"
    "ds_read_b128 a[36:39], %[rd1] offset:34816\n"
    "v_mfma_f32_16x16x32_bf16 v[160:163], a[84:87], a[12:15], v[160:163]\n"
    "v_mfma_f32_16x16x32_bf16 v[160:163], a[80:83], a[8:11], v[160:163]\n"
    "v_mfma_f32_16x16x32_bf16 v[164:167], a[104:107], a[8:11], v[164:167]\n"
    "ds_read_b128 a[8:11], %[rd0] offset:36864\n"
    "v_mfma_f32_16x16x32_bf16 v[164:167], a[108:111], a[12:15], v[164:167]\n"
    "s_add_u32 m0, s69, 8192\n"
    "s_add_u32 s57, s55, 8192\n"
    "v_mfma_f32_16x16x32_bf16 v[164:167], a[112:115], a[16:19], v[164:167]\n"
    "buffer_load_dwordx4 %[od1], %[srdX], s57 offen  lds\n"
    "v_mfma_f32_16x16x32_bf16 v[164:167], a[116:119], a[40:43], v[164:167]\n"
    "ds_read_b128 a[40:43], %[rd1] offset:36864\n"
    "v_mfma_f32_16x16x32_bf16 v[164:167], a[120:123], a[44:47], v[164:167]\n"
    "v_mfma_f32_16x16x32_bf16 v[164:167], a[124:127], a[48:51], v[164:167]\n"
    "v_mfma_f32_16x16x32_bf16 v[168:171], a[100:103], a[52:55], v[168:171]\n"
    "v_mfma_f32_16x16x32_bf16 v[168:171], a[96:99], a[48:51], v[168:171]\n"
    "v_mfma_f32_16x16x32_bf16 v[168:171], a[92:95], a[44:47], v[168:171]\n"
    "s_add_u32 m0, s69, 12288\n"
    "s_add_u32 s57, s55, 16384\n"
    "v_mfma_f32_16x16x32_bf16 v[168:171], a[88:91], a[20:23], v[168:171]\n"
    "buffer_load_dwordx4 %[od1], %[srdX], s57 offen  lds\n"
    "v_mfma_f32_16x16x32_bf16 v[168:171], a[84:87], a[16:19], v[168:171]\n"
    "v_mfma_f32_16x16x32_bf16 v[168:171], a[80:83], a[12:15], v[168:171]\n"
    "v_mfma_f32_16x16x32_bf16 v[172:175], a[104:107], a[12:15], v[172:175]\n"
    "ds_read_b128 a[12:15], %[rd0] offset:38912\n"
    "v_mfma_f32_16x16x32_bf16 v[172:175], a[108:111], a[16:19], v[172:175]\n"
    "v_mfma_f32_16x16x32_bf16 v[172:175], a[112:115], a[20:23], v[172:175]\n"
    "v_mfma_f32_16x16x32_bf16 v[172:175], a[116:119], a[44:47], v[172:175]\n"
    "ds_read_b128 a[44:47], %[rd1] offset:38912\n"
    "v_mfma_f32_16x16x32_bf16 v[172:175], a[120:123], a[48:51], v[172:175]\n"
    "v_mfma_f32_16x16x32_bf16 v[172:175], a[124:127], a[52:55], v[172:175]\n"
    "v_mfma_f32_16x16x32_bf16 v[176:179], a[100:103], a[56:59], v[176:179]\n"
    "v_mfma_f32_16x16x32_bf16 v[176:179], a[96:99], a[52:55], v[176:179]\n"
    "v_mfma_f32_16x16x32_bf16 v[176:179], a[92:95], a[48:51], v[176:179]\n"
    "s_add_u32 m0, s69, 16384\n"
    "s_add_u32 s57, s55, 24576\n"
    "v_mfma_f32_16x16x32_bf16 v[176:179], a[88:91], a[24:27], v[176:179]\n"
    "buffer_load_dwordx4 %[od1], %[srdX], s57 offen  lds\n"
    "v_mfma_f32_16x16x32_bf16 v[176:179], a[84:87], a[20:23], v[176:179]\n"
    "v_mfma_f32_16x16x32_bf16 v[176:179], a[80:83], a[16:19], v[176:179]\n"
    "v_mfma_f32_16x16x32_bf16 v[180:183], a[104:107], a[16:19], v[180:183]\n"
    "ds_read_b128 a[16:19], %[rd0] offset:40960\n"
    "v_mfma_f32_16x16x32_bf16 v[180:183], a[108:111], a[20:23], v[180:183]\n"
    "v_mfma_f32_16x16x32_bf16 v[180:183], a[112:115], a[24:27], v[180:183]\n"
    "v_mfma_f32_16x16x32_bf16 v[180:183], a[116:119], a[48:51], v[180:183]\n"
    "ds_read_b128 a[48:51], %[rd1] offset:40960\n"
    "v_mfma_f32_16x16x32_bf16 v[180:183], a[120:123], a[52:55], v[180:183]\n"
    "v_mfma_f32_16x16x32_bf16 v[180:183], a[124:127], a[56:59], v[180:183]\n"
    "v_mfma_f32_16x16x32_bf16 v[184:187], a[100:103], a[60:63], v[184:187]\n"
    "v_mfma_f32_16x16x32_bf16 v[184:187], a[96:99], a[56:59], v[184:187]\n"
    "v_mfma_f32_16x16x32_bf16 v[184:187], a[92:95], a[52:55], v[184:187]\n"
    "s_add_u32 m0, s69, 20480\n"
    "s_add_u32 s57, s55, 32768\n"
    "v_mfma_f32_16x16x32_bf16 v[184:187], a[88:91], a[28:31], v[184:187]\n"
    "buffer_load_dwordx4 %[od1], %[srdX], s57 offen  lds\n"
    "v_mfma_f32_16x16x32_bf16 v[184:187], a[84:87], a[24:27], v[184:187]\n"
    "v_mfma_f32_16x16x32_bf16 v[184:187], a[80:83], a[20:23], v[184:187]\n"
    "v_mfma_f32_16x16x32_bf16 v[188:191], a[104:107], a[20:23], v[188:191]\n"
    "ds_read_b128 a[20:23], %[rd0] offset:43008\n"
    "v_mfma_f32_16x16x32_bf16 v[188:191], a[108:111], a[24:27], v[188:191]\n"
    "v_mfma_f32_16x16x32_bf16 v[188:191], a[112:115], a[28:31], v[188:191]\n"
    "v_mfma_f32_16x16x32_bf16 v[188:191], a[116:119], a[52:55], v[188:191]\n"
    "ds_read_b128 a[52:55], %[rd1] offset:43008\n"
    "v_mfma_f32_16x16x32_bf16 v[188:191], a[120:123], a[56:59], v[188:191]\n"
    "v_mfma_f32_16x16x32_bf16 v[188:191], a[124:127], a[60:63], v[188:191]\n"
    "v_mfma_f32_16x16x32_bf16 v[192:195], a[100:103], a[72:75], v[192:195]\n"
    "v_mfma_f32_16x16x32_bf16 v[192:195], a[96:99], a[60:63], v[192:195]\n"
    "v_mfma_f32_16x16x32_bf16 v[192:195], a[92:95], a[56:59], v[192:195]\n"
    "s_add_u32 m0, s69, 24576\n"
    "s_add_u32 s57, s55, 40960\n"
    "v_mfma_f32_16x16x32_bf16 v[192:195], a[88:91], a[64:67], v[192:195]\n"
    "buffer_load_dwordx4 %[od1], %[srdX], s57 offen  lds\n"
    "v_mfma_f32_16x16x32_bf16 v[192:195], a[84:87], a[28:31], v[192:195]\n"
    "v_mfma_f32_16x16x32_bf16 v[192:195], a[80:83], a[24:27], v[192:195]\n"
    "v_mfma_f32_16x16x32_bf16 v[196:199], a[104:107], a[24:27], v[196:199]\n"
    "ds_read_b128 a[24:27], %[rd0] offset:45056\n"
    "v_mfma_f32_16x16x32_bf16 v[196:199], a[108:111], a[28:31], v[196:199]\n"
    "v_mfma_f32_16x16x32_bf16 v[196:199], a[112:115], a[64:67], v[196:199]\n"
    "v_mfma_f32_16x16x32_bf16 v[196:199], a[116:119], a[56:59], v[196:199]\n"
    "ds_read_b128 a[56:59], %[rd1] offset:45056\n"
    "v_mfma_f32_16x16x32_bf16 v[196:199], a[120:123], a[60:63], v[196:199]\n"
    "v_mfma_f32_16x16x32_bf16 v[196:199], a[124:127], a[72:75], v[196:199]\n"
    "v_mfma_f32_16x16x32_bf16 v[200:203], a[100:103], a[76:79], v[200:203]\n"
    "v_mfma_f32_16x16x32_bf16 v[200:203], a[96:99], a[72:75], v[200:203]\n"
    "v_mfma_f32_16x16x32_bf16 v[200:203], a[92:95], a[60:63], v[200:203]\n"
    "s_add_u32 m0, s69, 28672\n"
    "s_add_u32 s57, s55, 49152\n"
    "v_mfma_f32_16x16x32_bf16 v[200:203], a[88:91], a[68:71], v[200:203]\n"
    "buffer_load_dwordx4 %[od1], %[srdX], s57 offen  lds\n"
    "v_mfma_f32_16x16x32_bf16 v[200:203], a[84:87], a[64:67], v[200:203]\n"
    "v_mfma_f32_16x16x32_bf16 v[200:203], a[80:83], a[28:31], v[200:203]\n"
    "s_add_u32 s69, s69, 0x8000\n"
    "s_and_b32 s69, s69, 0x1ffff\n"
    "v_mfma_f32_16x16x32_bf16 v[204:207], a[104:107], a[28:31], v[204:207]\n"
    "ds_read_b128 a[28:31], %[rd0] offset:47104\n"
    "v_mfma_f32_16x16x32_bf16 v[204:207], a[108:111], a[64:67], v[204:207]\n"
    "s_add_u32 s55, s55, 128\n"
    "v_mfma_f32_16x16x32_bf16 v[204:207], a[112:115], a[68:71], v[204:207]\n"
    "v_mfma_f32_16x16x32_bf16 v[204:207], a[116:119], a[60:63], v[204:207]\n"
    "ds_read_b128 a[60:63], %[rd1] offset:47104\n"
    "v_mfma_f32_16x16x32_bf16 v[204:207], a[120:123], a[72:75], v[204:207]\n"
    "v_mfma_f32_16x16x32_bf16 v[204:207], a[124:127], a[76:79], v[204:207]\n"
    "s_waitcnt vmcnt(8)\n"
    "s_waitcnt lgkmcnt(8)\n"
    "s_cmp_lg_u32 s67, 0\n"
    "s_cbranch_scc1 Lc20%=\n"
    "v_mfma_f32_16x16x32_bf16 v[80:83], a[148:151], a[40:43], v[80:83]\n"
    "v_mfma_f32_16x16x32_bf16 v[80:83], a[144:147], a[36:39], v[80:83]\n"
    "v_mfma_f32_16x16x32_bf16 v[80:83], a[140:143], a[32:35], v[80:83]\n"
    "v_mfma_f32_16x16x32_bf16 v[80:83], a[136:139], a[8:11], v[80:83]\n"
    "v_mfma_f32_16x16x32_bf16 v[80:83], a[132:135], a[4:7], v[80:83]\n"
    "v_mfma_f32_16x16x32_bf16 v[80:83], a[128:131], a[0:3], v[80:83]\n"
    "v_mfma_f32_16x16x32_bf16 v[84:87], a[152:155], a[0:3], v[84:87]\n"
    "v_mfma_f32_16x16x32_bf16 v[84:87], a[156:159], a[4:7], v[84:87]\n"
    "v_mfma_f32_16x16x32_bf16 v[84:87], a[160:163], a[8:11], v[84:87]\n"
    "v_mfma_f32_16x16x32_bf16 v[84:87], a[164:167], a[32:35], v[84:87]\n"
    "v_mfma_f32_16x16x32_bf16 v[84:87], a[168:171], a[36:39], v[84:87]\n"
    "v_mfma_f32_16x16x32_bf16 v[84:87], a[172:175], a[40:43], v[84:87]\n"
    "Lc20%=:\n"
    "v_mfma_f32_16x16x32_bf16 v[88:91], a[148:151], a[44:47], v[88:91]\n"
    "ds_read_b128 a[0:3], %[rd0] offset:49152\n"
    "v_mfma_f32_16x16x32_bf16 v[88:91], a[144:147], a[40:43], v[88:91]\n"
    "s_mov_b32 s54, %[wso]\n"
    "v_mfma_f32_16x16x32_bf16 v[88:91], a[140:143], a[36:39], v[88:91]\n"
    "buffer_load_dwordx4 a[80:83], %[l16], %[srdW], s54 offen offset:0\n"
    "v_mfma_f32_16x16x32_bf16 v[88:91], a[136:139], a[12:15], v[88:91]\n"
    "v_mfma_f32_16x16x32_bf16 v[88:91], a[132:135], a[8:11], v[88:91]\n"
    "ds_read_b128 a[32:35], %[rd1] offset:49152\n"
    "v_mfma_f32_16x16x32_bf16 v[88:91], a[128:131], a[4:7], v[88:91]\n"
    "v_mfma_f32_16x16x32_bf16 v[92:95], a[152:155], a[4:7], v[92:95]\n"
    "ds_read_b128 a[4:7], %[rd0] offset:51200\n"
    "v_mfma_f32_16x16x32_bf16 v[92:95], a[156:159], a[8:11], v[92:95]\n"
    "v_mfma_f32_16x16x32_bf16 v[92:95], a[160:163], a[12:15], v[92:95]\n"
    "v_mfma_f32_16x16x32_bf16 v[92:95], a[164:167], a[36:39], v[92:95]\n"
    "ds_read_b128 a[36:39], %[rd1] offset:51200\n"
    "v_mfma_f32_16x16x32_bf16 v[92:95], a[168:171], a[40:43], v[92:95]\n"
    "buffer_load_dwordx4 a[84:87], %[l16], %[srdW], s54 offen offset:1024\n"
    "v_mfma_f32_16x16x32_bf16 v[92:95], a[172:175], a[44:47], v[92:95]\n"
    "s_waitcnt lgkmcnt(8)\n"
    "v_mfma_f32_16x16x32_bf16 v[96:99], a[148:151], a[48:51], v[96:99]\n"
    "v_mfma_f32_16x16x32_bf16 v[96:99], a[144:147], a[44:47], v[96:99]\n"
    "ds_read_b128 a[64:67], %[re0] offset:32768\n"
    "v_mfma_f32_16x16x32_bf16 v[96:99], a[140:143], a[40:43], v[96:99]\n"
    "buffer_load_dwordx4 a[88:91], %[l16], %[srdW], s54 offen offset:2048\n"
    "v_mfma_f32_16x16x32_bf16 v[96:99], a[136:139], a[16:19], v[96:99]\n"
    "v_mfma_f32_16x16x32_bf16 v[96:99], a[132:135], a[12:15], v[96:99]\n"
    "v_mfma_f32_16x16x32_bf16 v[96:99], a[128:131], a[8:11], v[96:99]\n"
    "v_mfma_f32_16x16x32_bf16 v[100:103], a[152:155], a[8:11], v[100:103]\n"
    "ds_read_b128 a[8:11], %[rd0] offset:53248\n"
    "v_mfma_f32_16x16x32_bf16 v[100:103], a[156:159], a[12:15], v[100:103]\n"
    "v_mfma_f32_16x16x32_bf16 v[100:103], a[160:163], a[16:19], v[100:103]\n"
    "v_mfma_f32_16x16x32_bf16 v[100:103], a[164:167], a[40:43], v[100:103]\n"
    "ds_read_b128 a[40:43], %[rd1] offset:53248\n"
    "v_mfma_f32_16x16x32_bf16 v[100:103], a[168:171], a[44:47], v[100:103]\n"
    "buffer_load_dwordx4 a[92:95], %[l16], %[srdW], s54 offen offset:3072\n"
    "v_mfma_f32_16x16x32_bf16 v[100:103], a[172:175], a[48:51], v[100:103]\n"
    "v_mfma_f32_16x16x32_bf16 v[104:107], a[148:151], a[52:55], v[104:107]\n"
    "s_add_u32 s54, s54, 4096\n"
    "v_mul_f32 v84, s73, v84\n"
    "v_mul_f32 v85, s73, v85\n"
    "v_mfma_f32_16x16x32_bf16 v[104:107], a[144:147], a[48:51], v[104:107]\n"
    "ds_read_b128 a[72:75], %[re1] offset:32768\n"
    "v_mul_f32 v86, s73, v86\n"
    "v_mfma_f32_16x16x32_bf16 v[104:107], a[140:143], a[44:47], v[104:107]\n"
    "buffer_load_dwordx4 a[96:99], %[l16], %[srdW], s54 offen offset:0\n"
    "v_mul_f32 v87, s73, v87\n"
    "v_mfma_f32_16x16x32_bf16 v[104:107], a[136:139], a[20:23], v[104:107]\n"
    "v_mul_f32 v92, s73, v92\n"
    "v_mul_f32 v93, s73, v93\n"
    "v_mfma_f32_16x16x32_bf16 v[104:107], a[132:135], a[16:19], v[104:107]\n"
    "v_mul_f32 v94, s73, v94\n"
    "v_mul_f32 v95, s73, v95\n"
    "v_mfma_f32_16x16x32_bf16 v[104:107], a[128:131], a[12:15], v[104:107]\n"
    "v_mul_f32 v80, s72, v80\n"
    "v_mul_f32 v81, s72, v81\n"
    "v_mfma_f32_16x16x32_bf16 v[108:111], a[152:155], a[12:15], v[108:111]\n"
    "ds_read_b128 a[12:15], %[rd0] offset:55296\n"
    "v_mul_f32 v82, s72, v82\n"
    "v_mfma_f32_16x16x32_bf16 v[108:111], a[156:159], a[16:19], v[108:111]\n"
    "v_mul_f32 v83, s72, v83\n"
    "v_mul_f32 v88, s72, v88\n"
    "v_mfma_f32_16x16x32_bf16 v[108:111], a[160:163], a[20:23], v[108:111]\n"
    "v_mul_f32 v89, s72, v89\n"
    "v_mul_f32 v90, s72, v90\n"
    "v_mfma_f32_16x16x32_bf16 v[108:111], a[164:167], a[44:47], v[108:111]\n"
    "ds_read_b128 a[44:47], %[rd1] offset:55296\n"
    "v_mul_f32 v91, s72, v91\n"
    "v_mfma_f32_16x16x32_bf16 v[108:111], a[168:171], a[48:51], v[108:111]\n"
    "buffer_load_dwordx4 a[100:103], %[l16], %[srdW], s54 offen offset:1024\n"
    "v_mfma_f32_16x16x32_bf16 v[108:111], a[172:175], a[52:55], v[108:111]\n"
    "s_waitcnt lgkmcnt(10)\n"
    "v_exp_f32 v84, v84\n"
    "v_mfma_f32_16x16x32_bf16 v[112:115], a[148:151], a[56:59], v[112:115]\n"
    "v_exp_f32 v85, v85\n"
    "v_mfma_f32_16x16x32_bf16 v[112:115], a[144:147], a[52:55], v[112:115]\n"
    "ds_read_b128 a[68:71], %[re0] offset:34816\n"
    "v_mfma_f32_16x16x32_bf16 v[112:115], a[140:143], a[48:51], v[112:115]\n"
    "buffer_load_dwordx4 a[104:107], %[l16], %[srdW], s54 offen offset:2048\n"
    "v_mfma_f32_16x16x32_bf16 v[112:115], a[136:139], a[24:27], v[112:115]\n"
    "v_exp_f32 v86, v86\n"
    "v_mfma_f32_16x16x32_bf16 v[112:115], a[132:135], a[20:23], v[112:115]\n"
    "v_exp_f32 v87, v87\n"
    "v_mfma_f32_16x16x32_bf16 v[112:115], a[128:131], a[16:19], v[112:115]\n"
    "v_exp_f32 v92, v92\n"
    "v_mfma_f32_16x16x32_bf16 v[116:119], a[152:155], a[16:19], v[116:119]\n"
    "ds_read_b128 a[16:19], %[rd0] offset:57344\n"
    "v_mfma_f32_16x16x32_bf16 v[116:119], a[156:159], a[20:23], v[116:119]\n"
    "v_exp_f32 v93, v93\n"
    "v_mfma_f32_16x16x32_bf16 v[116:119], a[160:163], a[24:27], v[116:119]\n"
    "v_exp_f32 v94, v94\n"
    "v_mfma_f32_16x16x32_bf16 v[116:119], a[164:167], a[48:51], v[116:119]\n"
    "ds_read_b128 a[48:51], %[rd1] offset:57344\n"
    "v_mfma_f32_16x16x32_bf16 v[116:119], a[168:171], a[52:55], v[116:119]\n"
    "buffer_load_dwordx4 a[108:111], %[l16], %[srdW], s54 offen offset:3072\n"
    "v_mfma_f32_16x16x32_bf16 v[116:119], a[172:175], a[56:59], v[116:119]\n"
    "v_exp_f32 v95, v95\n"
    "v_mfma_f32_16x16x32_bf16 v[120:123], a[148:151], a[60:63], v[120:123]\n"
    "s_add_u32 s54, s54, 4096\n"
    "v_exp_f32_e64 v16, -abs(v80)\n"
    "v_mfma_f32_16x16x32_bf16 v[120:123], a[144:147], a[56:59], v[120:123]\n"
    "ds_read_b128 a[76:79], %[re1] offset:34816\n"
    "v_mul_f32 v100, s73, v100\n"
    "v_mfma_f32_16x16x32_bf16 v[120:123], a[140:143], a[52:55], v[120:123]\n"
    "buffer_load_dwordx4 a[112:115], %[l16], %[srdW], s54 offen offset:0\n"
    "v_mul_f32 v101, s73, v101\n"
    "v_mfma_f32_16x16x32_bf16 v[120:123], a[136:139], a[28:31], v[120:123]\n"
    "v_exp_f32_e64 v17, -abs(v81)\n"
    "v_mfma_f32_16x16x32_bf16 v[120:123], a[132:135], a[24:27], v[120:123]\n"
    "v_exp_f32_e64 v18, -abs(v82)\n"
    "v_mfma_f32_16x16x32_bf16 v[120:123], a[128:131], a[20:23], v[120:123]\n"
    "v_exp_f32_e64 v19, -abs(v83)\n"
    "v_mfma_f32_16x16x32_bf16 v[124:127], a[152:155], a[20:23], v[124:127]\n"
    "ds_read_b128 a[20:23], %[rd0] offset:59392\n"
    "v_bfi_b32 v80, s61, 0.5, v80\n"
    "v_mfma_f32_16x16x32_bf16 v[124:127], a[156:159], a[24:27], v[124:127]\n"
    "v_bfi_b32 v81, s61, 0.5, v81\n"
    "v_bfi_b32 v82, s61, 0.5, v82\n"
    "v_mfma_f32_16x16x32_bf16 v[124:127], a[160:163], a[28:31], v[124:127]\n"
    "v_bfi_b32 v83, s61, 0.5, v83\n"
    "v_fma_f32 v80, v16, v80, v80\n"
    "v_mfma_f32_16x16x32_bf16 v[124:127], a[164:167], a[52:55], v[124:127]\n"
    "ds_read_b128 a[52:55], %[rd1] offset:59392\n"
    "v_fma_f32 v81, v17, v81, v81\n"
    "v_mfma_f32_16x16x32_bf16 v[124:127], a[168:171], a[56:59], v[124:127]\n"
    "buffer_load_dwordx4 a[116:119], %[l16], %[srdW], s54 offen offset:1024\n"
    "v_fma_f32 v82, v18, v82, v82\n"
    "v_mfma_f32_16x16x32_bf16 v[124:127], a[172:175], a[60:63], v[124:127]\n"
    "s_waitcnt lgkmcnt(12)\n"
    "v_fma_f32 v83, v19, v83, v83\n"
    "v_mul_f32 v102, s73, v102\n"
    "v_mfma_f32_16x16x32_bf16 v[128:131], a[148:151], a[32:35], v[128:131]\n"
    "v_exp_f32_e64 v16, -abs(v88)\n"
    "v_mfma_f32_16x16x32_bf16 v[128:131], a[144:147], a[60:63], v[128:131]\n"
    "v_exp_f32_e64 v17, -abs(v89)\n"
    "v_mfma_f32_16x16x32_bf16 v[128:131], a[140:143], a[56:59], v[128:131]\n"
    "buffer_load_dwordx4 a[120:123], %[l16], %[srdW], s54 offen offset:2048\n"
    "v_mul_f32 v103, s73, v103\n"
    "v_mfma_f32_16x16x32_bf16 v[128:131], a[136:139], a[0:3], v[128:131]\n"
    "v_exp_f32_e64 v18, -abs(v90)\n"
    "v_mfma_f32_16x16x32_bf16 v[128:131], a[132:135], a[28:31], v[128:131]\n"
    "v_exp_f32_e64 v19, -abs(v91)\n"
    "v_mfma_f32_16x16x32_bf16 v[128:131], a[128:131], a[24:27], v[128:131]\n"
    "v_bfi_b32 v88, s61, 0.5, v88\n"
    "v_bfi_b32 v89, s61, 0.5, v89\n"
    "v_mfma_f32_16x16x32_bf16 v[132:135], a[152:155], a[24:27], v[132:135]\n"
    "ds_read_b128 a[24:27], %[rd0] offset:61440\n"
    "v_bfi_b32 v90, s61, 0.5, v90\n"
    "v_mfma_f32_16x16x32_bf16 v[132:135], a[156:159], a[28:31], v[132:135]\n"
    "v_bfi_b32 v91, s61, 0.5, v91\n"
    "v_fma_f32 v88, v16, v88, v88\n"
    "v_mfma_f32_16x16x32_bf16 v[132:135], a[160:163], a[0:3], v[132:135]\n"
    "v_fma_f32 v89, v17, v89, v89\n"
    "v_fma_f32 v90, v18, v90, v90\n"
    "v_mfma_f32_16x16x32_bf16 v[132:135], a[164:167], a[56:59], v[132:135]\n"
    "ds_read_b128 a[56:59], %[rd1] offset:61440\n"
    "v_fma_f32 v91, v19, v91, v91\n"
    "v_mfma_f32_16x16x32_bf16 v[132:135], a[168:171], a[60:63], v[132:135]\n"
    "buffer_load_dwordx4 a[124:127], %[l16], %[srdW], s54 offen offset:3072\n"
    "v_fma_f32 v84, v84, v80, v80\n"
    "v_mfma_f32_16x16x32_bf16 v[132:135], a[172:175], a[32:35], v[132:135]\n"
    "v_fma_f32 v85, v85, v81, v81\n"
    "v_fma_f32 v86, v86, v82, v82\n"
    "v_mfma_f32_16x16x32_bf16 v[136:139], a[148:151], a[36:39], v[136:139]\n"
    "s_add_u32 s54, s54, 4096\n"
    "v_fma_f32 v87, v87, v83, v83\n"
    "v_fma_f32 v92, v92, v88, v88\n"
    "v_mfma_f32_16x16x32_bf16 v[136:139], a[144:147], a[32:35], v[136:139]\n"
    "s_add_u32 m0, s69, 0\n"
    "s_add_u32 s57, s55, 0\n"
    "v_subrev_u32 v13, 0x2000, %[od1]\n"
    "v_mfma_f32_16x16x32_bf16 v[136:139], a[140:143], a[60:63], v[136:139]\n"
    "buffer_load_dwordx4 v13, %[srdX], s57 offen  lds\n"
    "v_fma_f32 v93, v93, v89, v89\n"
    "v_mfma_f32_16x16x32_bf16 v[136:139], a[136:139], a[4:7], v[136:139]\n"
    "s_add_u32 m0, s69, 4096\n"
    "s_add_u32 s57, s55, 0\n"
    "v_fma_f32 v94, v94, v90, v90\n"
    "v_mfma_f32_16x16x32_bf16 v[136:139], a[132:135], a[0:3], v[136:139]\n"
    "buffer_load_dwordx4 %[od1], %[srdX], s57 offen  lds\n"
    "v_fma_f32 v95, v95, v91, v91\n"
    "v_mfma_f32_16x16x32_bf16 v[136:139], a[128:131], a[28:31], v[136:139]\n"
    "v_rcp_f32 v84, v84\n"
    "v_mfma_f32_16x16x32_bf16 v[140:143], a[152:155], a[28:31], v[140:143]\n"
    "ds_read_b128 a[28:31], %[rd0] offset:63488\n"
    "v_mul_f32 v108, s73, v108\n"
    "v_mfma_f32_16x16x32_bf16 v[140:143], a[156:159], a[0:3], v[140:143]\n"
    "v_rcp_f32 v85, v85\n"
    "v_mfma_f32_16x16x32_bf16 v[140:143], a[160:163], a[4:7], v[140:143]\n"
    "v_rcp_f32 v86, v86\n"
    "v_mfma_f32_16x16x32_bf16 v[140:143], a[164:167], a[60:63], v[140:143]\n"
    "ds_read_b128 a[60:63], %[rd1] offset:63488\n"
    "v_mul_f32 v109, s73, v109\n"
    "v_mfma_f32_16x16x32_bf16 v[140:143], a[168:171], a[32:35], v[140:143]\n"
    "v_rcp_f32 v87, v87\n"
    "v_mfma_f32_16x16x32_bf16 v[140:143], a[172:175], a[36:39], v[140:143]\n"
    "s_waitcnt lgkmcnt(10)\n"
    "v_rcp_f32 v92, v92\n"
    "v_mfma_f32_16x16x32_bf16 v[144:147], a[148:151], a[40:43], v[144:147]\n"
    "v_xor_b32 %[rd0], 0x10000, %[rd0]\n"
    "v_mul_f32 v110, s73, v110\n"
    "v_mfma_f32_16x16x32_bf16 v[144:147], a[144:147], a[36:39], v[144:147]\n"
    "v_xor_b32 %[rd1], 0x10000, %[rd1]\n"
    "v_mul_f32 v111, s73, v111\n"
    "v_mfma_f32_16x16x32_bf16 v[144:147], a[140:143], a[32:35], v[144:147]\n"
    "s_add_u32 s63, s63, 1\n"
    "v_rcp_f32 v93, v93\n"
    "v_mfma_f32_16x16x32_bf16 v[144:147], a[136:139], a[8:11], v[144:147]\n"
    "v_rcp_f32 v94, v94\n"
    "v_mfma_f32_16x16x32_bf16 v[144:147], a[132:135], a[4:7], v[144:147]\n"
    "v_rcp_f32 v95, v95\n"
    "v_mfma_f32_16x16x32_bf16 v[144:147], a[128:131], a[0:3], v[144:147]\n"
    "v_fma_f32 v84, -abs(v80), v84, v84\n"
    "v_fma_f32 v85, -abs(v81), v85, v85\n"
    "v_mfma_f32_16x16x32_bf16 v[148:151], a[152:155], a[0:3], v[148:151]\n"
    "s_waitcnt lgkmcnt(0)\n"
    "v_mov_b32 v13, s63\n"
    "v_mfma_f32_16x16x32_bf16 v[148:151], a[156:159], a[4:7], v[148:151]\n"
    "ds_write_b32 v14, v13\n"
    "v_mfma_f32_16x16x32_bf16 v[148:151], a[160:163], a[8:11], v[148:151]\n"
    "v_fma_f32 v86, -abs(v82), v86, v86\n"
    "v_fma_f32 v87, -abs(v83), v87, v87\n"
    "v_mfma_f32_16x16x32_bf16 v[148:151], a[164:167], a[32:35], v[148:151]\n"
    "v_fma_f32 v92, -abs(v88), v92, v92\n"
    "v_fma_f32 v93, -abs(v89), v93, v93\n"
    "v_mfma_f32_16x16x32_bf16 v[148:151], a[168:171], a[36:39], v[148:151]\n"
    "v_fma_f32 v94, -abs(v90), v94, v94\n"
    "v_fma_f32 v95, -abs(v91), v95, v95\n"
    "v_mfma_f32_16x16x32_bf16 v[148:151], a[172:175], a[40:43], v[148:151]\n"
    "v_cvt_pk_bf16_f32 v16, v84, v85\n"
    "v_cvt_pk_bf16_f32 v17, v86, v87\n"
    "v_mfma_f32_16x16x32_bf16 v[152:155], a[148:151], a[44:47], v[152:155]\n"
    "v_xor_b32 %[re0], 0x10000, %[re0]\n"
    "v_cvt_pk_bf16_f32 v18, v92, v93\n"
    "v_mfma_f32_16x16x32_bf16 v[152:155], a[144:147], a[40:43], v[152:155]\n"
    "v_xor_b32 %[re1], 0x10000, %[re1]\n"
    "v_cvt_pk_bf16_f32 v19, v94, v95\n"
    "v_mfma_f32_16x16x32_bf16 v[152:155], a[140:143], a[36:39], v[152:155]\n"
    "ds_write_b64 %[vsw], v[16:17] offset:0\n"
    "v_mfma_f32_16x16x32_bf16 v[152:155], a[136:139], a[12:15], v[152:155]\n"
    "ds_write_b64 %[vsw], v[18:19] offset:128\n"
    "v_mfma_f32_16x16x32_bf16 v[152:155], a[132:135], a[8:11], v[152:155]\n"
    "ds_read_b32 v13, v12\n"
    "v_mul_f32 v96, s72, v96\n"
    "v_mfma_f32_16x16x32_bf16 v[152:155], a[128:131], a[4:7], v[152:155]\n"
    "s_lshl_b32 s58, s66, 11\n"
    "v_add_u32 %[re0], s58, %[re0]\n"
    "v_add_u32 %[re1], s58, %[re1]\n"
    "v_mfma_f32_16x16x32_bf16 v[156:159], a[152:155], a[4:7], v[156:159]\n"
    "v_mul_f32 v97, s72, v97\n"
    "v_mul_f32 v98, s72, v98\n"
    "v_mfma_f32_16x16x32_bf16 v[156:159], a[156:159], a[8:11], v[156:159]\n"
    "v_mul_f32 v99, s72, v99\n"
    "v_mul_f32 v104, s72, v104\n"
    "v_mfma_f32_16x16x32_bf16 v[156:159], a[160:163], a[12:15], v[156:159]\n"
    "v_mul_f32 v105, s72, v105\n"
    "v_mul_f32 v106, s72, v106\n"
    "v_mfma_f32_16x16x32_bf16 v[156:159], a[164:167], a[36:39], v[156:159]\n"
    "v_mul_f32 v107, s72, v107\n"
    "v_mul_f32 v116, s73, v116\n"
    "v_mfma_f32_16x16x32_bf16 v[156:159], a[168:171], a[40:43], v[156:159]\n"
    "s_waitcnt lgkmcnt(0)\n"
    "v_cmp_gt_u32 vcc, s63, v13\n"
    "v_mul_f32 v117, s73, v117\n"
    "v_mfma_f32_16x16x32_bf16 v[156:159], a[172:175], a[44:47], v[156:159]\n"
    "s_cbranch_vccnz Ls20%=\n"
    "Lb20%=:\n"
    "v_mul_f32 v118, s73, v118\n"
    "v_mfma_f32_16x16x32_bf16 v[160:163], a[148:151], a[48:51], v[160:163]\n"
    "ds_read_b128 a[0:3], %[rd0] offset:0\n"
    "v_mul_f32 v119, s73, v119\n"
    "v_mfma_f32_16x16x32_bf16 v[160:163], a[144:147], a[44:47], v[160:163]\n"
    "ds_read_b128 a[32:35], %[rd1] offset:0\n"
    "v_mul_f32 v124, s73, v124\n"
    "v_mfma_f32_16x16x32_bf16 v[160:163], a[140:143], a[40:43], v[160:163]\n"
    "ds_read_b128 a[4:7], %[rd0] offset:2048\n"
    "v_mul_f32 v125, s73, v125\n"
    "v_mfma_f32_16x16x32_bf16 v[160:163], a[136:139], a[16:19], v[160:163]\n"
    "ds_read_b128 a[36:39], %[rd1] offset:2048\n"
    "v_mul_f32 v126, s73, v126\n"
    "v_mfma_f32_16x16x32_bf16 v[160:163], a[132:135], a[12:15], v[160:163]\n"
    "v_exp_f32 v100, v100\n"
    "v_mfma_f32_16x16x32_bf16 v[160:163], a[128:131], a[8:11], v[160:163]\n"
    "v_exp_f32 v101, v101\n"
    "v_mfma_f32_16x16x32_bf16 v[164:167], a[152:155], a[8:11], v[164:167]\n"
    "ds_read_b128 a[8:11], %[rd0] offset:4096\n"
    "v_mul_f32 v127, s73, v127\n"
    "v_mfma_f32_16x16x32_bf16 v[164:167], a[156:159], a[12:15], v[164:167]\n"
    "s_add_u32 m0, s69, 8192\n"
    "s_add_u32 s57, s55, 8192\n"
    "v_mul_f32 v112, s72, v112\n"
    "v_mfma_f32_16x16x32_bf16 v[164:167], a[160:163], a[16:19], v[164:167]\n"
    "buffer_load_dwordx4 %[od1], %[srdX], s57 offen  lds\n"
    "v_mul_f32 v113, s72, v113\n"
    "v_mfma_f32_16x16x32_bf16 v[164:167], a[164:167], a[40:43], v[164:167]\n"
    "ds_read_b128 a[40:43], %[rd1] offset:4096\n"
    "v_mul_f32 v114, s72, v114\n"
    "v_mfma_f32_16x16x32_bf16 v[164:167], a[168:171], a[44:47], v[164:167]\n"
    "v_exp_f32 v102, v102\n"
    "v_mfma_f32_16x16x32_bf16 v[164:167], a[172:175], a[48:51], v[164:167]\n"
    "v_exp_f32 v103, v103\n"
    "v_mfma_f32_16x16x32_bf16 v[168:171], a[148:151], a[52:55], v[168:171]\n"
    "v_exp_f32 v108, v108\n"
    "v_mfma_f32_16x16x32_bf16 v[168:171], a[144:147], a[48:51], v[168:171]\n"
    "v_exp_f32 v109, v109\n"
    "v_mfma_f32_16x16x32_bf16 v[168:171], a[140:143], a[44:47], v[168:171]\n"
    "s_add_u32 m0, s69, 12288\n"
    "s_add_u32 s57, s55, 16384\n"
    "v_mul_f32 v115, s72, v115\n"
    "v_mfma_f32_16x16x32_bf16 v[168:171], a[136:139], a[20:23], v[168:171]\n"
    "buffer_load_dwordx4 %[od1], %[srdX], s57 offen  lds\n"
    "v_mul_f32 v120, s72, v120\n"
    "v_mfma_f32_16x16x32_bf16 v[168:171], a[132:135], a[16:19], v[168:171]\n"
    "v_exp_f32 v110, v110\n"
    "v_mfma_f32_16x16x32_bf16 v[168:171], a[128:131], a[12:15], v[168:171]\n"
    "v_exp_f32 v111, v111\n"
    "v_mfma_f32_16x16x32_bf16 v[172:175], a[152:155], a[12:15], v[172:175]\n"
    "ds_read_b128 a[12:15], %[rd0] offset:6144\n"
    "v_mul_f32 v121, s72, v121\n"
    "v_mfma_f32_16x16x32_bf16 v[172:175], a[156:159], a[16:19], v[172:175]\n"
    "v_exp_f32_e64 v20, -abs(v96)\n"
    "v_mfma_f32_16x16x32_bf16 v[172:175], a[160:163], a[20:23], v[172:175]\n"
    "v_exp_f32_e64 v21, -abs(v97)\n"
    "v_mfma_f32_16x16x32_bf16 v[172:175], a[164:167], a[44:47], v[172:175]\n"
    "ds_read_b128 a[44:47], %[rd1] offset:6144\n"
    "v_mul_f32 v122, s72, v122\n"
    "v_mfma_f32_16x16x32_bf16 v[172:175], a[168:171], a[48:51], v[172:175]\n"
    "v_exp_f32_e64 v22, -abs(v98)\n"
    "v_mfma_f32_16x16x32_bf16 v[172:175], a[172:175], a[52:55], v[172:175]\n"
    "v_exp_f32_e64 v23, -abs(v99)\n"
    "v_mfma_f32_16x16x32_bf16 v[176:179], a[148:151], a[56:59], v[176:179]\n"
    "v_bfi_b32 v96, s61, 0.5, v96\n"
    "v_bfi_b32 v97, s61, 0.5, v97\n"
    "v_mfma_f32_16x16x32_bf16 v[176:179], a[144:147], a[52:55], v[176:179]\n"
    "v_bfi_b32 v98, s61, 0.5, v98\n"
    "v_bfi_b32 v99, s61, 0.5, v99\n"
    "v_mfma_f32_16x16x32_bf16 v[176:179], a[140:143], a[48:51], v[176:179]\n"
    "s_add_u32 m0, s69, 16384\n"
    "s_add_u32 s57, s55, 24576\n"
    "v_fma_f32 v96, v20, v96, v96\n"
    "v_mfma_f32_16x16x32_bf16 v[176:179], a[136:139], a[24:27], v[176:179]\n"
    "buffer_load_dwordx4 %[od1], %[srdX], s57 offen  lds\n"
    "v_fma_f32 v97, v21, v97, v97\n"
    "v_mfma_f32_16x16x32_bf16 v[176:179], a[132:135], a[20:23], v[176:179]\n"
    "v_fma_f32 v98, v22, v98, v98\n"
    "v_fma_f32 v99, v23, v99, v99\n"
    "v_mfma_f32_16x16x32_bf16 v[176:179], a[128:131], a[16:19], v[176:179]\n"
    "v_exp_f32_e64 v20, -abs(v104)\n"
    "v_mfma_f32_16x16x32_bf16 v[180:183], a[152:155], a[16:19], v[180:183]\n"
    "ds_read_b128 a[16:19], %[rd0] offset:8192\n"
    "v_mul_f32 v123, s72, v123\n"
    "v_mfma_f32_16x16x32_bf16 v[180:183], a[156:159], a[20:23], v[180:183]\n"
    "v_exp_f32_e64 v21, -abs(v105)\n"
    "v_mfma_f32_16x16x32_bf16 v[180:183], a[160:163], a[24:27], v[180:183]\n"
    "v_exp_f32_e64 v22, -abs(v106)\n"
    "v_mfma_f32_16x16x32_bf16 v[180:183], a[164:167], a[48:51], v[180:183]\n"
    "ds_read_b128 a[48:51], %[rd1] offset:8192\n"
    "v_mfma_f32_16x16x32_bf16 v[180:183], a[168:171], a[52:55], v[180:183]\n"
    "v_exp_f32_e64 v23, -abs(v107)\n"
    "v_mfma_f32_16x16x32_bf16 v[180:183], a[172:175], a[56:59], v[180:183]\n"
    "v_exp_f32 v116, v116\n"
    "v_mfma_f32_16x16x32_bf16 v[184:187], a[148:151], a[60:63], v[184:187]\n"
    "v_exp_f32 v117, v117\n"
    "v_mfma_f32_16x16x32_bf16 v[184:187], a[144:147], a[56:59], v[184:187]\n"
    "v_exp_f32 v118, v118\n"
    "v_mfma_f32_16x16x32_bf16 v[184:187], a[140:143], a[52:55], v[184:187]\n"
    "s_add_u32 m0, s69, 20480\n"
    "s_add_u32 s57, s55, 32768\n"
    "v_bfi_b32 v104, s61, 0.5, v104\n"
    "v_mfma_f32_16x16x32_bf16 v[184:187], a[136:139], a[28:31], v[184:187]\n"
    "buffer_load_dwordx4 %[od1], %[srdX], s57 offen  lds\n"
    "v_bfi_b32 v105, s61, 0.5, v105\n"
    "v_mfma_f32_16x16x32_bf16 v[184:187], a[132:135], a[24:27], v[184:187]\n"
    "v_exp_f32 v119, v119\n"
    "v_mfma_f32_16x16x32_bf16 v[184:187], a[128:131], a[20:23], v[184:187]\n"
    "v_exp_f32 v124, v124\n"
    "v_mfma_f32_16x16x32_bf16 v[188:191], a[152:155], a[20:23], v[188:191]\n"
    "ds_read_b128 a[20:23], %[rd0] offset:10240\n"
    "v_bfi_b32 v106, s61, 0.5, v106\n"
    "v_mfma_f32_16x16x32_bf16 v[188:191], a[156:159], a[24:27], v[188:191]\n"
    "v_exp_f32 v125, v125\n"
    "v_mfma_f32_16x16x32_bf16 v[188:191], a[160:163], a[28:31], v[188:191]\n"
    "v_exp_f32 v126, v126\n"
    "v_mfma_f32_16x16x32_bf16 v[188:191], a[164:167], a[52:55], v[188:191]\n"
    "ds_read_b128 a[52:55], %[rd1] offset:10240\n"
    "v_bfi_b32 v107, s61, 0.5, v107\n"
    "v_mfma_f32_16x16x32_bf16 v[188:191], a[168:171], a[56:59], v[188:191]\n"
    "v_exp_f32 v127, v127\n"
    "v_mfma_f32_16x16x32_bf16 v[188:191], a[172:175], a[60:63], v[188:191]\n"
    "v_exp_f32_e64 v16, -abs(v112)\n"
    "v_mfma_f32_16x16x32_bf16 v[192:195], a[148:151], a[72:75], v[192:195]\n"
    "v_exp_f32_e64 v17, -abs(v113)\n"
    "v_mfma_f32_16x16x32_bf16 v[192:195], a[144:147], a[60:63], v[192:195]\n"
    "v_exp_f32_e64 v18, -abs(v114)\n"
    "v_mfma_f32_16x16x32_bf16 v[192:195], a[140:143], a[56:59], v[192:195]\n"
    "s_add_u32 m0, s69, 24576\n"
    "s_add_u32 s57, s55, 40960\n"
    "v_fma_f32 v104, v20, v104, v104\n"
    "v_mfma_f32_16x16x32_bf16 v[192:195], a[136:139], a[64:67], v[192:195]\n"
    "buffer_load_dwordx4 %[od1], %[srdX], s57 offen  lds\n"
    "v_fma_f32 v105, v21, v105, v105\n"
    "v_mfma_f32_16x16x32_bf16 v[192:195], a[132:135], a[28:31], v[192:195]\n"
    "v_exp_f32_e64 v19, -abs(v115)\n"
    "v_mfma_f32_16x16x32_bf16 v[192:195], a[128:131], a[24:27], v[192:195]\n"
    "v_fma_f32 v106, v22, v106, v106\n"
    "v_fma_f32 v107, v23, v107, v107\n"
    "v_mfma_f32_16x16x32_bf16 v[196:199], a[152:155], a[24:27], v[196:199]\n"
    "ds_read_b128 a[24:27], %[rd0] offset:12288\n"
    "v_fma_f32 v100, v100, v96, v96\n"
    "v_mfma_f32_16x16x32_bf16 v[196:199], a[156:159], a[28:31], v[196:199]\n"
    "v_fma_f32 v101, v101, v97, v97\n"
    "v_fma_f32 v102, v102, v98, v98\n"
    "v_mfma_f32_16x16x32_bf16 v[196:199], a[160:163], a[64:67], v[196:199]\n"
    "v_fma_f32 v103, v103, v99, v99\n"
    "v_fma_f32 v108, v108, v104, v104\n"
    "v_mfma_f32_16x16x32_bf16 v[196:199], a[164:167], a[56:59], v[196:199]\n"
    "ds_read_b128 a[56:59], %[rd1] offset:12288\n"
    "v_fma_f32 v109, v109, v105, v105\n"
    "v_mfma_f32_16x16x32_bf16 v[196:199], a[168:171], a[60:63], v[196:199]\n"
    "v_fma_f32 v110, v110, v106, v106\n"
    "v_fma_f32 v111, v111, v107, v107\n"
    "v_mfma_f32_16x16x32_bf16 v[196:199], a[172:175], a[72:75], v[196:199]\n"
    "v_rcp_f32 v100, v100\n"
    "v_mfma_f32_16x16x32_bf16 v[200:203], a[148:151], a[76:79], v[200:203]\n"
    "v_rcp_f32 v101, v101\n"
    "v_mfma_f32_16x16x32_bf16 v[200:203], a[144:147], a[72:75], v[200:203]\n"
    "v_rcp_f32 v102, v102\n"
    "v_mfma_f32_16x16x32_bf16 v[200:203], a[140:143], a[60:63], v[200:203]\n"
    "s_add_u32 m0, s69, 28672\n"
    "s_add_u32 s57, s55, 49152\n"
    "v_bfi_b32 v112, s61, 0.5, v112\n"
    "v_mfma_f32_16x16x32_bf16 v[200:203], a[136:139], a[68:71], v[200:203]\n"
    "buffer_load_dwordx4 %[od1], %[srdX], s57 offen  lds\n"
    "v_bfi_b32 v113, s61, 0.5, v113\n"
    "v_mfma_f32_16x16x32_bf16 v[200:203], a[132:135], a[64:67], v[200:203]\n"
    "v_rcp_f32 v103, v103\n"
    "v_mfma_f32_16x16x32_bf16 v[200:203], a[128:131], a[28:31], v[200:203]\n"
    "s_add_u32 s69, s69, 0x8000\n"
    "s_and_b32 s69, s69, 0x1ffff\n"
    "v_bfi_b32 v114, s61, 0.5, v114\n"
    "v_mfma_f32_16x16x32_bf16 v[204:207], a[152:155], a[28:31], v[204:207]\n"
    "ds_read_b128 a[28:31], %[rd0] offset:14336\n"
    "v_bfi_b32 v115, s61, 0.5, v115\n"
    "v_mfma_f32_16x16x32_bf16 v[204:207], a[156:159], a[64:67], v[204:207]\n"
    "s_add_u32 s55, s55, 128\n"
    "v_rcp_f32 v108, v108\n"
    "v_mfma_f32_16x16x32_bf16 v[204:207], a[160:163], a[68:71], v[204:207]\n"
    "v_rcp_f32 v109, v109\n"
    "v_mfma_f32_16x16x32_bf16 v[204:207], a[164:167], a[60:63], v[204:207]\n"
    "ds_read_b128 a[60:63], %[rd1] offset:14336\n"
    "v_fma_f32 v112, v16, v112, v112\n"
    "v_mfma_f32_16x16x32_bf16 v[204:207], a[168:171], a[72:75], v[204:207]\n"
    "v_rcp_f32 v110, v110\n"
    "v_mfma_f32_16x16x32_bf16 v[204:207], a[172:175], a[76:79], v[204:207]\n"
    "s_nop 7\n"
    "s_mov_b32 s67, s66\n"
    "s_add_u32 s62, s62, 1\n"
    "s_cmp_eq_u32 s62, 1\n"
    "s_cbranch_scc1 Ltile%=\n"
    "v_subrev_u32 v12, 36864, %[vsw]\n"
    "v_subrev_u32 v13, 0x8000, v14\n"
    "v_rcp_f32 v111, v111\n"
    "v_fma_f32 v100, -abs(v96), v100, v100\n"
    "v_fma_f32 v101, -abs(v97), v101, v101\n"
    "v_fma_f32 v102, -abs(v98), v102, v102\n"
    "v_fma_f32 v103, -abs(v99), v103, v103\n"
    "v_fma_f32 v108, -abs(v104), v108, v108\n"
    "v_fma_f32 v109, -abs(v105), v109, v109\n"
    "v_fma_f32 v110, -abs(v106), v110, v110\n"
    "v_fma_f32 v111, -abs(v107), v111, v111\n"
    "v_cvt_pk_bf16_f32 v20, v100, v101\n"
    "v_cvt_pk_bf16_f32 v21, v102, v103\n"
    "v_cvt_pk_bf16_f32 v22, v108, v109\n"
    "v_cvt_pk_bf16_f32 v23, v110, v111\n"
    "v_fma_f32 v113, v17, v113, v113\n"
    "v_fma_f32 v114, v18, v114, v114\n"
    "v_fma_f32 v115, v19, v115, v115\n"
    "v_exp_f32_e64 v16, -abs(v120)\n"
    "v_exp_f32_e64 v17, -abs(v121)\n"
    "v_exp_f32_e64 v18, -abs(v122)\n"
    "v_exp_f32_e64 v19, -abs(v123)\n"
    "v_bfi_b32 v120, s61, 0.5, v120\n"
    "v_bfi_b32 v121, s61, 0.5, v121\n"
    "v_bfi_b32 v122, s61, 0.5, v122\n"
    "v_bfi_b32 v123, s61, 0.5, v123\n"
    "v_fma_f32 v120, v16, v120, v120\n"
    "v_fma_f32 v121, v17, v121, v121\n"
    "v_fma_f32 v122, v18, v122, v122\n"
    "v_fma_f32 v123, v19, v123, v123\n"
    "v_fma_f32 v116, v116, v112, v112\n"
    "v_fma_f32 v117, v117, v113, v113\n"
    "v_fma_f32 v118, v118, v114, v114\n"
    "v_fma_f32 v119, v119, v115, v115\n"
    "v_fma_f32 v124, v124, v120, v120\n"
    "v_fma_f32 v125, v125, v121, v121\n"
    "v_fma_f32 v126, v126, v122, v122\n"
    "v_fma_f32 v127, v127, v123, v123\n"
    "v_rcp_f32 v116, v116\n"
    "v_rcp_f32 v117, v117\n"
    "v_rcp_f32 v118, v118\n"
    "v_rcp_f32 v119, v119\n"
    "v_rcp_f32 v124, v124\n"
    "v_rcp_f32 v125, v125\n"
    "v_rcp_f32 v126, v126\n"
    "v_rcp_f32 v127, v127\n"
    "v_fma_f32 v116, -abs(v112), v116, v116\n"
    "v_fma_f32 v117, -abs(v113), v117, v117\n"
    "v_fma_f32 v118, -abs(v114), v118, v118\n"
    "v_fma_f32 v119, -abs(v115), v119, v119\n"
    "v_fma_f32 v124, -abs(v120), v124, v124\n"
    "v_fma_f32 v125, -abs(v121), v125, v125\n"
    "v_fma_f32 v126, -abs(v122), v126, v126\n"
    "v_fma_f32 v127, -abs(v123), v127, v127\n"
    "v_cvt_pk_bf16_f32 v16, v116, v117\n"
    "v_cvt_pk_bf16_f32 v17, v118, v119\n"
    "v_cvt_pk_bf16_f32 v18, v124, v125\n"
    "v_cvt_pk_bf16_f32 v19, v126, v127\n"
    "ds_write_b64 v12, v[20:21] offset:0\n"
    "ds_write_b64 v12, v[22:23] offset:128\n"
    "ds_write_b64 v12, v[16:17] offset:4096\n"
    "ds_write_b64 v12, v[18:19] offset:4224\n"
    "v_pk_mul_f32 v[132:133], v[132:133], s[72:73] op_sel:[0,1]\n"
    "v_pk_mul_f32 v[134:135], v[134:135], s[72:73] op_sel:[0,1]\n"
    "v_pk_mul_f32 v[140:141], v[140:141], s[72:73] op_sel:[0,1]\n"
    "v_pk_mul_f32 v[142:143], v[142:143], s[72:73] op_sel:[0,1]\n"
    "v_pk_mul_f32 v[248:249], v[128:129], s[72:73] op_sel_hi:[1,0]\n"
    "v_pk_mul_f32 v[250:251], v[130:131], s[72:73] op_sel_hi:[1,0]\n"
    "v_pk_mul_f32 v[252:253], v[136:137], s[72:73] op_sel_hi:[1,0]\n"
    "v_pk_mul_f32 v[254:255], v[138:139], s[72:73] op_sel_hi:[1,0]\n"
    "v_exp_f32_e64 v248, -abs(v248)\n"
    "v_exp_f32_e64 v249, -abs(v249)\n"
    "v_exp_f32_e64 v250, -abs(v250)\n"
    "v_exp_f32_e64 v251, -abs(v251)\n"
    "v_exp_f32_e64 v252, -abs(v252)\n"
    "v_exp_f32_e64 v253, -abs(v253)\n"
    "v_exp_f32_e64 v254, -abs(v254)\n"
    "v_exp_f32_e64 v255, -abs(v255)\n"
    "v_exp_f32 v132, v132\n"
    "v_exp_f32 v133, v133\n"
    "v_exp_f32 v134, v134\n"
    "v_exp_f32 v135, v135\n"
    "v_exp_f32 v140, v140\n"
    "v_exp_f32 v141, v141\n"
    "v_exp_f32 v142, v142\n"
    "v_exp_f32 v143, v143\n"
    "v_pk_fma_f32 v[248:249], v[248:249], 0.5, 0.5 op_sel_hi:[1,0,0]\n"
    "v_pk_fma_f32 v[250:251], v[250:251], 0.5, 0.5 op_sel_hi:[1,0,0]\n"
    "v_pk_fma_f32 v[252:253], v[252:253], 0.5, 0.5 op_sel_hi:[1,0,0]\n"
    "v_pk_fma_f32 v[254:255], v[254:255], 0.5, 0.5 op_sel_hi:[1,0,0]\n"
    "v_pk_fma_f32 v[132:133], v[132:133], v[248:249], v[248:249]\n"
    "v_pk_fma_f32 v[134:135], v[134:135], v[250:251], v[250:251]\n"
    "v_pk_fma_f32 v[140:141], v[140:141], v[252:253], v[252:253]\n"
    "v_pk_fma_f32 v[142:143], v[142:143], v[254:255], v[254:255]\n"
    "v_rcp_f32 v132, v132\n"
    "v_rcp_f32 v133, v133\n"
    "v_rcp_f32 v134, v134\n"
    "v_rcp_f32 v135, v135\n"
    "v_rcp_f32 v140, v140\n"
    "v_rcp_f32 v141, v141\n"
    "v_rcp_f32 v142, v142\n"
    "v_rcp_f32 v143, v143\n"
    "v_pk_fma_f32 v[132:133], v[248:249], v[132:133], v[132:133] neg_lo:[1,0,0] neg_hi:[1,0,0]\n"
    "v_pk_fma_f32 v[134:135], v[250:251], v[134:135], v[134:135] neg_lo:[1,0,0] neg_hi:[1,0,0]\n"
    "v_pk_fma_f32 v[140:141], v[252:253], v[140:141], v[140:141] neg_lo:[1,0,0] neg_hi:[1,0,0]\n"
    "v_pk_fma_f32 v[142:143], v[254:255], v[142:143], v[142:143] neg_lo:[1,0,0] neg_hi:[1,0,0]\n"
    "v_bfi_b32 v132, s61, v132, v128\n"
    "v_bfi_b32 v133, s61, v133, v129\n"
    "v_bfi_b32 v134, s61, v134, v130\n"
    "v_bfi_b32 v135, s61, v135, v131\n"
    "v_bfi_b32 v140, s61, v140, v136\n"
    "v_bfi_b32 v141, s61, v141, v137\n"
    "v_bfi_b32 v142, s61, v142, v138\n"
    "v_bfi_b32 v143, s61, v143, v139\n"
    "v_cvt_pk_bf16_f32 v20, v132, v133\n"
    "v_cvt_pk_bf16_f32 v21, v134, v135\n"
    "v_cvt_pk_bf16_f32 v22, v140, v141\n"
    "v_cvt_pk_bf16_f32 v23, v142, v143\n"
    "v_pk_mul_f32 v[148:149], v[148:149], s[72:73] op_sel:[0,1]\n"
    "v_pk_mul_f32 v[150:151], v[150:151], s[72:73] op_sel:[0,1]\n"
    "ds_write_b64 v12, v[20:21] offset:8192\n"
    "ds_write_b64 v12, v[22:23] offset:8320\n"
    "v_pk_mul_f32 v[156:157], v[156:157], s[72:73] op_sel:[0,1]\n"
    "v_pk_mul_f32 v[158:159], v[158:159], s[72:73] op_sel:[0,1]\n"
    "v_pk_mul_f32 v[248:249], v[144:145], s[72:73] op_sel_hi:[1,0]\n"
    "v_pk_mul_f32 v[250:251], v[146:147], s[72:73] op_sel_hi:[1,0]\n"
    "v_pk_mul_f32 v[252:253], v[152:153], s[72:73] op_sel_hi:[1,0]\n"
    "v_pk_mul_f32 v[254:255], v[154:155], s[72:73] op_sel_hi:[1,0]\n"
    "v_exp_f32_e64 v248, -abs(v248)\n"
    "v_exp_f32_e64 v249, -abs(v249)\n"
    "v_exp_f32_e64 v250, -abs(v250)\n"
    "v_exp_f32_e64 v251, -abs(v251)\n"
    "v_exp_f32_e64 v252, -abs(v252)\n"
    "v_exp_f32_e64 v253, -abs(v253)\n"
    "v_exp_f32_e64 v254, -abs(v254)\n"
    "v_exp_f32_e64 v255, -abs(v255)\n"
    "v_exp_f32 v148, v148\n"
    "v_exp_f32 v149, v149\n"
    "v_exp_f32 v150, v150\n"
    "v_exp_f32 v151, v151\n"
    "v_exp_f32 v156, v156\n"
    "v_exp_f32 v157, v157\n"
    "v_exp_f32 v158, v158\n"
    "v_exp_f32 v159, v159\n"
    "v_pk_fma_f32 v[248:249], v[248:249], 0.5, 0.5 op_sel_hi:[1,0,0]\n"
    "v_pk_fma_f32 v[250:251], v[250:251], 0.5, 0.5 op_sel_hi:[1,0,0]\n"
    "v_pk_fma_f32 v[252:253], v[252:253], 0.5, 0.5 op_sel_hi:[1,0,0]\n"
    "v_pk_fma_f32 v[254:255], v[254:255], 0.5, 0.5 op_sel_hi:[1,0,0]\n"
    "v_pk_fma_f32 v[148:149], v[148:149], v[248:249], v[248:249]\n"
    "v_pk_fma_f32 v[150:151], v[150:151], v[250:251], v[250:251]\n"
    "v_pk_fma_f32 v[156:157], v[156:157], v[252:253], v[252:253]\n"
    "v_pk_fma_f32 v[158:159], v[158:159], v[254:255], v[254:255]\n"
    "v_rcp_f32 v148, v148\n"
    "v_rcp_f32 v149, v149\n"
    "v_rcp_f32 v150, v150\n"
    "v_rcp_f32 v151, v151\n"
    "v_rcp_f32 v156, v156\n"
    "v_rcp_f32 v157, v157\n"
    "v_rcp_f32 v158, v158\n"
    "v_rcp_f32 v159, v159\n"
    "v_pk_fma_f32 v[148:149], v[248:249], v[148:149], v[148:149] neg_lo:[1,0,0] neg_hi:[1,0,0]\n"
    "v_pk_fma_f32 v[150:151], v[250:251], v[150:151], v[150:151] neg_lo:[1,0,0] neg_hi:[1,0,0]\n"
    "v_pk_fma_f32 v[156:157], v[252:253], v[156:157], v[156:157] neg_lo:[1,0,0] neg_hi:[1,0,0]\n"
    "v_pk_fma_f32 v[158:159], v[254:255], v[158:159], v[158:159] neg_lo:[1,0,0] neg_hi:[1,0,0]\n"
    "v_bfi_b32 v148, s61, v148, v144\n"
    "v_bfi_b32 v149, s61, v149, v145\n"
    "v_bfi_b32 v150, s61, v150, v146\n"
    "v_bfi_b32 v151, s61, v151, v147\n"
    "v_bfi_b32 v156, s61, v156, v152\n"
    "v_bfi_b32 v157, s61, v157, v153\n"
    "v_bfi_b32 v158, s61, v158, v154\n"
    "v_bfi_b32 v159, s61, v159, v155\n"
    "v_cvt_pk_bf16_f32 v16, v148, v149\n"
    "v_cvt_pk_bf16_f32 v17, v150, v151\n"
    "v_cvt_pk_bf16_f32 v18, v156, v157\n"
    "v_cvt_pk_bf16_f32 v19, v158, v159\n"
    "v_pk_mul_f32 v[164:165], v[164:165], s[72:73] op_sel:[0,1]\n"
    "v_pk_mul_f32 v[166:167], v[166:167], s[72:73] op_sel:[0,1]\n"
    "ds_write_b64 v12, v[16:17] offset:12288\n"
    "ds_write_b64 v12, v[18:19] offset:12416\n"
    "v_pk_mul_f32 v[172:173], v[172:173], s[72:73] op_sel:[0,1]\n"
    "v_pk_mul_f32 v[174:175], v[174:175], s[72:73] op_sel:[0,1]\n"
    "v_pk_mul_f32 v[248:249], v[160:161], s[72:73] op_sel_hi:[1,0]\n"
    "v_pk_mul_f32 v[250:251], v[162:163], s[72:73] op_sel_hi:[1,0]\n"
    "v_pk_mul_f32 v[252:253], v[168:169], s[72:73] op_sel_hi:[1,0]\n"
    "v_pk_mul_f32 v[254:255], v[170:171], s[72:73] op_sel_hi:[1,0]\n"
    "v_exp_f32_e64 v248, -abs(v248)\n"
    "v_exp_f32_e64 v249, -abs(v249)\n"
    "v_exp_f32_e64 v250, -abs(v250)\n"
    "v_exp_f32_e64 v251, -abs(v251)\n"
    "v_exp_f32_e64 v252, -abs(v252)\n"
    "v_exp_f32_e64 v253, -abs(v253)\n"
    "s_waitcnt lgkmcnt(0)\n"
    "s_barrier\n"
    "ds_read_b128 v[208:211], v14 offset:4096\n"
    "ds_read_b128 v[212:215], v13 offset:0\n"
    "ds_read_b128 v[216:219], v13 offset:4096\n"
    "ds_read_b128 v[220:223], v13 offset:8192\n"
    "ds_read_b128 v[224:227], v13 offset:12288\n"
    "v_exp_f32_e64 v254, -abs(v254)\n"
    "v_exp_f32_e64 v255, -abs(v255)\n"
    "v_exp_f32 v164, v164\n"
    "v_exp_f32 v165, v165\n"
    "v_exp_f32 v166, v166\n"
    "v_exp_f32 v167, v167\n"
    "v_exp_f32 v172, v172\n"
    "v_exp_f32 v173, v173\n"
    "v_exp_f32 v174, v174\n"
    "v_exp_f32 v175, v175\n"
    "v_pk_fma_f32 v[248:249], v[248:249], 0.5, 0.5 op_sel_hi:[1,0,0]\n"
    "v_pk_fma_f32 v[250:251], v[250:251], 0.5, 0.5 op_sel_hi:[1,0,0]\n"
    "v_pk_fma_f32 v[252:253], v[252:253], 0.5, 0.5 op_sel_hi:[1,0,0]\n"
    "v_pk_fma_f32 v[254:255], v[254:255], 0.5, 0.5 op_sel_hi:[1,0,0]\n"
    "v_pk_fma_f32 v[164:165], v[164:165], v[248:249], v[248:249]\n"
    "v_pk_fma_f32 v[166:167], v[166:167], v[250:251], v[250:251]\n"
    "s_waitcnt lgkmcnt(4)\n"
    "s_mov_b32 s60, 0\n"
    "buffer_store_dwordx4 v[208:211], v15, s[76:79], s60 offen sc0 sc1\n"
    "v_pk_fma_f32 v[172:173], v[172:173], v[252:253], v[252:253]\n"
    "v_pk_fma_f32 v[174:175], v[174:175], v[254:255], v[254:255]\n"
    "v_rcp_f32 v164, v164\n"
    "v_rcp_f32 v165, v165\n"
    "v_rcp_f32 v166, v166\n"
    "v_rcp_f32 v167, v167\n"
    "v_rcp_f32 v172, v172\n"
    "v_rcp_f32 v173, v173\n"
    "v_rcp_f32 v174, v174\n"
    "v_rcp_f32 v175, v175\n"
    "s_waitcnt lgkmcnt(3)\n"
    "s_mov_b32 s60, 8192\n"
    "buffer_store_dwordx4 v[212:215], %[oo], s[76:79], s60 offen sc0 sc1\n"
    "v_pk_fma_f32 v[164:165], v[248:249], v[164:165], v[164:165] neg_lo:[1,0,0] neg_hi:[1,0,0]\n"
    "v_pk_fma_f32 v[166:167], v[250:251], v[166:167], v[166:167] neg_lo:[1,0,0] neg_hi:[1,0,0]\n"
    "v_pk_fma_f32 v[172:173], v[252:253], v[172:173], v[172:173] neg_lo:[1,0,0] neg_hi:[1,0,0]\n"
    "v_pk_fma_f32 v[174:175], v[254:255], v[174:175], v[174:175] neg_lo:[1,0,0] neg_hi:[1,0,0]\n"
    "v_bfi_b32 v164, s61, v164, v160\n"
    "v_bfi_b32 v165, s61, v165, v161\n"
    "v_bfi_b32 v166, s61, v166, v162\n"
    "v_bfi_b32 v167, s61, v167, v163\n"
    "v_bfi_b32 v172, s61, v172, v168\n"
    "v_bfi_b32 v173, s61, v173, v169\n"
    "s_waitcnt lgkmcnt(2)\n"
    "s_mov_b32 s60, 16384\n"
    "buffer_store_dwordx4 v[216:219], %[oo], s[76:79], s60 offen sc0 sc1\n"
    "v_bfi_b32 v174, s61, v174, v170\n"
    "v_bfi_b32 v175, s61, v175, v171\n"
    "v_cvt_pk_bf16_f32 v20, v164, v165\n"
    "v_cvt_pk_bf16_f32 v21, v166, v167\n"
    "v_cvt_pk_bf16_f32 v22, v172, v173\n"
    "v_cvt_pk_bf16_f32 v23, v174, v175\n"
    "v_pk_mul_f32 v[180:181], v[180:181], s[72:73] op_sel:[0,1]\n"
    "v_pk_mul_f32 v[182:183], v[182:183], s[72:73] op_sel:[0,1]\n"
    "ds_write_b64 v12, v[20:21] offset:16384\n"
    "ds_write_b64 v12, v[22:23] offset:16512\n"
    "v_pk_mul_f32 v[188:189], v[188:189], s[72:73] op_sel:[0,1]\n"
    "v_pk_mul_f32 v[190:191], v[190:191], s[72:73] op_sel:[0,1]\n"
    "v_pk_mul_f32 v[248:249], v[176:177], s[72:73] op_sel_hi:[1,0]\n"
    "v_pk_mul_f32 v[250:251], v[178:179], s[72:73] op_sel_hi:[1,0]\n"
    "v_pk_mul_f32 v[252:253], v[184:185], s[72:73] op_sel_hi:[1,0]\n"
    "v_pk_mul_f32 v[254:255], v[186:187], s[72:73] op_sel_hi:[1,0]\n"
    "v_exp_f32_e64 v248, -abs(v248)\n"
    "v_exp_f32_e64 v249, -abs(v249)\n"
    "s_waitcnt lgkmcnt(3)\n"
    "s_mov_b32 s60, 24576\n"
    "buffer_store_dwordx4 v[220:223], %[oo], s[76:79], s60 offen sc0 sc1\n"
    "v_exp_f32_e64 v250, -abs(v250)\n"
    "v_exp_f32_e64 v251, -abs(v251)\n"
    "v_exp_f32_e64 v252, -abs(v252)\n"
    "v_exp_f32_e64 v253, -abs(v253)\n"
    "v_exp_f32_e64 v254, -abs(v254)\n"
    "v_exp_f32_e64 v255, -abs(v255)\n"
    "v_exp_f32 v180, v180\n"
    "v_exp_f32 v181, v181\n"
    "v_exp_f32 v182, v182\n"
    "v_exp_f32 v183, v183\n"
    "s_waitcnt lgkmcnt(2)\n"
    "s_mov_b32 s60, 32768\n"
    "buffer_store_dwordx4 v[224:227], %[oo], s[76:79], s60 offen sc0 sc1\n"
    "v_exp_f32 v188, v188\n"
    "v_exp_f32 v189, v189\n"
    "v_exp_f32 v190, v190\n"
    "v_exp_f32 v191, v191\n"
    "v_pk_fma_f32 v[248:249], v[248:249], 0.5, 0.5 op_sel_hi:[1,0,0]\n"
    "v_pk_fma_f32 v[250:251], v[250:251], 0.5, 0.5 op_sel_hi:[1,0,0]\n"
    "v_pk_fma_f32 v[252:253], v[252:253], 0.5, 0.5 op_sel_hi:[1,0,0]\n"
    "v_pk_fma_f32 v[254:255], v[254:255], 0.5, 0.5 op_sel_hi:[1,0,0]\n"
    "v_pk_fma_f32 v[180:181], v[180:181], v[248:249], v[248:249]\n"
    "v_pk_fma_f32 v[182:183], v[182:183], v[250:251], v[250:251]\n"
    "v_pk_fma_f32 v[188:189], v[188:189], v[252:253], v[252:253]\n"
    "v_pk_fma_f32 v[190:191], v[190:191], v[254:255], v[254:255]\n"
    "v_rcp_f32 v180, v180\n"
    "v_rcp_f32 v181, v181\n"
    "v_rcp_f32 v182, v182\n"
    "v_rcp_f32 v183, v183\n"
    "v_rcp_f32 v188, v188\n"
    "v_rcp_f32 v189, v189\n"
    "v_rcp_f32 v190, v190\n"
    "v_rcp_f32 v191, v191\n"
    "v_pk_fma_f32 v[180:181], v[248:249], v[180:181], v[180:181] neg_lo:[1,0,0] neg_hi:[1,0,0]\n"
    "v_pk_fma_f32 v[182:183], v[250:251], v[182:183], v[182:183] neg_lo:[1,0,0] neg_hi:[1,0,0]\n"
    "v_pk_fma_f32 v[188:189], v[252:253], v[188:189], v[188:189] neg_lo:[1,0,0] neg_hi:[1,0,0]\n"
    "v_pk_fma_f32 v[190:191], v[254:255], v[190:191], v[190:191] neg_lo:[1,0,0] neg_hi:[1,0,0]\n"
    "v_bfi_b32 v180, s61, v180, v176\n"
    "v_bfi_b32 v181, s61, v181, v177\n"
    "v_bfi_b32 v182, s61, v182, v178\n"
    "v_bfi_b32 v183, s61, v183, v179\n"
    "v_bfi_b32 v188, s61, v188, v184\n"
    "v_bfi_b32 v189, s61, v189, v185\n"
    "v_bfi_b32 v190, s61, v190, v186\n"
    "v_bfi_b32 v191, s61, v191, v187\n"
    "v_cvt_pk_bf16_f32 v16, v180, v181\n"
    "v_cvt_pk_bf16_f32 v17, v182, v183\n"
    "v_cvt_pk_bf16_f32 v18, v188, v189\n"
    "v_cvt_pk_bf16_f32 v19, v190, v191\n"
    "v_pk_mul_f32 v[196:197], v[196:197], s[72:73] op_sel:[0,1]\n"
    "v_pk_mul_f32 v[198:199], v[198:199], s[72:73] op_sel:[0,1]\n"
    "ds_write_b64 v12, v[16:17] offset:20480\n"
    "ds_write_b64 v12, v[18:19] offset:20608\n"
    "v_pk_mul_f32 v[204:205], v[204:205], s[72:73] op_sel:[0,1]\n"
    "v_pk_mul_f32 v[206:207], v[206:207], s[72:73] op_sel:[0,1]\n"
    "v_pk_mul_f32 v[248:249], v[192:193], s[72:73] op_sel_hi:[1,0]\n"
    "v_pk_mul_f32 v[250:251], v[194:195], s[72:73] op_sel_hi:[1,0]\n"
    "v_pk_mul_f32 v[252:253], v[200:201], s[72:73] op_sel_hi:[1,0]\n"
    "v_pk_mul_f32 v[254:255], v[202:203], s[72:73] op_sel_hi:[1,0]\n"
    "v_exp_f32_e64 v248, -abs(v248)\n"
    "v_exp_f32_e64 v249, -abs(v249)\n"
    "v_exp_f32_e64 v250, -abs(v250)\n"
    "v_exp_f32_e64 v251, -abs(v251)\n"
    "v_exp_f32_e64 v252, -abs(v252)\n"
    "v_exp_f32_e64 v253, -abs(v253)\n"
    "s_waitcnt lgkmcnt(0)\n"
    "s_barrier\n"
    "ds_read_b128 v[208:211], v13 offset:16384\n"
    "ds_read_b128 v[212:215], v13 offset:20480\n"
    "v_exp_f32_e64 v254, -abs(v254)\n"
    "v_exp_f32_e64 v255, -abs(v255)\n"
    "v_exp_f32 v196, v196\n"
    "v_exp_f32 v197, v197\n"
    "v_exp_f32 v198, v198\n"
    "v_exp_f32 v199, v199\n"
    "v_exp_f32 v204, v204\n"
    "v_exp_f32 v205, v205\n"
    "v_exp_f32 v206, v206\n"
    "v_exp_f32 v207, v207\n"
    "v_pk_fma_f32 v[248:249], v[248:249], 0.5, 0.5 op_sel_hi:[1,0,0]\n"
    "v_pk_fma_f32 v[250:251], v[250:251], 0.5, 0.5 op_sel_hi:[1,0,0]\n"
    "v_pk_fma_f32 v[252:253], v[252:253], 0.5, 0.5 op_sel_hi:[1,0,0]\n"
    "v_pk_fma_f32 v[254:255], v[254:255], 0.5, 0.5 op_sel_hi:[1,0,0]\n"
    "v_pk_fma_f32 v[196:197], v[196:197], v[248:249], v[248:249]\n"
    "v_pk_fma_f32 v[198:199], v[198:199], v[250:251], v[250:251]\n"
    "s_waitcnt lgkmcnt(1)\n"
    "s_mov_b32 s60, 40960\n"
    "buffer_store_dwordx4 v[208:211], %[oo], s[76:79], s60 offen sc0 sc1\n"
    "v_pk_fma_f32 v[204:205], v[204:205], v[252:253], v[252:253]\n"
    "v_pk_fma_f32 v[206:207], v[206:207], v[254:255], v[254:255]\n"
    "v_rcp_f32 v196, v196\n"
    "v_rcp_f32 v197, v197\n"
    "v_rcp_f32 v198, v198\n"
    "v_rcp_f32 v199, v199\n"
    "v_rcp_f32 v204, v204\n"
    "v_rcp_f32 v205, v205\n"
    "v_rcp_f32 v206, v206\n"
    "v_rcp_f32 v207, v207\n"
    "s_waitcnt lgkmcnt(0)\n"
    "s_mov_b32 s60, 49152\n"
    "buffer_store_dwordx4 v[212:215], %[oo], s[76:79], s60 offen sc0 sc1\n"
    "v_pk_fma_f32 v[196:197], v[248:249], v[196:197], v[196:197] neg_lo:[1,0,0] neg_hi:[1,0,0]\n"
    "v_pk_fma_f32 v[198:199], v[250:251], v[198:199], v[198:199] neg_lo:[1,0,0] neg_hi:[1,0,0]\n"
    "v_pk_fma_f32 v[204:205], v[252:253], v[204:205], v[204:205] neg_lo:[1,0,0] neg_hi:[1,0,0]\n"
    "v_pk_fma_f32 v[206:207], v[254:255], v[206:207], v[206:207] neg_lo:[1,0,0] neg_hi:[1,0,0]\n"
    "v_bfi_b32 v196, s61, v196, v192\n"
    "v_bfi_b32 v197, s61, v197, v193\n"
    "v_bfi_b32 v198, s61, v198, v194\n"
    "v_bfi_b32 v199, s61, v199, v195\n"
    "v_bfi_b32 v204, s61, v204, v200\n"
    "v_bfi_b32 v205, s61, v205, v201\n"
    "v_bfi_b32 v206, s61, v206, v202\n"
    "v_bfi_b32 v207, s61, v207, v203\n"
    "v_cvt_pk_bf16_f32 v20, v196, v197\n"
    "v_cvt_pk_bf16_f32 v21, v198, v199\n"
    "v_cvt_pk_bf16_f32 v22, v204, v205\n"
    "v_cvt_pk_bf16_f32 v23, v206, v207\n"
    "ds_write_b64 v12, v[20:21] offset:24576\n"
    "ds_write_b64 v12, v[22:23] offset:24704\n"
    "s_waitcnt lgkmcnt(0)\n"
    "s_barrier\n"
    "ds_read_b128 v[208:211], v13 offset:24576\n"
    "s_waitcnt lgkmcnt(0)\n"
    "s_mov_b32 s60, 57344\n"
    "buffer_store_dwordx4 v[208:211], %[oo], s[76:79], s60 offen sc0 sc1\n"
    "s_waitcnt vmcnt(0)\n"
    "s_branch Lend%=\n"
    "Ls1%=:\n"
    "ds_read_b32 v13, v12\n"
    "s_waitcnt lgkmcnt(0)\n"
    "v_cmp_gt_u32 vcc, s63, v13\n"
    "s_nop 1\n"
    "s_cbranch_vccnz Ls1%=\n"
    "s_branch Lb1%=\n"
    "Ls2%=:\n"
    "ds_read_b32 v13, v12\n"
    "s_waitcnt lgkmcnt(0)\n"
    "v_cmp_gt_u32 vcc, s63, v13\n"
    "s_nop 1\n"
    "s_cbranch_vccnz Ls2%=\n"
    "s_branch Lb2%=\n"
    "Ls3%=:\n"
    "ds_read_b32 v13, v12\n"
    "s_waitcnt lgkmcnt(0)\n"
    "v_cmp_gt_u32 vcc, s63, v13\n"
    "s_nop 1\n"
    "s_cbranch_vccnz Ls3%=\n"
    "s_branch Lb3%=\n"
    "Ls4%=:\n"
    "ds_read_b32 v13, v12\n"
    "s_waitcnt lgkmcnt(0)\n"
    "v_cmp_gt_u32 vcc, s63, v13\n"
    "s_nop 1\n"
    "s_cbranch_vccnz Ls4%=\n"
    "s_branch Lb4%=\n"
    "Ls5%=:\n"
    "ds_read_b32 v13, v12\n"
    "s_waitcnt lgkmcnt(0)\n"
    "v_cmp_gt_u32 vcc, s63, v13\n"
    "s_nop 1\n"
    "s_cbranch_vccnz Ls5%=\n"
    "s_branch Lb5%=\n"
    "Ls6%=:\n"
    "ds_read_b32 v13, v12\n"
    "s_waitcnt lgkmcnt(0)\n"
    "v_cmp_gt_u32 vcc, s63, v13\n"
    "s_nop 1\n"
    "s_cbranch_vccnz Ls6%=\n"
    "s_branch Lb6%=\n"
    "Ls7%=:\n"
    "ds_read_b32 v13, v12\n"
    "s_waitcnt lgkmcnt(0)\n"
    "v_cmp_gt_u32 vcc, s63, v13\n"
    "s_nop 1\n"
    "s_cbranch_vccnz Ls7%=\n"
    "s_branch Lb7%=\n"
    "Ls8%=:\n"
    "ds_read_b32 v13, v12\n"
    "s_waitcnt lgkmcnt(0)\n"
    "v_cmp_gt_u32 vcc, s63, v13\n"
    "s_nop 1\n"
    "s_cbranch_vccnz Ls8%=\n"
    "s_branch Lb8%=\n"
    "Ls9%=:\n"
    "ds_read_b32 v13, v12\n"
    "s_waitcnt lgkmcnt(0)\n"
    "v_cmp_gt_u32 vcc, s63, v13\n"
    "s_nop 1\n"
    "s_cbranch_vccnz Ls9%=\n"
    "s_branch Lb9%=\n"
    "Ls10%=:\n"
    "ds_read_b32 v13, v12\n"
    "s_waitcnt lgkmcnt(0)\n"
    "v_cmp_gt_u32 vcc, s63, v13\n"
    "s_nop 1\n"
    "s_cbranch_vccnz Ls10%=\n"
    "s_branch Lb10%=\n"
    "Ls11%=:\n"
    "ds_read_b32 v13, v12\n"
    "s_waitcnt lgkmcnt(0)\n"
    "v_cmp_gt_u32 vcc, s63, v13\n"
    "s_nop 1\n"
    "s_cbranch_vccnz Ls11%=\n"
    "s_branch Lb11%=\n"
    "Ls12%=:\n"
    "ds_read_b32 v13, v12\n"
    "s_waitcnt lgkmcnt(0)\n"
    "v_cmp_gt_u32 vcc, s63, v13\n"
    "s_nop 1\n"
    "s_cbranch_vccnz Ls12%=\n"
    "s_branch Lb12%=\n"
    "Ls13%=:\n"
    "ds_read_b32 v13, v12\n"
    "s_waitcnt lgkmcnt(0)\n"
    "v_cmp_gt_u32 vcc, s63, v13\n"
    "s_nop 1\n"
    "s_cbranch_vccnz Ls13%=\n"
    "s_branch Lb13%=\n"
    "Ls14%=:\n"
    "ds_read_b32 v13, v12\n"
    "s_waitcnt lgkmcnt(0)\n"
    "v_cmp_gt_u32 vcc, s63, v13\n"
    "s_nop 1\n"
    "s_cbranch_vccnz Ls14%=\n"
    "s_branch Lb14%=\n"
    "Ls15%=:\n"
    "ds_read_b32 v13, v12\n"
    "s_waitcnt lgkmcnt(0)\n"
    "v_cmp_gt_u32 vcc, s63, v13\n"
    "s_nop 1\n"
    "s_cbranch_vccnz Ls15%=\n"
    "s_branch Lb15%=\n"
    "Ls16%=:\n"
    "ds_read_b32 v13, v12\n"
    "s_waitcnt lgkmcnt(0)\n"
    "v_cmp_gt_u32 vcc, s63, v13\n"
    "s_nop 1\n"
    "s_cbranch_vccnz Ls16%=\n"
    "s_branch Lb16%=\n"
    "Ls17%=:\n"
    "ds_read_b32 v13, v12\n"
    "s_waitcnt lgkmcnt(0)\n"
    "v_cmp_gt_u32 vcc, s63, v13\n"
    "s_nop 1\n"
    "s_cbranch_vccnz Ls17%=\n"
    "s_branch Lb17%=\n"
    "Ls18%=:\n"
    "ds_read_b32 v13, v12\n"
    "s_waitcnt lgkmcnt(0)\n"
    "v_cmp_gt_u32 vcc, s63, v13\n"
    "s_nop 1\n"
    "s_cbranch_vccnz Ls18%=\n"
    "s_branch Lb18%=\n"
    "Ls19%=:\n"
    "ds_read_b32 v13, v12\n"
    "s_waitcnt lgkmcnt(0)\n"
    "v_cmp_gt_u32 vcc, s63, v13\n"
    "s_nop 1\n"
    "s_cbranch_vccnz Ls19%=\n"
    "s_branch Lb19%=\n"
    "Ls20%=:\n"
    "ds_read_b32 v13, v12\n"
    "s_waitcnt lgkmcnt(0)\n"
    "v_cmp_gt_u32 vcc, s63, v13\n"
    "s_nop 1\n"
    "s_cbranch_vccnz Ls20%=\n"
    "s_branch Lb20%=\n"
    "Ls41%=:\n"
    "ds_read_b32 v13, v12\n"
    "s_waitcnt lgkmcnt(0)\n"
    "v_cmp_gt_u32 vcc, s63, v13\n"
    "s_nop 1\n"
    "s_cbranch_vccnz Ls41%=\n"
    "s_branch Lb41%=\n"
    "Ls42%=:\n"
    "ds_read_b32 v13, v12\n"
    "s_waitcnt lgkmcnt(0)\n"
    "v_cmp_gt_u32 vcc, s63, v13\n"
    "s_nop 1\n"
    "s_cbranch_vccnz Ls42%=\n"
    "s_branch Lb42%=\n"
    "Ls43%=:\n"
    "ds_read_b32 v13, v12\n"
    "s_waitcnt lgkmcnt(0)\n"
    "v_cmp_gt_u32 vcc, s63, v13\n"
    "s_nop 1\n"
    "s_cbranch_vccnz Ls43%=\n"
    "s_branch Lb43%=\n"
    "Ls44%=:\n"
    "ds_read_b32 v13, v12\n"
    "s_waitcnt lgkmcnt(0)\n"
    "v_cmp_gt_u32 vcc, s63, v13\n"
    "s_nop 1\n"
    "s_cbranch_vccnz Ls44%=\n"
    "s_branch Lb44%=\n"
    "Lps%=:\n"
    "s_mov_b32 s60, 0x4000\n"
    "s_lshl_b32 s58, s62, 6\n"
    "Lpl%=:\n"
    "s_sleep 8\n"
    "buffer_load_dword v79, v78, %[srdW], s58 offen offset:0 sc0 sc1\n"
    "s_waitcnt vmcnt(0)\n"
    "s_sub_u32 s60, s60, 1\n"
    "s_cbranch_scc1 Lpb%=\n"
    "v_cmp_gt_u32 vcc, 32, v79\n"
    "s_nop 1\n"
    "s_cbranch_vccnz Lpl%=\n"
    "s_branch Lpb%=\n"
    "Lqs%=:\n"
    "s_mov_b32 s60, 0x4000\n"
    "s_lshl_b32 s58, s62, 6\n"
    "Lql%=:\n"
    "s_sleep 8\n"
    "buffer_load_dword v79, v78, %[srdW], s58 offen offset:128 sc0 sc1\n"
    "s_waitcnt vmcnt(0)\n"
    "s_sub_u32 s60, s60, 1\n"
    "s_cbranch_scc1 Lqb%=\n"
    "v_cmp_gt_u32 vcc, 32, v79\n"
    "s_nop 1\n"
    "s_cbranch_vccnz Lql%=\n"
    "s_branch Lqb%=\n"
    "Lend%=:\n"
    : [rd0] "+v"(rd0), [rd1] "+v"(rd1), [re0] "+v"(re0), [re1] "+v"(re1), [od1] "+v"(od1),
      [xr1] "+v"(xr1), [oo] "+v"(oo)
    : [l16] "v"(l16), [vsw] "v"(vsw), [vst] "v"(vst),
      [srdX] "s"(srdX), [srdW] "s"(srdW), [so0] "s"(so0), [so1] "s"(so1),
      [m0b] "s"(m0b), [wso] "s"(wso), [w8k] "s"(wv * 8192u), [wv8] "s"(wv * 8u), [side] "s"(side), [srdD] "s"(srdD), [prs] "s"((rs + 1u) * 4096u), [pcnt] "s"(32u * 262144u + 256u + grp * 256u), [flb] "s"(ldsb + 131072u), [flw] "s"(ldsb + 131072u + wv * 1024u)
    : "v12", "v13", "v14", "v15", "v16", "v17", "v18", "v19", "v20", "v21", "v22", "v23", "v24", "v25", "v26", "v27", "v28", "v29", "v30", "v31", "v32", "v33", "v34", "v35", "v36", "v37", "v38", "v39", "v40", "v41", "v42", "v43", "v44", "v45", "v46", "v47", "v48", "v49", "v50", "v51", "v52", "v53", "v54", "v55", "v56", "v57", "v58", "v59", "v60", "v61", "v62", "v63", "v64", "v65", "v66", "v67", "v68", "v69", "v70", "v71", "v72", "v73", "v74", "v75", "v76", "v77", "v78", "v79", "v80", "v81", "v82", "v83", "v84", "v85", "v86", "v87", "v88", "v89", "v90", "v91", "v92", "v93", "v94", "v95", "v96", "v97", "v98", "v99", "v100", "v101", "v102", "v103", "v104", "v105", "v106", "v107", "v108", "v109", "v110", "v111", "v112", "v113", "v114", "v115", "v116", "v117", "v118", "v119", "v120", "v121", "v122", "v123", "v124", "v125", "v126", "v127", "v128", "v129", "v130", "v131", "v132", "v133", "v134", "v135", "v136", "v137", "v138", "v139", "v140", "v141", "v142", "v143", "v144", "v145", "v146", "v147", "v148", "v149", "v150", "v151", "v152", "v153", "v154", "v155", "v156", "v157", "v158", "v159", "v160", "v161", "v162", "v163", "v164", "v165", "v166", "v167", "v168", "v169", "v170", "v171", "v172", "v173", "v174", "v175", "v176", "v177", "v178", "v179", "v180", "v181", "v182", "v183", "v184", "v185", "v186", "v187", "v188", "v189", "v190", "v191", "v192", "v193", "v194", "v195", "v196", "v197", "v198", "v199", "v200", "v201", "v202", "v203", "v204", "v205", "v206", "v207", "v208", "v209", "v210", "v211", "v212", "v213", "v214", "v215", "v216", "v217", "v218", "v219", "v220", "v221", "v222", "v223", "v224", "v225", "v226", "v227", "v228", "v229", "v230", "v231", "v232", "v233", "v234", "v235", "v236", "v237", "v238", "v239", "v240", "v241", "v242", "v243", "v244", "v245", "v246", "v247", "v248", "v249", "v250", "v251", "v252", "v253", "v254", "v255", "a0", "a1", "a2", "a3", "a4", "a5", "a6", "a7", "a8", "a9", "a10", "a11", "a12", "a13", "a14", "a15", "a16", "a17", "a18", "a19", "a20", "a21", "a22", "a23", "a24", "a25", "a26", "a27", "a28", "a29", "a30", "a31", "a32", "a33", "a34", "a35", "a36", "a37", "a38", "a39", "a40", "a41", "a42", "a43", "a44", "a45", "a46", "a47", "a48", "a49", "a50", "a51", "a52", "a53", "a54", "a55", "a56", "a57", "a58", "a59", "a60", "a61", "a62", "a63", "a64", "a65", "a66", "a67", "a68", "a69", "a70", "a71", "a72", "a73", "a74", "a75", "a76", "a77", "a78", "a79", "a80", "a81", "a82", "a83", "a84", "a85", "a86", "a87", "a88", "a89", "a90", "a91", "a92", "a93", "a94", "a95", "a96", "a97", "a98", "a99", "a100", "a101", "a102", "a103", "a104", "a105", "a106", "a107", "a108", "a109", "a110", "a111", "a112", "a113", "a114", "a115", "a116", "a117", "a118", "a119", "a120", "a121", "a122", "a123", "a124", "a125", "a126", "a127", "a128", "a129", "a130", "a131", "a132", "a133", "a134", "a135", "a136", "a137", "a138", "a139", "a140", "a141", "a142", "a143", "a144", "a145", "a146", "a147", "a148", "a149", "a150", "a151", "a152", "a153", "a154", "a155", "a156", "a157", "a158", "a159", "a160", "a161", "a162", "a163", "a164", "a165", "a166", "a167", "a168", "a169", "a170", "a171", "a172", "a173", "a174", "a175", "a176", "a177", "a178", "a179", "a180", "a181", "a182", "a183", "a184", "a185", "a186", "a187", "a188", "a189", "a190", "a191", "a192", "a193", "a194", "a195", "a196", "a197", "a198", "a199", "a200", "a201", "a202", "a203", "a204", "a205", "a206", "a207", "a208", "a209", "a210", "a211", "a212", "a213", "a214", "a215", "a216", "a217", "a218", "a219", "a220", "a221", "a222", "a223", "a224", "a225", "a226", "a227", "a228", "a229", "a230", "a231", "a232", "a233", "a234", "a235", "a236", "a237", "a238", "a239", "a240", "a241", "a242", "a243", "a244", "a245", "a246", "a247", "a248", "a249", "a250", "a251", "a252", "a253", "a254", "a255", "s54", "s55", "s56", "s57", "s58", "s59", "s60", "s61", "s62", "s63", "s64", "s65", "s66", "s67", "s68", "s69", "s70", "s71", "s72", "s73", "s74", "s75", "s76", "s77", "s78", "s79", "scc", "vcc", "memory");
}

#define PREP_CALL (void)0
extern "C" void kernel_launch(void* const* d_in, const int* in_sizes, int n_in,
                              void* d_out, int out_size, void* d_ws, size_t ws_size,
                              hipStream_t stream) {
    (void)hipFuncSetAttribute((const void*)conv_main_kernel, hipFuncAttributeMaxDynamicSharedMemorySize, 163840);
    u16* wt = (u16*)((char*)d_ws + ((ws_size - (44u << 20)) & ~(size_t)4095));
    wprep_kernel<<<384, 256, 0, stream>>>((const u16*)d_in[1], wt);
    conv_main_kernel<<<256, 256, 163840, stream>>>((const u16*)d_in[0], wt, (u16*)d_out);
}
```
